```python
import torch

def _softmax(x, axis=-1):
    return torch.softmax(x, dim=axis)

def _gelu(x):
    return torch.nn.functional.gelu(x, approximate='tanh')

def _silu(x):
    return torch.nn.functional.silu(x)

def _sigmoid(x):
    return torch.sigmoid(x)

def _layernorm(x, axis=-1, eps=1e-05):
    m = x.mean(dim=axis, keepdim=True)
    v = x.var(dim=axis, keepdim=True, unbiased=False)
    return (x - m) / torch.sqrt(v + eps)

def _scatter_add(tgt, idx, src, axis=0):
    return tgt.clone().index_add_(axis, idx, src)

def _segment_sum(src, seg, n):
    out = torch.zeros((n,) + src.shape[1:], dtype=src.dtype)
    return out.index_add_(0, seg, src)

def _pad(x, pw):
    flat = tuple((v for pair in reversed(pw) for v in pair))
    return torch.nn.functional.pad(x, flat)
kernel_name = 'standalone__mixer_gmlp__prompt'

def setup_inputs():

    _w = lambda a: (lambda t: t.to(torch.bfloat16) if t.is_floating_point() else t)(torch.as_tensor(a))
    B, S, D, F = (1, 2048, 576, 4608)
    Fh = F // 2
    return {'x': _w((lambda *s: torch.randn(*s, dtype=torch.bfloat16) * 0.05)(B, S, D)), 'w_in': _w((lambda *s: torch.randn(*s, dtype=torch.bfloat16) * 0.4)(D, 2 * Fh)), 'w_sp': _w(0.01 * (lambda *s: torch.randn(*s, dtype=torch.bfloat16) * 0.4)(S, S)), 'b_sp': _w(1.0 + 0.01 * (lambda *s: torch.randn(*s, dtype=torch.bfloat16) * 0.05)(S)), 'w_out': _w((lambda *s: torch.randn(*s, dtype=torch.bfloat16) * 0.05)(Fh, D))}

def reference(x, w_in, w_sp, b_sp, w_out):
    B, S, D = x.shape
    F = w_in.shape[-1]
    Fh = F // 2
    z = _gelu(torch.einsum('bsd,df->bsf', x, w_in))
    z1 = z[..., :Fh].clone()
    z2 = _layernorm(z[..., Fh:], axis=-1)
    g = torch.einsum('ts,bsf->btf', w_sp, z2) + b_sp[None, :, None]
    return torch.einsum('bsf,fd->bsd', z1 * g, w_out)
```

```cpp
#include <hip/hip_runtime.h>
#include <hip/hip_bf16.h>
typedef unsigned short u16;
typedef short bf16x8 __attribute__((ext_vector_type(8)));
typedef float f32x4 __attribute__((ext_vector_type(4)));
__device__ __forceinline__ float bf2f(u16 b) { return __uint_as_float(((unsigned)b) << 16); }
__device__ __forceinline__ u16 f2bf(float f) { unsigned u = __float_as_uint(f); u += 0x7FFFu + ((u >> 16) & 1u); return (u16)(u >> 16); }
__device__ __forceinline__ float gelu(float x) {
  const float k1 = -2.0f * 0.7978845608028654f * 1.4426950408889634f, k2 = k1 * 0.044715f;
  float e = __builtin_amdgcn_exp2f(x * (k1 + k2 * x * x));
  return x * __builtin_amdgcn_rcpf(1.0f + e);
}
template <int MODE>
__global__ void gemm(const u16* A, const u16* B, u16* C, int M, int N, int K, int lda, int ldb, int ldc, const u16* z1, int ldz, const u16* bias) {
  int wave = (blockIdx.x * blockDim.x + threadIdx.x) >> 6, lane = threadIdx.x & 63;
  int nt = N / 16, tm = wave / nt, tn = wave % nt;
  f32x4 acc = {0, 0, 0, 0};
  int r = lane & 15, q = lane >> 4;
  for (int k = 0; k < K; k += 32) {
    bf16x8 a = *(const bf16x8*)(A + (size_t)(tm * 16 + r) * lda + k + q * 8);
    bf16x8 b;
    for (int j = 0; j < 8; ++j) b[j] = (short)B[(size_t)(k + q * 8 + j) * ldb + tn * 16 + r];
    acc = __builtin_amdgcn_mfma_f32_16x16x32_bf16(a, b, acc, 0, 0, 0);
  }
  for (int j = 0; j < 4; ++j) {
    int row = tm * 16 + q * 4 + j, col = tn * 16 + r;
    float v = acc[j];
    if (MODE == 0) v = gelu(bf2f(f2bf(v)));
    if (MODE == 1) v = bf2f(z1[(size_t)row * ldz + col]) * bf2f(f2bf(bf2f(f2bf(v)) + bf2f(bias[row])));
    C[(size_t)row * ldc + col] = f2bf(v);
  }
}
__global__ void ln(const u16* Z, u16* Z2, int ldz, int off, int n) {
  int row = blockIdx.x; __shared__ float red[256];
  float s = 0; for (int i = threadIdx.x; i < n; i += 256) s += bf2f(Z[(size_t)row * ldz + off + i]);
  red[threadIdx.x] = s; __syncthreads(); for (int o = 128; o; o >>= 1) { if (threadIdx.x < o) red[threadIdx.x] += red[threadIdx.x + o]; __syncthreads(); }
  float m = red[0] / n; __syncthreads();
  float q = 0; for (int i = threadIdx.x; i < n; i += 256) { float d = bf2f(Z[(size_t)row * ldz + off + i]) - m; q += d * d; }
  red[threadIdx.x] = q; __syncthreads(); for (int o = 128; o; o >>= 1) { if (threadIdx.x < o) red[threadIdx.x] += red[threadIdx.x + o]; __syncthreads(); }
  float rs = rsqrtf(red[0] / n + 1e-5f);
  for (int i = threadIdx.x; i < n; i += 256) Z2[(size_t)row * n + i] = f2bf((bf2f(Z[(size_t)row * ldz + off + i]) - m) * rs);
}
extern "C" void kernel_launch(void* const* d_in, const int* in_sizes, int n_in, void* d_out, int out_size, void* d_ws, size_t ws_size, hipStream_t stream) {
  const u16 *x = (const u16*)d_in[0], *w_in = (const u16*)d_in[1], *w_sp = (const u16*)d_in[2], *b_sp = (const u16*)d_in[3], *w_out = (const u16*)d_in[4];
  const int S = 2048, D = 576, F = 4608, Fh = 2304;
  u16* Z = (u16*)d_ws; u16* Z2 = Z + (size_t)S * F; u16* P = Z2 + (size_t)S * Fh;
  gemm<0><<<(S / 16) * (F / 16) / 4, 256, 0, stream>>>(x, w_in, Z, S, F, D, D, F, F, nullptr, 0, nullptr);
  ln<<<S, 256, 0, stream>>>(Z, Z2, F, Fh, Fh);
  gemm<1><<<(S / 16) * (Fh / 16) / 4, 256, 0, stream>>>(w_sp, Z2, P, S, Fh, S, S, Fh, Fh, Z, F, b_sp);
  gemm<2><<<(S / 16) * (D / 16) / 4, 256, 0, stream>>>(P, w_out, (u16*)d_out, S, D, Fh, Fh, D, D, nullptr, 0, nullptr);
}
```

```cpp
#include <hip/hip_runtime.h>
#include <hip/hip_bf16.h>
typedef unsigned short u16;
typedef short s16x4 __attribute__((ext_vector_type(4)));
typedef short bf16x8 __attribute__((ext_vector_type(8)));
typedef float f32x2 __attribute__((ext_vector_type(2)));
typedef float f32x4 __attribute__((ext_vector_type(4)));
typedef unsigned u32x4 __attribute__((ext_vector_type(4)));
typedef unsigned u32x2 __attribute__((ext_vector_type(2)));
#define LAS __attribute__((address_space(3)))
#define DEV __device__ __forceinline__
#define BAR() __builtin_amdgcn_s_barrier()
#define SB() __builtin_amdgcn_sched_barrier(0)
#define MEMB() asm volatile("" ::: "memory")
#define WAITL0() __builtin_amdgcn_s_waitcnt(0xC07F)
#define WV(N) asm volatile("s_waitcnt vmcnt(%0)" ::"i"(N) : "memory")
#define MFMA(a, b, c) __builtin_amdgcn_mfma_f32_16x16x32_bf16(a, b, c, 0, 0, 0)
#define MFMA16(a, b, c) __builtin_amdgcn_mfma_f32_16x16x16bf16_1k(a, b, c, 0, 0, 0)
#define GL(R, D, V, S, I) __builtin_amdgcn_raw_ptr_buffer_load_lds(R, (LAS void*)(size_t)(D), 16, V, S, I, 0)
#define GL4(R, D, V0, V1, V2, V3, S) do { GL(R, D, V0, S, 0); GL(R, D, V1, S, 1024); GL(R, D, V2, S, 2048); GL(R, D, V3, S, 3072); } while (0)
#define RSRC(p, n) __builtin_amdgcn_make_buffer_rsrc((void*)(p), 0, (n), 0x00020000)

constexpr int CH = 4608;
constexpr int FRAG_BYTES = 16 * 128 * CH;
constexpr size_t WS_Z2 = 0, WS_Z1 = FRAG_BYTES, WS_P = 2 * (size_t)FRAG_BYTES, WS_ST = 3 * (size_t)FRAG_BYTES, WS_EP = WS_ST + 16 * 16 * 128 * 16;
constexpr int STG1 = 53248, STG2 = 34816, STG3 = 41984;

DEV unsigned cvt_pk(float lo, float hi) { unsigned r; asm("v_cvt_pk_bf16_f32 %0, %1, %2" : "=v"(r) : "v"(lo), "v"(hi)); return r; }
DEV bf16x8 ld128(unsigned a) { return *(LAS bf16x8*)(size_t)a; }
DEV s16x4 ld64(unsigned a) { return *(LAS s16x4*)(size_t)a; }
DEV s16x4 ldtr(unsigned a) { return __builtin_amdgcn_ds_read_tr16_b64_v4i16((LAS s16x4*)(size_t)a); }
DEV bf16x8 cat(s16x4 a, s16x4 b) { return __builtin_shufflevector(a, b, 0, 1, 2, 3, 4, 5, 6, 7); }
DEV float bflo(unsigned v) { return __uint_as_float(v << 16); }
DEV float bfhi(unsigned v) { return __uint_as_float(v & 0xffff0000u); }
DEV f32x2 gelu2(f32x2 x) {
  const float k1 = -2.0f * 0.7978845608028654f * 1.4426950408889634f, k2 = k1 * 0.044715f;
  f32x2 t = x * (x * x * k2 + k1), e;
  e.x = __builtin_amdgcn_exp2f(t.x); e.y = __builtin_amdgcn_exp2f(t.y);
  e = e + 1.0f;
  e.x = __builtin_amdgcn_rcpf(e.x); e.y = __builtin_amdgcn_rcpf(e.y);
  return x * e;
}
DEV f32x4 gelu4(f32x4 v) { f32x2 a = gelu2((f32x2){v[0], v[1]}), b = gelu2((f32x2){v[2], v[3]}); return (f32x4){a.x, a.y, b.x, b.y}; }
DEV void tile_of(int bid, int& il, int& jl, int& ti, int& fj) { const int x = bid & 7, r = bid >> 3; il = r & 7; jl = r >> 3; ti = (x & 1) * 8 + il; fj = (x >> 1) * 4 + jl; }

__global__ __launch_bounds__(512, 2) void k1_gemm_in(const char* __restrict__ X, const char* __restrict__ Win, char* __restrict__ ws) {
  extern __shared__ __attribute__((aligned(16))) char smem[];
  const int tid = threadIdx.x, wid = __builtin_amdgcn_readfirstlane(tid >> 6), lane = tid & 63;
  int il, jl, ti, fj; tile_of(blockIdx.x, il, jl, ti, fj);
  const unsigned lds0 = (unsigned)(size_t)(LAS char*)smem;
  if (wid >= 4) {
    const int lw = wid - 4;
    auto rX = RSRC(X, 2048 * 1152); auto rW = RSRC(Win, 576 * 9216);
    unsigned vX[4], vW[9];
#pragma unroll
    for (int q = 0; q < 4; ++q) { const int row = 8 * q + (lane >> 3); vX[q] = row * 1152 + (((lane & 7) ^ ((row >> 1) & 7)) << 4) - q * 1024; }
#pragma unroll
    for (int q = 0; q < 9; ++q) { const int b = q * 1024 + lane * 16, R = b / 288, c = b - R * 288, k = 8 * ((R >> 2) & 3) + 4 * (R >> 4) + (R & 3); vW[q] = k * 9216 + c - (q % 3) * 1024; }
    const unsigned sX = (ti * 128 + lw * 32) * 1152, sW = (lw & 1) * 32 * 9216 + (lw >> 1) * 4608 + fj * 288;
    const unsigned dX = lds0 + lw * 4096, dW = lds0 + 16384 + lw * 9216 - (lw & 1) * 9216 * 0;
    const int rot = il + 2 * jl;
    auto issue = [&](int k, int slot) {
      int kk = k + rot; kk = kk >= 9 ? kk - 9 : kk; kk = kk >= 9 ? kk - 9 : kk;
      const unsigned so = slot * STG1, sx = sX + kk * 128, sw = sW + kk * 64 * 9216;
      GL(rW, dW + so, vW[0], sw, 0); GL(rW, dW + so, vW[1], sw, 1024); GL(rW, dW + so, vW[2], sw, 2048);
      GL4(rX, dX + so, vX[0], vX[1], vX[2], vX[3], sx);
      GL(rW, dW + so + 3072, vW[3], sw, 0); GL(rW, dW + so + 3072, vW[4], sw, 1024); GL(rW, dW + so + 3072, vW[5], sw, 2048);
      GL(rW, dW + so + 6144, vW[6], sw, 0); GL(rW, dW + so + 6144, vW[7], sw, 1024); GL(rW, dW + so + 6144, vW[8], sw, 2048);
    };
    issue(0, 0); issue(1, 1); issue(2, 2);
    const unsigned long long* epw = (const unsigned long long*)(ws + WS_EP + ti * 256);
    WV(26); BAR();
    int slot = 0;
    for (int k = 1; k < 9; ++k) {
      if (k < 8) WV(13); else WV(0);
      BAR();
      if (k + 2 < 9) issue(k + 2, slot);
      slot = slot == 2 ? 0 : slot + 1;
    }
    const unsigned long long ep = __hip_atomic_load(epw, __ATOMIC_RELAXED, __HIP_MEMORY_SCOPE_AGENT);
    const unsigned tagA = (unsigned)ep + 0x9E3779B9u, tagB = (unsigned)(ep >> 32) + 0x85EBCA6Bu;
    BAR();
    BAR();
    auto rS = RSRC(ws + WS_ST, 16 * 16 * 128 * 16);
    if (lw < 2) {
      const int row = lw * 64 + lane;
      f32x2 s = {0.f, 0.f};
#pragma unroll
      for (int q = 0; q < 4; ++q) { f32x4 v = *(LAS f32x4*)(size_t)(lds0 + row * 64 + q * 16); s.x += v[0] + v[2]; s.y += v[1] + v[3]; }
      u32x4 g; g.x = tagA; g.y = __float_as_uint(s.x); g.z = tagB; g.w = __float_as_uint(s.y);
      __builtin_amdgcn_raw_buffer_store_b128(g, rS, ((ti * 16 + fj) * 128 + row) * 16, 0, 16);
      float s1, s2;
      for (unsigned spins = 0;; ++spins) {
        u32x4 v[16]; bool ok = true; s1 = 0.f; s2 = 0.f;
        MEMB();
#pragma unroll
        for (int p = 0; p < 16; ++p) v[p] = __builtin_amdgcn_raw_buffer_load_b128(rS, row * 16, (ti * 16 + p) * 2048, 16);
#pragma unroll
        for (int p = 0; p < 16; ++p) { ok &= (v[p].x == tagA) & (v[p].z == tagB); s1 += __uint_as_float(v[p].y); s2 += __uint_as_float(v[p].w); }
        if (__all(ok)) break;
        if (spins > (1u << 20)) { s1 = __builtin_nanf(""); break; }
        __builtin_amdgcn_s_sleep(2);
      }
      const float mean = s1 * (1.0f / 2304.0f), var = fmaxf(s2 * (1.0f / 2304.0f) - mean * mean, 0.f), rs = __builtin_amdgcn_rsqf(var + 1e-5f);
      *(LAS f32x2*)(size_t)(lds0 + 8192 + row * 8) = (f32x2){rs, -mean * rs};
      asm volatile("s_waitcnt lgkmcnt(0)" ::: "memory");
    }
    BAR();
    if (fj == 0 && tid == 256) __hip_atomic_store((unsigned long long*)(ws + WS_EP + ti * 256), ((unsigned long long)tagB << 32) | tagA, __ATOMIC_RELAXED, __HIP_MEMORY_SCOPE_AGENT);
  } else {
    const int wt = wid >> 1, wf = wid & 1;
    const int r16 = lane & 15, g = lane >> 4, qr = (lane >> 2) & 3, p = lane & 3;
    f32x4 acc[4][9];
#pragma unroll
    for (int m = 0; m < 4; ++m)
#pragma unroll
      for (int n = 0; n < 9; ++n) acc[m][n] = (f32x4){0, 0, 0, 0};
    const unsigned bX = lds0 + (64 * wt + r16) * 128 + ((g ^ (r16 >> 1)) << 4);
    const unsigned bW = lds0 + 16384 + (4 * g + qr) * 288 + p * 8 + wf * 128;
    const unsigned bE = lds0 + 16384 + (4 * g + qr) * 288 + p * 8 + wf * 18432 + 256;
    unsigned aX0 = bX, aX1 = bX ^ 64, aW = bW, aE = bE;
    bf16x8 xb[2][4]; s16x4 wb[6][2];
    auto rdW = [&](int G, unsigned w, unsigned e, int u) {
      const int kk = G / 9, n = G % 9;
      const unsigned a = n < 4 ? w + kk * 9216 + u * 4608 + n * 32 : n < 8 ? w + 18432 + kk * 9216 + u * 4608 + (n - 4) * 32 : e + kk * 9216 + u * 4608;
      wb[G % 6][u] = ldtr(a);
    };
    WAITL0();
    BAR();
    MEMB();
#pragma unroll
    for (int m = 0; m < 4; ++m) xb[0][m] = ld128(aX0 + m * 2048);
    rdW(0, aW, aE, 0); rdW(0, aW, aE, 1); rdW(1, aW, aE, 0); rdW(1, aW, aE, 1);
    unsigned so = 0;
    for (int k = 0; k < 9; ++k) {
      so = so == 2 * STG1 ? 0 : so + STG1;
      unsigned nX0 = bX + so, nX1 = (bX ^ 64) + so, nW = bW + so, nE = bE + so;
      SB();
#pragma unroll
      for (int G = 0; G < 18; ++G) {
        const int kk = G / 9, n = G % 9;
#pragma unroll
        for (int m = 0; m < 4; ++m) {
          acc[m][n] = MFMA(cat(wb[G % 6][0], wb[G % 6][1]), xb[kk][m], acc[m][n]);
          SB();
          if (G == 0) rdW(2 + (m >> 1), aW, aE, m & 1);
          else if (G <= 14) { if (m < 2) rdW(G + 3, aW, aE, m); else if (m == 2 && G >= 2 && G <= 5) xb[1][G - 2] = ld128(aX1 + (G - 2) * 2048); }
          else if (G >= 16) { if (m < 2) xb[0][(G - 16) * 2 + m] = ld128(nX0 + ((G - 16) * 2 + m) * 2048); else rdW(G - 16, nW, nE, m - 2); }
          SB();
        }
        if (G == 15) { WAITL0(); SB(); BAR(); MEMB(); SB(); }
      }
      aX0 = nX0; aX1 = nX1; aW = nW; aE = nE;
    }
    f32x2 s1[4], s2[4];
#pragma unroll
    for (int m = 0; m < 4; ++m) {
      s1[m] = (f32x2){0.f, 0.f}; s2[m] = (f32x2){0.f, 0.f};
#pragma unroll
      for (int n = 4; n < 8; ++n) { acc[m][n] = gelu4(acc[m][n]); f32x2 a = {acc[m][n][0], acc[m][n][1]}, b = {acc[m][n][2], acc[m][n][3]}; s1[m] += a + b; s2[m] += a * a; s2[m] += b * b; }
    }
    if (wf) {
#pragma unroll
      for (int m = 0; m < 4; ++m) { acc[m][8] = gelu4(acc[m][8]); f32x2 a = {acc[m][8][0], acc[m][8][1]}, b = {acc[m][8][2], acc[m][8][3]}; s1[m] += a + b; s2[m] += a * a; s2[m] += b * b; }
    }
    WAITL0();
#pragma unroll
    for (int m = 0; m < 4; ++m) *(LAS f32x2*)(size_t)(lds0 + (64 * wt + 16 * m + r16) * 64 + (wf * 4 + g) * 8) = (f32x2){s1[m].x + s1[m].y, s2[m].x + s2[m].y};
    asm volatile("s_waitcnt lgkmcnt(0)" ::: "memory");
    BAR();
    char* z1 = ws + WS_Z1;
#pragma unroll
    for (int m = 0; m < 4; ++m) {
      char* dst = z1 + ((size_t)(ti * 8 + wt * 4 + m) * 16 + fj) * CH;
#pragma unroll
      for (int pp = 0; pp < 2; ++pp) {
        const f32x4 a = gelu4(acc[m][2 * pp]), b = gelu4(acc[m][2 * pp + 1]);
        u32x4 v; v.x = cvt_pk(a[0], a[1]); v.y = cvt_pk(a[2], a[3]); v.z = cvt_pk(b[0], b[1]); v.w = cvt_pk(b[2], b[3]);
        *(u32x4*)(dst + (2 * wf + pp) * 1024 + lane * 16) = v;
      }
      if (!wf) { const f32x4 a = gelu4(acc[m][8]); u32x2 v; v.x = cvt_pk(a[0], a[1]); v.y = cvt_pk(a[2], a[3]); *(u32x2*)(dst + 4096 + lane * 8) = v; }
    }
    BAR();
    MEMB();
    char* z2 = ws + WS_Z2;
    const unsigned sl = g * 16 + (r16 ^ (4 * (g >> 1)));
    const bool sw = g & 1;
#pragma unroll
    for (int m = 0; m < 4; ++m) {
      const f32x2 rs = *(LAS f32x2*)(size_t)(lds0 + 8192 + (64 * wt + 16 * m + r16) * 8);
      char* dst = z2 + ((size_t)fj * 128 + ti * 8 + wt * 4 + m) * CH;
#pragma unroll
      for (int pp = 0; pp < 2; ++pp) {
        const f32x4 a = acc[m][4 + 2 * pp] * rs.x + rs.y, b = acc[m][5 + 2 * pp] * rs.x + rs.y;
        const unsigned a0 = cvt_pk(a[0], a[1]), a1 = cvt_pk(a[2], a[3]), b0 = cvt_pk(b[0], b[1]), b1 = cvt_pk(b[2], b[3]);
        u32x4 v; v.x = sw ? b0 : a0; v.y = sw ? b1 : a1; v.z = sw ? a0 : b0; v.w = sw ? a1 : b1;
        *(u32x4*)(dst + (2 * wf + pp) * 1024 + sl * 16) = v;
      }
      if (wf) { const f32x4 a = acc[m][8] * rs.x + rs.y; u32x2 v; v.x = cvt_pk(a[0], a[1]); v.y = cvt_pk(a[2], a[3]); *(u32x2*)(dst + 4096 + sl * 8) = v; }
    }
  }
}

struct G2Addr { unsigned w[2], z[2][2], s[2]; };
template <int WF, int OFF>
DEV void g2_step(const G2Addr& A, f32x4 (&acc)[4][5], bf16x8 (&wX)[4], s16x4 (&tX)[5][2], bf16x8 (&wY)[4], s16x4 (&tY)[4][2]) {
  constexpr int KX = WF, KY = 1 - WF;
  SB(); BAR(); MEMB(); SB();
#pragma unroll
  for (int i = 0; i < 16; ++i) {
    const int n = i >> 2, m = i & 3;
    acc[m][n] = MFMA(cat(tY[n][0], tY[n][1]), wY[m], acc[m][n]);
    SB();
    if (i == 0) wX[0] = ld128(A.w[KX] + OFF);
    else if (i == 1) tX[0][0] = ldtr(A.z[0][0] + OFF + KX * 9216);
    else if (i == 2) tX[0][1] = ldtr(A.z[1][0] + OFF + KX * 9216);
    else if (i < 6) wX[i - 2] = ld128(A.w[KX] + OFF + (i - 2) * 2048);
    else if (i < 12) { const int nn = 1 + ((i - 6) >> 1), u = (i - 6) & 1; tX[nn][u] = ldtr(A.z[u][nn & 1] + OFF + KX * 9216 + (nn >> 1) * 1024); }
    else if (i < 14) tX[4][i - 12] = ldtr(A.s[i - 12] + OFF + KX * 9216);
    SB();
  }
#pragma unroll
  for (int i = 0; i < 20; ++i) {
    const int n = i >> 2, m = i & 3;
    acc[m][n] = MFMA(cat(tX[n][0], tX[n][1]), wX[m], acc[m][n]);
    SB();
    if (i == 0) wY[0] = ld128(A.w[KY] + OFF);
    else if (i == 1) tY[0][0] = ldtr(A.z[0][0] + OFF + KY * 9216);
    else if (i == 2) tY[0][1] = ldtr(A.z[1][0] + OFF + KY * 9216);
    else if (i < 6) wY[i - 2] = ld128(A.w[KY] + OFF + (i - 2) * 2048);
    else if (i < 12) { const int nn = 1 + ((i - 6) >> 1), u = (i - 6) & 1; tY[nn][u] = ldtr(A.z[u][nn & 1] + OFF + KY * 9216 + (nn >> 1) * 1024); }
    SB();
  }
  WAITL0();
}
template <int WF>
DEV void g2_compute(unsigned lds0, int wt, int lane, f32x4 (&acc)[4][5]) {
  const int r16 = lane & 15, g = lane >> 4, qr = (lane >> 2) & 3, p = lane & 3;
  G2Addr A0, A1;
  A0.w[0] = lds0 + (64 * wt + r16) * 128 + ((g ^ (r16 >> 1)) << 4); A0.w[1] = A0.w[0] ^ 64;
#pragma unroll
  for (int u = 0; u < 2; ++u) {
    const unsigned sl = p * 16 + 8 * (g & 1) + 4 * (u ^ (p >> 1)) + qr;
#pragma unroll
    for (int h = 0; h < 2; ++h) A0.z[u][h] = lds0 + 16384 + (g >> 1) * CH + WF * 2048 + sl * 16 + ((h ^ (p & 1)) * 8);
    A0.s[u] = lds0 + 16384 + (g >> 1) * CH + 4096 + sl * 8;
  }
#pragma unroll
  for (int u = 0; u < 2; ++u) { A1.w[u] = A0.w[u] + 2 * STG2; A1.s[u] = A0.s[u] + 2 * STG2; A1.z[u][0] = A0.z[u][0] + 2 * STG2; A1.z[u][1] = A0.z[u][1] + 2 * STG2; }
  asm volatile("" : "+v"(A1.w[0]), "+v"(A1.w[1]), "+v"(A1.s[0]), "+v"(A1.s[1]), "+v"(A1.z[0][0]), "+v"(A1.z[0][1]), "+v"(A1.z[1][0]), "+v"(A1.z[1][1]));
  asm volatile("" : "+v"(A0.w[0]), "+v"(A0.w[1]), "+v"(A0.s[0]), "+v"(A0.s[1]), "+v"(A0.z[0][0]), "+v"(A0.z[0][1]), "+v"(A0.z[1][0]), "+v"(A0.z[1][1]));
  bf16x8 wX[4], wY[4]; s16x4 tX[5][2], tY[4][2];
#pragma unroll
  for (int m = 0; m < 4; ++m) { wY[m] = (bf16x8){0,0,0,0,0,0,0,0}; tY[m][0] = (s16x4){0,0,0,0}; tY[m][1] = (s16x4){0,0,0,0}; }
  WAITL0();
  for (int k = 0; k < 8; ++k) {
    g2_step<WF, 0>(A0, acc, wX, tX, wY, tY);
    g2_step<WF, STG2>(A0, acc, wX, tX, wY, tY);
    g2_step<WF, 0>(A1, acc, wX, tX, wY, tY);
    g2_step<WF, STG2>(A1, acc, wX, tX, wY, tY);
  }
#pragma unroll
  for (int n = 0; n < 4; ++n)
#pragma unroll
    for (int m = 0; m < 4; ++m) acc[m][n] = MFMA(cat(tY[n][0], tY[n][1]), wY[m], acc[m][n]);
}
__global__ __launch_bounds__(512, 2) void k2_gemm_sp(const char* __restrict__ W, const u16* __restrict__ bias, char* __restrict__ ws) {
  extern __shared__ __attribute__((aligned(16))) char smem[];
  const int tid = threadIdx.x, wid = __builtin_amdgcn_readfirstlane(tid >> 6), lane = tid & 63;
  int il, jl, ti, fj; tile_of(blockIdx.x, il, jl, ti, fj);
  const unsigned lds0 = (unsigned)(size_t)(LAS char*)smem;
  if (wid >= 4) {
    const int lw = wid - 4;
    auto rW = RSRC(W, 2048 * 4096); auto rZ = RSRC(ws + WS_Z2, FRAG_BYTES);
    unsigned vW[4];
#pragma unroll
    for (int q = 0; q < 4; ++q) { const int row = 8 * q + (lane >> 3); vW[q] = row * 4096 + (((lane & 7) ^ ((row >> 1) & 7)) << 4) - q * 1024; }
    const unsigned vZ = lane * 16, zp0 = lw < 2 ? lw * 5 : 2 + lw * 4;
    const unsigned sW = (ti * 128 + lw * 32) * 4096, sZ = fj * 128 * CH + zp0 * 1024;
    auto issue = [&](int k, int slot) {
      const unsigned d = lds0 + slot * STG2, dz = d + 16384 + zp0 * 1024, so = sZ + k * 18432;
      GL4(rW, d + lw * 4096, vW[0], vW[1], vW[2], vW[3], sW + k * 128);
      GL4(rZ, dz, vZ, vZ, vZ, vZ, so);
      if (lw < 2) GL(rZ, dz + 4096, vZ, so + 4096, 0);
    };
    issue(0, 0); issue(1, 1); issue(2, 2);
    int slot = 3;
    for (int k = 0; k < 32; ++k) {
      if (k < 30) { if (lw < 2) WV(18); else WV(16); } else if (k == 30) { if (lw < 2) WV(9); else WV(8); } else WV(0);
      BAR();
      if (k + 3 < 32) issue(k + 3, slot);
      slot = (slot + 1) & 3;
    }
    BAR(); BAR();
  } else {
    const int wt = wid >> 1, wf = wid & 1;
    u32x4 za[4][2]; u32x2 zs[4]; float bs[4];
#pragma unroll
    for (int m = 0; m < 4; ++m) {
      const char* src = ws + WS_Z1 + ((size_t)(ti * 8 + wt * 4 + m) * 16 + fj) * CH;
      za[m][0] = *(const u32x4*)(src + (2 * wf) * 1024 + lane * 16); za[m][1] = *(const u32x4*)(src + (2 * wf + 1) * 1024 + lane * 16);
      zs[m] = *(const u32x2*)(src + 4096 + lane * 8);
      bs[m] = __uint_as_float((unsigned)bias[ti * 128 + wt * 64 + m * 16 + (lane & 15)] << 16);
    }
    f32x4 acc[4][5];
#pragma unroll
    for (int m = 0; m < 4; ++m)
#pragma unroll
      for (int n = 0; n < 5; ++n) acc[m][n] = (f32x4){0, 0, 0, 0};
    if (wf == 0) g2_compute<0>(lds0, wt, lane, acc); else g2_compute<1>(lds0, wt, lane, acc);
    BAR();
    if (wf == 1) {
#pragma unroll
      for (int m = 0; m < 4; ++m) *(LAS f32x4*)(size_t)(lds0 + (wt * 4 + m) * 1024 + lane * 16) = acc[m][4];
      asm volatile("s_waitcnt lgkmcnt(0)" ::: "memory");
    }
    BAR();
    MEMB();
#pragma unroll
    for (int m = 0; m < 4; ++m) {
      char* dst = ws + WS_P + ((size_t)(ti * 8 + wt * 4 + m) * 16 + fj) * CH;
#pragma unroll
      for (int pp = 0; pp < 2; ++pp) {
        const f32x4 a = acc[m][2 * pp] + bs[m], b = acc[m][2 * pp + 1] + bs[m]; const u32x4 z = za[m][pp];
        u32x4 v; v.x = cvt_pk(a[0] * bflo(z.x), a[1] * bfhi(z.x)); v.y = cvt_pk(a[2] * bflo(z.y), a[3] * bfhi(z.y));
        v.z = cvt_pk(b[0] * bflo(z.z), b[1] * bfhi(z.z)); v.w = cvt_pk(b[2] * bflo(z.w), b[3] * bfhi(z.w));
        *(u32x4*)(dst + (2 * wf + pp) * 1024 + lane * 16) = v;
      }
      if (wf == 0) {
        const f32x4 a = acc[m][4] + *(LAS f32x4*)(size_t)(lds0 + (wt * 4 + m) * 1024 + lane * 16) + bs[m];
        u32x2 v; v.x = cvt_pk(a[0] * bflo(zs[m].x), a[1] * bfhi(zs[m].x)); v.y = cvt_pk(a[2] * bflo(zs[m].y), a[3] * bfhi(zs[m].y));
        *(u32x2*)(dst + 4096 + lane * 8) = v;
      }
    }
  }
}

template <int KH>
DEV void g3_compute(unsigned lds0, int dh, int lane, char* out, int tt, int dd) {
  f32x4 acc[5][2];
#pragma unroll
  for (int m = 0; m < 5; ++m) { acc[m][0] = (f32x4){0, 0, 0, 0}; acc[m][1] = (f32x4){0, 0, 0, 0}; }
  const int g = lane >> 4, qr = (lane >> 2) & 3, p = lane & 3;
  const int rr = 4 * g + qr;
  unsigned bW[2];
#pragma unroll
  for (int c = 0; c < 2; ++c) bW[c] = lds0 + 23552 + rr * 128 + ((((2 * dh + c) * 2 + (p >> 1)) ^ (((rr >> 1) & 3) << 1)) << 4) + (p & 1) * 8;
  const unsigned bP = lds0 + lane * 16 + KH * 2048, bS = lds0 + 20480 + lane * 8;
  unsigned so = 0;
  WAITL0();
  for (int j = 0; j < 16; ++j) {
    BAR(); MEMB();
    bf16x8 pf[2][5], wfr[2][2];
#pragma unroll
    for (int P = 0; P < 2; ++P) {
#pragma unroll
      for (int c = 0; c < 2; ++c) wfr[P][c] = cat(ldtr(bW[c] + so + (2 * KH + P) * 4096), ldtr(bW[c] + so + (2 * KH + P) * 4096 + 2048));
#pragma unroll
      for (int m = 0; m < 5; ++m) pf[P][m] = ld128(bP + so + m * 4096 + P * 1024);
    }
    constexpr int M0 = KH ? 3 : 0, M1 = KH ? 5 : 3;
    s16x4 ws_[2], ps[5];
#pragma unroll
    for (int c = 0; c < 2; ++c) ws_[c] = ldtr(bW[c] + so + 16384);
#pragma unroll
    for (int m = M0; m < M1; ++m) ps[m] = ld64(bS + so + m * 512);
#pragma unroll
    for (int P = 0; P < 2; ++P)
#pragma unroll
      for (int m = 0; m < 5; ++m)
#pragma unroll
        for (int c = 0; c < 2; ++c) acc[m][c] = MFMA(wfr[P][c], pf[P][m], acc[m][c]);
#pragma unroll
    for (int m = M0; m < M1; ++m)
#pragma unroll
      for (int c = 0; c < 2; ++c) acc[m][c] = MFMA16(ws_[c], ps[m], acc[m][c]);
    WAITL0();
    so = so == 2 * STG3 ? 0 : so + STG3;
  }
  BAR();
#pragma unroll
  for (int m = 0; m < 5; ++m) *(LAS f32x4*)(size_t)(lds0 + ((KH * 2 + dh) * 5 + m) * 1024 + lane * 16) = acc[m][1 - KH];
  asm volatile("s_waitcnt lgkmcnt(0)" ::: "memory");
  BAR(); MEMB();
#pragma unroll
  for (int m = 0; m < 5; ++m) {
    const f32x4 a = acc[m][KH] + *(LAS f32x4*)(size_t)(lds0 + (((1 - KH) * 2 + dh) * 5 + m) * 1024 + lane * 16);
    const int t = tt * 80 + m * 16 + (lane & 15);
    u32x2 v; v.x = cvt_pk(a[0], a[1]); v.y = cvt_pk(a[2], a[3]);
    if (t < 2048) *(u32x2*)(out + (size_t)t * 1152 + dd * 128 + (dh * 2 + KH) * 32 + (lane >> 4) * 8) = v;
  }
}
__global__ __launch_bounds__(512, 2) void k3_gemm_out(const char* __restrict__ Wo, char* __restrict__ out, char* __restrict__ ws) {
  extern __shared__ __attribute__((aligned(16))) char smem[];
  const int tid = threadIdx.x, wid = __builtin_amdgcn_readfirstlane(tid >> 6), lane = tid & 63;
  const int x = blockIdx.x & 7, r = blockIdx.x >> 3, L = x * 30 + r;
  if (r >= 30 || L >= 234) return;
  const int tt = L / 9, dd = L - tt * 9;
  const unsigned lds0 = (unsigned)(size_t)(LAS char*)smem;
  if (wid >= 4) {
    const int lw = wid - 4;
    auto rP = RSRC(ws + WS_P, FRAG_BYTES); auto rW = RSRC(Wo, 2304 * 1152);
    unsigned vW[4];
#pragma unroll
    for (int q = 0; q < 4; ++q) vW[q] = (8 * q + (lane >> 3)) * 1152 + (((lane & 7) ^ (((lane >> 4) & 3) << 1)) << 4) - q * 1024;
    const unsigned vP = lane * 16, vS = (lane >> 5) * 16 * CH + (lane & 31) * 16 + 4096;
    const unsigned sP = tt * 5 * 16 * CH, sW = dd * 128;
    auto issue = [&](int j, int slot) {
      const unsigned d = lds0 + slot * STG3, sp = sP + j * CH, sw = sW + j * 144 * 1152, dw = d + 23552;
      if (lw == 0) { GL4(rP, d, vP, vP, vP, vP, sp); GL4(rP, d + 4096, vP, vP, vP, vP, sp + 16 * CH); GL(rP, d + 20480, vS, sp, 0); GL(rP, d + 21504, vS, sp + 32 * CH, 0); }
      else if (lw == 1) { GL4(rP, d + 8192, vP, vP, vP, vP, sp + 32 * CH); GL4(rP, d + 12288, vP, vP, vP, vP, sp + 48 * CH); if (lane < 32) GL(rP, d + 22528, vS, sp + 64 * CH, 0);
                          GL(rW, dw + 16384, vW[0], sw + 128 * 1152, 0); GL(rW, dw + 16384, vW[1], sw + 128 * 1152, 1024); }
      else if (lw == 2) { GL4(rP, d + 16384, vP, vP, vP, vP, sp + 64 * CH); GL4(rW, dw, vW[0], vW[1], vW[2], vW[3], sw); GL(rW, dw + 4096, vW[0], sw + 32 * 1152, 0); GL(rW, dw + 4096, vW[1], sw + 32 * 1152, 1024); }
      else { GL(rW, dw + 4096, vW[2], sw + 32 * 1152, 2048); GL(rW, dw + 4096, vW[3], sw + 32 * 1152, 3072); GL4(rW, dw + 8192, vW[0], vW[1], vW[2], vW[3], sw + 64 * 1152); GL4(rW, dw + 12288, vW[0], vW[1], vW[2], vW[3], sw + 96 * 1152); }
    };
    issue(0, 0); issue(1, 1);
    int slot = 2;
    for (int j = 0; j < 16; ++j) {
      if (j < 15) { if (lw == 1) WV(11); else WV(10); } else WV(0);
      BAR();
      if (j + 2 < 16) issue(j + 2, slot);
      slot = slot == 2 ? 0 : slot + 1;
    }
    BAR(); BAR();
  } else {
    const int kh = wid >> 1, dh = wid & 1;
    if (kh == 0) g3_compute<0>(lds0, dh, lane, out, tt, dd); else g3_compute<1>(lds0, dh, lane, out, tt, dd);
  }
}

extern "C" void kernel_launch(void* const* d_in, const int* in_sizes, int n_in, void* d_out, int out_size, void* d_ws, size_t ws_size, hipStream_t stream) {
  (void)hipFuncSetAttribute((const void*)k1_gemm_in, hipFuncAttributeMaxDynamicSharedMemorySize, 3 * STG1);
  (void)hipFuncSetAttribute((const void*)k2_gemm_sp, hipFuncAttributeMaxDynamicSharedMemorySize, 4 * STG2);
  (void)hipFuncSetAttribute((const void*)k3_gemm_out, hipFuncAttributeMaxDynamicSharedMemorySize, 3 * STG3);
  k1_gemm_in<<<256, 512, 3 * STG1, stream>>>((const char*)d_in[0], (const char*)d_in[1], (char*)d_ws);
  k2_gemm_sp<<<256, 512, 4 * STG2, stream>>>((const char*)d_in[2], (const u16*)d_in[3], (char*)d_ws);
  k3_gemm_out<<<256, 512, 3 * STG3, stream>>>((const char*)d_in[4], (char*)d_out, (char*)d_ws);
}
```

```cpp
#include <hip/hip_runtime.h>
#include <hip/hip_bf16.h>
typedef unsigned short u16;
typedef unsigned long long u64;
typedef short s16x4 __attribute__((ext_vector_type(4)));
typedef short bf16x8 __attribute__((ext_vector_type(8)));
typedef float f32x2 __attribute__((ext_vector_type(2)));
typedef float f32x4 __attribute__((ext_vector_type(4)));
typedef unsigned u32x4 __attribute__((ext_vector_type(4)));
typedef unsigned u32x2 __attribute__((ext_vector_type(2)));
#define LAS __attribute__((address_space(3)))
#define DEV __device__ __forceinline__
#define BAR() __builtin_amdgcn_s_barrier()
#define SB() __builtin_amdgcn_sched_barrier(0)
#define MEMB() asm volatile("" ::: "memory")
#define WAITL0() __builtin_amdgcn_s_waitcnt(0xC07F)
#define WV(N) asm volatile("s_waitcnt vmcnt(%0)" ::"i"(N) : "memory")
#define MFMA(a, b, c) __builtin_amdgcn_mfma_f32_16x16x32_bf16(a, b, c, 0, 0, 0)
#define MFMA16(a, b, c) __builtin_amdgcn_mfma_f32_16x16x16bf16_1k(a, b, c, 0, 0, 0)
#define GL(R, D, V, S, I) __builtin_amdgcn_raw_ptr_buffer_load_lds(R, (LAS void*)(size_t)(D), 16, V, S, I, 0)
#define GL4(R, D, V0, V1, V2, V3, S) do { GL(R, D, V0, S, 0); GL(R, D, V1, S, 1024); GL(R, D, V2, S, 2048); GL(R, D, V3, S, 3072); } while (0)
#define GS(R, D, V, S, I) __builtin_amdgcn_raw_ptr_buffer_load_lds(R, (LAS void*)(size_t)(D), 16, V, S, I, HAUX)
#define GS4(R, D, V, S) do { GS(R, D, V, S, 0); GS(R, D, V, S, 1024); GS(R, D, V, S, 2048); GS(R, D, V, S, 3072); } while (0)
#define HAUX 16
#define RSRC(p, n) __builtin_amdgcn_make_buffer_rsrc((void*)(p), 0, (n), 0x00020000)
#define RLX_AGENT __ATOMIC_RELAXED, __HIP_MEMORY_SCOPE_AGENT
#define WARM1 0
#define ROT1 1

constexpr int CH = 4608;
constexpr int FRAG_BYTES = 16 * 128 * CH;
constexpr size_t WS_Z2 = 0, WS_P = FRAG_BYTES, WS_ST = 2 * (size_t)FRAG_BYTES, WS_F1 = WS_ST + 16 * 16 * 128 * 16, WS_F2 = WS_F1 + 256 * 128, WS_EP = WS_F2 + 256 * 128;
constexpr int STG1 = 53248, STG2 = 34816, STG3 = 41984, LDS_BYTES = 163840;
constexpr int L_T = 139264, L_R = L_T + 8192, L_DUMMY = 159744;
constexpr unsigned SPIN_CAP = 1u << 16;
#define STAMP(I)

DEV unsigned cvt_pk(float lo, float hi) { unsigned r; asm("v_cvt_pk_bf16_f32 %0, %1, %2" : "=v"(r) : "v"(lo), "v"(hi)); return r; }
DEV bf16x8 ld128(unsigned a) { return *(LAS bf16x8*)(size_t)a; }
DEV s16x4 ld64(unsigned a) { return *(LAS s16x4*)(size_t)a; }
DEV s16x4 ldtr(unsigned a) { return __builtin_amdgcn_ds_read_tr16_b64_v4i16((LAS s16x4*)(size_t)a); }
DEV bf16x8 cat(s16x4 a, s16x4 b) { return __builtin_shufflevector(a, b, 0, 1, 2, 3, 4, 5, 6, 7); }
DEV float bflo(unsigned v) { return __uint_as_float(v << 16); }
DEV float bfhi(unsigned v) { return __uint_as_float(v & 0xffff0000u); }
DEV f32x2 gelu2(f32x2 x) {
  const float k1 = -2.0f * 0.7978845608028654f * 1.4426950408889634f, k2 = k1 * 0.044715f;
  f32x2 t = x * (x * x * k2 + k1), e;
  e.x = __builtin_amdgcn_exp2f(t.x); e.y = __builtin_amdgcn_exp2f(t.y);
  e = e + 1.0f;
  e.x = __builtin_amdgcn_rcpf(e.x); e.y = __builtin_amdgcn_rcpf(e.y);
  return x * e;
}
DEV f32x4 gelu4(f32x4 v) { f32x2 a = gelu2((f32x2){v[0], v[1]}), b = gelu2((f32x2){v[2], v[3]}); return (f32x4){a.x, a.y, b.x, b.y}; }

struct G2Addr { unsigned w[2], z[2][2], s[2]; };
template <int WF, int OFF, bool PUB, bool FIRST>
DEV void g2_step(const G2Addr& A, f32x4 (&acc)[4][5], bf16x8 (&wX)[4], s16x4 (&tX)[5][2], bf16x8 (&wY)[4], s16x4 (&tY)[4][2], u64* flag, u64 tag, int lane) {
  constexpr int KX = WF, KY = 1 - WF;
  SB(); BAR(); MEMB(); SB();
#pragma unroll
  for (int i = 0; i < 16; ++i) {
    const int n = i >> 2, m = i & 3;
    if (!FIRST) acc[m][n] = MFMA(cat(tY[n][0], tY[n][1]), wY[m], acc[m][n]);
    SB();
    if (i == 0) wX[0] = ld128(A.w[KX] + OFF);
    else if (i == 1) tX[0][0] = ldtr(A.z[0][0] + OFF + KX * 9216);
    else if (i == 2) tX[0][1] = ldtr(A.z[1][0] + OFF + KX * 9216);
    else if (i < 6) wX[i - 2] = ld128(A.w[KX] + OFF + (i - 2) * 2048);
    else if (i < 12) { const int nn = 1 + ((i - 6) >> 1), u = (i - 6) & 1; tX[nn][u] = ldtr(A.z[u][nn & 1] + OFF + KX * 9216 + (nn >> 1) * 1024); }
    else if (i < 14) tX[4][i - 12] = ldtr(A.s[i - 12] + OFF + KX * 9216);
    SB();
  }
#pragma unroll
  for (int i = 0; i < 20; ++i) {
    const int n = i >> 2, m = i & 3;
    acc[m][n] = MFMA(cat(tX[n][0], tX[n][1]), wX[m], acc[m][n]);
    SB();
    if (i == 0) wY[0] = ld128(A.w[KY] + OFF);
    else if (i == 1) tY[0][0] = ldtr(A.z[0][0] + OFF + KY * 9216);
    else if (i == 2) tY[0][1] = ldtr(A.z[1][0] + OFF + KY * 9216);
    else if (i < 6) wY[i - 2] = ld128(A.w[KY] + OFF + (i - 2) * 2048);
    else if (i < 12) { const int nn = 1 + ((i - 6) >> 1), u = (i - 6) & 1; tY[nn][u] = ldtr(A.z[u][nn & 1] + OFF + KY * 9216 + (nn >> 1) * 1024); }
    SB();
  }
  if (PUB) {
    WV(0);
    if (lane == 0) __hip_atomic_store(flag, tag, RLX_AGENT);
  }
  WAITL0();
}

template <int KH>
DEV void g3_compute(unsigned lds0, int dh, int lane, char* out, int tt, int dd) {
  f32x4 acc[5][2];
#pragma unroll
  for (int m = 0; m < 5; ++m) { acc[m][0] = (f32x4){0, 0, 0, 0}; acc[m][1] = (f32x4){0, 0, 0, 0}; }
  const int g = lane >> 4, qr = (lane >> 2) & 3, p = lane & 3;
  const int rr = 4 * g + qr;
  unsigned bW[2];
#pragma unroll
  for (int c = 0; c < 2; ++c) bW[c] = lds0 + 23552 + rr * 128 + ((((2 * dh + c) * 2 + (p >> 1)) ^ (((rr >> 1) & 3) << 1)) << 4) + (p & 1) * 8;
  const unsigned bP = lds0 + lane * 16 + KH * 2048, bS = lds0 + 20480 + lane * 8;
  unsigned so = 0;
  WAITL0();
  for (int j = 0; j < 16; ++j) {
    BAR(); MEMB();
    bf16x8 pf[2][5], wfr[2][2];
#pragma unroll
    for (int P = 0; P < 2; ++P) {
#pragma unroll
      for (int c = 0; c < 2; ++c) wfr[P][c] = cat(ldtr(bW[c] + so + (2 * KH + P) * 4096), ldtr(bW[c] + so + (2 * KH + P) * 4096 + 2048));
#pragma unroll
      for (int m = 0; m < 5; ++m) pf[P][m] = ld128(bP + so + m * 4096 + P * 1024);
    }
    constexpr int M0 = KH ? 3 : 0, M1 = KH ? 5 : 3;
    s16x4 ws_[2], ps[5];
#pragma unroll
    for (int c = 0; c < 2; ++c) ws_[c] = ldtr(bW[c] + so + 16384);
#pragma unroll
    for (int m = M0; m < M1; ++m) ps[m] = ld64(bS + so + m * 512);
#pragma unroll
    for (int P = 0; P < 2; ++P)
#pragma unroll
      for (int m = 0; m < 5; ++m)
#pragma unroll
        for (int c = 0; c < 2; ++c) acc[m][c] = MFMA(wfr[P][c], pf[P][m], acc[m][c]);
#pragma unroll
    for (int m = M0; m < M1; ++m)
#pragma unroll
      for (int c = 0; c < 2; ++c) acc[m][c] = MFMA16(ws_[c], ps[m], acc[m][c]);
    WAITL0();
    so = so == 2 * STG3 ? 0 : so + STG3;
  }
  BAR();
#pragma unroll
  for (int m = 0; m < 5; ++m) *(LAS f32x4*)(size_t)(lds0 + ((KH * 2 + dh) * 5 + m) * 1024 + lane * 16) = acc[m][1 - KH];
  asm volatile("s_waitcnt lgkmcnt(0)" ::: "memory");
  BAR(); MEMB();
#pragma unroll
  for (int m = 0; m < 5; ++m) {
    const f32x4 a = acc[m][KH] + *(LAS f32x4*)(size_t)(lds0 + (((1 - KH) * 2 + dh) * 5 + m) * 1024 + lane * 16);
    const int t = tt * 80 + m * 16 + (lane & 15);
    u32x2 v; v.x = cvt_pk(a[0], a[1]); v.y = cvt_pk(a[2], a[3]);
    if (t < 2048) *(u32x2*)(out + (size_t)t * 1152 + dd * 128 + (dh * 2 + KH) * 32 + (lane >> 4) * 8) = v;
  }
}

template <int WF>
DEV void g2_phase(unsigned lds0, int wid, int wt, int lane, int ti, int fj, char* ws, u64 tag, const u32x4 (&za)[4][2], const u32x2 (&zs)[4], const float (&bs)[4]) {
  const int r16 = lane & 15, g = lane >> 4, qr = (lane >> 2) & 3, p = lane & 3;
  G2Addr A0, A1;
  A0.w[0] = lds0 + (64 * wt + r16) * 128 + ((g ^ (r16 >> 1)) << 4); A0.w[1] = A0.w[0] ^ 64;
#pragma unroll
  for (int u = 0; u < 2; ++u) {
    const unsigned sl = p * 16 + 8 * (g & 1) + 4 * (u ^ (p >> 1)) + qr;
#pragma unroll
    for (int h = 0; h < 2; ++h) A0.z[u][h] = lds0 + 16384 + (g >> 1) * CH + WF * 2048 + sl * 16 + ((h ^ (p & 1)) * 8);
    A0.s[u] = lds0 + 16384 + (g >> 1) * CH + 4096 + sl * 8;
  }
#pragma unroll
  for (int u = 0; u < 2; ++u) { A1.w[u] = A0.w[u] + 2 * STG2; A1.s[u] = A0.s[u] + 2 * STG2; A1.z[u][0] = A0.z[u][0] + 2 * STG2; A1.z[u][1] = A0.z[u][1] + 2 * STG2; }
  asm volatile("" : "+v"(A1.w[0]), "+v"(A1.w[1]), "+v"(A1.s[0]), "+v"(A1.s[1]), "+v"(A1.z[0][0]), "+v"(A1.z[0][1]), "+v"(A1.z[1][0]), "+v"(A1.z[1][1]));
  asm volatile("" : "+v"(A0.w[0]), "+v"(A0.w[1]), "+v"(A0.s[0]), "+v"(A0.s[1]), "+v"(A0.z[0][0]), "+v"(A0.z[0][1]), "+v"(A0.z[1][0]), "+v"(A0.z[1][1]));
  f32x4 acc[4][5];
#pragma unroll
  for (int m = 0; m < 4; ++m)
#pragma unroll
    for (int n = 0; n < 5; ++n) acc[m][n] = (f32x4){0, 0, 0, 0};
  bf16x8 wX[4], wY[4]; s16x4 tX[5][2], tY[4][2];
  u64* f1 = (u64*)(ws + WS_F1 + (fj * 16 + ti) * 128) + wid;
  WAITL0();
  g2_step<WF, 0, true, true>(A0, acc, wX, tX, wY, tY, f1, tag, lane);
  STAMP(7);
  g2_step<WF, STG2, false, false>(A0, acc, wX, tX, wY, tY, f1, tag, lane);
  STAMP(8);
  g2_step<WF, 0, false, false>(A1, acc, wX, tX, wY, tY, f1, tag, lane);
  STAMP(9);
  g2_step<WF, STG2, false, false>(A1, acc, wX, tX, wY, tY, f1, tag, lane);
  for (int k = 1; k < 8; ++k) {
    g2_step<WF, 0, false, false>(A0, acc, wX, tX, wY, tY, f1, tag, lane);
    g2_step<WF, STG2, false, false>(A0, acc, wX, tX, wY, tY, f1, tag, lane);
    g2_step<WF, 0, false, false>(A1, acc, wX, tX, wY, tY, f1, tag, lane);
    g2_step<WF, STG2, false, false>(A1, acc, wX, tX, wY, tY, f1, tag, lane);
  }
  BAR();
  if (WF == 1) {
#pragma unroll
    for (int m = 0; m < 4; ++m) *(LAS f32x4*)(size_t)(lds0 + L_T + (wt * 4 + m) * 1024 + lane * 16) = acc[m][4];
  }
#pragma unroll
  for (int n = 0; n < 4; ++n)
#pragma unroll
    for (int m = 0; m < 4; ++m) acc[m][n] = MFMA(cat(tY[n][0], tY[n][1]), wY[m], acc[m][n]);
  STAMP(10);
  asm volatile("s_waitcnt lgkmcnt(0)" ::: "memory");
  BAR();
  MEMB();
  auto rP = RSRC(ws + WS_P, FRAG_BYTES);
#pragma unroll
  for (int m = 0; m < 4; ++m) {
    const unsigned dst = ((ti * 8 + wt * 4 + m) * 16 + fj) * CH;
#pragma unroll
    for (int pp = 0; pp < 2; ++pp) {
      const f32x4 a = acc[m][2 * pp] + bs[m], b = acc[m][2 * pp + 1] + bs[m]; const u32x4 z = za[m][pp];
      u32x4 v; v.x = cvt_pk(a[0] * bflo(z.x), a[1] * bfhi(z.x)); v.y = cvt_pk(a[2] * bflo(z.y), a[3] * bfhi(z.y));
      v.z = cvt_pk(b[0] * bflo(z.z), b[1] * bfhi(z.z)); v.w = cvt_pk(b[2] * bflo(z.w), b[3] * bfhi(z.w));
      __builtin_amdgcn_raw_buffer_store_b128(v, rP, dst + lane * 16 + (2 * WF + pp) * 1024, 0, 16);
    }
    if (WF == 0) {
      const f32x4 a = acc[m][4] + *(LAS f32x4*)(size_t)(lds0 + L_T + (wt * 4 + m) * 1024 + lane * 16) + bs[m];
      u32x2 v; v.x = cvt_pk(a[0] * bflo(zs[m].x), a[1] * bfhi(zs[m].x)); v.y = cvt_pk(a[2] * bflo(zs[m].y), a[3] * bfhi(zs[m].y));
      __builtin_amdgcn_raw_buffer_store_b64(v, rP, dst + lane * 8 + 4096, 0, 16);
    }
  }
  STAMP(11);
  WV(0);
  if (lane == 0) __hip_atomic_store((u64*)(ws + WS_F2 + (ti * 16 + fj) * 128) + wid, tag, RLX_AGENT);
  STAMP(12);
}

DEV unsigned poll16(const char* base, u64 tag, int lane) {
  const u64 v = __hip_atomic_load((const u64*)(base + (lane >> 2) * 128) + (lane & 3), RLX_AGENT);
  u64 ok = __builtin_amdgcn_ballot_w64(v == tag);
  ok &= ok >> 1; ok &= ok >> 2;
  unsigned m = 0;
#pragma unroll
  for (int i = 0; i < 16; ++i) m |= (unsigned)((ok >> (4 * i)) & 1) << i;
  return m;
}

__global__ __launch_bounds__(512, 2) void gmlp_fused(const char* __restrict__ X, const char* __restrict__ Win, const char* __restrict__ Wsp, const u16* __restrict__ bias,
                                                     const char* __restrict__ Wo, char* __restrict__ out, char* __restrict__ ws) {
  extern __shared__ __attribute__((aligned(16))) char smem[];
  const int tid = threadIdx.x, wid = __builtin_amdgcn_readfirstlane(tid >> 6), lane = tid & 63;
  const int bid = blockIdx.x, xg = bid & 7, rk = bid >> 3, il = rk & 7, jl = rk >> 3, ti = (xg & 1) * 8 + il, fj = (xg >> 1) * 4 + jl;
  const int L3 = xg * 30 + rk, tt = L3 / 9, dd = L3 - tt * 9;
  const bool has3 = rk < 30 && L3 < 234;
  const unsigned lds0 = (unsigned)(size_t)(LAS char*)smem;
  STAMP(0);
  if (__builtin_expect(wid >= 4, 1)) {
    const int lw = wid - 4;
    auto rX = RSRC(X, 2048 * 1152); auto rWi = RSRC(Win, 576 * 9216);
    unsigned vX[4], vW[9];
    const unsigned sX = (ti * 128 + lw * 32) * 1152, sW = (lw & 1) * 32 * 9216 + (lw >> 1) * 4608 + fj * 288;
    const unsigned dX = lds0 + lw * 4096, dW = lds0 + 16384 + lw * 9216;
    const int rot = ROT1 == 0 ? 0 : ROT1 == 1 ? il + 2 * jl : 3 * ((il + jl) % 3);
    auto k1 = [&](int k) { int kk = k + rot; kk = kk >= 9 ? kk - 9 : kk; kk = kk >= 9 ? kk - 9 : kk; return kk; };
    auto issue1 = [&](int k, int slot) {
      const int kk = k1(k);
      const unsigned so = slot * STG1, sx = sX + kk * 128, sw = sW + kk * 64 * 9216;
      GL4(rX, dX + so, vX[0], vX[1], vX[2], vX[3], sx);
      GL(rWi, dW + so, vW[0], sw, 0); GL(rWi, dW + so, vW[1], sw, 1024); GL(rWi, dW + so, vW[2], sw, 2048);
      GL(rWi, dW + so + 3072, vW[3], sw, 0); GL(rWi, dW + so + 3072, vW[4], sw, 1024); GL(rWi, dW + so + 3072, vW[5], sw, 2048);
      GL(rWi, dW + so + 6144, vW[6], sw, 0); GL(rWi, dW + so + 6144, vW[7], sw, 1024); GL(rWi, dW + so + 6144, vW[8], sw, 2048);
    };
    const unsigned vL = lane * 16;
    {
#pragma unroll
      for (int q = 0; q < 4; ++q) { const int row = 8 * q + (lane >> 3); vX[q] = row * 1152 + (((lane & 7) ^ ((row >> 1) & 7)) << 4) - q * 1024; }
      GL4(rX, dX, vX[0], vX[1], vX[2], vX[3], sX + k1(0) * 128);
      SB();
      const unsigned sw = sW + k1(0) * 64 * 9216;
#pragma unroll
      for (int q = 0; q < 9; ++q) {
        const unsigned c16 = q * 64 + lane, R = (c16 * 3641u) >> 16, c = (c16 - R * 18) * 16, k = 8 * ((R >> 2) & 3) + 4 * (R >> 4) + (R & 3);
        vW[q] = k * 9216 + c - (q % 3) * 1024;
        if (q % 3 == 0) GL(rWi, dW + (q / 3) * 3072, vW[q], sw, 0); else if (q % 3 == 1) GL(rWi, dW + (q / 3) * 3072, vW[q], sw, 1024); else GL(rWi, dW + (q / 3) * 3072, vW[q], sw, 2048);
        SB();
      }
    }
    constexpr int NW1 = 0;
    issue1(1, 1); issue1(2, 2);
    auto rWs = RSRC(Wsp, 2048 * 4096); auto rWo = RSRC(Wo, 2304 * 1152);
    unsigned vS[4];
#pragma unroll
    for (int q = 0; q < 4; ++q) { const int row = 8 * q + (lane >> 3); vS[q] = row * 4096 + (((lane & 7) ^ ((row >> 1) & 7)) << 4) - q * 1024; }
    const unsigned sWs = (ti * 128 + lw * 32) * 4096;
    auto kpos = [&](int n) { int t2 = n < 2 ? 2 * ti + n : (n - 2 < 2 * ti ? n - 2 : n); return t2; };
    auto issueW = [&](int n) { GL4(rWs, lds0 + (n & 3) * STG2 + lw * 4096, vS[0], vS[1], vS[2], vS[3], sWs + kpos(n) * 128); };
    WV(26 + NW1); BAR();
    STAMP(1);
    WV(13); BAR(); issue1(3, 0);
    WV(13); BAR(); issue1(4, 1);
    WV(13); BAR(); issue1(5, 2);
    WV(13); BAR(); issue1(6, 0);
    WV(13); BAR(); issue1(7, 1);
    WV(13); BAR(); issue1(8, 2);
    const u64 ep = __hip_atomic_load((const u64*)(ws + WS_EP), RLX_AGENT);
#pragma unroll
    for (int q = 0; q < 2; ++q) GL4(rWs, lds0 + L_DUMMY, vL, vL, vL, vL, bid * 32768 + lw * 8192 + q * 4096);
    GL(rWo, lds0 + L_DUMMY, vL, bid * 10368 + lw * 2592, 0); GL(rWo, lds0 + L_DUMMY, vL, bid * 10368 + lw * 2592, 1024); GL(rWo, lds0 + L_DUMMY, vL, bid * 10368 + lw * 2592 + 2592 - 1024, 2048 - 2048);
    WV(13 + 12); BAR();
    WV(12); BAR();
    BAR();
    const unsigned tagA = __builtin_amdgcn_readfirstlane((unsigned)ep) + 0x9E3779B9u, tagB = __builtin_amdgcn_readfirstlane((unsigned)(ep >> 32)) + 0x85EBCA6Bu;
    const u64 tag = ((u64)tagB << 32) | tagA;
    issueW(0); issueW(1); issueW(2); issueW(3);
    BAR();
    STAMP(2);
    {
      auto rS = RSRC(ws + WS_ST, 16 * 16 * 128 * 16);
      if (lane < 32) {
        const int row = lw * 32 + lane;
        f32x4 v0, v1, v2, v3;
        asm volatile("ds_read_b128 %0, %4\n\tds_read_b128 %1, %4 offset:16\n\tds_read_b128 %2, %4 offset:32\n\tds_read_b128 %3, %4 offset:48\n\ts_waitcnt lgkmcnt(0)"
                     : "=&v"(v0), "=&v"(v1), "=&v"(v2), "=&v"(v3) : "v"(lds0 + L_T + row * 64) : "memory");
        const f32x4 s = (v0 + v1) + (v2 + v3);
        u32x4 gq; gq.x = tagA; gq.y = __float_as_uint(s[0] + s[2]); gq.z = tagB; gq.w = __float_as_uint(s[1] + s[3]);
        __builtin_amdgcn_raw_buffer_store_b128(gq, rS, ((ti * 16 + fj) * 128 + row) * 16, 0, 16);
      }
      for (unsigned spins = 0; spins < SPIN_CAP; ++spins) {
        MEMB();
        const u32x4 v = __builtin_amdgcn_raw_buffer_load_b128(rS, (lane & 15) * 512, (ti * 16 + lw * 4) * 2048, 16);
        if (__all((v.x == tagA) & (v.z == tagB))) break;
      }
      f32x4 s;
      for (unsigned spins = 0;; ++spins) {
        u32x4 v[8]; bool ok = true;
        MEMB();
#pragma unroll
        for (int p = 0; p < 8; ++p) v[p] = __builtin_amdgcn_raw_buffer_load_b128(rS, lane * 16 + (p & 1) * 1024, (ti * 16 + lw * 4 + (p >> 1)) * 2048, 16);
        s = (f32x4){0.f, 0.f, 0.f, 0.f};
#pragma unroll
        for (int p = 0; p < 8; ++p) { ok &= (v[p].x == tagA) & (v[p].z == tagB); s[(p & 1) * 2] += __uint_as_float(v[p].y); s[(p & 1) * 2 + 1] += __uint_as_float(v[p].w); }
        if (__all(ok)) break;
        if (spins > SPIN_CAP) { s[0] = s[2] = __builtin_nanf(""); break; }
      }
      const f32x2 o0 = {s[0], s[1]}, o1 = {s[2], s[3]};
      asm volatile("ds_write_b64 %0, %1\n\tds_write_b64 %0, %2 offset:2048\n\ts_waitcnt lgkmcnt(0)" :: "v"(lds0 + L_R + lane * 32 + lw * 8), "v"(o0), "v"(o1) : "memory");
    }
    STAMP(3);
    WV(0);
    BAR();
    BAR();
    BAR();
    auto rZ = RSRC(ws + WS_Z2, FRAG_BYTES);
    const unsigned zp0 = lw < 2 ? lw * 5 : 2 + lw * 4, sZ = fj * 128 * CH + zp0 * 1024;
    unsigned ready = 0;
    auto need = [&](const char* fl, int who) {
      if (!((ready >> who) & 1)) {
        for (unsigned spins = 0; spins < SPIN_CAP; ++spins) { ready = poll16(fl, tag, lane); if ((ready >> who) & 1) break; }
        MEMB();
      }
    };
    auto issueZ = [&](int n) {
      const int kp = kpos(n);
      need(ws + WS_F1 + fj * 16 * 128, kp >> 1);
      const unsigned dz = lds0 + (n & 3) * STG2 + 16384 + zp0 * 1024, so = sZ + kp * 18432;
      GS4(rZ, dz, vL, so);
      if (lw < 2) GS(rZ, dz + 4096, vL, so + 4096, 0);
    };
    issueZ(2);
    STAMP(4);
    issueZ(3); issueW(4); issueZ(4);
    if (lw < 2) WV(14); else WV(12);
    BAR();
    STAMP(5);
    issueW(5); issueZ(5);
    if (lw < 2) WV(18); else WV(16);
    BAR();
    for (int n = 6; n < 32; ++n) {
      issueW(n); issueZ(n);
      if (lw < 2) WV(18); else WV(16);
      BAR();
    }
    if (lw < 2) WV(9); else WV(8);
    BAR();
    WV(0);
    BAR();
    BAR();
    if (!has3) { BAR(); return; }
    auto rP = RSRC(ws + WS_P, FRAG_BYTES);
    unsigned vO[4];
#pragma unroll
    for (int q = 0; q < 4; ++q) vO[q] = (8 * q + (lane >> 3)) * 1152 + (((lane & 7) ^ (((lane >> 4) & 3) << 1)) << 4) - q * 1024;
    const unsigned vPs = (lane >> 5) * 16 * CH + (lane & 31) * 16 + 4096;
    const unsigned sP = tt * 5 * 16 * CH, sO = dd * 128;
    auto issueO = [&](int j, int slot) {
      const unsigned dw = lds0 + slot * STG3 + 23552, sw = sO + j * 144 * 1152;
      GL4(rWo, dw + lw * 4096, vO[0], vO[1], vO[2], vO[3], sw + lw * 32 * 1152);
      if (lw >= 2) GL(rWo, dw + 16384 + (lw - 2) * 1024, vO[0], sw + (128 + (lw - 2) * 8) * 1152, 0);
    };
    const int pa = (tt * 80) >> 7, pb = min((tt * 80 + 79) >> 7, 15);
    unsigned rdyA = 0, rdyB = 0;
    auto issueP = [&](int j, int slot) {
      if (!((rdyA & rdyB) >> j & 1)) {
        for (unsigned spins = 0; spins < SPIN_CAP; ++spins) { rdyA = poll16(ws + WS_F2 + pa * 16 * 128, tag, lane); rdyB = poll16(ws + WS_F2 + pb * 16 * 128, tag, lane); if ((rdyA & rdyB) >> j & 1) break; }
        MEMB();
      }
      const unsigned d = lds0 + slot * STG3, sp = sP + j * CH;
      GS4(rP, d + lw * 4096, vL, sp + lw * 16 * CH);
      GS(rP, d + 16384 + lw * 1024, vL, sp + 64 * CH + lw * 1024, 0);
      if (lw < 2) GS(rP, d + 20480 + lw * 1024, vPs, sp + lw * 32 * CH, 0);
      else if (lw == 2) { if (lane < 32) GS(rP, d + 22528, vPs, sp + 64 * CH, 0); }
    };
    issueO(0, 0); issueO(1, 1); issueO(2, 2);
    BAR();
    issueP(0, 0);
    STAMP(6);
    issueP(1, 1); issueP(2, 2);
    if (lw < 3) WV(12); else WV(10);
    BAR();
    STAMP(7);
    if (lw < 3) WV(6); else WV(5);
    BAR();
    int slot = 0;
    for (int j = 3; j < 16; ++j) {
      issueO(j, slot); issueP(j, slot);
      slot = slot == 2 ? 0 : slot + 1;
      if (lw == 2) WV(11); else WV(10);
      BAR();
    }
    WV(0);
    BAR();
    BAR(); BAR();
    STAMP(8);
    if (bid == 0 && tid == 256) __hip_atomic_store((u64*)(ws + WS_EP), tag, RLX_AGENT);
  } else {
    const int wt = wid >> 1, wf = wid & 1;
    const int r16 = lane & 15, g = lane >> 4, qr = (lane >> 2) & 3, p = lane & 3;
    f32x4 acc[4][9];
#pragma unroll
    for (int m = 0; m < 4; ++m)
#pragma unroll
      for (int n = 0; n < 9; ++n) acc[m][n] = (f32x4){0, 0, 0, 0};
    const unsigned bX = lds0 + (64 * wt + r16) * 128 + ((g ^ (r16 >> 1)) << 4);
    const unsigned bW = lds0 + 16384 + (4 * g + qr) * 288 + p * 8 + wf * 128;
    const unsigned bE = lds0 + 16384 + (4 * g + qr) * 288 + p * 8 + wf * 18432 + 256;
    unsigned aX1 = bX ^ 64, aW = bW, aE = bE;
    bf16x8 xb[2][4]; s16x4 wb[6][2];
    auto rdW = [&](int G, unsigned w, unsigned e, int u) {
      const int kk = G / 9, n = G % 9;
      const unsigned a = n < 4 ? w + kk * 9216 + u * 4608 + n * 32 : n < 8 ? w + 18432 + kk * 9216 + u * 4608 + (n - 4) * 32 : e + kk * 9216 + u * 4608;
      wb[G % 6][u] = ldtr(a);
    };
    WAITL0();
    BAR();
    STAMP(1);
    MEMB();
#pragma unroll
    for (int m = 0; m < 4; ++m) xb[0][m] = ld128(bX + m * 2048);
    rdW(0, aW, aE, 0); rdW(0, aW, aE, 1); rdW(1, aW, aE, 0); rdW(1, aW, aE, 1);
    unsigned so = 0;
    for (int k = 0; k < 9; ++k) {
      so = so == 2 * STG1 ? 0 : so + STG1;
      unsigned nX0 = bX + so, nX1 = (bX ^ 64) + so, nW = bW + so, nE = bE + so;
      SB();
#pragma unroll
      for (int G = 0; G < 18; ++G) {
        const int kk = G / 9, n = G % 9;
#pragma unroll
        for (int m = 0; m < 4; ++m) {
          acc[m][n] = MFMA(cat(wb[G % 6][0], wb[G % 6][1]), xb[kk][m], acc[m][n]);
          SB();
          if (G == 0) rdW(2 + (m >> 1), aW, aE, m & 1);
          else if (G <= 14) { if (m < 2) rdW(G + 3, aW, aE, m); else if (m == 2 && G >= 2 && G <= 5) xb[1][G - 2] = ld128(aX1 + (G - 2) * 2048); }
          else if (G >= 16) { if (m < 2) xb[0][(G - 16) * 2 + m] = ld128(nX0 + ((G - 16) * 2 + m) * 2048); else rdW(G - 16, nW, nE, m - 2); }
          SB();
        }
        if (G == 15) { WAITL0(); SB(); BAR(); MEMB(); SB(); }
      }
      aX1 = nX1; aW = nW; aE = nE;
    }
    STAMP(2);
    MEMB(); SB();
    const u64 ep = __hip_atomic_load((const u64*)(ws + WS_EP), RLX_AGENT);
    unsigned short bsr[4];
#pragma unroll
    for (int m = 0; m < 4; ++m) bsr[m] = bias[ti * 128 + wt * 64 + m * 16 + r16];
    SB();
    f32x2 s1[4], s2[4];
#pragma unroll
    for (int m = 0; m < 4; ++m) {
      s1[m] = (f32x2){0.f, 0.f}; s2[m] = (f32x2){0.f, 0.f};
#pragma unroll
      for (int n = 4; n < 8; ++n) { acc[m][n] = gelu4(acc[m][n]); f32x2 a = {acc[m][n][0], acc[m][n][1]}, b = {acc[m][n][2], acc[m][n][3]}; s1[m] += a + b; s2[m] += a * a; s2[m] += b * b; }
    }
    if (wf) {
#pragma unroll
      for (int m = 0; m < 4; ++m) { acc[m][8] = gelu4(acc[m][8]); f32x2 a = {acc[m][8][0], acc[m][8][1]}, b = {acc[m][8][2], acc[m][8][3]}; s1[m] += a + b; s2[m] += a * a; s2[m] += b * b; }
    }
#pragma unroll
    for (int m = 0; m < 4; ++m) *(LAS f32x2*)(size_t)(lds0 + L_T + (64 * wt + 16 * m + r16) * 64 + (wf * 4 + g) * 8) = (f32x2){s1[m].x + s1[m].y, s2[m].x + s2[m].y};
    asm volatile("s_waitcnt lgkmcnt(0)" ::: "memory");
    STAMP(3);
    SB();
    BAR();
#pragma unroll
    for (int m = 0; m < 4; ++m) asm volatile("" : "+v"(acc[m][0]), "+v"(acc[m][1]), "+v"(acc[m][2]), "+v"(acc[m][3]), "+v"(acc[m][8]));
    SB();
    u32x4 za[4][2]; u32x2 zs[4];
#pragma unroll
    for (int m = 0; m < 4; ++m) {
#pragma unroll
      for (int pp = 0; pp < 2; ++pp) {
        const f32x4 a = gelu4(acc[m][2 * pp]), b = gelu4(acc[m][2 * pp + 1]);
        za[m][pp].x = cvt_pk(a[0], a[1]); za[m][pp].y = cvt_pk(a[2], a[3]); za[m][pp].z = cvt_pk(b[0], b[1]); za[m][pp].w = cvt_pk(b[2], b[3]);
      }
      zs[m] = (u32x2){0u, 0u};
    }
    if (!wf) {
#pragma unroll
      for (int m = 0; m < 4; ++m) { const f32x4 a = gelu4(acc[m][8]); zs[m].x = cvt_pk(a[0], a[1]); zs[m].y = cvt_pk(a[2], a[3]); }
    }
    asm volatile("" : "+v"(za[0][0]), "+v"(za[0][1]), "+v"(za[1][0]), "+v"(za[1][1]), "+v"(za[2][0]), "+v"(za[2][1]), "+v"(za[3][0]), "+v"(za[3][1]));
    asm volatile("" : "+v"(zs[0]), "+v"(zs[1]), "+v"(zs[2]), "+v"(zs[3]));
    float bs[4];
#pragma unroll
    for (int m = 0; m < 4; ++m) bs[m] = __uint_as_float((unsigned)bsr[m] << 16);
    const u64 tag = ((u64)(__builtin_amdgcn_readfirstlane((unsigned)(ep >> 32)) + 0x85EBCA6Bu) << 32) | (__builtin_amdgcn_readfirstlane((unsigned)ep) + 0x9E3779B9u);
    asm volatile("" : "+v"(bs[0]), "+v"(bs[1]), "+v"(bs[2]), "+v"(bs[3]));
    STAMP(4);
    BAR();
    STAMP(5);
    MEMB();
    {
      auto rZ = RSRC(ws + WS_Z2, FRAG_BYTES);
      const unsigned sl = g * 16 + (r16 ^ (4 * (g >> 1)));
      const bool sw = g & 1;
#pragma unroll
      for (int m = 0; m < 4; ++m) {
        const f32x4 q4 = *(LAS f32x4*)(size_t)(lds0 + L_R + (64 * wt + 16 * m + r16) * 32) + *(LAS f32x4*)(size_t)(lds0 + L_R + (64 * wt + 16 * m + r16) * 32 + 16);
        const float mean = (q4[0] + q4[2]) * (1.0f / 2304.0f), var = fmaxf((q4[1] + q4[3]) * (1.0f / 2304.0f) - mean * mean, 0.f);
        f32x2 rs; rs.x = __builtin_amdgcn_rsqf(var + 1e-5f); rs.y = -mean * rs.x;
        const unsigned dst = (fj * 128 + ti * 8 + wt * 4 + m) * CH, ld = lds0 + wt * STG2 + 16384 + m * CH;
#pragma unroll
        for (int pp = 0; pp < 2; ++pp) {
          const f32x4 a = acc[m][4 + 2 * pp] * rs.x + rs.y, b = acc[m][5 + 2 * pp] * rs.x + rs.y;
          const unsigned a0 = cvt_pk(a[0], a[1]), a1 = cvt_pk(a[2], a[3]), b0 = cvt_pk(b[0], b[1]), b1 = cvt_pk(b[2], b[3]);
          u32x4 v; v.x = sw ? b0 : a0; v.y = sw ? b1 : a1; v.z = sw ? a0 : b0; v.w = sw ? a1 : b1;
          __builtin_amdgcn_raw_buffer_store_b128(v, rZ, dst + sl * 16 + wf * 2048 + pp * 1024, 0, 16);
          *(LAS u32x4*)(size_t)(ld + wf * 2048 + pp * 1024 + sl * 16) = v;
        }
        if (wf) {
          const f32x4 a = acc[m][8] * rs.x + rs.y; u32x2 v; v.x = cvt_pk(a[0], a[1]); v.y = cvt_pk(a[2], a[3]);
          __builtin_amdgcn_raw_buffer_store_b64(v, rZ, dst + sl * 8 + 4096, 0, 16);
          *(LAS u32x2*)(size_t)(ld + 4096 + sl * 8) = v;
        }
      }
    }
    STAMP(6);
    if (wf == 0) g2_phase<0>(lds0, wid, wt, lane, ti, fj, ws, tag, za, zs, bs); else g2_phase<1>(lds0, wid, wt, lane, ti, fj, ws, tag, za, zs, bs);
    if (!has3) return;
    if (wt == 0) g3_compute<0>(lds0, wf, lane, out, tt, dd); else g3_compute<1>(lds0, wf, lane, out, tt, dd);
    STAMP(13);
  }
}

extern "C" void kernel_launch(void* const* d_in, const int* in_sizes, int n_in, void* d_out, int out_size, void* d_ws, size_t ws_size, hipStream_t stream) {
  (void)hipFuncSetAttribute((const void*)gmlp_fused, hipFuncAttributeMaxDynamicSharedMemorySize, LDS_BYTES);
  gmlp_fused<<<256, 512, LDS_BYTES, stream>>>((const char*)d_in[0], (const char*)d_in[1], (const char*)d_in[2], (const u16*)d_in[3], (const char*)d_in[4], (char*)d_out, (char*)d_ws);
}
```

```cpp
#include <hip/hip_runtime.h>
#include <hip/hip_bf16.h>
#include <type_traits>
typedef unsigned short u16;
typedef unsigned long long u64;
typedef short s16x4 __attribute__((ext_vector_type(4)));
typedef short bf16x8 __attribute__((ext_vector_type(8)));
typedef float f32x2 __attribute__((ext_vector_type(2)));
typedef float f32x4 __attribute__((ext_vector_type(4)));
typedef unsigned u32x4 __attribute__((ext_vector_type(4)));
typedef unsigned u32x2 __attribute__((ext_vector_type(2)));
#define LAS __attribute__((address_space(3)))
#define DEV __device__ __forceinline__
#define BAR() __builtin_amdgcn_s_barrier()
#define SB() __builtin_amdgcn_sched_barrier(0)
#define MEMB() asm volatile("" ::: "memory")
#define WAITL0() __builtin_amdgcn_s_waitcnt(0xC07F)
#define WV(N) asm volatile("s_waitcnt vmcnt(%0)" ::"i"(N) : "memory")
#define MFMA(a, b, c) __builtin_amdgcn_mfma_f32_16x16x32_bf16(a, b, c, 0, 0, 0)
#define MFMA16(a, b, c) __builtin_amdgcn_mfma_f32_16x16x16bf16_1k(a, b, c, 0, 0, 0)
#define GL(R, D, V, S, I) __builtin_amdgcn_raw_ptr_buffer_load_lds(R, (LAS void*)(size_t)(D), 16, V, S, I, 0)
#define GL4(R, D, V0, V1, V2, V3, S) do { GL(R, D, V0, S, 0); GL(R, D, V1, S, 1024); GL(R, D, V2, S, 2048); GL(R, D, V3, S, 3072); } while (0)
#define GS(R, D, V, S, I) __builtin_amdgcn_raw_ptr_buffer_load_lds(R, (LAS void*)(size_t)(D), 16, V, S, I, HAUX)
#define GS4(R, D, V, S) do { GS(R, D, V, S, 0); GS(R, D, V, S, 1024); GS(R, D, V, S, 2048); GS(R, D, V, S, 3072); } while (0)
#define ABL 0
#define SAUX 16
#define HAUX 16
#define RSRC(p, n) __builtin_amdgcn_make_buffer_rsrc((void*)(p), 0, (n), 0x00020000)
#define RLX_AGENT __ATOMIC_RELAXED, __HIP_MEMORY_SCOPE_AGENT
#define WARM1 0
#define ROT1 2

constexpr int CH = 4608;
constexpr int FRAG_BYTES = 16 * 128 * CH;
constexpr size_t WS_Z2 = 0, WS_P = FRAG_BYTES, WS_ST = 2 * (size_t)FRAG_BYTES, WS_F1 = WS_ST + 16 * 16 * 128 * 16, WS_F2 = WS_F1 + 256 * 128, WS_EP = WS_F2 + 256 * 128;
constexpr int STG1 = 53248, STG2 = 34816, STGP = 23040, STGO = 18432, L_O = 3 * STGP, L_X = 151552, LDS_BYTES = 163840;
#define CODE_KB 31
#define PRE 1
#define WARM3 0
#define WU 2
#define ROT3 0
constexpr int L_T = 139264, L_R = L_T + 8192, L_BIAS = 159744, L_TAG = L_BIAS + 256, L_DUMMY = 161280;
constexpr unsigned SPIN_CAP = 1u << 16;
#define STAMP(I)
#define DUMP()

DEV unsigned cvt_pk(float lo, float hi) { unsigned r; asm("v_cvt_pk_bf16_f32 %0, %1, %2" : "=v"(r) : "v"(lo), "v"(hi)); return r; }
DEV bf16x8 ld128(unsigned a) { return *(LAS bf16x8*)(size_t)a; }
DEV s16x4 ld64(unsigned a) { return *(LAS s16x4*)(size_t)a; }
DEV s16x4 ldtr(unsigned a) { return __builtin_amdgcn_ds_read_tr16_b64_v4i16((LAS s16x4*)(size_t)a); }
DEV bf16x8 cat(s16x4 a, s16x4 b) { return __builtin_shufflevector(a, b, 0, 1, 2, 3, 4, 5, 6, 7); }
DEV float bflo(unsigned v) { return __uint_as_float(v << 16); }
DEV float bfhi(unsigned v) { return __uint_as_float(v & 0xffff0000u); }
DEV f32x2 gelu2(f32x2 x) {
  const float k1 = -2.0f * 0.7978845608028654f * 1.4426950408889634f, k2 = k1 * 0.044715f;
  f32x2 t = x * (x * x * k2 + k1), e;
  e.x = __builtin_amdgcn_exp2f(t.x); e.y = __builtin_amdgcn_exp2f(t.y);
  e = e + 1.0f;
  e.x = __builtin_amdgcn_rcpf(e.x); e.y = __builtin_amdgcn_rcpf(e.y);
  return x * e;
}
DEV f32x4 gelu4(f32x4 v) { f32x2 a = gelu2((f32x2){v[0], v[1]}), b = gelu2((f32x2){v[2], v[3]}); return (f32x4){a.x, a.y, b.x, b.y}; }

struct G2Addr { unsigned w[2], z[2][2], s[2]; };
template <int WF, int OFF, bool PUB, bool FIRST>
DEV void g2_step(G2Addr& A, int dlt, f32x4 (&acc)[4][5], bf16x8 (&wX)[4], s16x4 (&tX)[5][2], bf16x8 (&wY)[4], s16x4 (&tY)[4][2], u64* flag, u64 tag, int lane) {
  constexpr int KX = WF, KY = 1 - WF;
  SB(); BAR(); MEMB(); SB();
#pragma unroll
  for (int i = 0; i < 16; ++i) {
    const int n = i >> 2, m = i & 3;
    if (!FIRST) acc[m][n] = MFMA(cat(tY[n][0], tY[n][1]), wY[m], acc[m][n]);
    SB();
    if (i == 0) wX[0] = ld128(A.w[KX] + OFF);
    else if (i == 1) tX[0][0] = ldtr(A.z[0][0] + OFF + KX * 9216);
    else if (i == 2) tX[0][1] = ldtr(A.z[1][0] + OFF + KX * 9216);
    else if (i < 6) wX[i - 2] = ld128(A.w[KX] + OFF + (i - 2) * 2048);
    else if (i < 12) { const int nn = 1 + ((i - 6) >> 1), u = (i - 6) & 1; tX[nn][u] = ldtr(A.z[u][nn & 1] + OFF + KX * 9216 + (nn >> 1) * 1024); }
    else if (i < 14) tX[4][i - 12] = ldtr(A.s[i - 12] + OFF + KX * 9216);
    SB();
  }
#pragma unroll
  for (int i = 0; i < 20; ++i) {
    const int n = i >> 2, m = i & 3;
    acc[m][n] = MFMA(cat(tX[n][0], tX[n][1]), wX[m], acc[m][n]);
    SB();
    if (i == 0) wY[0] = ld128(A.w[KY] + OFF);
    else if (i == 1) tY[0][0] = ldtr(A.z[0][0] + OFF + KY * 9216);
    else if (i == 2) tY[0][1] = ldtr(A.z[1][0] + OFF + KY * 9216);
    else if (i < 6) wY[i - 2] = ld128(A.w[KY] + OFF + (i - 2) * 2048);
    else if (i < 12) { const int nn = 1 + ((i - 6) >> 1), u = (i - 6) & 1; tY[nn][u] = ldtr(A.z[u][nn & 1] + OFF + KY * 9216 + (nn >> 1) * 1024); }
    else if (OFF != 0 && i < 16) {
      const int u = i & 1;
      if (i < 14) { A.w[u] += dlt; A.s[u] += dlt; asm volatile("" : "+v"(A.w[u]), "+v"(A.s[u])); }
      else { A.z[u][0] += dlt; A.z[u][1] += dlt; asm volatile("" : "+v"(A.z[u][0]), "+v"(A.z[u][1])); }
    }
    SB();
  }
  if (PUB) {
    WV(0);
    if (lane == 0) __hip_atomic_store(flag, tag, RLX_AGENT);
  }
  WAITL0();
}

struct G3Frag { bf16x8 p[2][5]; s16x4 w[2][2][2], ws[2], ps[3]; };
template <int KH, bool MM, bool RD>
DEV void g3_stage(f32x4 (&acc)[5][2], const G3Frag& c, G3Frag& n, unsigned aP, unsigned aS, unsigned a0, unsigned a1) {
  constexpr int M0 = KH ? 3 : 0, NS = KH ? 2 : 3, NM = 20 + 2 * NS, NR = 20 + NS;
#pragma unroll
  for (int i = 0; i < NM; ++i) {
    if (MM && !(ABL & 64)) {
      if (i >= 10 && i < 10 + 2 * NS) acc[M0 + ((i - 10) >> 1)][i & 1] = MFMA16(c.ws[i & 1], c.ps[(i - 10) >> 1], acc[M0 + ((i - 10) >> 1)][i & 1]);
      else { const int ii = i < 10 ? i : i - 2 * NS, P = ii / 10, m = KH ? 4 - ((ii % 10) >> 1) : (ii % 10) >> 1, cc = ii & 1; acc[m][cc] = MFMA(cat(c.w[P][cc][0], c.w[P][cc][1]), c.p[P][m], acc[m][cc]); }
    }
    if (MM && (ABL & 64) && i == 0) { for (int P = 0; P < 2; ++P) { for (int m = 0; m < 5; ++m) asm volatile("" :: "v"(c.p[P][m])); for (int q = 0; q < 4; ++q) asm volatile("" :: "v"(c.w[P][q >> 1][q & 1])); } asm volatile("" :: "v"(c.ws[0]), "v"(c.ws[1]), "v"(c.ps[0]), "v"(c.ps[1])); if (NS == 3) asm volatile("" :: "v"(c.ps[2])); }
    SB();
    if (RD && !((ABL & 32) && MM)) {
#pragma unroll
      for (int r = (i * 3) / 2; r < ((i + 1) * 3) / 2; ++r) {
        if (r < 18) { const int P = r / 9, q = r % 9;
          if (q < 4) n.w[P][q >> 1][q & 1] = ldtr(((q >> 1) ? a1 : a0) + (2 * KH + P) * 4096 + (q & 1) * 2048);
          else n.p[P][q - 4] = ld128(aP + P * 1024 + (q - 4) * 4096); }
        else if (r < 20) n.ws[r - 18] = ldtr(((r - 18) ? a1 : a0) + 16384);
        else if (r < NR) n.ps[r - 20] = ld64(aS + (M0 + r - 20) * 512);
      }
    }
    SB();
  }
}
template <int KH>
DEV void g3_compute(unsigned lds0, int wid, int dh, int lane, char* out, int tt, int dd) {
  f32x4 acc[5][2];
#pragma unroll
  for (int m = 0; m < 5; ++m) { acc[m][0] = (f32x4){0, 0, 0, 0}; acc[m][1] = (f32x4){0, 0, 0, 0}; }
  const int g = lane >> 4, qr = (lane >> 2) & 3, p = lane & 3;
  const int rr = 4 * g + qr;
  unsigned bW[2];
#pragma unroll
  for (int c = 0; c < 2; ++c) bW[c] = lds0 + L_O + rr * 128 + ((((2 * dh + c) * 2 + (p >> 1)) ^ (((rr >> 1) & 3) << 1)) << 4) + (p & 1) * 8;
  const unsigned bP = lds0 + lane * 16 + KH * 2048, bS = lds0 + 20480 + lane * 8;
  G3Frag f0, f1;
  if (ABL & 32) g3_stage<KH, false, true>(acc, f0, f1, bP, bS, bW[0], bW[1]);
  WAITL0();
  SB(); BAR(); MEMB(); SB();
  g3_stage<KH, false, true>(acc, f1, f0, bP, bS, bW[0], bW[1]);
  WAITL0();
  unsigned sp = STGP, sw = STGO;
  for (int j = 1; j < 16; j += 2) {
    SB(); BAR(); MEMB(); SB();
    g3_stage<KH, true, true>(acc, f0, f1, bP + sp, bS + sp, bW[0] + sw, bW[1] + sw);
    sp = sp == 2 * STGP ? 0 : sp + STGP; sw = sw == 4 * STGO ? 0 : sw + STGO;
    WAITL0();
    SB(); BAR(); MEMB(); SB();
    g3_stage<KH, true, true>(acc, f1, f0, bP + sp, bS + sp, bW[0] + sw, bW[1] + sw);
    sp = sp == 2 * STGP ? 0 : sp + STGP; sw = sw == 4 * STGO ? 0 : sw + STGO;
    WAITL0();
  }
#pragma unroll
  for (int m = 0; m < 5; ++m) *(LAS f32x4*)(size_t)(lds0 + ((KH * 2 + dh) * 5 + m) * 1024 + lane * 16) = acc[m][1 - KH];
  asm volatile("s_waitcnt lgkmcnt(0)" ::: "memory");
  BAR(); MEMB();
#pragma unroll
  for (int m = 0; m < 5; ++m) {
    const f32x4 a = acc[m][KH] + *(LAS f32x4*)(size_t)(lds0 + (((1 - KH) * 2 + dh) * 5 + m) * 1024 + lane * 16);
    const int t = tt * 80 + m * 16 + (lane & 15);
    u32x2 v; v.x = cvt_pk(a[0], a[1]); v.y = cvt_pk(a[2], a[3]);
    if (t < 2048) *(u32x2*)(out + (size_t)t * 1152 + dd * 128 + (dh * 2 + KH) * 32 + (lane >> 4) * 8) = v;
  }
}

template <int WF>
DEV void g2_phase(unsigned lds0, int wid, int wt, int lane, int ti, int fj, char* ws, u64 tag, const u32x4 (&za)[4][2], const u32x2 (&zs)[4], const float (&bs)[4]) {
  const int r16 = lane & 15, g = lane >> 4, qr = (lane >> 2) & 3, p = lane & 3;
  G2Addr A0;
  A0.w[0] = lds0 + (64 * wt + r16) * 128 + ((g ^ (r16 >> 1)) << 4); A0.w[1] = A0.w[0] ^ 64;
#pragma unroll
  for (int u = 0; u < 2; ++u) {
    const unsigned sl = p * 16 + 8 * (g & 1) + 4 * (u ^ (p >> 1)) + qr;
#pragma unroll
    for (int h = 0; h < 2; ++h) A0.z[u][h] = lds0 + 16384 + (g >> 1) * CH + WF * 2048 + sl * 16 + ((h ^ (p & 1)) * 8);
    A0.s[u] = lds0 + 16384 + (g >> 1) * CH + 4096 + sl * 8;
  }
  asm volatile("" : "+v"(A0.w[0]), "+v"(A0.w[1]), "+v"(A0.s[0]), "+v"(A0.s[1]), "+v"(A0.z[0][0]), "+v"(A0.z[0][1]), "+v"(A0.z[1][0]), "+v"(A0.z[1][1]));
  f32x4 acc[4][5];
#pragma unroll
  for (int m = 0; m < 4; ++m)
#pragma unroll
    for (int n = 0; n < 5; ++n) { const float b0 = (n < 4 || WF == 0) ? bs[m] : 0.f; acc[m][n] = (f32x4){b0, b0, b0, b0}; }
  bf16x8 wX[4], wY[4]; s16x4 tX[5][2], tY[4][2];
  u64* f1 = (u64*)(ws + WS_F1 + (fj * 16 + ti) * 128) + wid;
  WAITL0();
  int dlt = 2 * STG2;
  g2_step<WF, 0, true, true>(A0, dlt, acc, wX, tX, wY, tY, f1, tag, lane);
  STAMP(7);
  g2_step<WF, STG2, false, false>(A0, dlt, acc, wX, tX, wY, tY, f1, tag, lane);
  STAMP(8);
  for (int k = 1; k < 16; ++k) {
    dlt = -dlt;
    g2_step<WF, 0, false, false>(A0, dlt, acc, wX, tX, wY, tY, f1, tag, lane);
    g2_step<WF, STG2, false, false>(A0, dlt, acc, wX, tX, wY, tY, f1, tag, lane);
  }
  BAR();
  if (WF == 1) {
#pragma unroll
    for (int m = 0; m < 4; ++m) *(LAS f32x4*)(size_t)(lds0 + L_X + (wt * 4 + m) * 1024 + lane * 16) = acc[m][4];
  }
#pragma unroll
  for (int n = 0; n < 4; ++n)
#pragma unroll
    for (int m = 0; m < 4; ++m) acc[m][n] = MFMA(cat(tY[n][0], tY[n][1]), wY[m], acc[m][n]);
  STAMP(10);
  auto rP = RSRC(ws + WS_P, FRAG_BYTES);
  auto gate = [&](f32x4 a, unsigned z0, unsigned z1, unsigned& o0, unsigned& o1) {
    const f32x2 x = (f32x2){a[0], a[1]} * (f32x2){bflo(z0), bfhi(z0)}, y = (f32x2){a[2], a[3]} * (f32x2){bflo(z1), bfhi(z1)};
    o0 = cvt_pk(x.x, x.y); o1 = cvt_pk(y.x, y.y);
  };
  asm volatile("s_waitcnt lgkmcnt(0)" ::: "memory");
  BAR();
  MEMB();
#pragma unroll
  for (int m = 0; m < 4; ++m) {
    const unsigned dst = ((ti * 8 + wt * 4 + m) * 16 + fj) * CH;
    if (WF == 0) {
      const f32x4 a = acc[m][4] + *(LAS f32x4*)(size_t)(lds0 + L_X + (wt * 4 + m) * 1024 + lane * 16);
      u32x2 v; unsigned o0, o1; gate(a, zs[m].x, zs[m].y, o0, o1); v.x = o0; v.y = o1;
      __builtin_amdgcn_raw_buffer_store_b64(v, rP, dst + lane * 8 + 4096, 0, SAUX);
    }
#pragma unroll
    for (int pp = 0; pp < 2; ++pp) {
      unsigned o0, o1, o2, o3; gate(acc[m][2 * pp], za[m][pp].x, za[m][pp].y, o0, o1); gate(acc[m][2 * pp + 1], za[m][pp].z, za[m][pp].w, o2, o3);
      __builtin_amdgcn_raw_buffer_store_b128((u32x4){o0, o1, o2, o3}, rP, dst + lane * 16 + (2 * WF + pp) * 1024, 0, SAUX);
    }
  }
  STAMP(11);
  WV(0);
  if (lane == 0) __hip_atomic_store((u64*)(ws + WS_F2 + (ti * 16 + fj) * 128) + wid, tag, RLX_AGENT);
  BAR();
  STAMP(12);
}

DEV u64 poll16(const char* base, u64 tag, int lane) {
  const u64 v = __hip_atomic_load((const u64*)(base + (lane >> 2) * 128) + (lane & 3), RLX_AGENT);
  return __builtin_amdgcn_ballot_w64(v == tag);
}
DEV bool got(u64 mask, int who) { return ((unsigned)(mask >> (4 * who)) & 15u) == 15u; }

__global__ __launch_bounds__(512, 2) void gmlp_fused(const char* __restrict__ X, const char* __restrict__ Win, const char* __restrict__ Wsp, const u16* __restrict__ bias,
                                                     const char* __restrict__ Wo, char* __restrict__ out, char* __restrict__ ws) {
  extern __shared__ __attribute__((aligned(16))) char smem[];
  u64 pc0; asm volatile("s_getpc_b64 %0" : "=s"(pc0));
  const int tid = threadIdx.x, wid = __builtin_amdgcn_readfirstlane(tid >> 6), lane = tid & 63;
  const int bid = blockIdx.x, xg = bid & 7, rk = bid >> 3, il = rk & 7, jl = rk >> 3, ti = (xg & 1) * 8 + il, fj = (xg >> 1) * 4 + jl;
  const int L3 = xg * 30 + rk, tt = L3 / 9, dd = L3 - tt * 9;
  const bool has3 = rk < 30 && L3 < 234;
  const unsigned lds0 = (unsigned)(size_t)(LAS char*)smem;
  STAMP(0);
  if (__builtin_expect(wid >= 4, 1)) {
    __builtin_amdgcn_s_setprio(3);
    const int lw = wid - 4;
    auto rX = RSRC(X, 2048 * 1152); auto rWi = RSRC(Win, 576 * 9216);
    unsigned vX[4], vW[9];
    const unsigned sX = (ti * 128 + lw * 32) * 1152, sW = (lw & 1) * 32 * 9216 + (lw >> 1) * 4608 + fj * 288;
    const unsigned dX = lds0 + lw * 4096, dW = lds0 + 16384 + lw * 9216;
    const int rot = ROT1 == 0 ? 0 : ROT1 == 1 ? il + 2 * jl : 3 * ((il + jl) % 3);
    auto k1 = [&](int k) { int kk = k + rot; kk = kk >= 9 ? kk - 9 : kk; kk = kk >= 9 ? kk - 9 : kk; return kk; };
    auto issue1 = [&](int k, int slot) {
      const int kk = k1(k);
      const unsigned so = slot * STG1, sx = sX + kk * 128, sw = sW + kk * 64 * 9216;
      GL4(rX, dX + so, vX[0], vX[1], vX[2], vX[3], sx);
      GL(rWi, dW + so, vW[0], sw, 0); GL(rWi, dW + so, vW[1], sw, 1024); GL(rWi, dW + so, vW[2], sw, 2048);
      GL(rWi, dW + so + 3072, vW[3], sw, 0); GL(rWi, dW + so + 3072, vW[4], sw, 1024); GL(rWi, dW + so + 3072, vW[5], sw, 2048);
      GL(rWi, dW + so + 6144, vW[6], sw, 0); GL(rWi, dW + so + 6144, vW[7], sw, 1024); GL(rWi, dW + so + 6144, vW[8], sw, 2048);
    };
    const unsigned vL = lane * 16;
    {
#pragma unroll
      for (int q = 0; q < 4; ++q) { const int row = 8 * q + (lane >> 3); vX[q] = row * 1152 + (((lane & 7) ^ ((row >> 1) & 7)) << 4) - q * 1024; }
      GL4(rX, dX, vX[0], vX[1], vX[2], vX[3], sX + k1(0) * 128);
      SB();
      const unsigned sw = sW + k1(0) * 64 * 9216;
#pragma unroll
      for (int q = 0; q < 9; ++q) {
        const unsigned c16 = q * 64 + lane, R = (c16 * 3641u) >> 16, c = (c16 - R * 18) * 16, k = 8 * ((R >> 2) & 3) + 4 * (R >> 4) + (R & 3);
        vW[q] = k * 9216 + c - (q % 3) * 1024;
        if (q % 3 == 0) GL(rWi, dW + (q / 3) * 3072, vW[q], sw, 0); else if (q % 3 == 1) GL(rWi, dW + (q / 3) * 3072, vW[q], sw, 1024); else GL(rWi, dW + (q / 3) * 3072, vW[q], sw, 2048);
        SB();
      }
    }
    constexpr int NW1 = 0;
    issue1(1, 1); issue1(2, 2);
    auto rWs = RSRC(Wsp, 2048 * 4096); auto rWo = RSRC(Wo, 2304 * 1152);
    unsigned vS[4];
#pragma unroll
    for (int q = 0; q < 4; ++q) { const int row = 8 * q + (lane >> 3); vS[q] = row * 4096 + (((lane & 7) ^ ((row >> 1) & 7)) << 4) - q * 1024; }
    const unsigned sWs = (ti * 128 + lw * 32) * 4096;
    auto kpos = [&](int n) { int t2 = n < 2 ? 2 * ti + n : (n - 2 < 2 * ti ? n - 2 : n); return t2; };
    auto issueW = [&](int n) { GL4(rWs, lds0 + (n & 3) * STG2 + lw * 4096, vS[0], vS[1], vS[2], vS[3], sWs + kpos(n) * 128); };
    WV(26 + NW1); BAR();
    STAMP(1);
    auto warm = [&](int q0, int q1) {
#pragma unroll
      for (int q = q0; q < q1; ++q) {
        if (q < 3) GL(rWo, lds0 + L_DUMMY, vL, bid * 10368 + lw * 2592 + (q < 2 ? q * 1024 : 1568), 0);
        else GL(rWs, lds0 + L_DUMMY, vL, bid * 32768 + lw * 8192 + (q - 3) * 1024, 0);
      }
    };
    WV(13); BAR(); issue1(3, 0);
    WV(13); BAR(); issue1(4, 1);
    WV(13); BAR(); issue1(5, 2); warm(0, 3);
    WV(16); BAR(); issue1(6, 0); warm(3, 6);
    WV(19); BAR(); issue1(7, 1); warm(6, 9);
    WV(19); BAR(); issue1(8, 2); warm(9, 11);
    WV(18); BAR();
    WV(2); BAR();
    const u64 ep = __hip_atomic_load((const u64*)(ws + WS_EP), RLX_AGENT);
    if (lw == 0) __builtin_amdgcn_raw_ptr_buffer_load_lds(RSRC(bias, 4096), (LAS void*)(size_t)(lds0 + L_BIAS), 4, lane * 4, ti * 256, 0, 0);
    BAR();
    const unsigned tagA = __builtin_amdgcn_readfirstlane((unsigned)ep) + 0x9E3779B9u, tagB = __builtin_amdgcn_readfirstlane((unsigned)(ep >> 32)) + 0x85EBCA6Bu;
    const u64 tag = ((u64)tagB << 32) | tagA;
    issueW(0); issueW(1);
    if (lw == 0) { const u32x2 tg = {tagA, tagB}; asm volatile("ds_write_b64 %0, %1\n\ts_waitcnt lgkmcnt(0)" :: "v"(lds0 + L_TAG), "v"(tg) : "memory"); }
    BAR();
    STAMP(2);
    {
      auto rS = RSRC(ws + WS_ST, 16 * 16 * 128 * 16);
      if (lane < 32) {
        const int row = lw * 32 + lane;
        f32x4 v0, v1, v2, v3;
        asm volatile("ds_read_b128 %0, %4\n\tds_read_b128 %1, %4 offset:16\n\tds_read_b128 %2, %4 offset:32\n\tds_read_b128 %3, %4 offset:48\n\ts_waitcnt lgkmcnt(0)"
                     : "=&v"(v0), "=&v"(v1), "=&v"(v2), "=&v"(v3) : "v"(lds0 + L_T + row * 64) : "memory");
        const f32x4 s = (v0 + v1) + (v2 + v3);
        u32x4 gq; gq.x = tagA; gq.y = __float_as_uint(s[0] + s[2]); gq.z = tagB; gq.w = __float_as_uint(s[1] + s[3]);
        __builtin_amdgcn_raw_buffer_store_b128(gq, rS, ((ti * 16 + fj) * 128 + row) * 16, 0, 16);
      }
      STAMP(9);
      for (unsigned spins = 0; spins < SPIN_CAP; ++spins) {
        MEMB();
        const u32x4 v = __builtin_amdgcn_raw_buffer_load_b128(rS, (lane & 15) * 512, (ti * 16 + lw * 4) * 2048, 16);
        if (__all((v.x == tagA) & (v.z == tagB))) break;
      }
      STAMP(10);
      f32x4 s;
      for (unsigned spins = 0;; ++spins) {
        u32x4 v[8]; bool ok = true;
        MEMB();
#pragma unroll
        for (int p = 0; p < 8; ++p) v[p] = __builtin_amdgcn_raw_buffer_load_b128(rS, lane * 16 + (p & 1) * 1024, (ti * 16 + lw * 4 + (p >> 1)) * 2048, 16);
        s = (f32x4){0.f, 0.f, 0.f, 0.f};
#pragma unroll
        for (int p = 0; p < 8; ++p) { ok &= (v[p].x == tagA) & (v[p].z == tagB); s[(p & 1) * 2] += __uint_as_float(v[p].y); s[(p & 1) * 2 + 1] += __uint_as_float(v[p].w); }
        if (__all(ok)) break;
        if (spins > SPIN_CAP) { s[0] = s[2] = __builtin_nanf(""); break; }
      }
      const f32x2 o0 = {s[0], s[1]}, o1 = {s[2], s[3]};
      asm volatile("ds_write_b64 %0, %1\n\tds_write_b64 %0, %2 offset:2048\n\ts_waitcnt lgkmcnt(0)" :: "v"(lds0 + L_R + lane * 32 + lw * 8), "v"(o0), "v"(o1) : "memory");
    }
    STAMP(3);
    WV(0);
    BAR();
    BAR();
    issueW(2); issueW(3);
    BAR();
    auto rZ = RSRC(ws + WS_Z2, FRAG_BYTES);
    const unsigned OOB = 0x80000000u;
    const unsigned zp0 = lw < 2 ? lw * 5 : 2 + lw * 4, sZ = fj * 128 * CH + zp0 * 1024;
    const unsigned vZ5 = lw < 2 ? vL : OOB;
    u64 ready = 0;
    auto issueZ = [&](int n) {
      const int kp = kpos(n);
      if (__builtin_expect(!got(ready, kp >> 1), 0)) { for (unsigned spins = 0; !got(ready, kp >> 1) && spins < SPIN_CAP; ++spins) ready = poll16(ws + WS_F1 + fj * 16 * 128, tag, lane); }
      MEMB();
      const unsigned dz = lds0 + (n & 3) * STG2 + 16384 + zp0 * 1024, so = sZ + kp * 18432;
      GS4(rZ, dz, vL, so);
      GS(rZ, lw < 2 ? dz + 4096 : lds0 + L_DUMMY, vZ5, so + 4096, 0);
    };
    issueZ(2);
    STAMP(4);
    issueZ(3); issueW(4); issueZ(4);
    WV(14);
    BAR();
    STAMP(5);
    for (int n = 5; n < 32; ++n) {
      issueW(n); issueZ(n);
      WV(18);
      BAR();
    }
    WV(9); BAR(); WV(0); BAR();
    unsigned vO[2], vOh;
#pragma unroll
    for (int q = 0; q < 2; ++q) { const int row = 36 * lw + 8 * q + (lane >> 3); vO[q] = row * 1152 + (((lane & 7) ^ (((row >> 1) & 3) << 1)) << 4); }
    { const int row = 36 * lw + 32 + ((lane >> 3) & 3); vOh = row * 1152 + (((lane & 7) ^ (((row >> 1) & 3) << 1)) << 4); }
    const unsigned vO1 = vO[1] - 1024, vO2 = vO[0] + 16 * 1152 - 2048, vO3 = vO[1] + 16 * 1152 - 3072;
    const int r3 = ROT3 ? (dd * 7) >> 2 : 0;
    const unsigned sO = dd * 128;
    BAR(); BAR(); BAR();
    if (!has3) return;
    auto issueO = [&](int n, int slot) {
      const unsigned d = lds0 + L_O + slot * STGO + lw * 4608, s0 = sO + ((n + r3) & 15) * 144 * 1152;
      GL4(rWo, d, vO[0], vO1, vO2, vO3, s0);
      if (lane < 32) GL(rWo, d + 4096, vOh, s0, 0);
    };
    issueO(0, 0); issueO(1, 1); issueO(2, 2); issueO(3, 3); issueO(4, 4);
    auto rP = RSRC(ws + WS_P, FRAG_BYTES);
    const unsigned vPs = lw < 3 ? (lane >> 5) * 16 * CH + (lane & 31) * 16 + 4096 : OOB;
    const int limS = lw == 2 ? 32 : 64;
    const unsigned sP = tt * 5 * 16 * CH;
    const int pa = (tt * 80) >> 7, pb = min((tt * 80 + 79) >> 7, 15);
    u64 rdy = 0;
    auto issueP = [&](int n, int slot) {
      const int j = (n + r3) & 15;
      if (__builtin_expect(!got(rdy, j), 0)) { for (unsigned spins = 0; !got(rdy, j) && spins < SPIN_CAP; ++spins) rdy = poll16(ws + WS_F2 + pa * 16 * 128, tag, lane) & poll16(ws + WS_F2 + pb * 16 * 128, tag, lane); }
      MEMB();
      const unsigned d = lds0 + slot * STGP, sp = sP + j * CH;
      GS4(rP, d + lw * 4096, vL, sp + lw * 16 * CH);
      GS(rP, d + 16384 + lw * 1024, vL, sp + 64 * CH + lw * 1024, 0);
      if (lane < limS) GS(rP, lw < 3 ? d + 20480 + lw * 1024 : lds0 + L_DUMMY, vPs, sp + lw * 32 * CH, 0);
    };
    issueP(0, 0);
    STAMP(6);
    issueP(1, 1); issueP(2, 2);
    WV(12);
    BAR();
    STAMP(7);
    WV(6);
    BAR(); issueP(3, 0); issueO(5, 0); WV(11);
    int sp3 = 1, so5 = 1;
    for (int n = 2; n < 12; ++n) {
      BAR();
      issueP(n + 2, sp3); issueO(n + 4, so5);
      sp3 = sp3 == 2 ? 0 : sp3 + 1; so5 = so5 == 4 ? 0 : so5 + 1;
      WV(16);
    }
    BAR(); issueP(14, 2); WV(6);
    BAR(); issueP(15, 0); WV(6);
    BAR(); WV(0);
    BAR();
    BAR(); BAR();
    STAMP(8);
    DUMP();
    if (bid == 0 && tid == 256) __hip_atomic_store((u64*)(ws + WS_EP), tag, RLX_AGENT);
  } else {
    const int wt = wid >> 1, wf = wid & 1;
    const int r16 = lane & 15, g = lane >> 4, qr = (lane >> 2) & 3, p = lane & 3;
    if ((PRE & 1) && wid == 0) {
      auto rC = RSRC((const char*)(pc0 & ~(u64)255), CODE_KB * 1024);
      GL(rC, lds0 + L_DUMMY, lane * 16, rk * 1024, 0); GL(rC, lds0 + L_DUMMY, lane * 16, rk * 1024 + 32768, 0);
    }
    if ((PRE & 2) && wid == 1) {
      auto rA = RSRC(ws, 21 << 20); auto rB = RSRC(Wsp, 2048 * 4096); auto rD = RSRC(Wo, 2304 * 1152); auto rE = RSRC(out, 2048 * 1152);
      if (lane < 10) __builtin_amdgcn_raw_ptr_buffer_load_lds(rA, (LAS void*)(size_t)(lds0 + L_DUMMY), 4, lane * 65536, rk * 655360, 0, 16);
      if (lane < 4) __builtin_amdgcn_raw_ptr_buffer_load_lds(rB, (LAS void*)(size_t)(lds0 + L_DUMMY), 4, lane * 65536, rk * 262144, 0, 0);
      if (lane < 2) { __builtin_amdgcn_raw_ptr_buffer_load_lds(rD, (LAS void*)(size_t)(lds0 + L_DUMMY), 4, lane * 65536, rk * 131072, 0, 0);
                      __builtin_amdgcn_raw_ptr_buffer_load_lds(rE, (LAS void*)(size_t)(lds0 + L_DUMMY), 4, lane * 65536, rk * 131072, 0, 16); }
    }
    f32x4 acc[4][9];
#pragma unroll
    for (int m = 0; m < 4; ++m)
#pragma unroll
      for (int n = 0; n < 9; ++n) acc[m][n] = (f32x4){0, 0, 0, 0};
    const unsigned bX = lds0 + (64 * wt + r16) * 128 + ((g ^ (r16 >> 1)) << 4);
    const unsigned bW = lds0 + 16384 + (4 * g + qr) * 288 + p * 8 + wf * 128;
    const unsigned bE = lds0 + 16384 + (4 * g + qr) * 288 + p * 8 + wf * 18432 + 256;
    unsigned aX1 = bX ^ 64, aW = bW, aE = bE;
    bf16x8 xb[2][4]; s16x4 wb[6][2];
    auto rdW = [&](int G, unsigned w, unsigned e, int u) {
      const int kk = G / 9, n = G % 9;
      const unsigned a = n < 4 ? w + kk * 9216 + u * 4608 + n * 32 : n < 8 ? w + 18432 + kk * 9216 + u * 4608 + (n - 4) * 32 : e + kk * 9216 + u * 4608;
      wb[G % 6][u] = ldtr(a);
    };
    WAITL0();
    BAR();
    STAMP(1);
    MEMB();
#pragma unroll
    for (int m = 0; m < 4; ++m) xb[0][m] = ld128(bX + m * 2048);
    rdW(0, aW, aE, 0); rdW(0, aW, aE, 1); rdW(1, aW, aE, 0); rdW(1, aW, aE, 1);
    unsigned so = 0;
    for (int k = 0; k < 9; ++k) {
      so = so == 2 * STG1 ? 0 : so + STG1;
      unsigned nX0 = bX + so, nX1 = (bX ^ 64) + so, nW = bW + so, nE = bE + so;
      SB();
#pragma unroll
      for (int G = 0; G < 18; ++G) {
        const int kk = G / 9, n = G % 9;
#pragma unroll
        for (int m = 0; m < 4; ++m) {
          acc[m][n] = MFMA(cat(wb[G % 6][0], wb[G % 6][1]), xb[kk][m], acc[m][n]);
          SB();
          if (G == 0) rdW(2 + (m >> 1), aW, aE, m & 1);
          else if (G <= 14) { if (m < 2) rdW(G + 3, aW, aE, m); else if (m == 2 && G >= 2 && G <= 5) xb[1][G - 2] = ld128(aX1 + (G - 2) * 2048); }
          else if (G >= 16) { if (m < 2) xb[0][(G - 16) * 2 + m] = ld128(nX0 + ((G - 16) * 2 + m) * 2048); else rdW(G - 16, nW, nE, m - 2); }
          SB();
        }
        if (G == 15) { WAITL0(); SB(); BAR(); MEMB(); SB(); }
      }
      aX1 = nX1; aW = nW; aE = nE;
    }
    STAMP(2);
    f32x2 s1[4], s2[4];
#pragma unroll
    for (int m = 0; m < 4; ++m) {
      s1[m] = (f32x2){0.f, 0.f}; s2[m] = (f32x2){0.f, 0.f};
#pragma unroll
      for (int n = 4; n < 8; ++n) { acc[m][n] = gelu4(acc[m][n]); f32x2 a = {acc[m][n][0], acc[m][n][1]}, b = {acc[m][n][2], acc[m][n][3]}; s1[m] += a + b; s2[m] += a * a; s2[m] += b * b; }
    }
    if (wf) {
#pragma unroll
      for (int m = 0; m < 4; ++m) { acc[m][8] = gelu4(acc[m][8]); f32x2 a = {acc[m][8][0], acc[m][8][1]}, b = {acc[m][8][2], acc[m][8][3]}; s1[m] += a + b; s2[m] += a * a; s2[m] += b * b; }
    }
#pragma unroll
    for (int m = 0; m < 4; ++m) *(LAS f32x2*)(size_t)(lds0 + L_T + (64 * wt + 16 * m + r16) * 64 + (wf * 4 + g) * 8) = (f32x2){s1[m].x + s1[m].y, s2[m].x + s2[m].y};
    asm volatile("s_waitcnt lgkmcnt(0)" ::: "memory");
    STAMP(3);
    SB();
    BAR();
#pragma unroll
    for (int m = 0; m < 4; ++m) asm volatile("" : "+v"(acc[m][0]), "+v"(acc[m][1]), "+v"(acc[m][2]), "+v"(acc[m][3]), "+v"(acc[m][8]));
    SB();
    u32x4 za[4][2]; u32x2 zs[4];
#pragma unroll
    for (int m = 0; m < 4; ++m) {
#pragma unroll
      for (int pp = 0; pp < 2; ++pp) {
        const f32x4 a = gelu4(acc[m][2 * pp]), b = gelu4(acc[m][2 * pp + 1]);
        za[m][pp].x = cvt_pk(a[0], a[1]); za[m][pp].y = cvt_pk(a[2], a[3]); za[m][pp].z = cvt_pk(b[0], b[1]); za[m][pp].w = cvt_pk(b[2], b[3]);
      }
      zs[m] = (u32x2){0u, 0u};
    }
    if (!wf) {
#pragma unroll
      for (int m = 0; m < 4; ++m) { const f32x4 a = gelu4(acc[m][8]); zs[m].x = cvt_pk(a[0], a[1]); zs[m].y = cvt_pk(a[2], a[3]); }
    }
    asm volatile("" : "+v"(za[0][0]), "+v"(za[0][1]), "+v"(za[1][0]), "+v"(za[1][1]), "+v"(za[2][0]), "+v"(za[2][1]), "+v"(za[3][0]), "+v"(za[3][1]));
    asm volatile("" : "+v"(zs[0]), "+v"(zs[1]), "+v"(zs[2]), "+v"(zs[3]));
    STAMP(4);
    BAR();
    STAMP(5);
    MEMB();
    float bs[4];
#pragma unroll
    for (int m = 0; m < 4; ++m) bs[m] = __uint_as_float((unsigned)*(LAS u16*)(size_t)(lds0 + L_BIAS + (wt * 64 + m * 16 + r16) * 2) << 16);
    const u32x2 tg = *(LAS u32x2*)(size_t)(lds0 + L_TAG);
    const u64 tag = ((u64)(unsigned)__builtin_amdgcn_readfirstlane(tg.y) << 32) | (unsigned)__builtin_amdgcn_readfirstlane(tg.x);
    {
      auto rZ = RSRC(ws + WS_Z2, FRAG_BYTES);
      const unsigned sl = g * 16 + (r16 ^ (4 * (g >> 1)));
      const bool sw = g & 1;
#pragma unroll
      for (int m = 0; m < 4; ++m) {
        const f32x4 q4 = *(LAS f32x4*)(size_t)(lds0 + L_R + (64 * wt + 16 * m + r16) * 32) + *(LAS f32x4*)(size_t)(lds0 + L_R + (64 * wt + 16 * m + r16) * 32 + 16);
        const float mean = (q4[0] + q4[2]) * (1.0f / 2304.0f), var = fmaxf((q4[1] + q4[3]) * (1.0f / 2304.0f) - mean * mean, 0.f);
        f32x2 rs; rs.x = __builtin_amdgcn_rsqf(var + 1e-5f); rs.y = -mean * rs.x;
        const unsigned dst = (fj * 128 + ti * 8 + wt * 4 + m) * CH, ld = lds0 + wt * STG2 + 16384 + m * CH;
#pragma unroll
        for (int pp = 0; pp < 2; ++pp) {
          const f32x4 a = acc[m][4 + 2 * pp] * rs.x + rs.y, b = acc[m][5 + 2 * pp] * rs.x + rs.y;
          const unsigned a0 = cvt_pk(a[0], a[1]), a1 = cvt_pk(a[2], a[3]), b0 = cvt_pk(b[0], b[1]), b1 = cvt_pk(b[2], b[3]);
          u32x4 v; v.x = sw ? b0 : a0; v.y = sw ? b1 : a1; v.z = sw ? a0 : b0; v.w = sw ? a1 : b1;
          __builtin_amdgcn_raw_buffer_store_b128(v, rZ, dst + sl * 16 + wf * 2048 + pp * 1024, 0, SAUX);
          *(LAS u32x4*)(size_t)(ld + wf * 2048 + pp * 1024 + sl * 16) = v;
        }
        if (wf) {
          const f32x4 a = acc[m][8] * rs.x + rs.y; u32x2 v; v.x = cvt_pk(a[0], a[1]); v.y = cvt_pk(a[2], a[3]);
          __builtin_amdgcn_raw_buffer_store_b64(v, rZ, dst + sl * 8 + 4096, 0, SAUX);
          *(LAS u32x2*)(size_t)(ld + 4096 + sl * 8) = v;
        }
      }
    }
    STAMP(6);
    if (wf == 0) g2_phase<0>(lds0, wid, wt, lane, ti, fj, ws, tag, za, zs, bs); else g2_phase<1>(lds0, wid, wt, lane, ti, fj, ws, tag, za, zs, bs);
    if (!has3) return;
    if (wt == 0) g3_compute<0>(lds0, wid, wf, lane, out, tt, dd); else g3_compute<1>(lds0, wid, wf, lane, out, tt, dd);
    STAMP(13);
    DUMP();
  }
}

extern "C" void kernel_launch(void* const* d_in, const int* in_sizes, int n_in, void* d_out, int out_size, void* d_ws, size_t ws_size, hipStream_t stream) {
  (void)hipFuncSetAttribute((const void*)gmlp_fused, hipFuncAttributeMaxDynamicSharedMemorySize, LDS_BYTES);
  gmlp_fused<<<256, 512, LDS_BYTES, stream>>>((const char*)d_in[0], (const char*)d_in[1], (const char*)d_in[2], (const u16*)d_in[3], (const char*)d_in[4], (char*)d_out, (char*)d_ws);
}
```

```cpp
#include <hip/hip_runtime.h>
#include <hip/hip_bf16.h>
#include <type_traits>
typedef unsigned short u16;
typedef unsigned long long u64;
typedef short s16x4 __attribute__((ext_vector_type(4)));
typedef short bf16x8 __attribute__((ext_vector_type(8)));
typedef float f32x2 __attribute__((ext_vector_type(2)));
typedef float f32x4 __attribute__((ext_vector_type(4)));
typedef unsigned u32x4 __attribute__((ext_vector_type(4)));
typedef unsigned u32x2 __attribute__((ext_vector_type(2)));
#define LAS __attribute__((address_space(3)))
#define DEV __device__ __forceinline__
#define BAR() __builtin_amdgcn_s_barrier()
#define SB() __builtin_amdgcn_sched_barrier(0)
#define MEMB() asm volatile("" ::: "memory")
#define WAITL0() __builtin_amdgcn_s_waitcnt(0xC07F)
#define WV(N) asm volatile("s_waitcnt vmcnt(%0)" ::"i"(N) : "memory")
#define MFMA(a, b, c) __builtin_amdgcn_mfma_f32_16x16x32_bf16(a, b, c, 0, 0, 0)
#define MFMA16(a, b, c) __builtin_amdgcn_mfma_f32_16x16x16bf16_1k(a, b, c, 0, 0, 0)
#define GL(R, D, V, S, I) __builtin_amdgcn_raw_ptr_buffer_load_lds(R, (LAS void*)(size_t)(D), 16, V, S, I, 0)
#define GL4(R, D, V0, V1, V2, V3, S) do { GL(R, D, V0, S, 0); GL(R, D, V1, S, 1024); GL(R, D, V2, S, 2048); GL(R, D, V3, S, 3072); } while (0)
#define GS(R, D, V, S, I) __builtin_amdgcn_raw_ptr_buffer_load_lds(R, (LAS void*)(size_t)(D), 16, V, S, I, HAUX)
#define GS4(R, D, V, S) do { GS(R, D, V, S, 0); GS(R, D, V, S, 1024); GS(R, D, V, S, 2048); GS(R, D, V, S, 3072); } while (0)
#define ABL 0
#define SAUX 16
#define HAUX 16
#define RSRC(p, n) __builtin_amdgcn_make_buffer_rsrc((void*)(p), 0, (n), 0x00020000)
#define RLX_AGENT __ATOMIC_RELAXED, __HIP_MEMORY_SCOPE_AGENT
#define WARM1 0
#define ROT1 2

constexpr int CH = 4608;
constexpr int FRAG_BYTES = 16 * 128 * CH;
constexpr size_t WS_Z2 = 0, WS_P = FRAG_BYTES, WS_ST = 2 * (size_t)FRAG_BYTES, WS_F1 = WS_ST + 16 * 16 * 128 * 16, WS_F2 = WS_F1 + 256 * 128, WS_EP = WS_F2 + 256 * 128;
constexpr int STG1 = 53248, STG2 = 34816, STGP = 23040, STGO = 18432, L_O = 3 * STGP, L_X = 151552, LDS_BYTES = 163840;
#define CODE_KB 31
#define PRE 1
#define WARM3 0
#define WU 1
#define ROT3 0
constexpr int L_T = 139264, L_R = L_T + 8192, L_BIAS = 159744, L_TAG = L_BIAS + 256, L_DUMMY = 161280;
constexpr unsigned SPIN_CAP = 1u << 16;
#define STAMP(I)
#define DUMP()

DEV unsigned cvt_pk(float lo, float hi) { unsigned r; asm("v_cvt_pk_bf16_f32 %0, %1, %2" : "=v"(r) : "v"(lo), "v"(hi)); return r; }
DEV bf16x8 ld128(unsigned a) { return *(LAS bf16x8*)(size_t)a; }
DEV s16x4 ld64(unsigned a) { return *(LAS s16x4*)(size_t)a; }
DEV s16x4 ldtr(unsigned a) { return __builtin_amdgcn_ds_read_tr16_b64_v4i16((LAS s16x4*)(size_t)a); }
DEV bf16x8 cat(s16x4 a, s16x4 b) { return __builtin_shufflevector(a, b, 0, 1, 2, 3, 4, 5, 6, 7); }
DEV float bflo(unsigned v) { return __uint_as_float(v << 16); }
DEV float bfhi(unsigned v) { return __uint_as_float(v & 0xffff0000u); }
DEV f32x2 gelu2(f32x2 x) {
  const float k1 = -2.0f * 0.7978845608028654f * 1.4426950408889634f, k2 = k1 * 0.044715f;
  f32x2 t = x * (x * x * k2 + k1), e;
  e.x = __builtin_amdgcn_exp2f(t.x); e.y = __builtin_amdgcn_exp2f(t.y);
  e = e + 1.0f;
  e.x = __builtin_amdgcn_rcpf(e.x); e.y = __builtin_amdgcn_rcpf(e.y);
  return x * e;
}
DEV f32x4 gelu4(f32x4 v) { f32x2 a = gelu2((f32x2){v[0], v[1]}), b = gelu2((f32x2){v[2], v[3]}); return (f32x4){a.x, a.y, b.x, b.y}; }

struct G2Addr { unsigned w[2], z[2][2], s[2]; };
template <int WF, int OFF, bool PUB, bool FIRST>
DEV void g2_step(G2Addr& A, int dlt, f32x4 (&acc)[4][5], bf16x8 (&wX)[4], s16x4 (&tX)[5][2], bf16x8 (&wY)[4], s16x4 (&tY)[4][2], u64* flag, u64 tag, int lane) {
  constexpr int KX = WF, KY = 1 - WF;
  SB(); BAR(); MEMB(); SB();
#pragma unroll
  for (int i = 0; i < 16; ++i) {
    const int n = i >> 2, m = i & 3;
    if (!FIRST) acc[m][n] = MFMA(cat(tY[n][0], tY[n][1]), wY[m], acc[m][n]);
    SB();
    if (i == 0) wX[0] = ld128(A.w[KX] + OFF);
    else if (i == 1) tX[0][0] = ldtr(A.z[0][0] + OFF + KX * 9216);
    else if (i == 2) tX[0][1] = ldtr(A.z[1][0] + OFF + KX * 9216);
    else if (i < 6) wX[i - 2] = ld128(A.w[KX] + OFF + (i - 2) * 2048);
    else if (i < 12) { const int nn = 1 + ((i - 6) >> 1), u = (i - 6) & 1; tX[nn][u] = ldtr(A.z[u][nn & 1] + OFF + KX * 9216 + (nn >> 1) * 1024); }
    else if (i < 14) tX[4][i - 12] = ldtr(A.s[i - 12] + OFF + KX * 9216);
    SB();
  }
#pragma unroll
  for (int i = 0; i < 20; ++i) {
    const int n = i >> 2, m = i & 3;
    acc[m][n] = MFMA(cat(tX[n][0], tX[n][1]), wX[m], acc[m][n]);
    SB();
    if (i == 0) wY[0] = ld128(A.w[KY] + OFF);
    else if (i == 1) tY[0][0] = ldtr(A.z[0][0] + OFF + KY * 9216);
    else if (i == 2) tY[0][1] = ldtr(A.z[1][0] + OFF + KY * 9216);
    else if (i < 6) wY[i - 2] = ld128(A.w[KY] + OFF + (i - 2) * 2048);
    else if (i < 12) { const int nn = 1 + ((i - 6) >> 1), u = (i - 6) & 1; tY[nn][u] = ldtr(A.z[u][nn & 1] + OFF + KY * 9216 + (nn >> 1) * 1024); }
    else if (OFF != 0 && i < 16) {
      const int u = i & 1;
      if (i < 14) { A.w[u] += dlt; A.s[u] += dlt; asm volatile("" : "+v"(A.w[u]), "+v"(A.s[u])); }
      else { A.z[u][0] += dlt; A.z[u][1] += dlt; asm volatile("" : "+v"(A.z[u][0]), "+v"(A.z[u][1])); }
    }
    SB();
  }
  if (PUB) {
    WV(0);
    if (lane == 0) __hip_atomic_store(flag, tag, RLX_AGENT);
  }
  WAITL0();
}

struct G3Frag { bf16x8 p[2][5]; s16x4 w[2][2][2], ws[2], ps[3]; };
template <int KH, bool MM, bool RD>
DEV void g3_stage(f32x4 (&acc)[5][2], const G3Frag& c, G3Frag& n, unsigned aP, unsigned aS, unsigned a0, unsigned a1) {
  constexpr int M0 = KH ? 3 : 0, NS = KH ? 2 : 3, NM = 20 + 2 * NS, NR = 20 + NS;
#pragma unroll
  for (int i = 0; i < NM; ++i) {
    if (MM && !(ABL & 64)) {
      if (i >= 10 && i < 10 + 2 * NS) acc[M0 + ((i - 10) >> 1)][i & 1] = MFMA16(c.ws[i & 1], c.ps[(i - 10) >> 1], acc[M0 + ((i - 10) >> 1)][i & 1]);
      else { const int ii = i < 10 ? i : i - 2 * NS, P = ii / 10, m = KH ? 4 - ((ii % 10) >> 1) : (ii % 10) >> 1, cc = ii & 1; acc[m][cc] = MFMA(cat(c.w[P][cc][0], c.w[P][cc][1]), c.p[P][m], acc[m][cc]); }
    }
    if (MM && (ABL & 64) && i == 0) { for (int P = 0; P < 2; ++P) { for (int m = 0; m < 5; ++m) asm volatile("" :: "v"(c.p[P][m])); for (int q = 0; q < 4; ++q) asm volatile("" :: "v"(c.w[P][q >> 1][q & 1])); } asm volatile("" :: "v"(c.ws[0]), "v"(c.ws[1]), "v"(c.ps[0]), "v"(c.ps[1])); if (NS == 3) asm volatile("" :: "v"(c.ps[2])); }
    SB();
    if (RD && !((ABL & 32) && MM)) {
#pragma unroll
      for (int r = (i * 3) / 2; r < ((i + 1) * 3) / 2; ++r) {
        if (r < 18) { const int P = r / 9, q = r % 9;
          if (q < 4) n.w[P][q >> 1][q & 1] = ldtr(((q >> 1) ? a1 : a0) + (2 * KH + P) * 4096 + (q & 1) * 2048);
          else n.p[P][q - 4] = ld128(aP + P * 1024 + (q - 4) * 4096); }
        else if (r < 20) n.ws[r - 18] = ldtr(((r - 18) ? a1 : a0) + 16384);
        else if (r < NR) n.ps[r - 20] = ld64(aS + (M0 + r - 20) * 512);
      }
    }
    SB();
  }
}
template <int KH>
DEV void g3_compute(unsigned lds0, int wid, int dh, int lane, char* out, int tt, int dd) {
  f32x4 acc[5][2];
#pragma unroll
  for (int m = 0; m < 5; ++m) { acc[m][0] = (f32x4){0, 0, 0, 0}; acc[m][1] = (f32x4){0, 0, 0, 0}; }
  const int g = lane >> 4, qr = (lane >> 2) & 3, p = lane & 3;
  const int rr = 4 * g + qr;
  unsigned bW[2];
#pragma unroll
  for (int c = 0; c < 2; ++c) bW[c] = lds0 + L_O + rr * 128 + ((((2 * dh + c) * 2 + (p >> 1)) ^ (((rr >> 1) & 3) << 1)) << 4) + (p & 1) * 8;
  const unsigned bP = lds0 + lane * 16 + KH * 2048, bS = lds0 + 20480 + lane * 8;
  G3Frag f0, f1;
  if (ABL & 32) g3_stage<KH, false, true>(acc, f0, f1, bP, bS, bW[0], bW[1]);
  WAITL0();
  SB(); BAR(); MEMB(); SB();
  g3_stage<KH, false, true>(acc, f1, f0, bP, bS, bW[0], bW[1]);
  WAITL0();
  unsigned sp = STGP, sw = STGO;
  for (int j = 1; j < 16; j += 2) {
    SB(); BAR(); MEMB(); SB();
    g3_stage<KH, true, true>(acc, f0, f1, bP + sp, bS + sp, bW[0] + sw, bW[1] + sw);
    sp = sp == 2 * STGP ? 0 : sp + STGP; sw = sw == 4 * STGO ? 0 : sw + STGO;
    WAITL0();
    SB(); BAR(); MEMB(); SB();
    g3_stage<KH, true, true>(acc, f1, f0, bP + sp, bS + sp, bW[0] + sw, bW[1] + sw);
    sp = sp == 2 * STGP ? 0 : sp + STGP; sw = sw == 4 * STGO ? 0 : sw + STGO;
    WAITL0();
  }
#pragma unroll
  for (int m = 0; m < 5; ++m) *(LAS f32x4*)(size_t)(lds0 + ((KH * 2 + dh) * 5 + m) * 1024 + lane * 16) = acc[m][1 - KH];
  asm volatile("s_waitcnt lgkmcnt(0)" ::: "memory");
  BAR(); MEMB();
#pragma unroll
  for (int m = 0; m < 5; ++m) {
    const f32x4 a = acc[m][KH] + *(LAS f32x4*)(size_t)(lds0 + (((1 - KH) * 2 + dh) * 5 + m) * 1024 + lane * 16);
    const int t = tt * 80 + m * 16 + (lane & 15);
    u32x2 v; v.x = cvt_pk(a[0], a[1]); v.y = cvt_pk(a[2], a[3]);
    if (t < 2048) *(u32x2*)(out + (size_t)t * 1152 + dd * 128 + (dh * 2 + KH) * 32 + (lane >> 4) * 8) = v;
  }
}

template <int WF>
DEV void g2_phase(unsigned lds0, int wid, int wt, int lane, int ti, int fj, char* ws, u64 tag, const u32x4 (&za)[4][2], const u32x2 (&zs)[4], const float (&bs)[4]) {
  const int r16 = lane & 15, g = lane >> 4, qr = (lane >> 2) & 3, p = lane & 3;
  G2Addr A0;
  A0.w[0] = lds0 + (64 * wt + r16) * 128 + ((g ^ (r16 >> 1)) << 4); A0.w[1] = A0.w[0] ^ 64;
#pragma unroll
  for (int u = 0; u < 2; ++u) {
    const unsigned sl = p * 16 + 8 * (g & 1) + 4 * (u ^ (p >> 1)) + qr;
#pragma unroll
    for (int h = 0; h < 2; ++h) A0.z[u][h] = lds0 + 16384 + (g >> 1) * CH + WF * 2048 + sl * 16 + ((h ^ (p & 1)) * 8);
    A0.s[u] = lds0 + 16384 + (g >> 1) * CH + 4096 + sl * 8;
  }
  asm volatile("" : "+v"(A0.w[0]), "+v"(A0.w[1]), "+v"(A0.s[0]), "+v"(A0.s[1]), "+v"(A0.z[0][0]), "+v"(A0.z[0][1]), "+v"(A0.z[1][0]), "+v"(A0.z[1][1]));
  f32x4 acc[4][5];
#pragma unroll
  for (int m = 0; m < 4; ++m)
#pragma unroll
    for (int n = 0; n < 5; ++n) { const float b0 = (n < 4 || WF == 0) ? bs[m] : 0.f; acc[m][n] = (f32x4){b0, b0, b0, b0}; }
  bf16x8 wX[4], wY[4]; s16x4 tX[5][2], tY[4][2];
  u64* f1 = (u64*)(ws + WS_F1 + (fj * 16 + ti) * 128) + wid;
  WAITL0();
  int dlt = 2 * STG2;
  g2_step<WF, 0, true, true>(A0, dlt, acc, wX, tX, wY, tY, f1, tag, lane);
  STAMP(7);
  g2_step<WF, STG2, false, false>(A0, dlt, acc, wX, tX, wY, tY, f1, tag, lane);
  STAMP(8);
  for (int k = 1; k < 16; ++k) {
    dlt = -dlt;
    g2_step<WF, 0, false, false>(A0, dlt, acc, wX, tX, wY, tY, f1, tag, lane);
    g2_step<WF, STG2, false, false>(A0, dlt, acc, wX, tX, wY, tY, f1, tag, lane);
  }
  BAR();
  if (WF == 1) {
#pragma unroll
    for (int m = 0; m < 4; ++m) *(LAS f32x4*)(size_t)(lds0 + L_X + (wt * 4 + m) * 1024 + lane * 16) = acc[m][4];
  }
#pragma unroll
  for (int n = 0; n < 4; ++n)
#pragma unroll
    for (int m = 0; m < 4; ++m) acc[m][n] = MFMA(cat(tY[n][0], tY[n][1]), wY[m], acc[m][n]);
  STAMP(10);
  auto rP = RSRC(ws + WS_P, FRAG_BYTES);
  auto gate = [&](f32x4 a, unsigned z0, unsigned z1, unsigned& o0, unsigned& o1) {
    const f32x2 x = (f32x2){a[0], a[1]} * (f32x2){bflo(z0), bfhi(z0)}, y = (f32x2){a[2], a[3]} * (f32x2){bflo(z1), bfhi(z1)};
    o0 = cvt_pk(x.x, x.y); o1 = cvt_pk(y.x, y.y);
  };
  asm volatile("s_waitcnt lgkmcnt(0)" ::: "memory");
  BAR();
  MEMB();
#pragma unroll
  for (int m = 0; m < 4; ++m) {
    const unsigned dst = ((ti * 8 + wt * 4 + m) * 16 + fj) * CH;
    if (WF == 0) {
      const f32x4 a = acc[m][4] + *(LAS f32x4*)(size_t)(lds0 + L_X + (wt * 4 + m) * 1024 + lane * 16);
      u32x2 v; unsigned o0, o1; gate(a, zs[m].x, zs[m].y, o0, o1); v.x = o0; v.y = o1;
      __builtin_amdgcn_raw_buffer_store_b64(v, rP, dst + lane * 8 + 4096, 0, SAUX);
    }
#pragma unroll
    for (int pp = 0; pp < 2; ++pp) {
      unsigned o0, o1, o2, o3; gate(acc[m][2 * pp], za[m][pp].x, za[m][pp].y, o0, o1); gate(acc[m][2 * pp + 1], za[m][pp].z, za[m][pp].w, o2, o3);
      __builtin_amdgcn_raw_buffer_store_b128((u32x4){o0, o1, o2, o3}, rP, dst + lane * 16 + (2 * WF + pp) * 1024, 0, SAUX);
    }
  }
  STAMP(11);
  WV(0);
  if (lane == 0) __hip_atomic_store((u64*)(ws + WS_F2 + (ti * 16 + fj) * 128) + wid, tag, RLX_AGENT);
  BAR();
  STAMP(12);
}

DEV u64 poll16(const char* base, u64 tag, int lane) {
  const u64 v = __hip_atomic_load((const u64*)(base + (lane >> 2) * 128) + (lane & 3), RLX_AGENT);
  return __builtin_amdgcn_ballot_w64(v == tag);
}
DEV bool got(u64 mask, int who) { return ((unsigned)(mask >> (4 * who)) & 15u) == 15u; }

__global__ __launch_bounds__(512, 2) void gmlp_fused(const char* __restrict__ X, const char* __restrict__ Win, const char* __restrict__ Wsp, const u16* __restrict__ bias,
                                                     const char* __restrict__ Wo, char* __restrict__ out, char* __restrict__ ws) {
  extern __shared__ __attribute__((aligned(16))) char smem[];
  u64 pc0; asm volatile("s_getpc_b64 %0" : "=s"(pc0));
  const int tid = threadIdx.x, wid = __builtin_amdgcn_readfirstlane(tid >> 6), lane = tid & 63;
  const int bid = blockIdx.x, xg = bid & 7, rk = bid >> 3, il = rk & 7, jl = rk >> 3, ti = (xg & 1) * 8 + il, fj = (xg >> 1) * 4 + jl;
  const int L3 = xg * 30 + rk, tt = L3 / 9, dd = L3 - tt * 9;
  const bool has3 = rk < 30 && L3 < 234;
  const unsigned lds0 = (unsigned)(size_t)(LAS char*)smem;
  STAMP(0);
  if (__builtin_expect(wid >= 4, 1)) {
    __builtin_amdgcn_s_setprio(3);
    const int lw = wid - 4;
    auto rX = RSRC(X, 2048 * 1152); auto rWi = RSRC(Win, 576 * 9216);
    unsigned vX[4], vW[9];
    const unsigned sX = (ti * 128 + lw * 32) * 1152, sW = (lw & 1) * 32 * 9216 + (lw >> 1) * 4608 + fj * 288;
    const unsigned dX = lds0 + lw * 4096, dW = lds0 + 16384 + lw * 9216;
    const int rot = ROT1 == 0 ? 0 : ROT1 == 1 ? il + 2 * jl : 3 * ((il + jl) % 3);
    auto k1 = [&](int k) { int kk = k + rot; kk = kk >= 9 ? kk - 9 : kk; kk = kk >= 9 ? kk - 9 : kk; return kk; };
    auto issue1 = [&](int k, int slot) {
      const int kk = k1(k);
      const unsigned so = slot * STG1, sx = sX + kk * 128, sw = sW + kk * 64 * 9216;
      GL4(rX, dX + so, vX[0], vX[1], vX[2], vX[3], sx);
      GL(rWi, dW + so, vW[0], sw, 0); GL(rWi, dW + so, vW[1], sw, 1024); GL(rWi, dW + so, vW[2], sw, 2048);
      GL(rWi, dW + so + 3072, vW[3], sw, 0); GL(rWi, dW + so + 3072, vW[4], sw, 1024); GL(rWi, dW + so + 3072, vW[5], sw, 2048);
      GL(rWi, dW + so + 6144, vW[6], sw, 0); GL(rWi, dW + so + 6144, vW[7], sw, 1024); GL(rWi, dW + so + 6144, vW[8], sw, 2048);
    };
    const unsigned vL = lane * 16;
    {
#pragma unroll
      for (int q = 0; q < 4; ++q) { const int row = 8 * q + (lane >> 3); vX[q] = row * 1152 + (((lane & 7) ^ ((row >> 1) & 7)) << 4) - q * 1024; }
      GL4(rX, dX, vX[0], vX[1], vX[2], vX[3], sX + k1(0) * 128);
      SB();
      const unsigned sw = sW + k1(0) * 64 * 9216;
#pragma unroll
      for (int q = 0; q < 9; ++q) {
        const unsigned c16 = q * 64 + lane, R = (c16 * 3641u) >> 16, c = (c16 - R * 18) * 16, k = 8 * ((R >> 2) & 3) + 4 * (R >> 4) + (R & 3);
        vW[q] = k * 9216 + c - (q % 3) * 1024;
        if (q % 3 == 0) GL(rWi, dW + (q / 3) * 3072, vW[q], sw, 0); else if (q % 3 == 1) GL(rWi, dW + (q / 3) * 3072, vW[q], sw, 1024); else GL(rWi, dW + (q / 3) * 3072, vW[q], sw, 2048);
        SB();
      }
    }
    constexpr int NW1 = 0;
    issue1(1, 1); issue1(2, 2);
    auto rWs = RSRC(Wsp, 2048 * 4096); auto rWo = RSRC(Wo, 2304 * 1152);
    unsigned vS[4];
#pragma unroll
    for (int q = 0; q < 4; ++q) { const int row = 8 * q + (lane >> 3); vS[q] = row * 4096 + (((lane & 7) ^ ((row >> 1) & 7)) << 4) - q * 1024; }
    const unsigned sWs = (ti * 128 + lw * 32) * 4096;
    auto kpos = [&](int n) { int t2 = n < 2 ? 2 * ti + n : (n - 2 < 2 * ti ? n - 2 : n); return t2; };
    auto issueW = [&](int n) { GL4(rWs, lds0 + (n & 3) * STG2 + lw * 4096, vS[0], vS[1], vS[2], vS[3], sWs + kpos(n) * 128); };
    WV(26 + NW1); BAR();
    STAMP(1);
    auto warm = [&](int q0, int q1) {
#pragma unroll
      for (int q = q0; q < q1; ++q) {
        if (q < 3) GL(rWo, lds0 + L_DUMMY, vL, bid * 10368 + lw * 2592 + (q < 2 ? q * 1024 : 1568), 0);
        else GL(rWs, lds0 + L_DUMMY, vL, bid * 32768 + lw * 8192 + (q - 3) * 1024, 0);
      }
    };
    WV(13); BAR(); issue1(3, 0);
    WV(13); BAR(); issue1(4, 1);
    WV(13); BAR(); issue1(5, 2);
    WV(13); BAR(); issue1(6, 0);
    WV(13); BAR(); issue1(7, 1);
    WV(13); BAR(); issue1(8, 2);
    WV(13); BAR();
    if (WU == 1) { warm(0, 11); WV(11); } else WV(0);
    BAR();
    if (WU == 0) warm(0, 11);
    const u64 ep = __hip_atomic_load((const u64*)(ws + WS_EP), RLX_AGENT);
    if (lw == 0) __builtin_amdgcn_raw_ptr_buffer_load_lds(RSRC(bias, 4096), (LAS void*)(size_t)(lds0 + L_BIAS), 4, lane * 4, ti * 256, 0, 0);
    BAR();
    const unsigned tagA = __builtin_amdgcn_readfirstlane((unsigned)ep) + 0x9E3779B9u, tagB = __builtin_amdgcn_readfirstlane((unsigned)(ep >> 32)) + 0x85EBCA6Bu;
    const u64 tag = ((u64)tagB << 32) | tagA;
    issueW(0); issueW(1);
    if (lw == 0) { const u32x2 tg = {tagA, tagB}; asm volatile("ds_write_b64 %0, %1\n\ts_waitcnt lgkmcnt(0)" :: "v"(lds0 + L_TAG), "v"(tg) : "memory"); }
    BAR();
    STAMP(2);
    {
      auto rS = RSRC(ws + WS_ST, 16 * 16 * 128 * 16);
      if (lane < 32) {
        const int row = lw * 32 + lane;
        f32x4 v0, v1, v2, v3;
        asm volatile("ds_read_b128 %0, %4\n\tds_read_b128 %1, %4 offset:16\n\tds_read_b128 %2, %4 offset:32\n\tds_read_b128 %3, %4 offset:48\n\ts_waitcnt lgkmcnt(0)"
                     : "=&v"(v0), "=&v"(v1), "=&v"(v2), "=&v"(v3) : "v"(lds0 + L_T + row * 64) : "memory");
        const f32x4 s = (v0 + v1) + (v2 + v3);
        u32x4 gq; gq.x = tagA; gq.y = __float_as_uint(s[0] + s[2]); gq.z = tagB; gq.w = __float_as_uint(s[1] + s[3]);
        __builtin_amdgcn_raw_buffer_store_b128(gq, rS, ((ti * 16 + fj) * 128 + row) * 16, 0, 16);
      }
      STAMP(9);
      for (unsigned spins = 0; spins < SPIN_CAP; ++spins) {
        MEMB();
        const u32x4 v = __builtin_amdgcn_raw_buffer_load_b128(rS, (lane & 15) * 512, (ti * 16 + lw * 4) * 2048, 16);
        if (__all((v.x == tagA) & (v.z == tagB))) break;
      }
      STAMP(10);
      f32x4 s;
      for (unsigned spins = 0;; ++spins) {
        u32x4 v[8]; bool ok = true;
        MEMB();
#pragma unroll
        for (int p = 0; p < 8; ++p) v[p] = __builtin_amdgcn_raw_buffer_load_b128(rS, lane * 16 + (p & 1) * 1024, (ti * 16 + lw * 4 + (p >> 1)) * 2048, 16);
        s = (f32x4){0.f, 0.f, 0.f, 0.f};
#pragma unroll
        for (int p = 0; p < 8; ++p) { ok &= (v[p].x == tagA) & (v[p].z == tagB); s[(p & 1) * 2] += __uint_as_float(v[p].y); s[(p & 1) * 2 + 1] += __uint_as_float(v[p].w); }
        if (__all(ok)) break;
        if (spins > SPIN_CAP) { s[0] = s[2] = __builtin_nanf(""); break; }
      }
      const f32x2 o0 = {s[0], s[1]}, o1 = {s[2], s[3]};
      asm volatile("ds_write_b64 %0, %1\n\tds_write_b64 %0, %2 offset:2048\n\ts_waitcnt lgkmcnt(0)" :: "v"(lds0 + L_R + lane * 32 + lw * 8), "v"(o0), "v"(o1) : "memory");
    }
    STAMP(3);
    WV(0);
    BAR();
    BAR();
    issueW(2); issueW(3);
    BAR();
    auto rZ = RSRC(ws + WS_Z2, FRAG_BYTES);
    const unsigned OOB = 0x80000000u;
    const unsigned zp0 = lw < 2 ? lw * 5 : 2 + lw * 4, sZ = fj * 128 * CH + zp0 * 1024;
    const unsigned vZ5 = lw < 2 ? vL : OOB;
    u64 ready = 0;
    auto issueZ = [&](int n) {
      const int kp = kpos(n);
      if (__builtin_expect(!got(ready, kp >> 1), 0)) { for (unsigned spins = 0; !got(ready, kp >> 1) && spins < SPIN_CAP; ++spins) ready = poll16(ws + WS_F1 + fj * 16 * 128, tag, lane); }
      MEMB();
      const unsigned dz = lds0 + (n & 3) * STG2 + 16384 + zp0 * 1024, so = sZ + kp * 18432;
      GS4(rZ, dz, vL, so);
      GS(rZ, lw < 2 ? dz + 4096 : lds0 + L_DUMMY, vZ5, so + 4096, 0);
    };
    issueZ(2);
    STAMP(4);
    issueZ(3); issueW(4); issueZ(4);
    WV(14);
    BAR();
    STAMP(5);
    for (int n = 5; n < 32; ++n) {
      issueW(n); issueZ(n);
      WV(18);
      BAR();
    }
    WV(9); BAR(); WV(0); BAR();
    unsigned vO[2], vOh;
#pragma unroll
    for (int q = 0; q < 2; ++q) { const int row = 36 * lw + 8 * q + (lane >> 3); vO[q] = row * 1152 + (((lane & 7) ^ (((row >> 1) & 3) << 1)) << 4); }
    { const int row = 36 * lw + 32 + ((lane >> 3) & 3); vOh = row * 1152 + (((lane & 7) ^ (((row >> 1) & 3) << 1)) << 4); }
    const unsigned vO1 = vO[1] - 1024, vO2 = vO[0] + 16 * 1152 - 2048, vO3 = vO[1] + 16 * 1152 - 3072;
    const int r3 = ROT3 ? (dd * 7) >> 2 : 0;
    const unsigned sO = dd * 128;
    BAR(); BAR(); BAR();
    if (!has3) return;
    auto issueO = [&](int n, int slot) {
      const unsigned d = lds0 + L_O + slot * STGO + lw * 4608, s0 = sO + ((n + r3) & 15) * 144 * 1152;
      GL4(rWo, d, vO[0], vO1, vO2, vO3, s0);
      if (lane < 32) GL(rWo, d + 4096, vOh, s0, 0);
    };
    issueO(0, 0); issueO(1, 1); issueO(2, 2); issueO(3, 3); issueO(4, 4);
    auto rP = RSRC(ws + WS_P, FRAG_BYTES);
    const unsigned vPs = lw < 3 ? (lane >> 5) * 16 * CH + (lane & 31) * 16 + 4096 : OOB;
    const int limS = lw == 2 ? 32 : 64;
    const unsigned sP = tt * 5 * 16 * CH;
    const int pa = (tt * 80) >> 7, pb = min((tt * 80 + 79) >> 7, 15);
    u64 rdy = 0;
    auto issueP = [&](int n, int slot) {
      const int j = (n + r3) & 15;
      if (__builtin_expect(!got(rdy, j), 0)) { for (unsigned spins = 0; !got(rdy, j) && spins < SPIN_CAP; ++spins) rdy = poll16(ws + WS_F2 + pa * 16 * 128, tag, lane) & poll16(ws + WS_F2 + pb * 16 * 128, tag, lane); }
      MEMB();
      const unsigned d = lds0 + slot * STGP, sp = sP + j * CH;
      GS4(rP, d + lw * 4096, vL, sp + lw * 16 * CH);
      GS(rP, d + 16384 + lw * 1024, vL, sp + 64 * CH + lw * 1024, 0);
      if (lane < limS) GS(rP, lw < 3 ? d + 20480 + lw * 1024 : lds0 + L_DUMMY, vPs, sp + lw * 32 * CH, 0);
    };
    issueP(0, 0);
    STAMP(6);
    issueP(1, 1); issueP(2, 2);
    WV(12);
    BAR();
    STAMP(7);
    WV(6);
    BAR(); issueP(3, 0); issueO(5, 0); WV(11);
    int sp3 = 1, so5 = 1;
    for (int n = 2; n < 12; ++n) {
      BAR();
      issueP(n + 2, sp3); issueO(n + 4, so5);
      sp3 = sp3 == 2 ? 0 : sp3 + 1; so5 = so5 == 4 ? 0 : so5 + 1;
      WV(16);
    }
    BAR(); issueP(14, 2); WV(6);
    BAR(); issueP(15, 0); WV(6);
    BAR(); WV(0);
    BAR();
    BAR(); BAR();
    STAMP(8);
    DUMP();
    if (bid == 0 && tid == 256) __hip_atomic_store((u64*)(ws + WS_EP), tag, RLX_AGENT);
  } else {
    const int wt = wid >> 1, wf = wid & 1;
    const int r16 = lane & 15, g = lane >> 4, qr = (lane >> 2) & 3, p = lane & 3;
    if ((PRE & 1) && wid == 0) {
      auto rC = RSRC((const char*)(pc0 & ~(u64)255), CODE_KB * 1024);
      GL(rC, lds0 + L_DUMMY, lane * 16, rk * 1024, 0); GL(rC, lds0 + L_DUMMY, lane * 16, rk * 1024 + 32768, 0);
    }
    if ((PRE & 2) && wid == 1) {
      auto rA = RSRC(ws, 21 << 20); auto rB = RSRC(Wsp, 2048 * 4096); auto rD = RSRC(Wo, 2304 * 1152); auto rE = RSRC(out, 2048 * 1152);
      if (lane < 10) __builtin_amdgcn_raw_ptr_buffer_load_lds(rA, (LAS void*)(size_t)(lds0 + L_DUMMY), 4, lane * 65536, rk * 655360, 0, 16);
      if (lane < 4) __builtin_amdgcn_raw_ptr_buffer_load_lds(rB, (LAS void*)(size_t)(lds0 + L_DUMMY), 4, lane * 65536, rk * 262144, 0, 0);
      if (lane < 2) { __builtin_amdgcn_raw_ptr_buffer_load_lds(rD, (LAS void*)(size_t)(lds0 + L_DUMMY), 4, lane * 65536, rk * 131072, 0, 0);
                      __builtin_amdgcn_raw_ptr_buffer_load_lds(rE, (LAS void*)(size_t)(lds0 + L_DUMMY), 4, lane * 65536, rk * 131072, 0, 16); }
    }
    f32x4 acc[4][9];
#pragma unroll
    for (int m = 0; m < 4; ++m)
#pragma unroll
      for (int n = 0; n < 9; ++n) acc[m][n] = (f32x4){0, 0, 0, 0};
    const unsigned bX = lds0 + (64 * wt + r16) * 128 + ((g ^ (r16 >> 1)) << 4);
    const unsigned bW = lds0 + 16384 + (4 * g + qr) * 288 + p * 8 + wf * 128;
    const unsigned bE = lds0 + 16384 + (4 * g + qr) * 288 + p * 8 + wf * 18432 + 256;
    unsigned aX1 = bX ^ 64, aW = bW, aE = bE;
    bf16x8 xb[2][4]; s16x4 wb[6][2];
    auto rdW = [&](int G, unsigned w, unsigned e, int u) {
      const int kk = G / 9, n = G % 9;
      const unsigned a = n < 4 ? w + kk * 9216 + u * 4608 + n * 32 : n < 8 ? w + 18432 + kk * 9216 + u * 4608 + (n - 4) * 32 : e + kk * 9216 + u * 4608;
      wb[G % 6][u] = ldtr(a);
    };
    WAITL0();
    BAR();
    STAMP(1);
    MEMB();
#pragma unroll
    for (int m = 0; m < 4; ++m) xb[0][m] = ld128(bX + m * 2048);
    rdW(0, aW, aE, 0); rdW(0, aW, aE, 1); rdW(1, aW, aE, 0); rdW(1, aW, aE, 1);
    unsigned so = 0;
    for (int k = 0; k < 9; ++k) {
      so = so == 2 * STG1 ? 0 : so + STG1;
      unsigned nX0 = bX + so, nX1 = (bX ^ 64) + so, nW = bW + so, nE = bE + so;
      SB();
#pragma unroll
      for (int G = 0; G < 18; ++G) {
        const int kk = G / 9, n = G % 9;
#pragma unroll
        for (int m = 0; m < 4; ++m) {
          acc[m][n] = MFMA(cat(wb[G % 6][0], wb[G % 6][1]), xb[kk][m], acc[m][n]);
          SB();
          if (G == 0) rdW(2 + (m >> 1), aW, aE, m & 1);
          else if (G <= 14) { if (m < 2) rdW(G + 3, aW, aE, m); else if (m == 2 && G >= 2 && G <= 5) xb[1][G - 2] = ld128(aX1 + (G - 2) * 2048); }
          else if (G >= 16) { if (m < 2) xb[0][(G - 16) * 2 + m] = ld128(nX0 + ((G - 16) * 2 + m) * 2048); else rdW(G - 16, nW, nE, m - 2); }
          SB();
        }
        if (G == 15) { WAITL0(); SB(); BAR(); MEMB(); SB(); }
      }
      aX1 = nX1; aW = nW; aE = nE;
    }
    STAMP(2);
    f32x2 s1[4], s2[4];
#pragma unroll
    for (int m = 0; m < 4; ++m) {
      s1[m] = (f32x2){0.f, 0.f}; s2[m] = (f32x2){0.f, 0.f};
#pragma unroll
      for (int n = 4; n < 8; ++n) { acc[m][n] = gelu4(acc[m][n]); f32x2 a = {acc[m][n][0], acc[m][n][1]}, b = {acc[m][n][2], acc[m][n][3]}; s1[m] += a + b; s2[m] += a * a; s2[m] += b * b; }
    }
    if (wf) {
#pragma unroll
      for (int m = 0; m < 4; ++m) { acc[m][8] = gelu4(acc[m][8]); f32x2 a = {acc[m][8][0], acc[m][8][1]}, b = {acc[m][8][2], acc[m][8][3]}; s1[m] += a + b; s2[m] += a * a; s2[m] += b * b; }
    }
#pragma unroll
    for (int m = 0; m < 4; ++m) *(LAS f32x2*)(size_t)(lds0 + L_T + (64 * wt + 16 * m + r16) * 64 + (wf * 4 + g) * 8) = (f32x2){s1[m].x + s1[m].y, s2[m].x + s2[m].y};
    asm volatile("s_waitcnt lgkmcnt(0)" ::: "memory");
    STAMP(3);
    SB();
    BAR();
#pragma unroll
    for (int m = 0; m < 4; ++m) asm volatile("" : "+v"(acc[m][0]), "+v"(acc[m][1]), "+v"(acc[m][2]), "+v"(acc[m][3]), "+v"(acc[m][8]));
    SB();
    u32x4 za[4][2]; u32x2 zs[4];
#pragma unroll
    for (int m = 0; m < 4; ++m) {
#pragma unroll
      for (int pp = 0; pp < 2; ++pp) {
        const f32x4 a = gelu4(acc[m][2 * pp]), b = gelu4(acc[m][2 * pp + 1]);
        za[m][pp].x = cvt_pk(a[0], a[1]); za[m][pp].y = cvt_pk(a[2], a[3]); za[m][pp].z = cvt_pk(b[0], b[1]); za[m][pp].w = cvt_pk(b[2], b[3]);
      }
      zs[m] = (u32x2){0u, 0u};
    }
    if (!wf) {
#pragma unroll
      for (int m = 0; m < 4; ++m) { const f32x4 a = gelu4(acc[m][8]); zs[m].x = cvt_pk(a[0], a[1]); zs[m].y = cvt_pk(a[2], a[3]); }
    }
    asm volatile("" : "+v"(za[0][0]), "+v"(za[0][1]), "+v"(za[1][0]), "+v"(za[1][1]), "+v"(za[2][0]), "+v"(za[2][1]), "+v"(za[3][0]), "+v"(za[3][1]));
    asm volatile("" : "+v"(zs[0]), "+v"(zs[1]), "+v"(zs[2]), "+v"(zs[3]));
    STAMP(4);
    BAR();
    STAMP(5);
    MEMB();
    float bs[4];
#pragma unroll
    for (int m = 0; m < 4; ++m) bs[m] = __uint_as_float((unsigned)*(LAS u16*)(size_t)(lds0 + L_BIAS + (wt * 64 + m * 16 + r16) * 2) << 16);
    const u32x2 tg = *(LAS u32x2*)(size_t)(lds0 + L_TAG);
    const u64 tag = ((u64)(unsigned)__builtin_amdgcn_readfirstlane(tg.y) << 32) | (unsigned)__builtin_amdgcn_readfirstlane(tg.x);
    {
      auto rZ = RSRC(ws + WS_Z2, FRAG_BYTES);
      const unsigned sl = g * 16 + (r16 ^ (4 * (g >> 1)));
      const bool sw = g & 1;
#pragma unroll
      for (int m = 0; m < 4; ++m) {
        const f32x4 q4 = *(LAS f32x4*)(size_t)(lds0 + L_R + (64 * wt + 16 * m + r16) * 32) + *(LAS f32x4*)(size_t)(lds0 + L_R + (64 * wt + 16 * m + r16) * 32 + 16);
        const float mean = (q4[0] + q4[2]) * (1.0f / 2304.0f), var = fmaxf((q4[1] + q4[3]) * (1.0f / 2304.0f) - mean * mean, 0.f);
        f32x2 rs; rs.x = __builtin_amdgcn_rsqf(var + 1e-5f); rs.y = -mean * rs.x;
        const unsigned dst = (fj * 128 + ti * 8 + wt * 4 + m) * CH, ld = lds0 + wt * STG2 + 16384 + m * CH;
#pragma unroll
        for (int pp = 0; pp < 2; ++pp) {
          const f32x4 a = acc[m][4 + 2 * pp] * rs.x + rs.y, b = acc[m][5 + 2 * pp] * rs.x + rs.y;
          const unsigned a0 = cvt_pk(a[0], a[1]), a1 = cvt_pk(a[2], a[3]), b0 = cvt_pk(b[0], b[1]), b1 = cvt_pk(b[2], b[3]);
          u32x4 v; v.x = sw ? b0 : a0; v.y = sw ? b1 : a1; v.z = sw ? a0 : b0; v.w = sw ? a1 : b1;
          __builtin_amdgcn_raw_buffer_store_b128(v, rZ, dst + sl * 16 + wf * 2048 + pp * 1024, 0, SAUX);
          *(LAS u32x4*)(size_t)(ld + wf * 2048 + pp * 1024 + sl * 16) = v;
        }
        if (wf) {
          const f32x4 a = acc[m][8] * rs.x + rs.y; u32x2 v; v.x = cvt_pk(a[0], a[1]); v.y = cvt_pk(a[2], a[3]);
          __builtin_amdgcn_raw_buffer_store_b64(v, rZ, dst + sl * 8 + 4096, 0, SAUX);
          *(LAS u32x2*)(size_t)(ld + 4096 + sl * 8) = v;
        }
      }
    }
    STAMP(6);
    if (wf == 0) g2_phase<0>(lds0, wid, wt, lane, ti, fj, ws, tag, za, zs, bs); else g2_phase<1>(lds0, wid, wt, lane, ti, fj, ws, tag, za, zs, bs);
    if (!has3) return;
    if (wt == 0) g3_compute<0>(lds0, wid, wf, lane, out, tt, dd); else g3_compute<1>(lds0, wid, wf, lane, out, tt, dd);
    STAMP(13);
    DUMP();
  }
}

extern "C" void kernel_launch(void* const* d_in, const int* in_sizes, int n_in, void* d_out, int out_size, void* d_ws, size_t ws_size, hipStream_t stream) {
  (void)hipFuncSetAttribute((const void*)gmlp_fused, hipFuncAttributeMaxDynamicSharedMemorySize, LDS_BYTES);
  gmlp_fused<<<256, 512, LDS_BYTES, stream>>>((const char*)d_in[0], (const char*)d_in[1], (const char*)d_in[2], (const u16*)d_in[3], (const char*)d_in[4], (char*)d_out, (char*)d_ws);
}
```

```cpp
#include <hip/hip_runtime.h>
#include <hip/hip_bf16.h>
typedef unsigned short u16;
typedef unsigned long long u64;
typedef short s16x4 __attribute__((ext_vector_type(4)));
typedef short bf16x8 __attribute__((ext_vector_type(8)));
typedef float f32x2 __attribute__((ext_vector_type(2)));
typedef float f32x4 __attribute__((ext_vector_type(4)));
typedef unsigned u32x4 __attribute__((ext_vector_type(4)));
typedef unsigned u32x2 __attribute__((ext_vector_type(2)));
#define LAS __attribute__((address_space(3)))
#define DEV __device__ __forceinline__
#define BAR() __builtin_amdgcn_s_barrier()
#define SB() __builtin_amdgcn_sched_barrier(0)
#define MEMB() asm volatile("" ::: "memory")
#define WAITL0() __builtin_amdgcn_s_waitcnt(0xC07F)
#define WL0() asm volatile("s_waitcnt lgkmcnt(0)" ::: "memory")
#define WV(N) asm volatile("s_waitcnt vmcnt(%0)" ::"i"(N) : "memory")
#define MFMA(a, b, c) __builtin_amdgcn_mfma_f32_16x16x32_bf16(a, b, c, 0, 0, 0)
#define MFMA16(a, b, c) __builtin_amdgcn_mfma_f32_16x16x16bf16_1k(a, b, c, 0, 0, 0)
#define GL(R, D, V, S, I) __builtin_amdgcn_raw_ptr_buffer_load_lds(R, (LAS void*)(size_t)(D), 16, V, S, I, 0)
#define GL4(R, D, V0, V1, V2, V3, S) do { GL(R, D, V0, S, 0); GL(R, D, V1, S, 1024); GL(R, D, V2, S, 2048); GL(R, D, V3, S, 3072); } while (0)
#define GS(R, D, V, S, I) __builtin_amdgcn_raw_ptr_buffer_load_lds(R, (LAS void*)(size_t)(D), 16, V, S, I, 16)
#define GS4(R, D, V, S) do { GS(R, D, V, S, 0); GS(R, D, V, S, 1024); GS(R, D, V, S, 2048); GS(R, D, V, S, 3072); } while (0)
#define RSRC(p, n) __builtin_amdgcn_make_buffer_rsrc((void*)(p), 0, (n), 0x00020000)
#define RLX_AGENT __ATOMIC_RELAXED, __HIP_MEMORY_SCOPE_AGENT
#define ROT1 2
#define FLAGM 0
#define LA2 2
#define CODE_KB 48

constexpr int CH = 4608;
constexpr int FRAG_BYTES = 16 * 128 * CH;
constexpr size_t WS_Z2 = 0, WS_P = FRAG_BYTES, WS_ST = 2 * (size_t)FRAG_BYTES, WS_F1 = WS_ST + 16 * 16 * 128 * 16, WS_F2 = WS_F1 + 256 * 128, WS_EP = WS_F2 + 256 * 128;
constexpr int STG = 34816, STGP = 23040, STGO = 18432, L_O = 0, L_P = 5 * STGO, LDS_BYTES = 163840;
constexpr int L_T = 139264, L_R = L_T + 8192, L_X = 151552, L_BIAS = 159744, L_TAG = L_BIAS + 256, L_DUMMY = 161280;
constexpr unsigned SPIN_CAP = 1u << 16, OOB = 0x80000000u;
#define STAMP(I)
#define DUMP()

DEV unsigned cvt_pk(float lo, float hi) { unsigned r; asm("v_cvt_pk_bf16_f32 %0, %1, %2" : "=v"(r) : "v"(lo), "v"(hi)); return r; }
DEV bf16x8 ld128(unsigned a) { return *(LAS bf16x8*)(size_t)a; }
DEV s16x4 ld64(unsigned a) { return *(LAS s16x4*)(size_t)a; }
DEV s16x4 ldtr(unsigned a) { return __builtin_amdgcn_ds_read_tr16_b64_v4i16((LAS s16x4*)(size_t)a); }
DEV bf16x8 cat(s16x4 a, s16x4 b) { return __builtin_shufflevector(a, b, 0, 1, 2, 3, 4, 5, 6, 7); }
DEV float bflo(unsigned v) { return __uint_as_float(v << 16); }
DEV float bfhi(unsigned v) { return __uint_as_float(v & 0xffff0000u); }
DEV f32x2 gelu2(f32x2 x) {
  const float k1 = -2.0f * 0.7978845608028654f * 1.4426950408889634f, k2 = k1 * 0.044715f;
  f32x2 t = x * (x * x * k2 + k1), e;
  e.x = __builtin_amdgcn_exp2f(t.x); e.y = __builtin_amdgcn_exp2f(t.y);
  e = e + 1.0f;
  e.x = __builtin_amdgcn_rcpf(e.x); e.y = __builtin_amdgcn_rcpf(e.y);
  return x * e;
}
DEV u32x2 gelu_pack(f32x4 v) { const f32x2 a = gelu2((f32x2){v[0], v[1]}), b = gelu2((f32x2){v[2], v[3]}); return (u32x2){cvt_pk(a.x, a.y), cvt_pk(b.x, b.y)}; }
DEV u32x2 gelu_pack_sum(f32x4 v, f32x2& s1, f32x2& s2) {
  const f32x2 a = gelu2((f32x2){v[0], v[1]}), b = gelu2((f32x2){v[2], v[3]});
  s1 += a + b; s2 += a * a; s2 += b * b;
  return (u32x2){cvt_pk(a.x, a.y), cvt_pk(b.x, b.y)};
}

struct TAddr { unsigned w[2], z[2][2], s[2]; };
struct TFrag { bf16x8 wX[4], wY[4]; s16x4 tX[5][2], tY[4][2]; };
template <int WF, int PS, int OFF, bool FIRST, bool SYNC = true>
DEV void t_step(TAddr& A, int dlt, f32x4 (&acc)[4][5], TFrag& F) {
  constexpr int KX = WF, KY = 1 - WF;
  SB(); if (SYNC) BAR(); MEMB(); SB();
#pragma unroll
  for (int i = 0; i < 16; ++i) {
    const int n = i >> 2, m = i & 3;
    if (!FIRST) acc[m][n] = MFMA(cat(F.tY[n][0], F.tY[n][1]), F.wY[m], acc[m][n]);
    SB();
    if (i == 0) F.wX[0] = ld128(A.w[KX] + OFF);
    else if (i == 1) F.tX[0][0] = ldtr(A.z[0][0] + OFF + KX * 9216);
    else if (i == 2) F.tX[0][1] = ldtr(A.z[1][0] + OFF + KX * 9216);
    else if (i < 6) F.wX[i - 2] = ld128(A.w[KX] + OFF + (i - 2) * 2048);
    else if (i < 12) { const int nn = 1 + ((i - 6) >> 1), u = (i - 6) & 1; F.tX[nn][u] = ldtr(A.z[u][nn & 1] + OFF + KX * 9216 + (nn >> 1) * PS); }
    else if (i < 14) F.tX[4][i - 12] = ldtr(A.s[i - 12] + OFF + KX * 9216);
    SB();
  }
#pragma unroll
  for (int i = 0; i < 20; ++i) {
    const int n = i >> 2, m = i & 3;
    acc[m][n] = MFMA(cat(F.tX[n][0], F.tX[n][1]), F.wX[m], acc[m][n]);
    SB();
    if (i == 0) F.wY[0] = ld128(A.w[KY] + OFF);
    else if (i == 1) F.tY[0][0] = ldtr(A.z[0][0] + OFF + KY * 9216);
    else if (i == 2) F.tY[0][1] = ldtr(A.z[1][0] + OFF + KY * 9216);
    else if (i < 6) F.wY[i - 2] = ld128(A.w[KY] + OFF + (i - 2) * 2048);
    else if (i < 12) { const int nn = 1 + ((i - 6) >> 1), u = (i - 6) & 1; F.tY[nn][u] = ldtr(A.z[u][nn & 1] + OFF + KY * 9216 + (nn >> 1) * PS); }
    else if (OFF != 0 && i < 16) {
      const int u = i & 1;
      if (i < 14) { A.w[u] += dlt; A.s[u] += dlt; asm volatile("" : "+v"(A.w[u]), "+v"(A.s[u])); }
      else { A.z[u][0] += dlt; A.z[u][1] += dlt; asm volatile("" : "+v"(A.z[u][0]), "+v"(A.z[u][1])); }
    }
    SB();
  }
  WAITL0();
}
template <int WF>
DEV void t_finish(unsigned lds0, int wt, int lane, f32x4 (&acc)[4][5], TFrag& F) {
#pragma unroll
  for (int h = 0; h < 2; ++h) *(LAS f32x4*)(size_t)(lds0 + L_X + ((wt * 2 + WF) * 2 + h) * 1024 + lane * 16) = acc[2 * (1 - WF) + h][4];
#pragma unroll
  for (int n = 0; n < 4; ++n)
#pragma unroll
    for (int m = 0; m < 4; ++m) acc[m][n] = MFMA(cat(F.tY[n][0], F.tY[n][1]), F.wY[m], acc[m][n]);
  WL0();
  BAR(); MEMB();
#pragma unroll
  for (int h = 0; h < 2; ++h) acc[2 * WF + h][4] += *(LAS f32x4*)(size_t)(lds0 + L_X + ((wt * 2 + 1 - WF) * 2 + h) * 1024 + lane * 16);
}
template <int WF, bool MID, class Mid>
DEV void in_pass(unsigned lds0, int wt, int lane, TAddr& A, f32x4 (&acc)[4][5], Mid mid) {
  TFrag F;
#pragma unroll
  for (int m = 0; m < 4; ++m)
#pragma unroll
    for (int n = 0; n < 5; ++n) acc[m][n] = (f32x4){0, 0, 0, 0};
  int dlt = 2 * STG;
  WAITL0();
  t_step<WF, 64, 0, true>(A, dlt, acc, F);
  for (int k = 0; k < (MID ? 3 : 4); ++k) {
    t_step<WF, 64, STG, false>(A, dlt, acc, F);
    dlt = -dlt;
    t_step<WF, 64, 0, false>(A, dlt, acc, F);
  }
  if (MID) {
    t_step<WF, 64, STG, false>(A, -2 * STG, acc, F);
    SB(); mid(); SB();
    t_step<WF, 64, 0, false>(A, 0, acc, F);
  }
  t_finish<WF>(lds0, wt, lane, acc, F);
}

struct G3Frag { bf16x8 p[2][5]; s16x4 w[2][2][2], ws[2], ps[3]; };
template <int KH, bool MM>
DEV void g3_stage(f32x4 (&acc)[5][2], const G3Frag& c, G3Frag& n, unsigned aP, unsigned aS, unsigned a0, unsigned a1) {
  constexpr int M0 = KH ? 3 : 0, NS = KH ? 2 : 3, NM = 20 + 2 * NS, NR = 20 + NS;
#pragma unroll
  for (int i = 0; i < NM; ++i) {
    if (MM) {
      if (i >= 10 && i < 10 + 2 * NS) acc[M0 + ((i - 10) >> 1)][i & 1] = MFMA16(c.ws[i & 1], c.ps[(i - 10) >> 1], acc[M0 + ((i - 10) >> 1)][i & 1]);
      else { const int ii = i < 10 ? i : i - 2 * NS, P = ii / 10, m = (ii % 10) >> 1, cc = ii & 1; acc[m][cc] = MFMA(cat(c.w[P][cc][0], c.w[P][cc][1]), c.p[P][m], acc[m][cc]); }
    }
    SB();
#pragma unroll
    for (int r = (i * 3) / 2; r < ((i + 1) * 3) / 2; ++r) {
      if (r < 18) { const int P = r / 9, q = r % 9;
        if (q < 4) n.w[P][q >> 1][q & 1] = ldtr(((q >> 1) ? a1 : a0) + (2 * KH + P) * 4096 + (q & 1) * 2048);
        else n.p[P][q - 4] = ld128(aP + P * 1024 + (q - 4) * 4096); }
      else if (r < 20) n.ws[r - 18] = ldtr(((r - 18) ? a1 : a0) + 16384);
      else if (r < NR) n.ps[r - 20] = ld64(aS + (M0 + r - 20) * 512);
    }
    SB();
  }
}
template <int KH>
DEV void g3_compute(unsigned lds0, int dh, int lane, char* out, int tt, int dd) {
  f32x4 acc[5][2];
#pragma unroll
  for (int m = 0; m < 5; ++m) { acc[m][0] = (f32x4){0, 0, 0, 0}; acc[m][1] = (f32x4){0, 0, 0, 0}; }
  const int g = lane >> 4, qr = (lane >> 2) & 3, p = lane & 3;
  const int rr = 4 * g + qr;
  unsigned bW[2];
#pragma unroll
  for (int c = 0; c < 2; ++c) bW[c] = lds0 + L_O + rr * 128 + ((((2 * dh + c) * 2 + (p >> 1)) ^ (((rr >> 1) & 3) << 1)) << 4) + (p & 1) * 8;
  const unsigned bP = lds0 + L_P + lane * 16 + KH * 2048, bS = lds0 + L_P + 20480 + lane * 8;
  G3Frag f0, f1;
  WAITL0();
  SB(); BAR(); MEMB(); SB();
  g3_stage<KH, false>(acc, f1, f0, bP, bS, bW[0], bW[1]);
  WAITL0();
  unsigned sp = STGP, sw = STGO;
  for (int j = 1; j < 16; j += 2) {
    SB(); BAR(); MEMB(); SB();
    g3_stage<KH, true>(acc, f0, f1, bP + sp, bS + sp, bW[0] + sw, bW[1] + sw);
    sp = sp == 2 * STGP ? 0 : sp + STGP; sw = sw == 4 * STGO ? 0 : sw + STGO;
    WAITL0();
    SB(); BAR(); MEMB(); SB();
    g3_stage<KH, true>(acc, f1, f0, bP + sp, bS + sp, bW[0] + sw, bW[1] + sw);
    sp = sp == 2 * STGP ? 0 : sp + STGP; sw = sw == 4 * STGO ? 0 : sw + STGO;
    WAITL0();
  }
#pragma unroll
  for (int m = 0; m < 5; ++m) *(LAS f32x4*)(size_t)(lds0 + ((KH * 2 + dh) * 5 + m) * 1024 + lane * 16) = acc[m][1 - KH];
  WL0();
  BAR(); MEMB();
#pragma unroll
  for (int m = 0; m < 5; ++m) {
    const f32x4 a = acc[m][KH] + *(LAS f32x4*)(size_t)(lds0 + (((1 - KH) * 2 + dh) * 5 + m) * 1024 + lane * 16);
    const int t = tt * 80 + m * 16 + (lane & 15);
    u32x2 v; v.x = cvt_pk(a[0], a[1]); v.y = cvt_pk(a[2], a[3]);
    if (t < 2048) *(u32x2*)(out + (size_t)t * 1152 + dd * 128 + (dh * 2 + KH) * 32 + (lane >> 4) * 8) = v;
  }
}

template <int WF>
DEV void front(unsigned lds0, int wid, int wt, int lane, int ti, int fj, char* ws) {
  const int r16 = lane & 15, g = lane >> 4, qr = (lane >> 2) & 3, p = lane & 3;
  TAddr A;
  A.w[0] = lds0 + (64 * wt + r16) * 128 + ((g ^ (r16 >> 1)) << 4); A.w[1] = A.w[0] ^ 64;
#pragma unroll
  for (int u = 0; u < 2; ++u) {
    const unsigned b = lds0 + 16384 + (16 * u + 4 * g + qr) * 288 + p * 8;
    A.z[u][0] = b + WF * 128; A.z[u][1] = b + WF * 128 + 32; A.s[u] = b + 256;
  }
  asm volatile("" : "+v"(A.w[0]), "+v"(A.w[1]), "+v"(A.s[0]), "+v"(A.s[1]), "+v"(A.z[0][0]), "+v"(A.z[0][1]), "+v"(A.z[1][0]), "+v"(A.z[1][1]));
  f32x4 acc[4][5];
  in_pass<WF, false>(lds0, wt, lane, A, acc, []() {});
  STAMP(2);
  u32x2 ua[4][4], ub[2];
  {
    f32x2 s1[4], s2[4];
#pragma unroll
    for (int m = 0; m < 4; ++m) {
      s1[m] = (f32x2){0.f, 0.f}; s2[m] = (f32x2){0.f, 0.f};
#pragma unroll
      for (int n = 0; n < 4; ++n) ua[m][n] = gelu_pack_sum(acc[m][n], s1[m], s2[m]);
      if ((m >> 1) == WF) ub[m & 1] = gelu_pack_sum(acc[m][4], s1[m], s2[m]);
    }
#pragma unroll
    for (int m = 0; m < 4; ++m) *(LAS f32x2*)(size_t)(lds0 + L_T + (64 * wt + 16 * m + r16) * 64 + (WF * 4 + g) * 8) = (f32x2){s1[m].x + s1[m].y, s2[m].x + s2[m].y};
  }
#pragma unroll
  for (int m = 0; m < 4; ++m) asm volatile("" : "+v"(ua[m][0]), "+v"(ua[m][1]), "+v"(ua[m][2]), "+v"(ua[m][3]));
  asm volatile("" : "+v"(ub[0]), "+v"(ub[1]));
  STAMP(3);
  in_pass<WF, true>(lds0, wt, lane, A, acc, [&]() {
    STAMP(4);
#pragma unroll
  for (int m = 0; m < 4; ++m) asm volatile("" : "+v"(ua[m][0]), "+v"(ua[m][1]), "+v"(ua[m][2]), "+v"(ua[m][3]));
  asm volatile("" : "+v"(ub[0]), "+v"(ub[1]));
  int lz = lane; asm volatile("" : "+v"(lz));
  {
    const int r16 = lz & 15, g = lz >> 4;
    auto rZ = RSRC(ws + WS_Z2, FRAG_BYTES);
    const unsigned sl = g * 16 + (r16 ^ (4 * (g >> 1)));
    const bool sw = g & 1;
#pragma unroll
    for (int m = 0; m < 4; ++m) {
      const f32x4 q4 = *(LAS f32x4*)(size_t)(lds0 + L_R + (64 * wt + 16 * m + r16) * 32) + *(LAS f32x4*)(size_t)(lds0 + L_R + (64 * wt + 16 * m + r16) * 32 + 16);
      const float mean = (q4[0] + q4[2]) * (1.0f / 2304.0f), var = fmaxf((q4[1] + q4[3]) * (1.0f / 2304.0f) - mean * mean, 0.f);
      const float rs = __builtin_amdgcn_rsqf(var + 1e-5f), sh = -mean * rs;
      auto norm = [&](unsigned v) { const f32x2 y = (f32x2){bflo(v), bfhi(v)} * rs + sh; return cvt_pk(y.x, y.y); };
      const unsigned dst = (fj * 128 + ti * 8 + wt * 4 + m) * CH;
#pragma unroll
      for (int pp = 0; pp < 2; ++pp) {
        const u32x2 a = sw ? ua[m][2 * pp + 1] : ua[m][2 * pp], b = sw ? ua[m][2 * pp] : ua[m][2 * pp + 1];
        const u32x4 v = {norm(a.x), norm(a.y), norm(b.x), norm(b.y)};
        __builtin_amdgcn_raw_buffer_store_b128(v, rZ, dst + sl * 16 + WF * 2048 + pp * 1024, 0, 16);
        ua[m][2 * pp] = (u32x2){v.x, v.y}; ua[m][2 * pp + 1] = (u32x2){v.z, v.w};
      }
      if ((m >> 1) == WF) {
        const u32x2 v = {norm(ub[m & 1].x), norm(ub[m & 1].y)};
        __builtin_amdgcn_raw_buffer_store_b64(v, rZ, dst + sl * 8 + 4096, 0, 16);
        ub[m & 1] = v;
      }
    }
  }
    STAMP(5);
  });
#pragma unroll
  for (int m = 0; m < 4; ++m) asm volatile("" : "+v"(acc[m][0]), "+v"(acc[m][1]), "+v"(acc[m][2]), "+v"(acc[m][3]), "+v"(acc[m][4]));
  const u32x2 tg = *(LAS u32x2*)(size_t)(lds0 + L_TAG);
  const u64 tag = ((u64)(unsigned)__builtin_amdgcn_readfirstlane(tg.y) << 32) | (unsigned)__builtin_amdgcn_readfirstlane(tg.x);
  {
    const unsigned ld = lds0 + wt * STG + 16384, sl = g * 16 + (r16 ^ (4 * (g >> 1)));
#pragma unroll
    for (int m = 0; m < 4; ++m) {
#pragma unroll
      for (int pp = 0; pp < 2; ++pp) *(LAS u32x4*)(size_t)(ld + m * CH + WF * 2048 + pp * 1024 + sl * 16) = (u32x4){ua[m][2 * pp].x, ua[m][2 * pp].y, ua[m][2 * pp + 1].x, ua[m][2 * pp + 1].y};
      if ((m >> 1) == WF) *(LAS u32x2*)(size_t)(ld + m * CH + 4096 + sl * 8) = ub[m & 1];
    }
  }
  STAMP(7);
  SB();
  u32x2 za[4][4], zb[2];
#pragma unroll
  for (int m = 0; m < 4; ++m) {
#pragma unroll
    for (int n = 0; n < 4; ++n) za[m][n] = gelu_pack(acc[m][n]);
    if ((m >> 1) == WF) zb[m & 1] = gelu_pack(acc[m][4]);
    asm volatile("" : "+v"(za[m][0]), "+v"(za[m][1]), "+v"(za[m][2]), "+v"(za[m][3]));
    if (m == FLAGM) {
      SB();
      asm volatile("s_waitcnt vmcnt(0) lgkmcnt(0)" ::: "memory");
      if (lane == 0) __hip_atomic_store((u64*)(ws + WS_F1 + (fj * 16 + ti) * 128) + wid, tag, RLX_AGENT);
      BAR();
      SB();
    }
  }
  asm volatile("" : "+v"(zb[0]), "+v"(zb[1]));
  STAMP(6);
#pragma unroll
  for (int u = 0; u < 2; ++u) {
    const unsigned sl = p * 16 + 8 * (g & 1) + 4 * (u ^ (p >> 1)) + qr;
#pragma unroll
    for (int h = 0; h < 2; ++h) A.z[u][h] = lds0 + 16384 + (g >> 1) * CH + WF * 2048 + sl * 16 + ((h ^ (p & 1)) * 8);
    A.s[u] = lds0 + 16384 + (g >> 1) * CH + 4096 + sl * 8;
  }
  asm volatile("" : "+v"(A.s[0]), "+v"(A.s[1]), "+v"(A.z[0][0]), "+v"(A.z[0][1]), "+v"(A.z[1][0]), "+v"(A.z[1][1]));
#pragma unroll
  for (int m = 0; m < 4; ++m) {
    const float b = __uint_as_float((unsigned)*(LAS u16*)(size_t)(lds0 + L_BIAS + (wt * 64 + m * 16 + r16) * 2) << 16);
#pragma unroll
    for (int n = 0; n < 5; ++n) { const float b0 = (n < 4 || (m >> 1) == WF) ? b : 0.f; acc[m][n] = (f32x4){b0, b0, b0, b0}; }
  }
  TFrag F;
  int dlt = 2 * STG;
  WAITL0();
  t_step<WF, 1024, 0, true, false>(A, dlt, acc, F);
  t_step<WF, 1024, STG, false, false>(A, dlt, acc, F);
  STAMP(8);
  for (int k = 0; k < 15; ++k) {
    dlt = -dlt;
    t_step<WF, 1024, 0, false>(A, dlt, acc, F);
    t_step<WF, 1024, STG, false>(A, dlt, acc, F);
  }
  t_finish<WF>(lds0, wt, lane, acc, F);
  STAMP(10);
  auto rP = RSRC(ws + WS_P, FRAG_BYTES);
  auto gate = [&](f32x4 a, u32x2 z) {
    const f32x2 x = (f32x2){a[0], a[1]} * (f32x2){bflo(z.x), bfhi(z.x)}, y = (f32x2){a[2], a[3]} * (f32x2){bflo(z.y), bfhi(z.y)};
    return (u32x2){cvt_pk(x.x, x.y), cvt_pk(y.x, y.y)};
  };
#pragma unroll
  for (int m = 0; m < 4; ++m) {
    const unsigned dst = ((ti * 8 + wt * 4 + m) * 16 + fj) * CH;
#pragma unroll
    for (int pp = 0; pp < 2; ++pp) {
      const u32x2 a = gate(acc[m][2 * pp], za[m][2 * pp]), b = gate(acc[m][2 * pp + 1], za[m][2 * pp + 1]);
      __builtin_amdgcn_raw_buffer_store_b128((u32x4){a.x, a.y, b.x, b.y}, rP, dst + lane * 16 + (2 * WF + pp) * 1024, 0, 16);
    }
    if ((m >> 1) == WF) __builtin_amdgcn_raw_buffer_store_b64(gate(acc[m][4], zb[m & 1]), rP, dst + lane * 8 + 4096, 0, 16);
  }
  STAMP(11);
  WV(0);
  if (lane == 0) __hip_atomic_store((u64*)(ws + WS_F2 + (ti * 16 + fj) * 128) + wid, tag, RLX_AGENT);
  BAR();
  STAMP(12);
}

DEV u64 poll16(const char* base, u64 tag, int lane) {
  const u64 v = __hip_atomic_load((const u64*)(base + (lane >> 2) * 128) + (lane & 3), RLX_AGENT);
  return __builtin_amdgcn_ballot_w64(v == tag);
}
DEV bool got(u64 mask, int who) { return ((unsigned)(mask >> (4 * who)) & 15u) == 15u; }

__global__ __launch_bounds__(512, 2) void gmlp_fused(const char* __restrict__ X, const char* __restrict__ Win, const char* __restrict__ Wsp, const u16* __restrict__ bias,
                                                     const char* __restrict__ Wo, char* __restrict__ out, char* __restrict__ ws) {
  extern __shared__ __attribute__((aligned(16))) char smem[];
  u64 pc0; asm volatile("s_getpc_b64 %0" : "=s"(pc0));
  const int tid = threadIdx.x, wid = __builtin_amdgcn_readfirstlane(tid >> 6), lane = tid & 63;
  const int bid = blockIdx.x, xg = bid & 7, rk = bid >> 3, il = rk & 7, jl = rk >> 3, ti = (xg & 1) * 8 + il, fj = (xg >> 1) * 4 + jl;
  const int L3 = xg * 30 + rk, tt = L3 / 9, dd = L3 - tt * 9;
  const bool has3 = rk < 30 && L3 < 234;
  const unsigned lds0 = (unsigned)(size_t)(LAS char*)smem;
  STAMP(0);
  if (__builtin_expect(wid >= 4, 1)) {
    __builtin_amdgcn_s_setprio(3);
    const int lw = wid - 4;
    auto rX = RSRC(X, 2048 * 1152); auto rWi = RSRC(Win, 576 * 9216);
    unsigned vX[4], vW[5];
    const unsigned zp0 = lw < 2 ? lw * 5 : 2 + lw * 4;
    const unsigned sX = (ti * 128 + lw * 32) * 1152, sW = fj * 288;
    const int rot = ROT1 == 0 ? 0 : ROT1 == 1 ? il + 2 * jl : 3 * ((il + jl) % 3);
    auto k1 = [&](int k) { int kk = k + rot; kk = kk >= 9 ? kk - 9 : kk; kk = kk >= 9 ? kk - 9 : kk; return kk; };
    auto issue1 = [&](int half, int k) {
      const int kk = k1(k);
      const unsigned d = lds0 + (k & 3) * STG, dz = d + 16384 + zp0 * 1024, sw = sW + half * 4608 + kk * 64 * 9216;
      GL4(rX, d + lw * 4096, vX[0], vX[1], vX[2], vX[3], sX + kk * 128);
      GL4(rWi, dz, vW[0], vW[1], vW[2], vW[3], sw);
      GL(rWi, lw < 2 ? dz + 4096 : lds0 + L_DUMMY, vW[4], sw, 0);
    };
    {
#pragma unroll
      for (int q = 0; q < 4; ++q) { const int row = 8 * q + (lane >> 3); vX[q] = row * 1152 + (((lane & 7) ^ ((row >> 1) & 7)) << 4) - q * 1024; }
      GL4(rX, lds0 + lw * 4096, vX[0], vX[1], vX[2], vX[3], sX + k1(0) * 128);
      SB();
#pragma unroll
      for (int q = 0; q < 5; ++q) {
        const unsigned c16 = (zp0 + q) * 64 + lane, R = (c16 * 3641u) >> 16, c = (c16 - R * 18) * 16, k = (R & 32) + 8 * ((R >> 2) & 3) + 4 * ((R >> 4) & 1) + (R & 3);
        vW[q] = k * 9216 + c - (q & 3) * 1024;
      }
      if (lw >= 2) vW[4] = OOB;
      const unsigned dz = lds0 + 16384 + zp0 * 1024, sw = sW + 4608 + k1(0) * 64 * 9216;
      GL4(rWi, dz, vW[0], vW[1], vW[2], vW[3], sw);
      GL(rWi, lw < 2 ? dz + 4096 : lds0 + L_DUMMY, vW[4], sw, 0);
    }
    const unsigned vL = lane * 16;
    auto rWs = RSRC(Wsp, 2048 * 4096); auto rWo = RSRC(Wo, 2304 * 1152);
    issue1(1, 1);
    WV(9); BAR();
    STAMP(1);
    issue1(1, 2);
    WV(9);
    for (int k = 1; k < 5; ++k) { BAR(); issue1(1, k + 2); WV(9); }
    BAR(); issue1(1, 7); issue1(1, 8); WV(18);
    BAR(); issue1(0, 1); WV(18);
    BAR(); issue1(0, 2); WV(18);
    BAR(); issue1(0, 3);
    const u64 ep = __hip_atomic_load((const u64*)(ws + WS_EP), RLX_AGENT);
    if (lw == 0) __builtin_amdgcn_raw_ptr_buffer_load_lds(RSRC(bias, 4096), (LAS void*)(size_t)(lds0 + L_BIAS), 4, lane * 4, ti * 256, 0, 0);
    BAR();
    issue1(0, 0);
#pragma unroll
    for (int q = 0; q < 8; ++q) GL(rWs, lds0 + L_DUMMY, vL, bid * 32768 + lw * 8192 + q * 1024, 0);
    GL(rWo, lds0 + L_DUMMY, vL, bid * 10368 + lw * 2592, 0); GL(rWo, lds0 + L_DUMMY, vL, bid * 10368 + lw * 2592 + 1024, 0); GL(rWo, lds0 + L_DUMMY, vL, bid * 10368 + lw * 2592 + 1568, 0);
    const unsigned tagA = __builtin_amdgcn_readfirstlane((unsigned)ep) + 0x9E3779B9u, tagB = __builtin_amdgcn_readfirstlane((unsigned)(ep >> 32)) + 0x85EBCA6Bu;
    const u64 tag = ((u64)tagB << 32) | tagA;
    if (lw == 0) { const u32x2 tg = {tagA, tagB}; asm volatile("ds_write_b64 %0, %1\n\ts_waitcnt lgkmcnt(0)" :: "v"(lds0 + L_TAG), "v"(tg) : "memory"); }
    unsigned vS[4];
#pragma unroll
    for (int q = 0; q < 4; ++q) { const int row = 8 * q + (lane >> 3); vS[q] = row * 4096 + (((lane & 7) ^ ((row >> 1) & 7)) << 4) - q * 1024; }
    const unsigned sWs = (ti * 128 + lw * 32) * 4096;
    auto kpos = [&](int n) { return n < 2 ? 2 * ti + n : (n - 2 < 2 * ti ? n - 2 : n); };
    auto issueW = [&](int n) { GL4(rWs, lds0 + (n & 3) * STG + lw * 4096, vS[0], vS[1], vS[2], vS[3], sWs + kpos(n) * 128); };
    WV(11);
    BAR();
    STAMP(2);
    auto rS = RSRC(ws + WS_ST, 16 * 16 * 128 * 16);
    if (lane < 32) {
      const int row = lw * 32 + lane;
      f32x4 v0, v1, v2, v3;
      asm volatile("ds_read_b128 %0, %4\n\tds_read_b128 %1, %4 offset:16\n\tds_read_b128 %2, %4 offset:32\n\tds_read_b128 %3, %4 offset:48\n\ts_waitcnt lgkmcnt(0)"
                   : "=&v"(v0), "=&v"(v1), "=&v"(v2), "=&v"(v3) : "v"(lds0 + L_T + row * 64) : "memory");
      const f32x4 s = (v0 + v1) + (v2 + v3);
      u32x4 gq; gq.x = tagA; gq.y = __float_as_uint(s[0] + s[2]); gq.z = tagB; gq.w = __float_as_uint(s[1] + s[3]);
      __builtin_amdgcn_raw_buffer_store_b128(gq, rS, ((ti * 16 + fj) * 128 + row) * 16, 0, 16);
    }
    u32x4 sv[8];
    auto sweep = [&]() {
      MEMB();
#pragma unroll
      for (int p = 0; p < 8; ++p) sv[p] = __builtin_amdgcn_raw_buffer_load_b128(rS, lane * 16 + (p & 1) * 1024, (ti * 16 + lw * 4 + (p >> 1)) * 2048, 16);
      SB();
    };
    BAR(); issue1(0, 4);
    BAR(); issue1(0, 5);
    BAR(); issue1(0, 6); WV(18);
    BAR(); sweep(); issue1(0, 7); WV(9 + 8 + 9);
    BAR(); issue1(0, 8); WV(18);
    BAR();
    {
      f32x4 s;
      for (unsigned spins = 0;; ++spins) {
        bool ok = true;
        s = (f32x4){0.f, 0.f, 0.f, 0.f};
#pragma unroll
        for (int p = 0; p < 8; ++p) { ok &= (sv[p].x == tagA) & (sv[p].z == tagB); s[(p & 1) * 2] += __uint_as_float(sv[p].y); s[(p & 1) * 2 + 1] += __uint_as_float(sv[p].w); }
        if (__all(ok)) break;
        if (spins > SPIN_CAP) { s[0] = s[2] = __builtin_nanf(""); break; }
        sweep();
      }
      const f32x2 o0 = {s[0], s[1]}, o1 = {s[2], s[3]};
      asm volatile("ds_write_b64 %0, %1\n\tds_write_b64 %0, %2 offset:2048\n\ts_waitcnt lgkmcnt(0)" :: "v"(lds0 + L_R + lane * 32 + lw * 8), "v"(o0), "v"(o1) : "memory");
    }
    STAMP(3);
    issueW(1); WV(9 + 4);
    BAR(); WV(4);
    BAR();
    BAR();
    issueW(0);
    WV(0);
    BAR();
    auto rZ = RSRC(ws + WS_Z2, FRAG_BYTES);
    const unsigned sZ = fj * 128 * CH + zp0 * 1024, vZ5 = lw < 2 ? vL : OOB;
    u64 ready = 0;
    auto issueZ = [&](int n) {
      const int kp = kpos(n);
      if (__builtin_expect(!got(ready, kp >> 1), 0)) { for (unsigned spins = 0; !got(ready, kp >> 1) && spins < SPIN_CAP; ++spins) ready = poll16(ws + WS_F1 + fj * 16 * 128, tag, lane); }
      MEMB();
      const unsigned dz = lds0 + (n & 3) * STG + 16384 + zp0 * 1024, so = sZ + kp * 18432;
      GS4(rZ, dz, vL, so);
      GS(rZ, lw < 2 ? dz + 4096 : lds0 + L_DUMMY, vZ5, so + 4096, 0);
    };
    issueW(2); issueW(3);
    issueZ(2); issueZ(3);
    STAMP(4);
    WV(5);
    BAR();
    STAMP(5);
    for (int n = 4; n < 31; ++n) {
      issueW(n); issueZ(n);
      WV(9);
      BAR();
    }
    issueW(31); issueZ(31);
    constexpr int TAIL = 9;
    unsigned vO[2], vOh;
#pragma unroll
    for (int q = 0; q < 2; ++q) { const int row = 36 * lw + 8 * q + (lane >> 3); vO[q] = row * 1152 + (((lane & 7) ^ (((row >> 1) & 3) << 1)) << 4); }
    { const int row = 36 * lw + 32 + ((lane >> 3) & 3); vOh = row * 1152 + (((lane & 7) ^ (((row >> 1) & 3) << 1)) << 4); }
    const unsigned vO1 = vO[1] - 1024, vO2 = vO[0] + 16 * 1152 - 2048, vO3 = vO[1] + 16 * 1152 - 3072;
    const unsigned sO = min(dd, 8) * 128;
    auto issueO = [&](int n, int slot) {
      const unsigned d = lds0 + L_O + slot * STGO + lw * 4608, s0 = sO + n * 144 * 1152;
      GL4(rWo, d, vO[0], vO1, vO2, vO3, s0);
      if (lane < 32) GL(rWo, d + 4096, vOh, s0, 0);
    };
    issueO(0, 0);
    WV(9 + 5); BAR();
    issueO(1, 1); issueO(2, 2);
    WV(15); BAR();
    BAR(); BAR();
    if (!has3) { WV(0); return; }
    issueO(3, 3); issueO(4, 4);
    auto rP = RSRC(ws + WS_P, FRAG_BYTES);
    const unsigned vPs = lw < 3 ? (lane >> 5) * 16 * CH + (lane & 31) * 16 + 4096 : OOB;
    const int limS = lw == 2 ? 32 : 64;
    const unsigned sP = tt * 5 * 16 * CH;
    const int pa = (tt * 80) >> 7, pb = min((tt * 80 + 79) >> 7, 15);
    u64 rdy = 0;
    auto issueP = [&](int j, int slot) {
      if (__builtin_expect(!got(rdy, j), 0)) { for (unsigned spins = 0; !got(rdy, j) && spins < SPIN_CAP; ++spins) rdy = poll16(ws + WS_F2 + pa * 16 * 128, tag, lane) & poll16(ws + WS_F2 + pb * 16 * 128, tag, lane); }
      MEMB();
      const unsigned d = lds0 + L_P + slot * STGP, sp = sP + j * CH;
      GS4(rP, d + lw * 4096, vL, sp + lw * 16 * CH);
      GS(rP, d + 16384 + lw * 1024, vL, sp + 64 * CH + lw * 1024, 0);
      if (lane < limS) GS(rP, lw < 3 ? d + 20480 + lw * 1024 : lds0 + L_DUMMY, vPs, sp + lw * 32 * CH, 0);
    };
    issueP(0, 0);
    STAMP(6);
    issueP(1, 1); issueP(2, 2);
    WV(12);
    BAR();
    STAMP(7);
    WV(6);
    BAR(); issueP(3, 0); issueO(5, 0); WV(11);
    int sp3 = 1, so5 = 1;
    for (int n = 2; n < 12; ++n) {
      BAR();
      issueP(n + 2, sp3); issueO(n + 4, so5);
      sp3 = sp3 == 2 ? 0 : sp3 + 1; so5 = so5 == 4 ? 0 : so5 + 1;
      WV(16);
    }
    BAR(); issueP(14, 2); WV(6);
    BAR(); issueP(15, 0); WV(6);
    BAR(); WV(0);
    BAR();
    BAR(); BAR();
    STAMP(8);
    DUMP();
    if (bid == 0 && tid == 256) __hip_atomic_store((u64*)(ws + WS_EP), tag, RLX_AGENT);
  } else {
    const int wt = wid >> 1, wf = wid & 1;
    if (wid == 0) {
      auto rC = RSRC((const char*)(pc0 & ~(u64)255), CODE_KB * 1024);
      GL(rC, lds0 + L_DUMMY, lane * 16, rk * 1024, 0); GL(rC, lds0 + L_DUMMY, lane * 16, rk * 1024 + 32768, 0);
      WV(0);
    }
    if (wf == 0) front<0>(lds0, wid, wt, lane, ti, fj, ws); else front<1>(lds0, wid, wt, lane, ti, fj, ws);
    if (!has3) return;
    if (wt == 0) g3_compute<0>(lds0, wf, lane, out, tt, dd); else g3_compute<1>(lds0, wf, lane, out, tt, dd);
    STAMP(13);
    DUMP();
  }
}

extern "C" void kernel_launch(void* const* d_in, const int* in_sizes, int n_in, void* d_out, int out_size, void* d_ws, size_t ws_size, hipStream_t stream) {
  (void)hipFuncSetAttribute((const void*)gmlp_fused, hipFuncAttributeMaxDynamicSharedMemorySize, LDS_BYTES);
  gmlp_fused<<<256, 512, LDS_BYTES, stream>>>((const char*)d_in[0], (const char*)d_in[1], (const char*)d_in[2], (const u16*)d_in[3], (const char*)d_in[4], (char*)d_out, (char*)d_ws);
}
```

```cpp
#include <hip/hip_runtime.h>
#include <hip/hip_bf16.h>
typedef unsigned short u16;
typedef unsigned long long u64;
typedef short s16x4 __attribute__((ext_vector_type(4)));
typedef short bf16x8 __attribute__((ext_vector_type(8)));
typedef float f32x2 __attribute__((ext_vector_type(2)));
typedef float f32x4 __attribute__((ext_vector_type(4)));
typedef unsigned u32x4 __attribute__((ext_vector_type(4)));
typedef unsigned u32x2 __attribute__((ext_vector_type(2)));
#define LAS __attribute__((address_space(3)))
#define DEV __device__ __forceinline__
#define BAR() __builtin_amdgcn_s_barrier()
#define SB() __builtin_amdgcn_sched_barrier(0)
#define MEMB() asm volatile("" ::: "memory")
#define WAITL0() __builtin_amdgcn_s_waitcnt(0xC07F)
#define WL0() asm volatile("s_waitcnt lgkmcnt(0)" ::: "memory")
#define WV(N) asm volatile("s_waitcnt vmcnt(%0)" ::"i"(N) : "memory")
#define MFMA(a, b, c) __builtin_amdgcn_mfma_f32_16x16x32_bf16(a, b, c, 0, 0, 0)
#define MFMA16(a, b, c) __builtin_amdgcn_mfma_f32_16x16x16bf16_1k(a, b, c, 0, 0, 0)
#define GL(R, D, V, S, I) __builtin_amdgcn_raw_ptr_buffer_load_lds(R, (LAS void*)(size_t)(D), 16, V, S, I, 0)
#define GL4(R, D, V0, V1, V2, V3, S) do { GL(R, D, V0, S, 0); GL(R, D, V1, S, 1024); GL(R, D, V2, S, 2048); GL(R, D, V3, S, 3072); } while (0)
#define GS(R, D, V, S, I) __builtin_amdgcn_raw_ptr_buffer_load_lds(R, (LAS void*)(size_t)(D), 16, V, S, I, 16)
#define GS4(R, D, V, S) do { GS(R, D, V, S, 0); GS(R, D, V, S, 1024); GS(R, D, V, S, 2048); GS(R, D, V, S, 3072); } while (0)
#define RSRC(p, n) __builtin_amdgcn_make_buffer_rsrc((void*)(p), 0, (n), 0x00020000)
#define RLX_AGENT __ATOMIC_RELAXED, __HIP_MEMORY_SCOPE_AGENT
#define ROT1 2
#define OAUX 2
#define WAUX 0
#define FLAGM 0
#define LA2 2
#define CODE_KB 48

constexpr int CH = 4608;
constexpr int FRAG_BYTES = 16 * 128 * CH;
constexpr size_t WS_Z2 = 0, WS_P = FRAG_BYTES, WS_ST = 2 * (size_t)FRAG_BYTES, WS_F1 = WS_ST + 16 * 16 * 128 * 16, WS_F2 = WS_F1 + 256 * 128, WS_EP = WS_F2 + 256 * 128;
constexpr int STG = 34816, STGP = 23040, STGO = 18432, L_O = 0, L_P = 5 * STGO, LDS_BYTES = 163840;
constexpr int L_T = 139264, L_R = L_T + 8192, L_X = 151552, L_BIAS = 159744, L_TAG = L_BIAS + 256, L_DUMMY = 161280;
constexpr unsigned SPIN_CAP = 1u << 16, OOB = 0x80000000u;
#define STAMP(I)
#define DUMP()

DEV unsigned cvt_pk(float lo, float hi) { unsigned r; asm("v_cvt_pk_bf16_f32 %0, %1, %2" : "=v"(r) : "v"(lo), "v"(hi)); return r; }
DEV bf16x8 ld128(unsigned a) { return *(LAS bf16x8*)(size_t)a; }
DEV s16x4 ld64(unsigned a) { return *(LAS s16x4*)(size_t)a; }
DEV s16x4 ldtr(unsigned a) { return __builtin_amdgcn_ds_read_tr16_b64_v4i16((LAS s16x4*)(size_t)a); }
DEV bf16x8 cat(s16x4 a, s16x4 b) { return __builtin_shufflevector(a, b, 0, 1, 2, 3, 4, 5, 6, 7); }
DEV float bflo(unsigned v) { return __uint_as_float(v << 16); }
DEV float bfhi(unsigned v) { return __uint_as_float(v & 0xffff0000u); }
DEV f32x2 gelu2(f32x2 x) {
  const float k1 = -2.0f * 0.7978845608028654f * 1.4426950408889634f, k2 = k1 * 0.044715f;
  f32x2 t = x * (x * x * k2 + k1), e;
  e.x = __builtin_amdgcn_exp2f(t.x); e.y = __builtin_amdgcn_exp2f(t.y);
  e = e + 1.0f;
  e.x = __builtin_amdgcn_rcpf(e.x); e.y = __builtin_amdgcn_rcpf(e.y);
  return x * e;
}
DEV u32x2 gelu_pack(f32x4 v) { const f32x2 a = gelu2((f32x2){v[0], v[1]}), b = gelu2((f32x2){v[2], v[3]}); return (u32x2){cvt_pk(a.x, a.y), cvt_pk(b.x, b.y)}; }
DEV u32x2 gelu_pack_sum(f32x4 v, f32x2& s1, f32x2& s2) {
  const f32x2 a = gelu2((f32x2){v[0], v[1]}), b = gelu2((f32x2){v[2], v[3]});
  s1 += a + b; s2 += a * a; s2 += b * b;
  return (u32x2){cvt_pk(a.x, a.y), cvt_pk(b.x, b.y)};
}

struct TAddr { unsigned w[2], z[2][2], s[2]; };
struct TFrag { bf16x8 wX[4], wY[4]; s16x4 tX[5][2], tY[4][2]; };
template <int WF, int PS, int OFF, bool FIRST, bool SYNC = true>
DEV void t_step(TAddr& A, int dlt, f32x4 (&acc)[4][5], TFrag& F) {
  constexpr int KX = WF, KY = 1 - WF;
  SB(); if (SYNC) BAR(); MEMB(); SB();
#pragma unroll
  for (int i = 0; i < 16; ++i) {
    const int n = i >> 2, m = i & 3;
    if (!FIRST) acc[m][n] = MFMA(cat(F.tY[n][0], F.tY[n][1]), F.wY[m], acc[m][n]);
    SB();
    if (i == 0) F.wX[0] = ld128(A.w[KX] + OFF);
    else if (i == 1) F.tX[0][0] = ldtr(A.z[0][0] + OFF + KX * 9216);
    else if (i == 2) F.tX[0][1] = ldtr(A.z[1][0] + OFF + KX * 9216);
    else if (i < 6) F.wX[i - 2] = ld128(A.w[KX] + OFF + (i - 2) * 2048);
    else if (i < 12) { const int nn = 1 + ((i - 6) >> 1), u = (i - 6) & 1; F.tX[nn][u] = ldtr(A.z[u][nn & 1] + OFF + KX * 9216 + (nn >> 1) * PS); }
    else if (i < 14) F.tX[4][i - 12] = ldtr(A.s[i - 12] + OFF + KX * 9216);
    SB();
  }
#pragma unroll
  for (int i = 0; i < 20; ++i) {
    const int n = i >> 2, m = i & 3;
    acc[m][n] = MFMA(cat(F.tX[n][0], F.tX[n][1]), F.wX[m], acc[m][n]);
    SB();
    if (i == 0) F.wY[0] = ld128(A.w[KY] + OFF);
    else if (i == 1) F.tY[0][0] = ldtr(A.z[0][0] + OFF + KY * 9216);
    else if (i == 2) F.tY[0][1] = ldtr(A.z[1][0] + OFF + KY * 9216);
    else if (i < 6) F.wY[i - 2] = ld128(A.w[KY] + OFF + (i - 2) * 2048);
    else if (i < 12) { const int nn = 1 + ((i - 6) >> 1), u = (i - 6) & 1; F.tY[nn][u] = ldtr(A.z[u][nn & 1] + OFF + KY * 9216 + (nn >> 1) * PS); }
    else if (OFF != 0 && i < 16) {
      const int u = i & 1;
      if (i < 14) { A.w[u] += dlt; A.s[u] += dlt; asm volatile("" : "+v"(A.w[u]), "+v"(A.s[u])); }
      else { A.z[u][0] += dlt; A.z[u][1] += dlt; asm volatile("" : "+v"(A.z[u][0]), "+v"(A.z[u][1])); }
    }
    SB();
  }
  WAITL0();
}
template <int WF>
DEV void t_finish(unsigned lds0, int wt, int lane, f32x4 (&acc)[4][5], TFrag& F) {
#pragma unroll
  for (int h = 0; h < 2; ++h) *(LAS f32x4*)(size_t)(lds0 + L_X + ((wt * 2 + WF) * 2 + h) * 1024 + lane * 16) = acc[2 * (1 - WF) + h][4];
#pragma unroll
  for (int n = 0; n < 4; ++n)
#pragma unroll
    for (int m = 0; m < 4; ++m) acc[m][n] = MFMA(cat(F.tY[n][0], F.tY[n][1]), F.wY[m], acc[m][n]);
  WL0();
  BAR(); MEMB();
#pragma unroll
  for (int h = 0; h < 2; ++h) acc[2 * WF + h][4] += *(LAS f32x4*)(size_t)(lds0 + L_X + ((wt * 2 + 1 - WF) * 2 + h) * 1024 + lane * 16);
}
template <int WF, bool MID, class Mid>
DEV void in_pass(unsigned lds0, int wt, int lane, TAddr& A, f32x4 (&acc)[4][5], Mid mid) {
  TFrag F;
#pragma unroll
  for (int m = 0; m < 4; ++m)
#pragma unroll
    for (int n = 0; n < 5; ++n) acc[m][n] = (f32x4){0, 0, 0, 0};
  int dlt = 2 * STG;
  WAITL0();
  t_step<WF, 64, 0, true>(A, dlt, acc, F);
  for (int k = 0; k < (MID ? 3 : 4); ++k) {
    t_step<WF, 64, STG, false>(A, dlt, acc, F);
    dlt = -dlt;
    t_step<WF, 64, 0, false>(A, dlt, acc, F);
  }
  if (MID) {
    t_step<WF, 64, STG, false>(A, -2 * STG, acc, F);
    SB(); mid(); SB();
    t_step<WF, 64, 0, false>(A, 0, acc, F);
  }
  t_finish<WF>(lds0, wt, lane, acc, F);
}

struct G3Frag { bf16x8 p[2][5]; s16x4 w[2][2][2], ws[2], ps[3]; };
template <int KH, bool MM>
DEV void g3_stage(f32x4 (&acc)[5][2], const G3Frag& c, G3Frag& n, unsigned aP, unsigned aS, unsigned a0, unsigned a1) {
  constexpr int M0 = KH ? 3 : 0, NS = KH ? 2 : 3, NM = 20 + 2 * NS, NR = 20 + NS;
#pragma unroll
  for (int i = 0; i < NM; ++i) {
    if (MM) {
      if (i >= 10 && i < 10 + 2 * NS) acc[M0 + ((i - 10) >> 1)][i & 1] = MFMA16(c.ws[i & 1], c.ps[(i - 10) >> 1], acc[M0 + ((i - 10) >> 1)][i & 1]);
      else { const int ii = i < 10 ? i : i - 2 * NS, P = ii / 10, m = (ii % 10) >> 1, cc = ii & 1; acc[m][cc] = MFMA(cat(c.w[P][cc][0], c.w[P][cc][1]), c.p[P][m], acc[m][cc]); }
    }
    SB();
#pragma unroll
    for (int r = (i * 3) / 2; r < ((i + 1) * 3) / 2; ++r) {
      if (r < 18) { const int P = r / 9, q = r % 9;
        if (q < 4) n.w[P][q >> 1][q & 1] = ldtr(((q >> 1) ? a1 : a0) + (2 * KH + P) * 4096 + (q & 1) * 2048);
        else n.p[P][q - 4] = ld128(aP + P * 1024 + (q - 4) * 4096); }
      else if (r < 20) n.ws[r - 18] = ldtr(((r - 18) ? a1 : a0) + 16384);
      else if (r < NR) n.ps[r - 20] = ld64(aS + (M0 + r - 20) * 512);
    }
    SB();
  }
}
template <int KH>
DEV void g3_compute(unsigned lds0, int dh, int lane, char* out, int tt, int dd) {
  f32x4 acc[5][2];
#pragma unroll
  for (int m = 0; m < 5; ++m) { acc[m][0] = (f32x4){0, 0, 0, 0}; acc[m][1] = (f32x4){0, 0, 0, 0}; }
  const int g = lane >> 4, qr = (lane >> 2) & 3, p = lane & 3;
  const int rr = 4 * g + qr;
  unsigned bW[2];
#pragma unroll
  for (int c = 0; c < 2; ++c) bW[c] = lds0 + L_O + rr * 128 + (((4 * dh + p) ^ (((rr >> 1) & 1) | (rr & 4))) << 4) + ((c ^ (p & 1)) * 8);
  const unsigned bP = lds0 + L_P + lane * 16 + KH * 2048, bS = lds0 + L_P + 20480 + lane * 8;
  G3Frag f0, f1;
  WAITL0();
  SB(); BAR(); MEMB(); SB();
  g3_stage<KH, false>(acc, f1, f0, bP, bS, bW[0], bW[1]);
  WAITL0();
  unsigned sp = STGP, sw = STGO;
  for (int j = 1; j < 16; j += 2) {
    SB(); BAR(); MEMB(); SB();
    g3_stage<KH, true>(acc, f0, f1, bP + sp, bS + sp, bW[0] + sw, bW[1] + sw);
    sp = sp == 2 * STGP ? 0 : sp + STGP; sw = sw == 4 * STGO ? 0 : sw + STGO;
    WAITL0();
    SB(); BAR(); MEMB(); SB();
    g3_stage<KH, true>(acc, f1, f0, bP + sp, bS + sp, bW[0] + sw, bW[1] + sw);
    sp = sp == 2 * STGP ? 0 : sp + STGP; sw = sw == 4 * STGO ? 0 : sw + STGO;
    WAITL0();
  }
#pragma unroll
  for (int m = 0; m < 5; ++m)
    if ((m >= 3) != KH) { *(LAS f32x4*)(size_t)(lds0 + (dh * 10 + m * 2) * 1024 + lane * 16) = acc[m][0]; *(LAS f32x4*)(size_t)(lds0 + (dh * 10 + m * 2 + 1) * 1024 + lane * 16) = acc[m][1]; }
  WL0();
  BAR(); MEMB();
  auto rO = RSRC(out, 2048 * 1152);
  const bool sw2 = g & 1;
#pragma unroll
  for (int m = 0; m < 5; ++m)
    if ((m >= 3) == KH) {
      const f32x4 a = acc[m][0] + *(LAS f32x4*)(size_t)(lds0 + (dh * 10 + m * 2) * 1024 + lane * 16), b = acc[m][1] + *(LAS f32x4*)(size_t)(lds0 + (dh * 10 + m * 2 + 1) * 1024 + lane * 16);
      const unsigned a0 = cvt_pk(a[0], a[1]), a1 = cvt_pk(a[2], a[3]), b0 = cvt_pk(b[0], b[1]), b1 = cvt_pk(b[2], b[3]);
      const u32x4 v = {sw2 ? b0 : a0, sw2 ? b1 : a1, sw2 ? a0 : b0, sw2 ? a1 : b1};
      __builtin_amdgcn_raw_buffer_store_b128(v, rO, (tt * 80 + m * 16 + (lane & 15)) * 1152 + dd * 128 + dh * 64 + g * 16, 0, OAUX);
    }
}

template <int WF>
DEV void front(unsigned lds0, int wid, int wt, int lane, int ti, int fj, char* ws) {
  const int r16 = lane & 15, g = lane >> 4, qr = (lane >> 2) & 3, p = lane & 3;
  TAddr A;
  A.w[0] = lds0 + (64 * wt + r16) * 128 + ((g ^ (r16 >> 1)) << 4); A.w[1] = A.w[0] ^ 64;
#pragma unroll
  for (int u = 0; u < 2; ++u) {
    const unsigned b = lds0 + 16384 + (16 * u + 4 * g + qr) * 288 + p * 8;
    A.z[u][0] = b + WF * 128; A.z[u][1] = b + WF * 128 + 32; A.s[u] = b + 256;
  }
  asm volatile("" : "+v"(A.w[0]), "+v"(A.w[1]), "+v"(A.s[0]), "+v"(A.s[1]), "+v"(A.z[0][0]), "+v"(A.z[0][1]), "+v"(A.z[1][0]), "+v"(A.z[1][1]));
  f32x4 acc[4][5];
  in_pass<WF, false>(lds0, wt, lane, A, acc, []() {});
  STAMP(2);
  u32x2 ua[4][4], ub[2];
  {
    f32x2 s1[4], s2[4];
#pragma unroll
    for (int m = 0; m < 4; ++m) {
      s1[m] = (f32x2){0.f, 0.f}; s2[m] = (f32x2){0.f, 0.f};
#pragma unroll
      for (int n = 0; n < 4; ++n) ua[m][n] = gelu_pack_sum(acc[m][n], s1[m], s2[m]);
      if ((m >> 1) == WF) ub[m & 1] = gelu_pack_sum(acc[m][4], s1[m], s2[m]);
    }
#pragma unroll
    for (int m = 0; m < 4; ++m) *(LAS f32x2*)(size_t)(lds0 + L_T + (64 * wt + 16 * m + r16) * 64 + (WF * 4 + g) * 8) = (f32x2){s1[m].x + s1[m].y, s2[m].x + s2[m].y};
  }
#pragma unroll
  for (int m = 0; m < 4; ++m) asm volatile("" : "+v"(ua[m][0]), "+v"(ua[m][1]), "+v"(ua[m][2]), "+v"(ua[m][3]));
  asm volatile("" : "+v"(ub[0]), "+v"(ub[1]));
  STAMP(3);
  in_pass<WF, true>(lds0, wt, lane, A, acc, [&]() {
    STAMP(4);
#pragma unroll
  for (int m = 0; m < 4; ++m) asm volatile("" : "+v"(ua[m][0]), "+v"(ua[m][1]), "+v"(ua[m][2]), "+v"(ua[m][3]));
  asm volatile("" : "+v"(ub[0]), "+v"(ub[1]));
  int lz = lane; asm volatile("" : "+v"(lz));
  {
    const int r16 = lz & 15, g = lz >> 4;
    auto rZ = RSRC(ws + WS_Z2, FRAG_BYTES);
    const unsigned sl = g * 16 + (r16 ^ (4 * (g >> 1)));
    const bool sw = g & 1;
#pragma unroll
    for (int m = 0; m < 4; ++m) {
      const f32x4 q4 = *(LAS f32x4*)(size_t)(lds0 + L_R + (64 * wt + 16 * m + r16) * 32) + *(LAS f32x4*)(size_t)(lds0 + L_R + (64 * wt + 16 * m + r16) * 32 + 16);
      const float mean = (q4[0] + q4[2]) * (1.0f / 2304.0f), var = fmaxf((q4[1] + q4[3]) * (1.0f / 2304.0f) - mean * mean, 0.f);
      const float rs = __builtin_amdgcn_rsqf(var + 1e-5f), sh = -mean * rs;
      auto norm = [&](unsigned v) { const f32x2 y = (f32x2){bflo(v), bfhi(v)} * rs + sh; return cvt_pk(y.x, y.y); };
      const unsigned dst = (fj * 128 + ti * 8 + wt * 4 + m) * CH;
#pragma unroll
      for (int pp = 0; pp < 2; ++pp) {
        const u32x2 a = sw ? ua[m][2 * pp + 1] : ua[m][2 * pp], b = sw ? ua[m][2 * pp] : ua[m][2 * pp + 1];
        const u32x4 v = {norm(a.x), norm(a.y), norm(b.x), norm(b.y)};
        __builtin_amdgcn_raw_buffer_store_b128(v, rZ, dst + sl * 16 + WF * 2048 + pp * 1024, 0, 16);
        ua[m][2 * pp] = (u32x2){v.x, v.y}; ua[m][2 * pp + 1] = (u32x2){v.z, v.w};
      }
      if ((m >> 1) == WF) {
        const u32x2 v = {norm(ub[m & 1].x), norm(ub[m & 1].y)};
        __builtin_amdgcn_raw_buffer_store_b64(v, rZ, dst + sl * 8 + 4096, 0, 16);
        ub[m & 1] = v;
      }
    }
  }
    STAMP(5);
  });
#pragma unroll
  for (int m = 0; m < 4; ++m) asm volatile("" : "+v"(acc[m][0]), "+v"(acc[m][1]), "+v"(acc[m][2]), "+v"(acc[m][3]), "+v"(acc[m][4]));
  const u32x2 tg = *(LAS u32x2*)(size_t)(lds0 + L_TAG);
  const u64 tag = ((u64)(unsigned)__builtin_amdgcn_readfirstlane(tg.y) << 32) | (unsigned)__builtin_amdgcn_readfirstlane(tg.x);
  {
    const unsigned ld = lds0 + wt * STG + 16384, sl = g * 16 + (r16 ^ (4 * (g >> 1)));
#pragma unroll
    for (int m = 0; m < 4; ++m) {
#pragma unroll
      for (int pp = 0; pp < 2; ++pp) *(LAS u32x4*)(size_t)(ld + m * CH + WF * 2048 + pp * 1024 + sl * 16) = (u32x4){ua[m][2 * pp].x, ua[m][2 * pp].y, ua[m][2 * pp + 1].x, ua[m][2 * pp + 1].y};
      if ((m >> 1) == WF) *(LAS u32x2*)(size_t)(ld + m * CH + 4096 + sl * 8) = ub[m & 1];
    }
  }
  STAMP(7);
  SB();
  u32x2 za[4][4], zb[2];
#pragma unroll
  for (int m = 0; m < 4; ++m) {
#pragma unroll
    for (int n = 0; n < 4; ++n) za[m][n] = gelu_pack(acc[m][n]);
    if ((m >> 1) == WF) zb[m & 1] = gelu_pack(acc[m][4]);
    asm volatile("" : "+v"(za[m][0]), "+v"(za[m][1]), "+v"(za[m][2]), "+v"(za[m][3]));
    if (m == FLAGM) {
      SB();
      asm volatile("s_waitcnt vmcnt(0) lgkmcnt(0)" ::: "memory");
      if (lane == 0) __hip_atomic_store((u64*)(ws + WS_F1 + (fj * 16 + ti) * 128) + wid, tag, RLX_AGENT);
      BAR();
      SB();
    }
  }
  asm volatile("" : "+v"(zb[0]), "+v"(zb[1]));
  STAMP(6);
#pragma unroll
  for (int u = 0; u < 2; ++u) {
    const unsigned sl = p * 16 + 8 * (g & 1) + 4 * (u ^ (p >> 1)) + qr;
#pragma unroll
    for (int h = 0; h < 2; ++h) A.z[u][h] = lds0 + 16384 + (g >> 1) * CH + WF * 2048 + sl * 16 + ((h ^ (p & 1)) * 8);
    A.s[u] = lds0 + 16384 + (g >> 1) * CH + 4096 + sl * 8;
  }
  asm volatile("" : "+v"(A.s[0]), "+v"(A.s[1]), "+v"(A.z[0][0]), "+v"(A.z[0][1]), "+v"(A.z[1][0]), "+v"(A.z[1][1]));
#pragma unroll
  for (int m = 0; m < 4; ++m) {
    const float b = __uint_as_float((unsigned)*(LAS u16*)(size_t)(lds0 + L_BIAS + (wt * 64 + m * 16 + r16) * 2) << 16);
#pragma unroll
    for (int n = 0; n < 5; ++n) { const float b0 = (n < 4 || (m >> 1) == WF) ? b : 0.f; acc[m][n] = (f32x4){b0, b0, b0, b0}; }
  }
  TFrag F;
  int dlt = 2 * STG;
  WAITL0();
  t_step<WF, 1024, 0, true, false>(A, dlt, acc, F);
  t_step<WF, 1024, STG, false, false>(A, dlt, acc, F);
  STAMP(8);
  for (int k = 0; k < 15; ++k) {
    dlt = -dlt;
    t_step<WF, 1024, 0, false>(A, dlt, acc, F);
    t_step<WF, 1024, STG, false>(A, dlt, acc, F);
  }
  t_finish<WF>(lds0, wt, lane, acc, F);
  STAMP(10);
  auto rP = RSRC(ws + WS_P, FRAG_BYTES);
  auto gate = [&](f32x4 a, u32x2 z) {
    const f32x2 x = (f32x2){a[0], a[1]} * (f32x2){bflo(z.x), bfhi(z.x)}, y = (f32x2){a[2], a[3]} * (f32x2){bflo(z.y), bfhi(z.y)};
    return (u32x2){cvt_pk(x.x, x.y), cvt_pk(y.x, y.y)};
  };
#pragma unroll
  for (int m = 0; m < 4; ++m) {
    const unsigned dst = ((ti * 8 + wt * 4 + m) * 16 + fj) * CH;
#pragma unroll
    for (int pp = 0; pp < 2; ++pp) {
      const u32x2 a = gate(acc[m][2 * pp], za[m][2 * pp]), b = gate(acc[m][2 * pp + 1], za[m][2 * pp + 1]);
      __builtin_amdgcn_raw_buffer_store_b128((u32x4){a.x, a.y, b.x, b.y}, rP, dst + lane * 16 + (2 * WF + pp) * 1024, 0, 16);
    }
    if ((m >> 1) == WF) __builtin_amdgcn_raw_buffer_store_b64(gate(acc[m][4], zb[m & 1]), rP, dst + lane * 8 + 4096, 0, 16);
  }
  STAMP(11);
  WV(0);
  if (lane == 0) __hip_atomic_store((u64*)(ws + WS_F2 + (ti * 16 + fj) * 128) + wid, tag, RLX_AGENT);
  BAR();
  STAMP(12);
}

DEV u64 poll16(const char* base, u64 tag, int lane) {
  const u64 v = __hip_atomic_load((const u64*)(base + (lane >> 2) * 128) + (lane & 3), RLX_AGENT);
  return __builtin_amdgcn_ballot_w64(v == tag);
}
DEV bool got(u64 mask, int who) { return ((unsigned)(mask >> (4 * who)) & 15u) == 15u; }

__global__ __launch_bounds__(512, 2) void gmlp_fused(const char* __restrict__ X, const char* __restrict__ Win, const char* __restrict__ Wsp, const u16* __restrict__ bias,
                                                     const char* __restrict__ Wo, char* __restrict__ out, char* __restrict__ ws) {
  extern __shared__ __attribute__((aligned(16))) char smem[];
  u64 pc0; asm volatile("s_getpc_b64 %0" : "=s"(pc0));
  const int tid = threadIdx.x, wid = __builtin_amdgcn_readfirstlane(tid >> 6), lane = tid & 63;
  const int bid = blockIdx.x, xg = bid & 7, rk = bid >> 3, il = rk & 7, jl = rk >> 3, ti = (xg & 1) * 8 + il, fj = (xg >> 1) * 4 + jl;
  const int L3 = xg * 30 + rk, tt = L3 / 9, dd = L3 - tt * 9;
  const bool has3 = rk < 30 && L3 < 234;
  const unsigned lds0 = (unsigned)(size_t)(LAS char*)smem;
  STAMP(0);
  if (__builtin_expect(wid >= 4, 1)) {
    __builtin_amdgcn_s_setprio(3);
    const int lw = wid - 4;
    auto rX = RSRC(X, 2048 * 1152); auto rWi = RSRC(Win, 576 * 9216);
    unsigned vX[4], vW[5];
    const unsigned zp0 = lw < 2 ? lw * 5 : 2 + lw * 4;
    const unsigned sX = (ti * 128 + lw * 32) * 1152, sW = fj * 288;
    const int rot = ROT1 == 0 ? 0 : ROT1 == 1 ? il + 2 * jl : 3 * ((il + jl) % 3);
    auto k1 = [&](int k) { int kk = k + rot; kk = kk >= 9 ? kk - 9 : kk; kk = kk >= 9 ? kk - 9 : kk; return kk; };
    auto issue1 = [&](int half, int k) {
      const int kk = k1(k);
      const unsigned d = lds0 + (k & 3) * STG, dz = d + 16384 + zp0 * 1024, sw = sW + half * 4608 + kk * 64 * 9216;
      GL4(rX, d + lw * 4096, vX[0], vX[1], vX[2], vX[3], sX + kk * 128);
      GL4(rWi, dz, vW[0], vW[1], vW[2], vW[3], sw);
      GL(rWi, lw < 2 ? dz + 4096 : lds0 + L_DUMMY, vW[4], sw, 0);
    };
    {
#pragma unroll
      for (int q = 0; q < 4; ++q) { const int row = 8 * q + (lane >> 3); vX[q] = row * 1152 + (((lane & 7) ^ ((row >> 1) & 7)) << 4) - q * 1024; }
      GL4(rX, lds0 + lw * 4096, vX[0], vX[1], vX[2], vX[3], sX + k1(0) * 128);
      SB();
#pragma unroll
      for (int q = 0; q < 5; ++q) {
        const unsigned c16 = (zp0 + q) * 64 + lane, R = (c16 * 3641u) >> 16, c = (c16 - R * 18) * 16, k = (R & 32) + 8 * ((R >> 2) & 3) + 4 * ((R >> 4) & 1) + (R & 3);
        vW[q] = k * 9216 + c - (q & 3) * 1024;
      }
      if (lw >= 2) vW[4] = OOB;
      const unsigned dz = lds0 + 16384 + zp0 * 1024, sw = sW + 4608 + k1(0) * 64 * 9216;
      GL4(rWi, dz, vW[0], vW[1], vW[2], vW[3], sw);
      GL(rWi, lw < 2 ? dz + 4096 : lds0 + L_DUMMY, vW[4], sw, 0);
    }
    const unsigned vL = lane * 16;
    auto rWs = RSRC(Wsp, 2048 * 4096); auto rWo = RSRC(Wo, 2304 * 1152);
    issue1(1, 1);
    WV(9); BAR();
    STAMP(1);
    issue1(1, 2);
    WV(9);
    for (int k = 1; k < 5; ++k) { BAR(); issue1(1, k + 2); WV(9); }
    BAR(); issue1(1, 7); issue1(1, 8); WV(18);
    BAR(); issue1(0, 1); WV(18);
    BAR(); issue1(0, 2); WV(18);
    BAR(); issue1(0, 3);
    const u64 ep = __hip_atomic_load((const u64*)(ws + WS_EP), RLX_AGENT);
    if (lw == 0) __builtin_amdgcn_raw_ptr_buffer_load_lds(RSRC(bias, 4096), (LAS void*)(size_t)(lds0 + L_BIAS), 4, lane * 4, ti * 256, 0, 0);
    BAR();
    issue1(0, 0);
#pragma unroll
    for (int q = 0; q < 8; ++q) __builtin_amdgcn_raw_ptr_buffer_load_lds(rWs, (LAS void*)(size_t)(lds0 + L_DUMMY), 16, vL, bid * 32768 + lw * 8192 + q * 1024, 0, WAUX);
    GL(rWo, lds0 + L_DUMMY, vL, bid * 10368 + lw * 2592, 0); GL(rWo, lds0 + L_DUMMY, vL, bid * 10368 + lw * 2592 + 1024, 0); GL(rWo, lds0 + L_DUMMY, vL, bid * 10368 + lw * 2592 + 1568, 0);
    const unsigned tagA = __builtin_amdgcn_readfirstlane((unsigned)ep) + 0x9E3779B9u, tagB = __builtin_amdgcn_readfirstlane((unsigned)(ep >> 32)) + 0x85EBCA6Bu;
    const u64 tag = ((u64)tagB << 32) | tagA;
    if (lw == 0) { const u32x2 tg = {tagA, tagB}; asm volatile("ds_write_b64 %0, %1\n\ts_waitcnt lgkmcnt(0)" :: "v"(lds0 + L_TAG), "v"(tg) : "memory"); }
    unsigned vS[4];
#pragma unroll
    for (int q = 0; q < 4; ++q) { const int row = 8 * q + (lane >> 3); vS[q] = row * 4096 + (((lane & 7) ^ ((row >> 1) & 7)) << 4) - q * 1024; }
    const unsigned sWs = (ti * 128 + lw * 32) * 4096;
    auto kpos = [&](int n) { return n < 2 ? 2 * ti + n : (n - 2 < 2 * ti ? n - 2 : n); };
    auto issueW = [&](int n) { GL4(rWs, lds0 + (n & 3) * STG + lw * 4096, vS[0], vS[1], vS[2], vS[3], sWs + kpos(n) * 128); };
    WV(11);
    BAR();
    STAMP(2);
    auto rS = RSRC(ws + WS_ST, 16 * 16 * 128 * 16);
    if (lane < 32) {
      const int row = lw * 32 + lane;
      f32x4 v0, v1, v2, v3;
      asm volatile("ds_read_b128 %0, %4\n\tds_read_b128 %1, %4 offset:16\n\tds_read_b128 %2, %4 offset:32\n\tds_read_b128 %3, %4 offset:48\n\ts_waitcnt lgkmcnt(0)"
                   : "=&v"(v0), "=&v"(v1), "=&v"(v2), "=&v"(v3) : "v"(lds0 + L_T + row * 64) : "memory");
      const f32x4 s = (v0 + v1) + (v2 + v3);
      u32x4 gq; gq.x = tagA; gq.y = __float_as_uint(s[0] + s[2]); gq.z = tagB; gq.w = __float_as_uint(s[1] + s[3]);
      __builtin_amdgcn_raw_buffer_store_b128(gq, rS, ((ti * 16 + fj) * 128 + row) * 16, 0, 16);
    }
    u32x4 sv[8];
    auto sweep = [&]() {
      MEMB();
#pragma unroll
      for (int p = 0; p < 8; ++p) sv[p] = __builtin_amdgcn_raw_buffer_load_b128(rS, lane * 16 + (p & 1) * 1024, (ti * 16 + lw * 4 + (p >> 1)) * 2048, 16);
      SB();
    };
    BAR(); issue1(0, 4);
    BAR(); issue1(0, 5);
    BAR(); issue1(0, 6); WV(18);
    BAR(); sweep(); issue1(0, 7); WV(9 + 8 + 9);
    BAR(); issue1(0, 8); WV(18);
    BAR();
    {
      f32x4 s;
      for (unsigned spins = 0;; ++spins) {
        bool ok = true;
        s = (f32x4){0.f, 0.f, 0.f, 0.f};
#pragma unroll
        for (int p = 0; p < 8; ++p) { ok &= (sv[p].x == tagA) & (sv[p].z == tagB); s[(p & 1) * 2] += __uint_as_float(sv[p].y); s[(p & 1) * 2 + 1] += __uint_as_float(sv[p].w); }
        if (__all(ok)) break;
        if (spins > SPIN_CAP) { s[0] = s[2] = __builtin_nanf(""); break; }
        sweep();
      }
      const f32x2 o0 = {s[0], s[1]}, o1 = {s[2], s[3]};
      asm volatile("ds_write_b64 %0, %1\n\tds_write_b64 %0, %2 offset:2048\n\ts_waitcnt lgkmcnt(0)" :: "v"(lds0 + L_R + lane * 32 + lw * 8), "v"(o0), "v"(o1) : "memory");
    }
    STAMP(3);
    issueW(1); WV(9 + 4);
    BAR(); WV(4);
    BAR();
    BAR();
    issueW(0);
    WV(0);
    BAR();
    auto rZ = RSRC(ws + WS_Z2, FRAG_BYTES);
    const unsigned sZ = fj * 128 * CH + zp0 * 1024, vZ5 = lw < 2 ? vL : OOB;
    u64 ready = 0;
    auto issueZ = [&](int n) {
      const int kp = kpos(n);
      if (__builtin_expect(!got(ready, kp >> 1), 0)) { for (unsigned spins = 0; !got(ready, kp >> 1) && spins < SPIN_CAP; ++spins) ready = poll16(ws + WS_F1 + fj * 16 * 128, tag, lane); }
      MEMB();
      const unsigned dz = lds0 + (n & 3) * STG + 16384 + zp0 * 1024, so = sZ + kp * 18432;
      GS4(rZ, dz, vL, so);
      GS(rZ, lw < 2 ? dz + 4096 : lds0 + L_DUMMY, vZ5, so + 4096, 0);
    };
    issueW(2); issueW(3);
    issueZ(2); issueZ(3);
    STAMP(4);
    WV(5);
    BAR();
    STAMP(5);
    for (int n = 4; n < 31; ++n) {
      issueW(n); issueZ(n);
      WV(9);
      BAR();
    }
    issueW(31); issueZ(31);
    constexpr int TAIL = 9;
    unsigned vO[2], vOh;
#pragma unroll
    for (int q = 0; q < 2; ++q) { const int row = 36 * lw + 8 * q + (lane >> 3); vO[q] = row * 1152 + (((lane & 7) ^ (((row >> 1) & 1) | (row & 4))) << 4); }
    { const int row = 36 * lw + 32 + ((lane >> 3) & 3); vOh = row * 1152 + (((lane & 7) ^ (((row >> 1) & 1) | (row & 4))) << 4); }
    const unsigned vO1 = vO[1] - 1024, vO2 = vO[0] + 16 * 1152 - 2048, vO3 = vO[1] + 16 * 1152 - 3072;
    const unsigned sO = min(dd, 8) * 128;
    auto issueO = [&](int n, int slot) {
      const unsigned d = lds0 + L_O + slot * STGO + lw * 4608, s0 = sO + n * 144 * 1152;
      GL4(rWo, d, vO[0], vO1, vO2, vO3, s0);
      if (lane < 32) GL(rWo, d + 4096, vOh, s0, 0);
    };
    issueO(0, 0);
    WV(9 + 5); BAR();
    issueO(1, 1); issueO(2, 2);
    WV(15); BAR();
    BAR(); BAR();
    if (!has3) { WV(0); return; }
    issueO(3, 3); issueO(4, 4);
    auto rP = RSRC(ws + WS_P, FRAG_BYTES);
    const unsigned vPs = lw < 3 ? (lane >> 5) * 16 * CH + (lane & 31) * 16 + 4096 : OOB;
    const int limS = lw == 2 ? 32 : 64;
    const unsigned sP = tt * 5 * 16 * CH;
    const int pa = (tt * 80) >> 7, pb = min((tt * 80 + 79) >> 7, 15);
    u64 rdy = 0;
    auto issueP = [&](int j, int slot) {
      if (__builtin_expect(!got(rdy, j), 0)) { for (unsigned spins = 0; !got(rdy, j) && spins < SPIN_CAP; ++spins) rdy = poll16(ws + WS_F2 + pa * 16 * 128, tag, lane) & poll16(ws + WS_F2 + pb * 16 * 128, tag, lane); }
      MEMB();
      const unsigned d = lds0 + L_P + slot * STGP, sp = sP + j * CH;
      GS4(rP, d + lw * 4096, vL, sp + lw * 16 * CH);
      GS(rP, d + 16384 + lw * 1024, vL, sp + 64 * CH + lw * 1024, 0);
      if (lane < limS) GS(rP, lw < 3 ? d + 20480 + lw * 1024 : lds0 + L_DUMMY, vPs, sp + lw * 32 * CH, 0);
    };
    issueP(0, 0);
    STAMP(6);
    issueP(1, 1); issueP(2, 2);
    WV(12);
    BAR();
    STAMP(7);
    WV(6);
    BAR(); issueP(3, 0); issueO(5, 0); WV(11);
    int sp3 = 1, so5 = 1;
    for (int n = 2; n < 12; ++n) {
      BAR();
      issueP(n + 2, sp3); issueO(n + 4, so5);
      sp3 = sp3 == 2 ? 0 : sp3 + 1; so5 = so5 == 4 ? 0 : so5 + 1;
      WV(16);
    }
    BAR(); issueP(14, 2); WV(6);
    BAR(); issueP(15, 0); WV(6);
    BAR(); WV(0);
    BAR();
    BAR(); BAR();
    STAMP(8);
    DUMP();
    if (bid == 0 && tid == 256) __hip_atomic_store((u64*)(ws + WS_EP), tag, RLX_AGENT);
  } else {
    const int wt = wid >> 1, wf = wid & 1;
    if (wid == 0) {
      auto rC = RSRC((const char*)(pc0 & ~(u64)255), CODE_KB * 1024);
      GL(rC, lds0 + L_DUMMY, lane * 16, rk * 1024, 0); GL(rC, lds0 + L_DUMMY, lane * 16, rk * 1024 + 32768, 0);
      WV(0);
    }
    if (wf == 0) front<0>(lds0, wid, wt, lane, ti, fj, ws); else front<1>(lds0, wid, wt, lane, ti, fj, ws);
    if (!has3) return;
    if (wt == 0) g3_compute<0>(lds0, wf, lane, out, tt, dd); else g3_compute<1>(lds0, wf, lane, out, tt, dd);
    STAMP(13);
    DUMP();
  }
}

extern "C" void kernel_launch(void* const* d_in, const int* in_sizes, int n_in, void* d_out, int out_size, void* d_ws, size_t ws_size, hipStream_t stream) {
  (void)hipFuncSetAttribute((const void*)gmlp_fused, hipFuncAttributeMaxDynamicSharedMemorySize, LDS_BYTES);
  gmlp_fused<<<256, 512, LDS_BYTES, stream>>>((const char*)d_in[0], (const char*)d_in[1], (const char*)d_in[2], (const u16*)d_in[3], (const char*)d_in[4], (char*)d_out, (char*)d_ws);
}
```

```cpp
#define PROBE 0
#include <hip/hip_runtime.h>
#include <hip/hip_bf16.h>
typedef unsigned short u16;
typedef unsigned long long u64;
typedef short s16x4 __attribute__((ext_vector_type(4)));
typedef short bf16x8 __attribute__((ext_vector_type(8)));
typedef float f32x2 __attribute__((ext_vector_type(2)));
typedef float f32x4 __attribute__((ext_vector_type(4)));
typedef unsigned u32x4 __attribute__((ext_vector_type(4)));
typedef unsigned u32x2 __attribute__((ext_vector_type(2)));
#define LAS __attribute__((address_space(3)))
#define DEV __device__ __forceinline__
#define BAR() __builtin_amdgcn_s_barrier()
#define SB() __builtin_amdgcn_sched_barrier(0)
#define MEMB() asm volatile("" ::: "memory")
#define WAITL0() __builtin_amdgcn_s_waitcnt(0xC07F)
#define WL0() asm volatile("s_waitcnt lgkmcnt(0)" ::: "memory")
#define WV(N) asm volatile("s_waitcnt vmcnt(%0)" ::"i"(N) : "memory")
#define MFMA(a, b, c) __builtin_amdgcn_mfma_f32_16x16x32_bf16(a, b, c, 0, 0, 0)
#define MFMA16(a, b, c) __builtin_amdgcn_mfma_f32_16x16x16bf16_1k(a, b, c, 0, 0, 0)
#define GL(R, D, V, S, I) __builtin_amdgcn_raw_ptr_buffer_load_lds(R, (LAS void*)(size_t)(D), 16, V, S, I, 0)
#define GL4(R, D, V0, V1, V2, V3, S) do { GL(R, D, V0, S, 0); GL(R, D, V1, S, 1024); GL(R, D, V2, S, 2048); GL(R, D, V3, S, 3072); } while (0)
#define GS(R, D, V, S, I) __builtin_amdgcn_raw_ptr_buffer_load_lds(R, (LAS void*)(size_t)(D), 16, V, S, I, 16)
#define GS4(R, D, V, S) do { GS(R, D, V, S, 0); GS(R, D, V, S, 1024); GS(R, D, V, S, 2048); GS(R, D, V, S, 3072); } while (0)
#define RSRC(p, n) __builtin_amdgcn_make_buffer_rsrc((void*)(p), 0, (n), 0x00020000)
#define RLX_AGENT __ATOMIC_RELAXED, __HIP_MEMORY_SCOPE_AGENT
#define ROT1 2
#define OAUX 16
#define WAUX 0
#define FLAGM 0
#define LA2 2
#define CODE_KB 48

constexpr int CH = 4608;
constexpr int FRAG_BYTES = 16 * 128 * CH;
constexpr size_t WS_Z2 = 0, WS_P = FRAG_BYTES, WS_ST = 2 * (size_t)FRAG_BYTES, WS_F1 = WS_ST + 16 * 16 * 128 * 16, WS_F2 = WS_F1 + 256 * 128, WS_EP = WS_F2 + 256 * 128;
constexpr int STG = 34816, STGP = 23040, STGO = 18432, L_O = 0, L_P = 5 * STGO, LDS_BYTES = 163840;
constexpr int L_T = 139264, L_R = L_T + 8192, L_X = 151552, L_BIAS = 159744, L_TAG = L_BIAS + 256, L_DUMMY = 161280;
constexpr unsigned SPIN_CAP = 1u << 16, OOB = 0x80000000u;
#define STAMP(I)
#define DUMP()

DEV unsigned cvt_pk(float lo, float hi) { unsigned r; asm("v_cvt_pk_bf16_f32 %0, %1, %2" : "=v"(r) : "v"(lo), "v"(hi)); return r; }
DEV bf16x8 ld128(unsigned a) { return *(LAS bf16x8*)(size_t)a; }
DEV s16x4 ld64(unsigned a) { return *(LAS s16x4*)(size_t)a; }
DEV s16x4 ldtr(unsigned a) { return __builtin_amdgcn_ds_read_tr16_b64_v4i16((LAS s16x4*)(size_t)a); }
DEV bf16x8 cat(s16x4 a, s16x4 b) { return __builtin_shufflevector(a, b, 0, 1, 2, 3, 4, 5, 6, 7); }
DEV float bflo(unsigned v) { return __uint_as_float(v << 16); }
DEV float bfhi(unsigned v) { return __uint_as_float(v & 0xffff0000u); }
DEV f32x2 gelu2(f32x2 x) {
  const float k1 = -2.0f * 0.7978845608028654f * 1.4426950408889634f, k2 = k1 * 0.044715f;
  f32x2 t = x * (x * x * k2 + k1), e;
  e.x = __builtin_amdgcn_exp2f(t.x); e.y = __builtin_amdgcn_exp2f(t.y);
  e = e + 1.0f;
  e.x = __builtin_amdgcn_rcpf(e.x); e.y = __builtin_amdgcn_rcpf(e.y);
  return x * e;
}
DEV u32x2 gelu_pack(f32x4 v) { const f32x2 a = gelu2((f32x2){v[0], v[1]}), b = gelu2((f32x2){v[2], v[3]}); return (u32x2){cvt_pk(a.x, a.y), cvt_pk(b.x, b.y)}; }
DEV u32x2 gelu_pack_sum(f32x4 v, f32x2& s1, f32x2& s2) {
  const f32x2 a = gelu2((f32x2){v[0], v[1]}), b = gelu2((f32x2){v[2], v[3]});
  s1 += a + b; s2 += a * a; s2 += b * b;
  return (u32x2){cvt_pk(a.x, a.y), cvt_pk(b.x, b.y)};
}

struct TAddr { unsigned w[2], z[2][2], s[2]; };
struct TFrag { bf16x8 wX[4], wY[4]; s16x4 tX[5][2], tY[4][2]; };
template <int WF, int PS, int OFF, bool FIRST, bool SYNC = true>
DEV void t_step(TAddr& A, int dlt, f32x4 (&acc)[4][5], TFrag& F) {
  constexpr int KX = WF, KY = 1 - WF;
  SB(); if (SYNC) BAR(); MEMB(); SB();
#pragma unroll
  for (int i = 0; i < 16; ++i) {
    const int n = i >> 2, m = i & 3;
    if (!FIRST) acc[m][n] = MFMA(cat(F.tY[n][0], F.tY[n][1]), F.wY[m], acc[m][n]);
    SB();
    if (i == 0) F.wX[0] = ld128(A.w[KX] + OFF);
    else if (i == 1) F.tX[0][0] = ldtr(A.z[0][0] + OFF + KX * 9216);
    else if (i == 2) F.tX[0][1] = ldtr(A.z[1][0] + OFF + KX * 9216);
    else if (i < 6) F.wX[i - 2] = ld128(A.w[KX] + OFF + (i - 2) * 2048);
    else if (i < 12) { const int nn = 1 + ((i - 6) >> 1), u = (i - 6) & 1; F.tX[nn][u] = ldtr(A.z[u][nn & 1] + OFF + KX * 9216 + (nn >> 1) * PS); }
    else if (i < 14) F.tX[4][i - 12] = ldtr(A.s[i - 12] + OFF + KX * 9216);
    SB();
  }
#pragma unroll
  for (int i = 0; i < 20; ++i) {
    const int n = i >> 2, m = i & 3;
    acc[m][n] = MFMA(cat(F.tX[n][0], F.tX[n][1]), F.wX[m], acc[m][n]);
    SB();
    if (i == 0) F.wY[0] = ld128(A.w[KY] + OFF);
    else if (i == 1) F.tY[0][0] = ldtr(A.z[0][0] + OFF + KY * 9216);
    else if (i == 2) F.tY[0][1] = ldtr(A.z[1][0] + OFF + KY * 9216);
    else if (i < 6) F.wY[i - 2] = ld128(A.w[KY] + OFF + (i - 2) * 2048);
    else if (i < 12) { const int nn = 1 + ((i - 6) >> 1), u = (i - 6) & 1; F.tY[nn][u] = ldtr(A.z[u][nn & 1] + OFF + KY * 9216 + (nn >> 1) * PS); }
    else if (OFF != 0 && i < 16) {
      const int u = i & 1;
      if (i < 14) { A.w[u] += dlt; A.s[u] += dlt; asm volatile("" : "+v"(A.w[u]), "+v"(A.s[u])); }
      else { A.z[u][0] += dlt; A.z[u][1] += dlt; asm volatile("" : "+v"(A.z[u][0]), "+v"(A.z[u][1])); }
    }
    SB();
  }
  WAITL0();
}
template <int WF>
DEV void t_finish(unsigned lds0, int wt, int lane, f32x4 (&acc)[4][5], TFrag& F) {
#pragma unroll
  for (int h = 0; h < 2; ++h) *(LAS f32x4*)(size_t)(lds0 + L_X + ((wt * 2 + WF) * 2 + h) * 1024 + lane * 16) = acc[2 * (1 - WF) + h][4];
#pragma unroll
  for (int n = 0; n < 4; ++n)
#pragma unroll
    for (int m = 0; m < 4; ++m) acc[m][n] = MFMA(cat(F.tY[n][0], F.tY[n][1]), F.wY[m], acc[m][n]);
  WL0();
  BAR(); MEMB();
#pragma unroll
  for (int h = 0; h < 2; ++h) acc[2 * WF + h][4] += *(LAS f32x4*)(size_t)(lds0 + L_X + ((wt * 2 + 1 - WF) * 2 + h) * 1024 + lane * 16);
}
template <int WF, bool MID, class Mid>
DEV void in_pass(unsigned lds0, int wt, int lane, TAddr& A, f32x4 (&acc)[4][5], Mid mid) {
  TFrag F;
#pragma unroll
  for (int m = 0; m < 4; ++m)
#pragma unroll
    for (int n = 0; n < 5; ++n) acc[m][n] = (f32x4){0, 0, 0, 0};
  int dlt = 2 * STG;
  WAITL0();
  t_step<WF, 64, 0, true>(A, dlt, acc, F);
  for (int k = 0; k < (MID ? 3 : 4); ++k) {
    t_step<WF, 64, STG, false>(A, dlt, acc, F);
    dlt = -dlt;
    t_step<WF, 64, 0, false>(A, dlt, acc, F);
  }
  if (MID) {
    t_step<WF, 64, STG, false>(A, -2 * STG, acc, F);
    SB(); mid(); SB();
    t_step<WF, 64, 0, false>(A, 0, acc, F);
  }
  t_finish<WF>(lds0, wt, lane, acc, F);
}

struct G3Frag { bf16x8 p[2][5]; s16x4 w[2][2][2], ws[2], ps[3]; };
template <int KH, bool MM>
DEV void g3_stage(f32x4 (&acc)[5][2], const G3Frag& c, G3Frag& n, unsigned aP, unsigned aS, unsigned a0, unsigned a1) {
  constexpr int M0 = KH ? 3 : 0, NS = KH ? 2 : 3, NM = 20 + 2 * NS, NR = 20 + NS;
#pragma unroll
  for (int i = 0; i < NM; ++i) {
    if (MM) {
      if (i >= 10 && i < 10 + 2 * NS) acc[M0 + ((i - 10) >> 1)][i & 1] = MFMA16(c.ws[i & 1], c.ps[(i - 10) >> 1], acc[M0 + ((i - 10) >> 1)][i & 1]);
      else { const int ii = i < 10 ? i : i - 2 * NS, P = ii / 10, m = (ii % 10) >> 1, cc = ii & 1; acc[m][cc] = MFMA(cat(c.w[P][cc][0], c.w[P][cc][1]), c.p[P][m], acc[m][cc]); }
    }
    SB();
#pragma unroll
    for (int r = (i * 3) / 2; r < ((i + 1) * 3) / 2; ++r) {
      if (r < 18) { const int P = r / 9, q = r % 9;
        if (q < 4) n.w[P][q >> 1][q & 1] = ldtr(((q >> 1) ? a1 : a0) + (2 * KH + P) * 4096 + (q & 1) * 2048);
        else n.p[P][q - 4] = ld128(aP + P * 1024 + (q - 4) * 4096); }
      else if (r < 20) n.ws[r - 18] = ldtr(((r - 18) ? a1 : a0) + 16384);
      else if (r < NR) n.ps[r - 20] = ld64(aS + (M0 + r - 20) * 512);
    }
    SB();
  }
}
template <int KH>
DEV void g3_compute(unsigned lds0, int dh, int lane, char* out, int tt, int dd) {
  f32x4 acc[5][2];
#pragma unroll
  for (int m = 0; m < 5; ++m) { acc[m][0] = (f32x4){0, 0, 0, 0}; acc[m][1] = (f32x4){0, 0, 0, 0}; }
  const int g = lane >> 4, qr = (lane >> 2) & 3, p = lane & 3;
  const int rr = 4 * g + qr;
  unsigned bW[2];
#pragma unroll
  for (int c = 0; c < 2; ++c) bW[c] = lds0 + L_O + rr * 128 + (((4 * dh + p) ^ (((rr >> 1) & 1) | (rr & 4))) << 4) + ((c ^ (p & 1)) * 8);
  const unsigned bP = lds0 + L_P + lane * 16 + KH * 2048, bS = lds0 + L_P + 20480 + lane * 8;
  G3Frag f0, f1;
  WAITL0();
  SB(); BAR(); MEMB(); SB();
  g3_stage<KH, false>(acc, f1, f0, bP, bS, bW[0], bW[1]);
  WAITL0();
  unsigned sp = STGP, sw = STGO;
  for (int j = 1; j < 16; j += 2) {
    SB(); BAR(); MEMB(); SB();
    g3_stage<KH, true>(acc, f0, f1, bP + sp, bS + sp, bW[0] + sw, bW[1] + sw);
    sp = sp == 2 * STGP ? 0 : sp + STGP; sw = sw == 4 * STGO ? 0 : sw + STGO;
    WAITL0();
    SB(); BAR(); MEMB(); SB();
    g3_stage<KH, true>(acc, f1, f0, bP + sp, bS + sp, bW[0] + sw, bW[1] + sw);
    sp = sp == 2 * STGP ? 0 : sp + STGP; sw = sw == 4 * STGO ? 0 : sw + STGO;
    WAITL0();
  }
#pragma unroll
  for (int m = 0; m < 5; ++m)
    if ((m >= 3) != KH) { *(LAS f32x4*)(size_t)(lds0 + (dh * 10 + m * 2) * 1024 + lane * 16) = acc[m][0]; *(LAS f32x4*)(size_t)(lds0 + (dh * 10 + m * 2 + 1) * 1024 + lane * 16) = acc[m][1]; }
  WL0();
  BAR(); MEMB();
  auto rO = RSRC(out, 2048 * 1152);
  const bool sw2 = g & 1;
#pragma unroll
  for (int m = 0; m < 5; ++m)
    if ((m >= 3) == KH) {
      const f32x4 a = acc[m][0] + *(LAS f32x4*)(size_t)(lds0 + (dh * 10 + m * 2) * 1024 + lane * 16), b = acc[m][1] + *(LAS f32x4*)(size_t)(lds0 + (dh * 10 + m * 2 + 1) * 1024 + lane * 16);
      const unsigned a0 = cvt_pk(a[0], a[1]), a1 = cvt_pk(a[2], a[3]), b0 = cvt_pk(b[0], b[1]), b1 = cvt_pk(b[2], b[3]);
      const u32x4 v = {sw2 ? b0 : a0, sw2 ? b1 : a1, sw2 ? a0 : b0, sw2 ? a1 : b1};
      __builtin_amdgcn_raw_buffer_store_b128(v, rO, (tt * 80 + m * 16 + (lane & 15)) * 1152 + dd * 128 + dh * 64 + g * 16, 0, OAUX);
    }
}

template <int WF>
DEV void front(unsigned lds0, int wid, int wt, int lane, int ti, int fj, char* ws) {
  const int r16 = lane & 15, g = lane >> 4, qr = (lane >> 2) & 3, p = lane & 3;
  TAddr A;
  A.w[0] = lds0 + (64 * wt + r16) * 128 + ((g ^ (r16 >> 1)) << 4); A.w[1] = A.w[0] ^ 64;
#pragma unroll
  for (int u = 0; u < 2; ++u) {
    const unsigned b = lds0 + 16384 + (16 * u + 4 * g + qr) * 288 + p * 8;
    A.z[u][0] = b + WF * 128; A.z[u][1] = b + WF * 128 + 32; A.s[u] = b + 256;
  }
  asm volatile("" : "+v"(A.w[0]), "+v"(A.w[1]), "+v"(A.s[0]), "+v"(A.s[1]), "+v"(A.z[0][0]), "+v"(A.z[0][1]), "+v"(A.z[1][0]), "+v"(A.z[1][1]));
  f32x4 acc[4][5];
  in_pass<WF, false>(lds0, wt, lane, A, acc, []() {});
  STAMP(2);
  u32x2 ua[4][4], ub[2];
  {
    f32x2 s1[4], s2[4];
#pragma unroll
    for (int m = 0; m < 4; ++m) {
      s1[m] = (f32x2){0.f, 0.f}; s2[m] = (f32x2){0.f, 0.f};
#pragma unroll
      for (int n = 0; n < 4; ++n) ua[m][n] = gelu_pack_sum(acc[m][n], s1[m], s2[m]);
      if ((m >> 1) == WF) ub[m & 1] = gelu_pack_sum(acc[m][4], s1[m], s2[m]);
    }
#pragma unroll
    for (int m = 0; m < 4; ++m) *(LAS f32x2*)(size_t)(lds0 + L_T + (64 * wt + 16 * m + r16) * 64 + (WF * 4 + g) * 8) = (f32x2){s1[m].x + s1[m].y, s2[m].x + s2[m].y};
  }
#pragma unroll
  for (int m = 0; m < 4; ++m) asm volatile("" : "+v"(ua[m][0]), "+v"(ua[m][1]), "+v"(ua[m][2]), "+v"(ua[m][3]));
  asm volatile("" : "+v"(ub[0]), "+v"(ub[1]));
  STAMP(3);
  in_pass<WF, true>(lds0, wt, lane, A, acc, [&]() {
    STAMP(4);
#pragma unroll
  for (int m = 0; m < 4; ++m) asm volatile("" : "+v"(ua[m][0]), "+v"(ua[m][1]), "+v"(ua[m][2]), "+v"(ua[m][3]));
  asm volatile("" : "+v"(ub[0]), "+v"(ub[1]));
  int lz = lane; asm volatile("" : "+v"(lz));
  {
    const int r16 = lz & 15, g = lz >> 4;
    auto rZ = RSRC(ws + WS_Z2, FRAG_BYTES);
    const unsigned sl = g * 16 + (r16 ^ (4 * (g >> 1)));
    const bool sw = g & 1;
#pragma unroll
    for (int m = 0; m < 4; ++m) {
      const f32x4 q4 = *(LAS f32x4*)(size_t)(lds0 + L_R + (64 * wt + 16 * m + r16) * 32) + *(LAS f32x4*)(size_t)(lds0 + L_R + (64 * wt + 16 * m + r16) * 32 + 16);
      const float mean = (q4[0] + q4[2]) * (1.0f / 2304.0f), var = fmaxf((q4[1] + q4[3]) * (1.0f / 2304.0f) - mean * mean, 0.f);
      const float rs = __builtin_amdgcn_rsqf(var + 1e-5f), sh = -mean * rs;
      auto norm = [&](unsigned v) { const f32x2 y = (f32x2){bflo(v), bfhi(v)} * rs + sh; return cvt_pk(y.x, y.y); };
      const unsigned dst = (fj * 128 + ti * 8 + wt * 4 + m) * CH;
#pragma unroll
      for (int pp = 0; pp < 2; ++pp) {
        const u32x2 a = sw ? ua[m][2 * pp + 1] : ua[m][2 * pp], b = sw ? ua[m][2 * pp] : ua[m][2 * pp + 1];
        const u32x4 v = {norm(a.x), norm(a.y), norm(b.x), norm(b.y)};
        __builtin_amdgcn_raw_buffer_store_b128(v, rZ, dst + sl * 16 + WF * 2048 + pp * 1024, 0, 16);
        ua[m][2 * pp] = (u32x2){v.x, v.y}; ua[m][2 * pp + 1] = (u32x2){v.z, v.w};
      }
      if ((m >> 1) == WF) {
        const u32x2 v = {norm(ub[m & 1].x), norm(ub[m & 1].y)};
        __builtin_amdgcn_raw_buffer_store_b64(v, rZ, dst + sl * 8 + 4096, 0, 16);
        ub[m & 1] = v;
      }
    }
  }
    STAMP(5);
  });
#pragma unroll
  for (int m = 0; m < 4; ++m) asm volatile("" : "+v"(acc[m][0]), "+v"(acc[m][1]), "+v"(acc[m][2]), "+v"(acc[m][3]), "+v"(acc[m][4]));
  const u32x2 tg = *(LAS u32x2*)(size_t)(lds0 + L_TAG);
  const u64 tag = ((u64)(unsigned)__builtin_amdgcn_readfirstlane(tg.y) << 32) | (unsigned)__builtin_amdgcn_readfirstlane(tg.x);
  {
    const unsigned ld = lds0 + wt * STG + 16384, sl = g * 16 + (r16 ^ (4 * (g >> 1)));
#pragma unroll
    for (int m = 0; m < 4; ++m) {
#pragma unroll
      for (int pp = 0; pp < 2; ++pp) *(LAS u32x4*)(size_t)(ld + m * CH + WF * 2048 + pp * 1024 + sl * 16) = (u32x4){ua[m][2 * pp].x, ua[m][2 * pp].y, ua[m][2 * pp + 1].x, ua[m][2 * pp + 1].y};
      if ((m >> 1) == WF) *(LAS u32x2*)(size_t)(ld + m * CH + 4096 + sl * 8) = ub[m & 1];
    }
  }
  STAMP(7);
  SB();
  u32x2 za[4][4], zb[2];
#pragma unroll
  for (int m = 0; m < 4; ++m) {
#pragma unroll
    for (int n = 0; n < 4; ++n) za[m][n] = gelu_pack(acc[m][n]);
    if ((m >> 1) == WF) zb[m & 1] = gelu_pack(acc[m][4]);
    asm volatile("" : "+v"(za[m][0]), "+v"(za[m][1]), "+v"(za[m][2]), "+v"(za[m][3]));
    if (m == FLAGM) {
      SB();
      asm volatile("s_waitcnt vmcnt(0) lgkmcnt(0)" ::: "memory");
      if (lane == 0) __hip_atomic_store((u64*)(ws + WS_F1 + (fj * 16 + ti) * 128) + wid, tag, RLX_AGENT);
      BAR();
      SB();
    }
  }
  asm volatile("" : "+v"(zb[0]), "+v"(zb[1]));
  STAMP(6);
#pragma unroll
  for (int u = 0; u < 2; ++u) {
    const unsigned sl = p * 16 + 8 * (g & 1) + 4 * (u ^ (p >> 1)) + qr;
#pragma unroll
    for (int h = 0; h < 2; ++h) A.z[u][h] = lds0 + 16384 + (g >> 1) * CH + WF * 2048 + sl * 16 + ((h ^ (p & 1)) * 8);
    A.s[u] = lds0 + 16384 + (g >> 1) * CH + 4096 + sl * 8;
  }
  asm volatile("" : "+v"(A.s[0]), "+v"(A.s[1]), "+v"(A.z[0][0]), "+v"(A.z[0][1]), "+v"(A.z[1][0]), "+v"(A.z[1][1]));
#pragma unroll
  for (int m = 0; m < 4; ++m) {
    const float b = __uint_as_float((unsigned)*(LAS u16*)(size_t)(lds0 + L_BIAS + (wt * 64 + m * 16 + r16) * 2) << 16);
#pragma unroll
    for (int n = 0; n < 5; ++n) { const float b0 = (n < 4 || (m >> 1) == WF) ? b : 0.f; acc[m][n] = (f32x4){b0, b0, b0, b0}; }
  }
  TFrag F;
  int dlt = 2 * STG;
  WAITL0();
  t_step<WF, 1024, 0, true, false>(A, dlt, acc, F);
  t_step<WF, 1024, STG, false, false>(A, dlt, acc, F);
  STAMP(8);
  for (int k = 0; k < 15; ++k) {
    dlt = -dlt;
    t_step<WF, 1024, 0, false>(A, dlt, acc, F);
    t_step<WF, 1024, STG, false>(A, dlt, acc, F);
  }
  t_finish<WF>(lds0, wt, lane, acc, F);
  STAMP(10);
  auto rP = RSRC(ws + WS_P, FRAG_BYTES);
  auto gate = [&](f32x4 a, u32x2 z) {
    const f32x2 x = (f32x2){a[0], a[1]} * (f32x2){bflo(z.x), bfhi(z.x)}, y = (f32x2){a[2], a[3]} * (f32x2){bflo(z.y), bfhi(z.y)};
    return (u32x2){cvt_pk(x.x, x.y), cvt_pk(y.x, y.y)};
  };
#pragma unroll
  for (int m = 0; m < 4; ++m) {
    const unsigned dst = ((ti * 8 + wt * 4 + m) * 16 + fj) * CH;
#pragma unroll
    for (int pp = 0; pp < 2; ++pp) {
      const u32x2 a = gate(acc[m][2 * pp], za[m][2 * pp]), b = gate(acc[m][2 * pp + 1], za[m][2 * pp + 1]);
      __builtin_amdgcn_raw_buffer_store_b128((u32x4){a.x, a.y, b.x, b.y}, rP, dst + lane * 16 + (2 * WF + pp) * 1024, 0, 16);
    }
    if ((m >> 1) == WF) __builtin_amdgcn_raw_buffer_store_b64(gate(acc[m][4], zb[m & 1]), rP, dst + lane * 8 + 4096, 0, 16);
  }
  STAMP(11);
  WV(0);
  if (lane == 0) __hip_atomic_store((u64*)(ws + WS_F2 + (ti * 16 + fj) * 128) + wid, tag, RLX_AGENT);
  BAR();
  STAMP(12);
}

DEV u64 poll16(const char* base, u64 tag, int lane) {
  const u64 v = __hip_atomic_load((const u64*)(base + (lane >> 2) * 128) + (lane & 3), RLX_AGENT);
  return __builtin_amdgcn_ballot_w64(v == tag);
}
DEV bool got(u64 mask, int who) { return ((unsigned)(mask >> (4 * who)) & 15u) == 15u; }

__global__ __launch_bounds__(512, 2) void gmlp_fused(const char* __restrict__ X, const char* __restrict__ Win, const char* __restrict__ Wsp, const u16* __restrict__ bias,
                                                     const char* __restrict__ Wo, char* __restrict__ out, char* __restrict__ ws) {
  extern __shared__ __attribute__((aligned(16))) char smem[];
  u64 pc0; asm volatile("s_getpc_b64 %0" : "=s"(pc0));
  const int tid = threadIdx.x, wid = __builtin_amdgcn_readfirstlane(tid >> 6), lane = tid & 63;
  const int bid = blockIdx.x, xg = bid & 7, rk = bid >> 3, il = rk & 7, jl = rk >> 3, ti = (xg & 1) * 8 + il, fj = (xg >> 1) * 4 + jl;
  const int L3 = xg * 30 + rk, tt = L3 / 9, dd = L3 - tt * 9;
  const bool has3 = rk < 30 && L3 < 234;
  const unsigned lds0 = (unsigned)(size_t)(LAS char*)smem;
  STAMP(0);
  if (__builtin_expect(wid >= 4, 1)) {
    __builtin_amdgcn_s_setprio(3);
    const u64 rt0 = __builtin_amdgcn_s_memrealtime(); u64 rt1 = rt0;
#define MARK(I) do { if (PROBE == (I)) { rt1 = __builtin_amdgcn_s_memrealtime(); WAITL0(); } } while (0)
    const int lw = wid - 4;
    auto rX = RSRC(X, 2048 * 1152); auto rWi = RSRC(Win, 576 * 9216);
    unsigned vX[4], vW[5];
    const unsigned zp0 = lw < 2 ? lw * 5 : 2 + lw * 4;
    const unsigned sX = (ti * 128 + lw * 32) * 1152, sW = fj * 288;
    const int rot = ROT1 == 0 ? 0 : ROT1 == 1 ? il + 2 * jl : 3 * ((il + jl) % 3);
    auto k1 = [&](int k) { int kk = k + rot; kk = kk >= 9 ? kk - 9 : kk; kk = kk >= 9 ? kk - 9 : kk; return kk; };
    auto issue1 = [&](int half, int k) {
      const int kk = k1(k);
      const unsigned d = lds0 + (k & 3) * STG, dz = d + 16384 + zp0 * 1024, sw = sW + half * 4608 + kk * 64 * 9216;
      GL4(rX, d + lw * 4096, vX[0], vX[1], vX[2], vX[3], sX + kk * 128);
      GL4(rWi, dz, vW[0], vW[1], vW[2], vW[3], sw);
      GL(rWi, lw < 2 ? dz + 4096 : lds0 + L_DUMMY, vW[4], sw, 0);
    };
    {
#pragma unroll
      for (int q = 0; q < 4; ++q) { const int row = 8 * q + (lane >> 3); vX[q] = row * 1152 + (((lane & 7) ^ ((row >> 1) & 7)) << 4) - q * 1024; }
      GL4(rX, lds0 + lw * 4096, vX[0], vX[1], vX[2], vX[3], sX + k1(0) * 128);
      SB();
#pragma unroll
      for (int q = 0; q < 5; ++q) {
        const unsigned c16 = (zp0 + q) * 64 + lane, R = (c16 * 3641u) >> 16, c = (c16 - R * 18) * 16, k = (R & 32) + 8 * ((R >> 2) & 3) + 4 * ((R >> 4) & 1) + (R & 3);
        vW[q] = k * 9216 + c - (q & 3) * 1024;
      }
      if (lw >= 2) vW[4] = OOB;
      const unsigned dz = lds0 + 16384 + zp0 * 1024, sw = sW + 4608 + k1(0) * 64 * 9216;
      GL4(rWi, dz, vW[0], vW[1], vW[2], vW[3], sw);
      GL(rWi, lw < 2 ? dz + 4096 : lds0 + L_DUMMY, vW[4], sw, 0);
    }
    const unsigned vL = lane * 16;
    auto rWs = RSRC(Wsp, 2048 * 4096); auto rWo = RSRC(Wo, 2304 * 1152);
    issue1(1, 1);
    WV(9); BAR();
    MARK(1);
    STAMP(1);
    issue1(1, 2);
    WV(9);
    for (int k = 1; k < 5; ++k) { BAR(); issue1(1, k + 2); WV(9); }
    BAR(); issue1(1, 7); issue1(1, 8); WV(18);
    BAR(); issue1(0, 1); WV(18);
    BAR(); issue1(0, 2); WV(18);
    BAR(); issue1(0, 3);
    const u64 ep = __hip_atomic_load((const u64*)(ws + WS_EP), RLX_AGENT);
    if (lw == 0) __builtin_amdgcn_raw_ptr_buffer_load_lds(RSRC(bias, 4096), (LAS void*)(size_t)(lds0 + L_BIAS), 4, lane * 4, ti * 256, 0, 0);
    BAR();
    issue1(0, 0);
#pragma unroll
    for (int q = 0; q < 8; ++q) __builtin_amdgcn_raw_ptr_buffer_load_lds(rWs, (LAS void*)(size_t)(lds0 + L_DUMMY), 16, vL, bid * 32768 + lw * 8192 + q * 1024, 0, WAUX);
    GL(rWo, lds0 + L_DUMMY, vL, bid * 10368 + lw * 2592, 0); GL(rWo, lds0 + L_DUMMY, vL, bid * 10368 + lw * 2592 + 1024, 0); GL(rWo, lds0 + L_DUMMY, vL, bid * 10368 + lw * 2592 + 1568, 0);
    const unsigned tagA = __builtin_amdgcn_readfirstlane((unsigned)ep) + 0x9E3779B9u, tagB = __builtin_amdgcn_readfirstlane((unsigned)(ep >> 32)) + 0x85EBCA6Bu;
    const u64 tag = ((u64)tagB << 32) | tagA;
    if (lw == 0) { const u32x2 tg = {tagA, tagB}; asm volatile("ds_write_b64 %0, %1\n\ts_waitcnt lgkmcnt(0)" :: "v"(lds0 + L_TAG), "v"(tg) : "memory"); }
    unsigned vS[4];
#pragma unroll
    for (int q = 0; q < 4; ++q) { const int row = 8 * q + (lane >> 3); vS[q] = row * 4096 + (((lane & 7) ^ ((row >> 1) & 7)) << 4) - q * 1024; }
    const unsigned sWs = (ti * 128 + lw * 32) * 4096;
    auto kpos = [&](int n) { return n < 2 ? 2 * ti + n : (n - 2 < 2 * ti ? n - 2 : n); };
    auto issueW = [&](int n) { GL4(rWs, lds0 + (n & 3) * STG + lw * 4096, vS[0], vS[1], vS[2], vS[3], sWs + kpos(n) * 128); };
    WV(11);
    BAR();
    STAMP(2);
    auto rS = RSRC(ws + WS_ST, 16 * 16 * 128 * 16);
    if (lane < 32) {
      const int row = lw * 32 + lane;
      f32x4 v0, v1, v2, v3;
      asm volatile("ds_read_b128 %0, %4\n\tds_read_b128 %1, %4 offset:16\n\tds_read_b128 %2, %4 offset:32\n\tds_read_b128 %3, %4 offset:48\n\ts_waitcnt lgkmcnt(0)"
                   : "=&v"(v0), "=&v"(v1), "=&v"(v2), "=&v"(v3) : "v"(lds0 + L_T + row * 64) : "memory");
      const f32x4 s = (v0 + v1) + (v2 + v3);
      u32x4 gq; gq.x = tagA; gq.y = __float_as_uint(s[0] + s[2]); gq.z = tagB; gq.w = __float_as_uint(s[1] + s[3]);
      __builtin_amdgcn_raw_buffer_store_b128(gq, rS, ((ti * 16 + fj) * 128 + row) * 16, 0, 16);
    }
    u32x4 sv[8];
    auto sweep = [&]() {
      MEMB();
#pragma unroll
      for (int p = 0; p < 8; ++p) sv[p] = __builtin_amdgcn_raw_buffer_load_b128(rS, lane * 16 + (p & 1) * 1024, (ti * 16 + lw * 4 + (p >> 1)) * 2048, 16);
      SB();
    };
    BAR(); issue1(0, 4);
    BAR(); issue1(0, 5);
    BAR(); issue1(0, 6); WV(18);
    BAR(); sweep(); issue1(0, 7); WV(9 + 8 + 9);
    BAR(); issue1(0, 8); WV(18);
    BAR();
    {
      f32x4 s;
      for (unsigned spins = 0;; ++spins) {
        bool ok = true;
        s = (f32x4){0.f, 0.f, 0.f, 0.f};
#pragma unroll
        for (int p = 0; p < 8; ++p) { ok &= (sv[p].x == tagA) & (sv[p].z == tagB); s[(p & 1) * 2] += __uint_as_float(sv[p].y); s[(p & 1) * 2 + 1] += __uint_as_float(sv[p].w); }
        if (__all(ok)) break;
        if (spins > SPIN_CAP) { s[0] = s[2] = __builtin_nanf(""); break; }
        sweep();
      }
      const f32x2 o0 = {s[0], s[1]}, o1 = {s[2], s[3]};
      asm volatile("ds_write_b64 %0, %1\n\tds_write_b64 %0, %2 offset:2048\n\ts_waitcnt lgkmcnt(0)" :: "v"(lds0 + L_R + lane * 32 + lw * 8), "v"(o0), "v"(o1) : "memory");
    }
    STAMP(3);
    issueW(1); WV(9 + 4);
    BAR(); WV(4);
    BAR();
    BAR();
    MARK(2);
    issueW(0);
    WV(0);
    BAR();
    auto rZ = RSRC(ws + WS_Z2, FRAG_BYTES);
    const unsigned sZ = fj * 128 * CH + zp0 * 1024, vZ5 = lw < 2 ? vL : OOB;
    u64 ready = 0;
    auto issueZ = [&](int n) {
      const int kp = kpos(n);
      if (__builtin_expect(!got(ready, kp >> 1), 0)) { for (unsigned spins = 0; !got(ready, kp >> 1) && spins < SPIN_CAP; ++spins) ready = poll16(ws + WS_F1 + fj * 16 * 128, tag, lane); }
      MEMB();
      const unsigned dz = lds0 + (n & 3) * STG + 16384 + zp0 * 1024, so = sZ + kp * 18432;
      GS4(rZ, dz, vL, so);
      GS(rZ, lw < 2 ? dz + 4096 : lds0 + L_DUMMY, vZ5, so + 4096, 0);
    };
    issueW(2); issueW(3);
    issueZ(2); issueZ(3);
    STAMP(4);
    WV(5);
    BAR();
    STAMP(5);
    for (int n = 4; n < 31; ++n) {
      issueW(n); issueZ(n);
      WV(9);
      BAR();
    }
    issueW(31); issueZ(31);
    constexpr int TAIL = 9;
    unsigned vO[2], vOh;
#pragma unroll
    for (int q = 0; q < 2; ++q) { const int row = 36 * lw + 8 * q + (lane >> 3); vO[q] = row * 1152 + (((lane & 7) ^ (((row >> 1) & 1) | (row & 4))) << 4); }
    { const int row = 36 * lw + 32 + ((lane >> 3) & 3); vOh = row * 1152 + (((lane & 7) ^ (((row >> 1) & 1) | (row & 4))) << 4); }
    const unsigned vO1 = vO[1] - 1024, vO2 = vO[0] + 16 * 1152 - 2048, vO3 = vO[1] + 16 * 1152 - 3072;
    const unsigned sO = min(dd, 8) * 128;
    auto issueO = [&](int n, int slot) {
      const unsigned d = lds0 + L_O + slot * STGO + lw * 4608, s0 = sO + n * 144 * 1152;
      GL4(rWo, d, vO[0], vO1, vO2, vO3, s0);
      if (lane < 32) GL(rWo, d + 4096, vOh, s0, 0);
    };
    issueO(0, 0);
    WV(9 + 5); BAR();
    issueO(1, 1); issueO(2, 2);
    WV(15); BAR();
    BAR(); MARK(3); BAR();
    if (!has3) { WV(0); return; }
    issueO(3, 3); issueO(4, 4);
    auto rP = RSRC(ws + WS_P, FRAG_BYTES);
    const unsigned vPs = lw < 3 ? (lane >> 5) * 16 * CH + (lane & 31) * 16 + 4096 : OOB;
    const int limS = lw == 2 ? 32 : 64;
    const unsigned sP = tt * 5 * 16 * CH;
    const int pa = (tt * 80) >> 7, pb = min((tt * 80 + 79) >> 7, 15);
    u64 rdy = 0;
    auto issueP = [&](int j, int slot) {
      if (__builtin_expect(!got(rdy, j), 0)) { for (unsigned spins = 0; !got(rdy, j) && spins < SPIN_CAP; ++spins) rdy = poll16(ws + WS_F2 + pa * 16 * 128, tag, lane) & poll16(ws + WS_F2 + pb * 16 * 128, tag, lane); }
      MEMB();
      const unsigned d = lds0 + L_P + slot * STGP, sp = sP + j * CH;
      GS4(rP, d + lw * 4096, vL, sp + lw * 16 * CH);
      GS(rP, d + 16384 + lw * 1024, vL, sp + 64 * CH + lw * 1024, 0);
      if (lane < limS) GS(rP, lw < 3 ? d + 20480 + lw * 1024 : lds0 + L_DUMMY, vPs, sp + lw * 32 * CH, 0);
    };
    issueP(0, 0);
    STAMP(6);
    issueP(1, 1); issueP(2, 2);
    WV(12);
    BAR();
    MARK(4);
    STAMP(7);
    WV(6);
    BAR(); issueP(3, 0); issueO(5, 0); WV(11);
    int sp3 = 1, so5 = 1;
    for (int n = 2; n < 12; ++n) {
      BAR();
      issueP(n + 2, sp3); issueO(n + 4, so5);
      sp3 = sp3 == 2 ? 0 : sp3 + 1; so5 = so5 == 4 ? 0 : so5 + 1;
      WV(16);
    }
    BAR(); issueP(14, 2); WV(6);
    BAR(); issueP(15, 0); WV(6);
    BAR(); WV(0);
    BAR();
    BAR(); BAR();
    MARK(5);
    { const u64 e = __builtin_amdgcn_s_memrealtime() + 2 * (rt1 - rt0); while (__builtin_amdgcn_s_memrealtime() < e) __builtin_amdgcn_s_sleep(4); }
    STAMP(8);
    DUMP();
    if (bid == 0 && tid == 256) __hip_atomic_store((u64*)(ws + WS_EP), tag, RLX_AGENT);
  } else {
    const int wt = wid >> 1, wf = wid & 1;
    if (wid == 0) {
      auto rC = RSRC((const char*)(pc0 & ~(u64)255), CODE_KB * 1024);
      GL(rC, lds0 + L_DUMMY, lane * 16, rk * 1024, 0); GL(rC, lds0 + L_DUMMY, lane * 16, rk * 1024 + 32768, 0);
      WV(0);
    }
    if (wf == 0) front<0>(lds0, wid, wt, lane, ti, fj, ws); else front<1>(lds0, wid, wt, lane, ti, fj, ws);
    if (!has3) return;
    if (wt == 0) g3_compute<0>(lds0, wf, lane, out, tt, dd); else g3_compute<1>(lds0, wf, lane, out, tt, dd);
    STAMP(13);
    DUMP();
  }
}

extern "C" void kernel_launch(void* const* d_in, const int* in_sizes, int n_in, void* d_out, int out_size, void* d_ws, size_t ws_size, hipStream_t stream) {
  (void)hipFuncSetAttribute((const void*)gmlp_fused, hipFuncAttributeMaxDynamicSharedMemorySize, LDS_BYTES);
  gmlp_fused<<<256, 512, LDS_BYTES, stream>>>((const char*)d_in[0], (const char*)d_in[1], (const char*)d_in[2], (const u16*)d_in[3], (const char*)d_in[4], (char*)d_out, (char*)d_ws);
}
```

```cpp
#define PROBE 0
#include <hip/hip_runtime.h>
#include <hip/hip_bf16.h>
typedef unsigned short u16;
typedef unsigned long long u64;
typedef short s16x4 __attribute__((ext_vector_type(4)));
typedef short bf16x8 __attribute__((ext_vector_type(8)));
typedef float f32x2 __attribute__((ext_vector_type(2)));
typedef float f32x4 __attribute__((ext_vector_type(4)));
typedef unsigned u32x4 __attribute__((ext_vector_type(4)));
typedef unsigned u32x2 __attribute__((ext_vector_type(2)));
#define LAS __attribute__((address_space(3)))
#define DEV __device__ __forceinline__
#define BAR() __builtin_amdgcn_s_barrier()
#define SB() __builtin_amdgcn_sched_barrier(0)
#define MEMB() asm volatile("" ::: "memory")
#define WAITL0() __builtin_amdgcn_s_waitcnt(0xC07F)
#define WL0() asm volatile("s_waitcnt lgkmcnt(0)" ::: "memory")
#define WV(N) asm volatile("s_waitcnt vmcnt(%0)" ::"i"(N) : "memory")
#define MFMA(a, b, c) __builtin_amdgcn_mfma_f32_16x16x32_bf16(a, b, c, 0, 0, 0)
#define MFMA16(a, b, c) __builtin_amdgcn_mfma_f32_16x16x16bf16_1k(a, b, c, 0, 0, 0)
#define GL(R, D, V, S, I) __builtin_amdgcn_raw_ptr_buffer_load_lds(R, (LAS void*)(size_t)(D), 16, V, S, I, 0)
#define GL4(R, D, V0, V1, V2, V3, S) do { GL(R, D, V0, S, 0); GL(R, D, V1, S, 1024); GL(R, D, V2, S, 2048); GL(R, D, V3, S, 3072); } while (0)
#define GS(R, D, V, S, I) __builtin_amdgcn_raw_ptr_buffer_load_lds(R, (LAS void*)(size_t)(D), 16, V, S, I, 16)
#define GS4(R, D, V, S) do { GS(R, D, V, S, 0); GS(R, D, V, S, 1024); GS(R, D, V, S, 2048); GS(R, D, V, S, 3072); } while (0)
#define RSRC(p, n) __builtin_amdgcn_make_buffer_rsrc((void*)(p), 0, (n), 0x00020000)
#define RLX_AGENT __ATOMIC_RELAXED, __HIP_MEMORY_SCOPE_AGENT
#define ROT1 2
#define OAUX 16
#define WAUX 0
#define FLAGM 0
#define LA2 2
#define CODE_KB 48

constexpr int CH = 4608;
constexpr int FRAG_BYTES = 16 * 128 * CH;
constexpr size_t WS_Z2 = 0, WS_P = FRAG_BYTES, WS_ST = 2 * (size_t)FRAG_BYTES, WS_F1 = WS_ST + 16 * 16 * 128 * 16, WS_F2 = WS_F1 + 256 * 128, WS_EP = WS_F2 + 256 * 128;
constexpr int STG = 34816, STGP = 23040, STGO = 18432, L_O = 0, L_P = 5 * STGO, LDS_BYTES = 163840;
constexpr int L_T = 139264, L_R = L_T + 8192, L_X = 151552, L_BIAS = 159744, L_TAG = L_BIAS + 256, L_DUMMY = 161280;
constexpr unsigned SPIN_CAP = 1u << 16, OOB = 0x80000000u;
#define STAMP(I)
#define DUMP()

DEV unsigned cvt_pk(float lo, float hi) { unsigned r; asm("v_cvt_pk_bf16_f32 %0, %1, %2" : "=v"(r) : "v"(lo), "v"(hi)); return r; }
DEV bf16x8 ld128(unsigned a) { return *(LAS bf16x8*)(size_t)a; }
DEV s16x4 ld64(unsigned a) { return *(LAS s16x4*)(size_t)a; }
DEV s16x4 ldtr(unsigned a) { return __builtin_amdgcn_ds_read_tr16_b64_v4i16((LAS s16x4*)(size_t)a); }
DEV bf16x8 cat(s16x4 a, s16x4 b) { return __builtin_shufflevector(a, b, 0, 1, 2, 3, 4, 5, 6, 7); }
DEV float bflo(unsigned v) { return __uint_as_float(v << 16); }
DEV float bfhi(unsigned v) { return __uint_as_float(v & 0xffff0000u); }
DEV f32x2 gelu2(f32x2 x) {
  const float k1 = -2.0f * 0.7978845608028654f * 1.4426950408889634f, k2 = k1 * 0.044715f;
  f32x2 t = x * (x * x * k2 + k1), e;
  e.x = __builtin_amdgcn_exp2f(t.x); e.y = __builtin_amdgcn_exp2f(t.y);
  e = e + 1.0f;
  e.x = __builtin_amdgcn_rcpf(e.x); e.y = __builtin_amdgcn_rcpf(e.y);
  return x * e;
}
DEV u32x2 gelu_pack(f32x4 v) { const f32x2 a = gelu2((f32x2){v[0], v[1]}), b = gelu2((f32x2){v[2], v[3]}); return (u32x2){cvt_pk(a.x, a.y), cvt_pk(b.x, b.y)}; }
DEV u32x2 gelu_pack_sum(f32x4 v, f32x2& s1, f32x2& s2) {
  const f32x2 a = gelu2((f32x2){v[0], v[1]}), b = gelu2((f32x2){v[2], v[3]});
  s1 += a + b; s2 += a * a; s2 += b * b;
  return (u32x2){cvt_pk(a.x, a.y), cvt_pk(b.x, b.y)};
}

struct TAddr { unsigned w[2], z[2][2], s[2]; };
struct TFrag { bf16x8 wX[4], wY[4]; s16x4 tX[5][2], tY[4][2]; };
template <int WF, int PS, int OFF, bool FIRST, bool SYNC = true>
DEV void t_step(TAddr& A, int dlt, f32x4 (&acc)[4][5], TFrag& F) {
  constexpr int KX = WF, KY = 1 - WF;
  SB(); if (SYNC) BAR(); MEMB(); SB();
#pragma unroll
  for (int i = 0; i < 16; ++i) {
    const int n = i >> 2, m = i & 3;
    if (!FIRST) acc[m][n] = MFMA(cat(F.tY[n][0], F.tY[n][1]), F.wY[m], acc[m][n]);
    SB();
    if (i == 0) F.wX[0] = ld128(A.w[KX] + OFF);
    else if (i == 1) F.tX[0][0] = ldtr(A.z[0][0] + OFF + KX * 9216);
    else if (i == 2) F.tX[0][1] = ldtr(A.z[1][0] + OFF + KX * 9216);
    else if (i < 6) F.wX[i - 2] = ld128(A.w[KX] + OFF + (i - 2) * 2048);
    else if (i < 12) { const int nn = 1 + ((i - 6) >> 1), u = (i - 6) & 1; F.tX[nn][u] = ldtr(A.z[u][nn & 1] + OFF + KX * 9216 + (nn >> 1) * PS); }
    else if (i < 14) F.tX[4][i - 12] = ldtr(A.s[i - 12] + OFF + KX * 9216);
    SB();
  }
#pragma unroll
  for (int i = 0; i < 20; ++i) {
    const int n = i >> 2, m = i & 3;
    acc[m][n] = MFMA(cat(F.tX[n][0], F.tX[n][1]), F.wX[m], acc[m][n]);
    SB();
    if (i == 0) F.wY[0] = ld128(A.w[KY] + OFF);
    else if (i == 1) F.tY[0][0] = ldtr(A.z[0][0] + OFF + KY * 9216);
    else if (i == 2) F.tY[0][1] = ldtr(A.z[1][0] + OFF + KY * 9216);
    else if (i < 6) F.wY[i - 2] = ld128(A.w[KY] + OFF + (i - 2) * 2048);
    else if (i < 12) { const int nn = 1 + ((i - 6) >> 1), u = (i - 6) & 1; F.tY[nn][u] = ldtr(A.z[u][nn & 1] + OFF + KY * 9216 + (nn >> 1) * PS); }
    else if (OFF != 0 && i < 16) {
      const int u = i & 1;
      if (i < 14) { A.w[u] += dlt; A.s[u] += dlt; asm volatile("" : "+v"(A.w[u]), "+v"(A.s[u])); }
      else { A.z[u][0] += dlt; A.z[u][1] += dlt; asm volatile("" : "+v"(A.z[u][0]), "+v"(A.z[u][1])); }
    }
    SB();
  }
  WAITL0();
}
template <int WF>
DEV void t_finish(unsigned lds0, int wt, int lane, f32x4 (&acc)[4][5], TFrag& F) {
#pragma unroll
  for (int h = 0; h < 2; ++h) *(LAS f32x4*)(size_t)(lds0 + L_X + ((wt * 2 + WF) * 2 + h) * 1024 + lane * 16) = acc[2 * (1 - WF) + h][4];
#pragma unroll
  for (int n = 0; n < 4; ++n)
#pragma unroll
    for (int m = 0; m < 4; ++m) acc[m][n] = MFMA(cat(F.tY[n][0], F.tY[n][1]), F.wY[m], acc[m][n]);
  WL0();
  BAR(); MEMB();
#pragma unroll
  for (int h = 0; h < 2; ++h) acc[2 * WF + h][4] += *(LAS f32x4*)(size_t)(lds0 + L_X + ((wt * 2 + 1 - WF) * 2 + h) * 1024 + lane * 16);
}
template <int WF, bool MID, class Mid>
DEV void in_pass(unsigned lds0, int wt, int lane, TAddr& A, f32x4 (&acc)[4][5], Mid mid) {
  TFrag F;
#pragma unroll
  for (int m = 0; m < 4; ++m)
#pragma unroll
    for (int n = 0; n < 5; ++n) acc[m][n] = (f32x4){0, 0, 0, 0};
  int dlt = 2 * STG;
  WAITL0();
  t_step<WF, 64, 0, true>(A, dlt, acc, F);
  for (int k = 0; k < (MID ? 3 : 4); ++k) {
    t_step<WF, 64, STG, false>(A, dlt, acc, F);
    dlt = -dlt;
    t_step<WF, 64, 0, false>(A, dlt, acc, F);
  }
  if (MID) {
    t_step<WF, 64, STG, false>(A, -2 * STG, acc, F);
    SB(); mid(); SB();
    t_step<WF, 64, 0, false>(A, 0, acc, F);
  }
  t_finish<WF>(lds0, wt, lane, acc, F);
}

struct G3Frag { bf16x8 p[2][5]; s16x4 w[2][2][2], ws[2], ps[3]; };
template <int KH, bool MM>
DEV void g3_stage(f32x4 (&acc)[5][2], const G3Frag& c, G3Frag& n, unsigned aP, unsigned aS, unsigned a0, unsigned a1) {
  constexpr int M0 = KH ? 3 : 0, NS = KH ? 2 : 3, NM = 20 + 2 * NS, NR = 20 + NS;
#pragma unroll
  for (int i = 0; i < NM; ++i) {
    if (MM) {
      if (i >= 10 && i < 10 + 2 * NS) acc[M0 + ((i - 10) >> 1)][i & 1] = MFMA16(c.ws[i & 1], c.ps[(i - 10) >> 1], acc[M0 + ((i - 10) >> 1)][i & 1]);
      else { const int ii = i < 10 ? i : i - 2 * NS, P = ii / 10, m = (ii % 10) >> 1, cc = ii & 1; acc[m][cc] = MFMA(cat(c.w[P][cc][0], c.w[P][cc][1]), c.p[P][m], acc[m][cc]); }
    }
    SB();
#pragma unroll
    for (int r = (i * 3) / 2; r < ((i + 1) * 3) / 2; ++r) {
      if (r < 18) { const int P = r / 9, q = r % 9;
        if (q < 4) n.w[P][q >> 1][q & 1] = ldtr(((q >> 1) ? a1 : a0) + (2 * KH + P) * 4096 + (q & 1) * 2048);
        else n.p[P][q - 4] = ld128(aP + P * 1024 + (q - 4) * 4096); }
      else if (r < 20) n.ws[r - 18] = ldtr(((r - 18) ? a1 : a0) + 16384);
      else if (r < NR) n.ps[r - 20] = ld64(aS + (M0 + r - 20) * 512);
    }
    SB();
  }
}
template <int KH>
DEV void g3_compute(unsigned lds0, int dh, int lane, char* out, int tt, int dd) {
  f32x4 acc[5][2];
#pragma unroll
  for (int m = 0; m < 5; ++m) { acc[m][0] = (f32x4){0, 0, 0, 0}; acc[m][1] = (f32x4){0, 0, 0, 0}; }
  const int g = lane >> 4, qr = (lane >> 2) & 3, p = lane & 3;
  const int rr = 4 * g + qr;
  unsigned bW[2];
#pragma unroll
  for (int c = 0; c < 2; ++c) bW[c] = lds0 + L_O + rr * 128 + (((4 * dh + p) ^ (((rr >> 1) & 1) | (rr & 4))) << 4) + ((c ^ (p & 1)) * 8);
  const unsigned bP = lds0 + L_P + lane * 16 + KH * 2048, bS = lds0 + L_P + 20480 + lane * 8;
  G3Frag f0, f1;
  WAITL0();
  SB(); BAR(); MEMB(); SB();
  g3_stage<KH, false>(acc, f1, f0, bP, bS, bW[0], bW[1]);
  WAITL0();
  unsigned sp = STGP, sw = STGO;
  for (int j = 1; j < 16; j += 2) {
    SB(); BAR(); MEMB(); SB();
    g3_stage<KH, true>(acc, f0, f1, bP + sp, bS + sp, bW[0] + sw, bW[1] + sw);
    sp = sp == 2 * STGP ? 0 : sp + STGP; sw = sw == 4 * STGO ? 0 : sw + STGO;
    WAITL0();
    SB(); BAR(); MEMB(); SB();
    g3_stage<KH, true>(acc, f1, f0, bP + sp, bS + sp, bW[0] + sw, bW[1] + sw);
    sp = sp == 2 * STGP ? 0 : sp + STGP; sw = sw == 4 * STGO ? 0 : sw + STGO;
    WAITL0();
  }
#pragma unroll
  for (int m = 0; m < 5; ++m)
    if ((m >= 3) != KH) { *(LAS f32x4*)(size_t)(lds0 + (dh * 10 + m * 2) * 1024 + lane * 16) = acc[m][0]; *(LAS f32x4*)(size_t)(lds0 + (dh * 10 + m * 2 + 1) * 1024 + lane * 16) = acc[m][1]; }
  WL0();
  BAR(); MEMB();
  auto rO = RSRC(out, 2048 * 1152);
  const bool sw2 = g & 1;
#pragma unroll
  for (int m = 0; m < 5; ++m)
    if ((m >= 3) == KH) {
      const f32x4 a = acc[m][0] + *(LAS f32x4*)(size_t)(lds0 + (dh * 10 + m * 2) * 1024 + lane * 16), b = acc[m][1] + *(LAS f32x4*)(size_t)(lds0 + (dh * 10 + m * 2 + 1) * 1024 + lane * 16);
      const unsigned a0 = cvt_pk(a[0], a[1]), a1 = cvt_pk(a[2], a[3]), b0 = cvt_pk(b[0], b[1]), b1 = cvt_pk(b[2], b[3]);
      const u32x4 v = {sw2 ? b0 : a0, sw2 ? b1 : a1, sw2 ? a0 : b0, sw2 ? a1 : b1};
      __builtin_amdgcn_raw_buffer_store_b128(v, rO, (tt * 80 + m * 16 + (lane & 15)) * 1152 + dd * 128 + dh * 64 + g * 16, 0, OAUX);
    }
}

template <int WF>
DEV void front(unsigned lds0, int wid, int wt, int lane, int ti, int fj, char* ws) {
  const int r16 = lane & 15, g = lane >> 4, qr = (lane >> 2) & 3, p = lane & 3;
  TAddr A;
  A.w[0] = lds0 + (64 * wt + r16) * 128 + ((g ^ (r16 >> 1)) << 4); A.w[1] = A.w[0] ^ 64;
#pragma unroll
  for (int u = 0; u < 2; ++u) {
    const unsigned b = lds0 + 16384 + (16 * u + 4 * g + qr) * 288 + p * 8;
    A.z[u][0] = b + WF * 128; A.z[u][1] = b + WF * 128 + 32; A.s[u] = b + 256;
  }
  asm volatile("" : "+v"(A.w[0]), "+v"(A.w[1]), "+v"(A.s[0]), "+v"(A.s[1]), "+v"(A.z[0][0]), "+v"(A.z[0][1]), "+v"(A.z[1][0]), "+v"(A.z[1][1]));
  f32x4 acc[4][5];
  in_pass<WF, false>(lds0, wt, lane, A, acc, []() {});
  STAMP(2);
  u32x2 ua[4][4], ub[2];
  {
    f32x2 s1[4], s2[4];
#pragma unroll
    for (int m = 0; m < 4; ++m) {
      s1[m] = (f32x2){0.f, 0.f}; s2[m] = (f32x2){0.f, 0.f};
#pragma unroll
      for (int n = 0; n < 4; ++n) ua[m][n] = gelu_pack_sum(acc[m][n], s1[m], s2[m]);
      if ((m >> 1) == WF) ub[m & 1] = gelu_pack_sum(acc[m][4], s1[m], s2[m]);
    }
#pragma unroll
    for (int m = 0; m < 4; ++m) *(LAS f32x2*)(size_t)(lds0 + L_T + (64 * wt + 16 * m + r16) * 64 + (WF * 4 + g) * 8) = (f32x2){s1[m].x + s1[m].y, s2[m].x + s2[m].y};
  }
#pragma unroll
  for (int m = 0; m < 4; ++m) asm volatile("" : "+v"(ua[m][0]), "+v"(ua[m][1]), "+v"(ua[m][2]), "+v"(ua[m][3]));
  asm volatile("" : "+v"(ub[0]), "+v"(ub[1]));
  STAMP(3);
  in_pass<WF, true>(lds0, wt, lane, A, acc, [&]() {
    STAMP(4);
#pragma unroll
  for (int m = 0; m < 4; ++m) asm volatile("" : "+v"(ua[m][0]), "+v"(ua[m][1]), "+v"(ua[m][2]), "+v"(ua[m][3]));
  asm volatile("" : "+v"(ub[0]), "+v"(ub[1]));
  int lz = lane; asm volatile("" : "+v"(lz));
  {
    const int r16 = lz & 15, g = lz >> 4;
    auto rZ = RSRC(ws + WS_Z2, FRAG_BYTES);
    const unsigned sl = g * 16 + (r16 ^ (4 * (g >> 1)));
    const bool sw = g & 1;
#pragma unroll
    for (int m = 0; m < 4; ++m) {
      const f32x4 q4 = *(LAS f32x4*)(size_t)(lds0 + L_R + (64 * wt + 16 * m + r16) * 32) + *(LAS f32x4*)(size_t)(lds0 + L_R + (64 * wt + 16 * m + r16) * 32 + 16);
      const float mean = (q4[0] + q4[2]) * (1.0f / 2304.0f), var = fmaxf((q4[1] + q4[3]) * (1.0f / 2304.0f) - mean * mean, 0.f);
      const float rs = __builtin_amdgcn_rsqf(var + 1e-5f), sh = -mean * rs;
      auto norm = [&](unsigned v) { const f32x2 y = (f32x2){bflo(v), bfhi(v)} * rs + sh; return cvt_pk(y.x, y.y); };
      const unsigned dst = (fj * 128 + ti * 8 + wt * 4 + m) * CH;
#pragma unroll
      for (int pp = 0; pp < 2; ++pp) {
        const u32x2 a = sw ? ua[m][2 * pp + 1] : ua[m][2 * pp], b = sw ? ua[m][2 * pp] : ua[m][2 * pp + 1];
        const u32x4 v = {norm(a.x), norm(a.y), norm(b.x), norm(b.y)};
        __builtin_amdgcn_raw_buffer_store_b128(v, rZ, dst + sl * 16 + WF * 2048 + pp * 1024, 0, 16);
        ua[m][2 * pp] = (u32x2){v.x, v.y}; ua[m][2 * pp + 1] = (u32x2){v.z, v.w};
      }
      if ((m >> 1) == WF) {
        const u32x2 v = {norm(ub[m & 1].x), norm(ub[m & 1].y)};
        __builtin_amdgcn_raw_buffer_store_b64(v, rZ, dst + sl * 8 + 4096, 0, 16);
        ub[m & 1] = v;
      }
    }
  }
    STAMP(5);
  });
#pragma unroll
  for (int m = 0; m < 4; ++m) asm volatile("" : "+v"(acc[m][0]), "+v"(acc[m][1]), "+v"(acc[m][2]), "+v"(acc[m][3]), "+v"(acc[m][4]));
  const u32x2 tg = *(LAS u32x2*)(size_t)(lds0 + L_TAG);
  const u64 tag = ((u64)(unsigned)__builtin_amdgcn_readfirstlane(tg.y) << 32) | (unsigned)__builtin_amdgcn_readfirstlane(tg.x);
  {
    const unsigned ld = lds0 + wt * STG + 16384, sl = g * 16 + (r16 ^ (4 * (g >> 1)));
#pragma unroll
    for (int m = 0; m < 4; ++m) {
#pragma unroll
      for (int pp = 0; pp < 2; ++pp) *(LAS u32x4*)(size_t)(ld + m * CH + WF * 2048 + pp * 1024 + sl * 16) = (u32x4){ua[m][2 * pp].x, ua[m][2 * pp].y, ua[m][2 * pp + 1].x, ua[m][2 * pp + 1].y};
      if ((m >> 1) == WF) *(LAS u32x2*)(size_t)(ld + m * CH + 4096 + sl * 8) = ub[m & 1];
    }
  }
  STAMP(7);
  SB();
  u32x2 za[4][4], zb[2];
#pragma unroll
  for (int m = 0; m < 4; ++m) {
#pragma unroll
    for (int n = 0; n < 4; ++n) za[m][n] = gelu_pack(acc[m][n]);
    if ((m >> 1) == WF) zb[m & 1] = gelu_pack(acc[m][4]);
    asm volatile("" : "+v"(za[m][0]), "+v"(za[m][1]), "+v"(za[m][2]), "+v"(za[m][3]));
    if (m == FLAGM) {
      SB();
      asm volatile("s_waitcnt vmcnt(0) lgkmcnt(0)" ::: "memory");
      if (lane == 0) __hip_atomic_store((u64*)(ws + WS_F1 + (fj * 16 + ti) * 128) + wid, tag, RLX_AGENT);
      BAR();
      SB();
    }
  }
  asm volatile("" : "+v"(zb[0]), "+v"(zb[1]));
  STAMP(6);
#pragma unroll
  for (int u = 0; u < 2; ++u) {
    const unsigned sl = p * 16 + 8 * (g & 1) + 4 * (u ^ (p >> 1)) + qr;
#pragma unroll
    for (int h = 0; h < 2; ++h) A.z[u][h] = lds0 + 16384 + (g >> 1) * CH + WF * 2048 + sl * 16 + ((h ^ (p & 1)) * 8);
    A.s[u] = lds0 + 16384 + (g >> 1) * CH + 4096 + sl * 8;
  }
  asm volatile("" : "+v"(A.s[0]), "+v"(A.s[1]), "+v"(A.z[0][0]), "+v"(A.z[0][1]), "+v"(A.z[1][0]), "+v"(A.z[1][1]));
#pragma unroll
  for (int m = 0; m < 4; ++m) {
    const float b = __uint_as_float((unsigned)*(LAS u16*)(size_t)(lds0 + L_BIAS + (wt * 64 + m * 16 + r16) * 2) << 16);
#pragma unroll
    for (int n = 0; n < 5; ++n) { const float b0 = (n < 4 || (m >> 1) == WF) ? b : 0.f; acc[m][n] = (f32x4){b0, b0, b0, b0}; }
  }
  TFrag F;
  int dlt = 2 * STG;
  WAITL0();
  t_step<WF, 1024, 0, true, false>(A, dlt, acc, F);
  t_step<WF, 1024, STG, false, false>(A, dlt, acc, F);
  STAMP(8);
  for (int k = 0; k < 15; ++k) {
    dlt = -dlt;
    t_step<WF, 1024, 0, false>(A, dlt, acc, F);
    t_step<WF, 1024, STG, false>(A, dlt, acc, F);
  }
  t_finish<WF>(lds0, wt, lane, acc, F);
  STAMP(10);
  auto rP = RSRC(ws + WS_P, FRAG_BYTES);
  auto gate = [&](f32x4 a, u32x2 z) {
    const f32x2 x = (f32x2){a[0], a[1]} * (f32x2){bflo(z.x), bfhi(z.x)}, y = (f32x2){a[2], a[3]} * (f32x2){bflo(z.y), bfhi(z.y)};
    return (u32x2){cvt_pk(x.x, x.y), cvt_pk(y.x, y.y)};
  };
#pragma unroll
  for (int m = 0; m < 4; ++m) {
    const unsigned dst = ((ti * 8 + wt * 4 + m) * 16 + fj) * CH;
#pragma unroll
    for (int pp = 0; pp < 2; ++pp) {
      const u32x2 a = gate(acc[m][2 * pp], za[m][2 * pp]), b = gate(acc[m][2 * pp + 1], za[m][2 * pp + 1]);
      __builtin_amdgcn_raw_buffer_store_b128((u32x4){a.x, a.y, b.x, b.y}, rP, dst + lane * 16 + (2 * WF + pp) * 1024, 0, 16);
    }
    if ((m >> 1) == WF) __builtin_amdgcn_raw_buffer_store_b64(gate(acc[m][4], zb[m & 1]), rP, dst + lane * 8 + 4096, 0, 16);
  }
  STAMP(11);
  WV(0);
  if (lane == 0) __hip_atomic_store((u64*)(ws + WS_F2 + (ti * 16 + fj) * 128) + wid, tag, RLX_AGENT);
  BAR();
  STAMP(12);
}

DEV u64 poll16(const char* base, u64 tag, int lane) {
  const u64 v = __hip_atomic_load((const u64*)(base + (lane >> 2) * 128) + (lane & 3), RLX_AGENT);
  return __builtin_amdgcn_ballot_w64(v == tag);
}
DEV bool got(u64 mask, int who) { return ((unsigned)(mask >> (4 * who)) & 15u) == 15u; }

__global__ __launch_bounds__(512, 2) void gmlp_fused(const char* __restrict__ X, const char* __restrict__ Win, const char* __restrict__ Wsp, const u16* __restrict__ bias,
                                                     const char* __restrict__ Wo, char* __restrict__ out, char* __restrict__ ws) {
  extern __shared__ __attribute__((aligned(16))) char smem[];
  u64 rt0; asm volatile("s_memrealtime %0\n\ts_waitcnt lgkmcnt(0)" : "=s"(rt0));
  u64 pc0; asm volatile("s_getpc_b64 %0" : "=s"(pc0));
  const int tid = threadIdx.x, wid = __builtin_amdgcn_readfirstlane(tid >> 6), lane = tid & 63;
  const int bid = blockIdx.x, xg = bid & 7, rk = bid >> 3, il = rk & 7, jl = rk >> 3, ti = (xg & 1) * 8 + il, fj = (xg >> 1) * 4 + jl;
  const int L3 = xg * 30 + rk, tt = L3 / 9, dd = L3 - tt * 9;
  const bool has3 = rk < 30 && L3 < 234;
  const unsigned lds0 = (unsigned)(size_t)(LAS char*)smem;
  STAMP(0);
  if (__builtin_expect(wid >= 4, 1)) {
    __builtin_amdgcn_s_setprio(3);
    u64 rt1 = rt0;
#define MARK(I) do { if (PROBE == (I)) { SB(); asm volatile("s_memrealtime %0\n\ts_waitcnt lgkmcnt(0)" : "=s"(rt1) :: "memory"); SB(); } } while (0)
    MARK(1);
    const int lw = wid - 4;
    auto rX = RSRC(X, 2048 * 1152); auto rWi = RSRC(Win, 576 * 9216);
    unsigned vX[4], vW[5];
    const unsigned zp0 = lw < 2 ? lw * 5 : 2 + lw * 4;
    const unsigned sX = (ti * 128 + lw * 32) * 1152, sW = fj * 288;
    const int rot = ROT1 == 0 ? 0 : ROT1 == 1 ? il + 2 * jl : 3 * ((il + jl) % 3);
    auto k1 = [&](int k) { int kk = k + rot; kk = kk >= 9 ? kk - 9 : kk; kk = kk >= 9 ? kk - 9 : kk; return kk; };
    auto issue1 = [&](int half, int k) {
      const int kk = k1(k);
      const unsigned d = lds0 + (k & 3) * STG, dz = d + 16384 + zp0 * 1024, sw = sW + half * 4608 + kk * 64 * 9216;
      GL4(rX, d + lw * 4096, vX[0], vX[1], vX[2], vX[3], sX + kk * 128);
      GL4(rWi, dz, vW[0], vW[1], vW[2], vW[3], sw);
      GL(rWi, lw < 2 ? dz + 4096 : lds0 + L_DUMMY, vW[4], sw, 0);
    };
    {
#pragma unroll
      for (int q = 0; q < 4; ++q) { const int row = 8 * q + (lane >> 3); vX[q] = row * 1152 + (((lane & 7) ^ ((row >> 1) & 7)) << 4) - q * 1024; }
      MARK(2);
      GL4(rX, lds0 + lw * 4096, vX[0], vX[1], vX[2], vX[3], sX + k1(0) * 128);
      SB();
#pragma unroll
      for (int q = 0; q < 5; ++q) {
        const unsigned c16 = (zp0 + q) * 64 + lane, R = (c16 * 3641u) >> 16, c = (c16 - R * 18) * 16, k = (R & 32) + 8 * ((R >> 2) & 3) + 4 * ((R >> 4) & 1) + (R & 3);
        vW[q] = k * 9216 + c - (q & 3) * 1024;
      }
      if (lw >= 2) vW[4] = OOB;
      const unsigned dz = lds0 + 16384 + zp0 * 1024, sw = sW + 4608 + k1(0) * 64 * 9216;
      GL4(rWi, dz, vW[0], vW[1], vW[2], vW[3], sw);
      GL(rWi, lw < 2 ? dz + 4096 : lds0 + L_DUMMY, vW[4], sw, 0);
    }
    MARK(3);
    const unsigned vL = lane * 16;
    auto rWs = RSRC(Wsp, 2048 * 4096); auto rWo = RSRC(Wo, 2304 * 1152);
    issue1(1, 1);
    WV(9); BAR();
    MARK(4);
    STAMP(1);
    issue1(1, 2);
    WV(9);
    for (int k = 1; k < 5; ++k) { BAR(); issue1(1, k + 2); WV(9); }
    BAR(); issue1(1, 7); issue1(1, 8); WV(18);
    BAR(); issue1(0, 1); WV(18);
    BAR(); issue1(0, 2); WV(18);
    BAR(); issue1(0, 3);
    const u64 ep = __hip_atomic_load((const u64*)(ws + WS_EP), RLX_AGENT);
    if (lw == 0) __builtin_amdgcn_raw_ptr_buffer_load_lds(RSRC(bias, 4096), (LAS void*)(size_t)(lds0 + L_BIAS), 4, lane * 4, ti * 256, 0, 0);
    BAR();
    issue1(0, 0);
#pragma unroll
    for (int q = 0; q < 8; ++q) __builtin_amdgcn_raw_ptr_buffer_load_lds(rWs, (LAS void*)(size_t)(lds0 + L_DUMMY), 16, vL, bid * 32768 + lw * 8192 + q * 1024, 0, WAUX);
    GL(rWo, lds0 + L_DUMMY, vL, bid * 10368 + lw * 2592, 0); GL(rWo, lds0 + L_DUMMY, vL, bid * 10368 + lw * 2592 + 1024, 0); GL(rWo, lds0 + L_DUMMY, vL, bid * 10368 + lw * 2592 + 1568, 0);
    const unsigned tagA = __builtin_amdgcn_readfirstlane((unsigned)ep) + 0x9E3779B9u, tagB = __builtin_amdgcn_readfirstlane((unsigned)(ep >> 32)) + 0x85EBCA6Bu;
    const u64 tag = ((u64)tagB << 32) | tagA;
    if (lw == 0) { const u32x2 tg = {tagA, tagB}; asm volatile("ds_write_b64 %0, %1\n\ts_waitcnt lgkmcnt(0)" :: "v"(lds0 + L_TAG), "v"(tg) : "memory"); }
    unsigned vS[4];
#pragma unroll
    for (int q = 0; q < 4; ++q) { const int row = 8 * q + (lane >> 3); vS[q] = row * 4096 + (((lane & 7) ^ ((row >> 1) & 7)) << 4) - q * 1024; }
    const unsigned sWs = (ti * 128 + lw * 32) * 4096;
    auto kpos = [&](int n) { return n < 2 ? 2 * ti + n : (n - 2 < 2 * ti ? n - 2 : n); };
    auto issueW = [&](int n) { GL4(rWs, lds0 + (n & 3) * STG + lw * 4096, vS[0], vS[1], vS[2], vS[3], sWs + kpos(n) * 128); };
    WV(11);
    BAR();
    STAMP(2);
    auto rS = RSRC(ws + WS_ST, 16 * 16 * 128 * 16);
    if (lane < 32) {
      const int row = lw * 32 + lane;
      f32x4 v0, v1, v2, v3;
      asm volatile("ds_read_b128 %0, %4\n\tds_read_b128 %1, %4 offset:16\n\tds_read_b128 %2, %4 offset:32\n\tds_read_b128 %3, %4 offset:48\n\ts_waitcnt lgkmcnt(0)"
                   : "=&v"(v0), "=&v"(v1), "=&v"(v2), "=&v"(v3) : "v"(lds0 + L_T + row * 64) : "memory");
      const f32x4 s = (v0 + v1) + (v2 + v3);
      u32x4 gq; gq.x = tagA; gq.y = __float_as_uint(s[0] + s[2]); gq.z = tagB; gq.w = __float_as_uint(s[1] + s[3]);
      __builtin_amdgcn_raw_buffer_store_b128(gq, rS, ((ti * 16 + fj) * 128 + row) * 16, 0, 16);
    }
    u32x4 sv[8];
    auto sweep = [&]() {
      MEMB();
#pragma unroll
      for (int p = 0; p < 8; ++p) sv[p] = __builtin_amdgcn_raw_buffer_load_b128(rS, lane * 16 + (p & 1) * 1024, (ti * 16 + lw * 4 + (p >> 1)) * 2048, 16);
      SB();
    };
    BAR(); issue1(0, 4);
    BAR(); issue1(0, 5);
    BAR(); issue1(0, 6); WV(18);
    BAR(); sweep(); issue1(0, 7); WV(9 + 8 + 9);
    BAR(); issue1(0, 8); WV(18);
    BAR();
    {
      f32x4 s;
      for (unsigned spins = 0;; ++spins) {
        bool ok = true;
        s = (f32x4){0.f, 0.f, 0.f, 0.f};
#pragma unroll
        for (int p = 0; p < 8; ++p) { ok &= (sv[p].x == tagA) & (sv[p].z == tagB); s[(p & 1) * 2] += __uint_as_float(sv[p].y); s[(p & 1) * 2 + 1] += __uint_as_float(sv[p].w); }
        if (__all(ok)) break;
        if (spins > SPIN_CAP) { s[0] = s[2] = __builtin_nanf(""); break; }
        sweep();
      }
      const f32x2 o0 = {s[0], s[1]}, o1 = {s[2], s[3]};
      asm volatile("ds_write_b64 %0, %1\n\tds_write_b64 %0, %2 offset:2048\n\ts_waitcnt lgkmcnt(0)" :: "v"(lds0 + L_R + lane * 32 + lw * 8), "v"(o0), "v"(o1) : "memory");
    }
    STAMP(3);
    issueW(1); WV(9 + 4);
    BAR(); WV(4);
    BAR();
    BAR();
    issueW(0);
    WV(0);
    BAR();
    auto rZ = RSRC(ws + WS_Z2, FRAG_BYTES);
    const unsigned sZ = fj * 128 * CH + zp0 * 1024, vZ5 = lw < 2 ? vL : OOB;
    u64 ready = 0;
    auto issueZ = [&](int n) {
      const int kp = kpos(n);
      if (__builtin_expect(!got(ready, kp >> 1), 0)) { for (unsigned spins = 0; !got(ready, kp >> 1) && spins < SPIN_CAP; ++spins) ready = poll16(ws + WS_F1 + fj * 16 * 128, tag, lane); }
      MEMB();
      const unsigned dz = lds0 + (n & 3) * STG + 16384 + zp0 * 1024, so = sZ + kp * 18432;
      GS4(rZ, dz, vL, so);
      GS(rZ, lw < 2 ? dz + 4096 : lds0 + L_DUMMY, vZ5, so + 4096, 0);
    };
    issueW(2); issueW(3);
    issueZ(2); issueZ(3);
    STAMP(4);
    WV(5);
    BAR();
    STAMP(5);
    for (int n = 4; n < 31; ++n) {
      issueW(n); issueZ(n);
      WV(9);
      BAR();
    }
    issueW(31); issueZ(31);
    constexpr int TAIL = 9;
    unsigned vO[2], vOh;
#pragma unroll
    for (int q = 0; q < 2; ++q) { const int row = 36 * lw + 8 * q + (lane >> 3); vO[q] = row * 1152 + (((lane & 7) ^ (((row >> 1) & 1) | (row & 4))) << 4); }
    { const int row = 36 * lw + 32 + ((lane >> 3) & 3); vOh = row * 1152 + (((lane & 7) ^ (((row >> 1) & 1) | (row & 4))) << 4); }
    const unsigned vO1 = vO[1] - 1024, vO2 = vO[0] + 16 * 1152 - 2048, vO3 = vO[1] + 16 * 1152 - 3072;
    const unsigned sO = min(dd, 8) * 128;
    auto issueO = [&](int n, int slot) {
      const unsigned d = lds0 + L_O + slot * STGO + lw * 4608, s0 = sO + n * 144 * 1152;
      GL4(rWo, d, vO[0], vO1, vO2, vO3, s0);
      if (lane < 32) GL(rWo, d + 4096, vOh, s0, 0);
    };
    issueO(0, 0);
    WV(9 + 5); BAR();
    issueO(1, 1); issueO(2, 2);
    WV(15); BAR();
    BAR(); BAR();
    if (!has3) { WV(0); return; }
    issueO(3, 3); issueO(4, 4);
    auto rP = RSRC(ws + WS_P, FRAG_BYTES);
    const unsigned vPs = lw < 3 ? (lane >> 5) * 16 * CH + (lane & 31) * 16 + 4096 : OOB;
    const int limS = lw == 2 ? 32 : 64;
    const unsigned sP = tt * 5 * 16 * CH;
    const int pa = (tt * 80) >> 7, pb = min((tt * 80 + 79) >> 7, 15);
    u64 rdy = 0;
    auto issueP = [&](int j, int slot) {
      if (__builtin_expect(!got(rdy, j), 0)) { for (unsigned spins = 0; !got(rdy, j) && spins < SPIN_CAP; ++spins) rdy = poll16(ws + WS_F2 + pa * 16 * 128, tag, lane) & poll16(ws + WS_F2 + pb * 16 * 128, tag, lane); }
      MEMB();
      const unsigned d = lds0 + L_P + slot * STGP, sp = sP + j * CH;
      GS4(rP, d + lw * 4096, vL, sp + lw * 16 * CH);
      GS(rP, d + 16384 + lw * 1024, vL, sp + 64 * CH + lw * 1024, 0);
      if (lane < limS) GS(rP, lw < 3 ? d + 20480 + lw * 1024 : lds0 + L_DUMMY, vPs, sp + lw * 32 * CH, 0);
    };
    issueP(0, 0);
    STAMP(6);
    issueP(1, 1); issueP(2, 2);
    WV(12);
    BAR();
    STAMP(7);
    WV(6);
    BAR(); issueP(3, 0); issueO(5, 0); WV(11);
    int sp3 = 1, so5 = 1;
    for (int n = 2; n < 12; ++n) {
      BAR();
      issueP(n + 2, sp3); issueO(n + 4, so5);
      sp3 = sp3 == 2 ? 0 : sp3 + 1; so5 = so5 == 4 ? 0 : so5 + 1;
      WV(16);
    }
    BAR(); issueP(14, 2); WV(6);
    BAR(); issueP(15, 0); WV(6);
    BAR(); WV(0);
    BAR();
    BAR(); BAR();
    MARK(5);
    { const u64 e = __builtin_amdgcn_s_memrealtime() + (PROBE == 5 ? 2 : 10) * (rt1 - rt0); while (__builtin_amdgcn_s_memrealtime() < e) __builtin_amdgcn_s_sleep(4); }
    STAMP(8);
    DUMP();
    if (bid == 0 && tid == 256) __hip_atomic_store((u64*)(ws + WS_EP), tag, RLX_AGENT);
  } else {
    const int wt = wid >> 1, wf = wid & 1;
    if (wid == 0) {
      auto rC = RSRC((const char*)(pc0 & ~(u64)255), CODE_KB * 1024);
      GL(rC, lds0 + L_DUMMY, lane * 16, rk * 1024, 0); GL(rC, lds0 + L_DUMMY, lane * 16, rk * 1024 + 32768, 0);
      WV(0);
    }
    if (wf == 0) front<0>(lds0, wid, wt, lane, ti, fj, ws); else front<1>(lds0, wid, wt, lane, ti, fj, ws);
    if (!has3) return;
    if (wt == 0) g3_compute<0>(lds0, wf, lane, out, tt, dd); else g3_compute<1>(lds0, wf, lane, out, tt, dd);
    STAMP(13);
    DUMP();
  }
}

extern "C" void kernel_launch(void* const* d_in, const int* in_sizes, int n_in, void* d_out, int out_size, void* d_ws, size_t ws_size, hipStream_t stream) {
  (void)hipFuncSetAttribute((const void*)gmlp_fused, hipFuncAttributeMaxDynamicSharedMemorySize, LDS_BYTES);
  gmlp_fused<<<256, 512, LDS_BYTES, stream>>>((const char*)d_in[0], (const char*)d_in[1], (const char*)d_in[2], (const u16*)d_in[3], (const char*)d_in[4], (char*)d_out, (char*)d_ws);
}
```

```cpp
#include <hip/hip_runtime.h>
#include <hip/hip_bf16.h>
typedef unsigned short u16;
typedef unsigned long long u64;
typedef short s16x4 __attribute__((ext_vector_type(4)));
typedef short bf16x8 __attribute__((ext_vector_type(8)));
typedef float f32x2 __attribute__((ext_vector_type(2)));
typedef float f32x4 __attribute__((ext_vector_type(4)));
typedef unsigned u32x4 __attribute__((ext_vector_type(4)));
typedef unsigned u32x2 __attribute__((ext_vector_type(2)));
#define LAS __attribute__((address_space(3)))
#define DEV __device__ __forceinline__
#define BAR() __builtin_amdgcn_s_barrier()
#define SB() __builtin_amdgcn_sched_barrier(0)
#define MEMB() asm volatile("" ::: "memory")
#define WAITL0() __builtin_amdgcn_s_waitcnt(0xC07F)
#define WL0() asm volatile("s_waitcnt lgkmcnt(0)" ::: "memory")
#define WV(N) asm volatile("s_waitcnt vmcnt(%0)" ::"i"(N) : "memory")
#define MFMA(a, b, c) __builtin_amdgcn_mfma_f32_16x16x32_bf16(a, b, c, 0, 0, 0)
#define MFMA16(a, b, c) __builtin_amdgcn_mfma_f32_16x16x16bf16_1k(a, b, c, 0, 0, 0)
#define GL(R, D, V, S, I) __builtin_amdgcn_raw_ptr_buffer_load_lds(R, (LAS void*)(size_t)(D), 16, V, S, I, 0)
#define GL4(R, D, V0, V1, V2, V3, S) do { GL(R, D, V0, S, 0); GL(R, D, V1, S, 1024); GL(R, D, V2, S, 2048); GL(R, D, V3, S, 3072); } while (0)
#define GS(R, D, V, S, I) __builtin_amdgcn_raw_ptr_buffer_load_lds(R, (LAS void*)(size_t)(D), 16, V, S, I, 16)
#define GS4(R, D, V, S) do { GS(R, D, V, S, 0); GS(R, D, V, S, 1024); GS(R, D, V, S, 2048); GS(R, D, V, S, 3072); } while (0)
#define RSRC(p, n) __builtin_amdgcn_make_buffer_rsrc((void*)(p), 0, (n), 0x00020000)
#define RLX_AGENT __ATOMIC_RELAXED, __HIP_MEMORY_SCOPE_AGENT
#define ROT1 2
#define OAUX 16
#define WAUX 0
#define FLAGM 0
#define LA2 2
#define CODE_KB 48

constexpr int CH = 4608;
constexpr int FRAG_BYTES = 16 * 128 * CH;
constexpr size_t WS_Z2 = 0, WS_P = FRAG_BYTES, WS_ST = 2 * (size_t)FRAG_BYTES, WS_F1 = WS_ST + 16 * 16 * 128 * 16, WS_F2 = WS_F1 + 256 * 128, WS_EP = WS_F2 + 256 * 128;
constexpr int STG = 34816, STGP = 23040, STGO = 18432, L_O = 0, L_P = 5 * STGO, LDS_BYTES = 163840;
constexpr int L_T = 139264, L_R = L_T + 8192, L_X = 151552, L_BIAS = 159744, L_TAG = L_BIAS + 256, L_DUMMY = 161280;
constexpr unsigned SPIN_CAP = 1u << 16, OOB = 0x80000000u;
#define STAMP(I)
#define DUMP()

DEV unsigned cvt_pk(float lo, float hi) { unsigned r; asm("v_cvt_pk_bf16_f32 %0, %1, %2" : "=v"(r) : "v"(lo), "v"(hi)); return r; }
DEV bf16x8 ld128(unsigned a) { return *(LAS bf16x8*)(size_t)a; }
DEV s16x4 ld64(unsigned a) { return *(LAS s16x4*)(size_t)a; }
DEV s16x4 ldtr(unsigned a) { return __builtin_amdgcn_ds_read_tr16_b64_v4i16((LAS s16x4*)(size_t)a); }
DEV bf16x8 cat(s16x4 a, s16x4 b) { return __builtin_shufflevector(a, b, 0, 1, 2, 3, 4, 5, 6, 7); }
DEV float bflo(unsigned v) { return __uint_as_float(v << 16); }
DEV float bfhi(unsigned v) { return __uint_as_float(v & 0xffff0000u); }
DEV f32x2 gelu2(f32x2 x) {
  const float k1 = -2.0f * 0.7978845608028654f * 1.4426950408889634f, k2 = k1 * 0.044715f;
  f32x2 t = x * (x * x * k2 + k1), e;
  e.x = __builtin_amdgcn_exp2f(t.x); e.y = __builtin_amdgcn_exp2f(t.y);
  e = e + 1.0f;
  e.x = __builtin_amdgcn_rcpf(e.x); e.y = __builtin_amdgcn_rcpf(e.y);
  return x * e;
}
DEV u32x2 gelu_pack(f32x4 v) { const f32x2 a = gelu2((f32x2){v[0], v[1]}), b = gelu2((f32x2){v[2], v[3]}); return (u32x2){cvt_pk(a.x, a.y), cvt_pk(b.x, b.y)}; }
DEV u32x2 gelu_pack_sum(f32x4 v, f32x2& s1, f32x2& s2) {
  const f32x2 a = gelu2((f32x2){v[0], v[1]}), b = gelu2((f32x2){v[2], v[3]});
  s1 += a + b; s2 += a * a; s2 += b * b;
  return (u32x2){cvt_pk(a.x, a.y), cvt_pk(b.x, b.y)};
}

struct TAddr { unsigned w[2], z[2][2], s[2]; };
struct TFrag { bf16x8 wX[4], wY[4]; s16x4 tX[5][2], tY[4][2]; };
template <int WF, int PS, int OFF, bool FIRST, bool SYNC = true>
DEV void t_step(TAddr& A, int dlt, f32x4 (&acc)[4][5], TFrag& F) {
  constexpr int KX = WF, KY = 1 - WF;
  SB(); if (SYNC) BAR(); MEMB(); SB();
#pragma unroll
  for (int i = 0; i < 16; ++i) {
    const int n = i >> 2, m = i & 3;
    if (!FIRST) acc[m][n] = MFMA(cat(F.tY[n][0], F.tY[n][1]), F.wY[m], acc[m][n]);
    SB();
    if (i == 0) F.wX[0] = ld128(A.w[KX] + OFF);
    else if (i == 1) F.tX[0][0] = ldtr(A.z[0][0] + OFF + KX * 9216);
    else if (i == 2) F.tX[0][1] = ldtr(A.z[1][0] + OFF + KX * 9216);
    else if (i < 6) F.wX[i - 2] = ld128(A.w[KX] + OFF + (i - 2) * 2048);
    else if (i < 12) { const int nn = 1 + ((i - 6) >> 1), u = (i - 6) & 1; F.tX[nn][u] = ldtr(A.z[u][nn & 1] + OFF + KX * 9216 + (nn >> 1) * PS); }
    else if (i < 14) F.tX[4][i - 12] = ldtr(A.s[i - 12] + OFF + KX * 9216);
    SB();
  }
#pragma unroll
  for (int i = 0; i < 20; ++i) {
    const int n = i >> 2, m = i & 3;
    acc[m][n] = MFMA(cat(F.tX[n][0], F.tX[n][1]), F.wX[m], acc[m][n]);
    SB();
    if (i == 0) F.wY[0] = ld128(A.w[KY] + OFF);
    else if (i == 1) F.tY[0][0] = ldtr(A.z[0][0] + OFF + KY * 9216);
    else if (i == 2) F.tY[0][1] = ldtr(A.z[1][0] + OFF + KY * 9216);
    else if (i < 6) F.wY[i - 2] = ld128(A.w[KY] + OFF + (i - 2) * 2048);
    else if (i < 12) { const int nn = 1 + ((i - 6) >> 1), u = (i - 6) & 1; F.tY[nn][u] = ldtr(A.z[u][nn & 1] + OFF + KY * 9216 + (nn >> 1) * PS); }
    else if (OFF != 0 && i < 16) {
      const int u = i & 1;
      if (i < 14) { A.w[u] += dlt; A.s[u] += dlt; asm volatile("" : "+v"(A.w[u]), "+v"(A.s[u])); }
      else { A.z[u][0] += dlt; A.z[u][1] += dlt; asm volatile("" : "+v"(A.z[u][0]), "+v"(A.z[u][1])); }
    }
    SB();
  }
  WAITL0();
}
template <int WF>
DEV void t_finish(unsigned lds0, int wt, int lane, f32x4 (&acc)[4][5], TFrag& F) {
#pragma unroll
  for (int h = 0; h < 2; ++h) *(LAS f32x4*)(size_t)(lds0 + L_X + ((wt * 2 + WF) * 2 + h) * 1024 + lane * 16) = acc[2 * (1 - WF) + h][4];
#pragma unroll
  for (int n = 0; n < 4; ++n)
#pragma unroll
    for (int m = 0; m < 4; ++m) acc[m][n] = MFMA(cat(F.tY[n][0], F.tY[n][1]), F.wY[m], acc[m][n]);
  WL0();
  BAR(); MEMB();
#pragma unroll
  for (int h = 0; h < 2; ++h) acc[2 * WF + h][4] += *(LAS f32x4*)(size_t)(lds0 + L_X + ((wt * 2 + 1 - WF) * 2 + h) * 1024 + lane * 16);
}
template <int WF, bool MID, class Mid>
DEV void in_pass(unsigned lds0, int wt, int lane, TAddr& A, f32x4 (&acc)[4][5], Mid mid) {
  TFrag F;
#pragma unroll
  for (int m = 0; m < 4; ++m)
#pragma unroll
    for (int n = 0; n < 5; ++n) acc[m][n] = (f32x4){0, 0, 0, 0};
  int dlt = 2 * STG;
  WAITL0();
  t_step<WF, 64, 0, true>(A, dlt, acc, F);
  for (int k = 0; k < (MID ? 3 : 4); ++k) {
    t_step<WF, 64, STG, false>(A, dlt, acc, F);
    dlt = -dlt;
    t_step<WF, 64, 0, false>(A, dlt, acc, F);
  }
  if (MID) {
    t_step<WF, 64, STG, false>(A, -2 * STG, acc, F);
    SB(); mid(); SB();
    t_step<WF, 64, 0, false>(A, 0, acc, F);
  }
  t_finish<WF>(lds0, wt, lane, acc, F);
}

struct G3Frag { bf16x8 p[2][5]; s16x4 w[2][2][2], ws[2], ps[3]; };
template <int KH, bool MM>
DEV void g3_stage(f32x4 (&acc)[5][2], const G3Frag& c, G3Frag& n, unsigned aP, unsigned aS, unsigned a0, unsigned a1) {
  constexpr int M0 = KH ? 3 : 0, NS = KH ? 2 : 3, NM = 20 + 2 * NS, NR = 20 + NS;
#pragma unroll
  for (int i = 0; i < NM; ++i) {
    if (MM) {
      if (i >= 10 && i < 10 + 2 * NS) acc[M0 + ((i - 10) >> 1)][i & 1] = MFMA16(c.ws[i & 1], c.ps[(i - 10) >> 1], acc[M0 + ((i - 10) >> 1)][i & 1]);
      else { const int ii = i < 10 ? i : i - 2 * NS, P = ii / 10, m = (ii % 10) >> 1, cc = ii & 1; acc[m][cc] = MFMA(cat(c.w[P][cc][0], c.w[P][cc][1]), c.p[P][m], acc[m][cc]); }
    }
    SB();
#pragma unroll
    for (int r = (i * 3) / 2; r < ((i + 1) * 3) / 2; ++r) {
      if (r < 18) { const int P = r / 9, q = r % 9;
        if (q < 4) n.w[P][q >> 1][q & 1] = ldtr(((q >> 1) ? a1 : a0) + (2 * KH + P) * 4096 + (q & 1) * 2048);
        else n.p[P][q - 4] = ld128(aP + P * 1024 + (q - 4) * 4096); }
      else if (r < 20) n.ws[r - 18] = ldtr(((r - 18) ? a1 : a0) + 16384);
      else if (r < NR) n.ps[r - 20] = ld64(aS + (M0 + r - 20) * 512);
    }
    SB();
  }
}
template <int KH>
DEV void g3_compute(unsigned lds0, int dh, int lane, char* out, int tt, int dd) {
  f32x4 acc[5][2];
#pragma unroll
  for (int m = 0; m < 5; ++m) { acc[m][0] = (f32x4){0, 0, 0, 0}; acc[m][1] = (f32x4){0, 0, 0, 0}; }
  const int g = lane >> 4, qr = (lane >> 2) & 3, p = lane & 3;
  const int rr = 4 * g + qr;
  unsigned bW[2];
#pragma unroll
  for (int c = 0; c < 2; ++c) bW[c] = lds0 + L_O + rr * 128 + (((4 * dh + p) ^ (((rr >> 1) & 1) | (rr & 4))) << 4) + ((c ^ (p & 1)) * 8);
  const unsigned bP = lds0 + L_P + lane * 16 + KH * 2048, bS = lds0 + L_P + 20480 + lane * 8;
  G3Frag f0, f1;
  WAITL0();
  SB(); BAR(); MEMB(); SB();
  g3_stage<KH, false>(acc, f1, f0, bP, bS, bW[0], bW[1]);
  WAITL0();
  unsigned sp = STGP, sw = STGO;
  for (int j = 1; j < 16; j += 2) {
    SB(); BAR(); MEMB(); SB();
    g3_stage<KH, true>(acc, f0, f1, bP + sp, bS + sp, bW[0] + sw, bW[1] + sw);
    sp = sp == 2 * STGP ? 0 : sp + STGP; sw = sw == 4 * STGO ? 0 : sw + STGO;
    WAITL0();
    SB(); BAR(); MEMB(); SB();
    g3_stage<KH, true>(acc, f1, f0, bP + sp, bS + sp, bW[0] + sw, bW[1] + sw);
    sp = sp == 2 * STGP ? 0 : sp + STGP; sw = sw == 4 * STGO ? 0 : sw + STGO;
    WAITL0();
  }
#pragma unroll
  for (int m = 0; m < 5; ++m)
    if ((m >= 3) != KH) { *(LAS f32x4*)(size_t)(lds0 + (dh * 10 + m * 2) * 1024 + lane * 16) = acc[m][0]; *(LAS f32x4*)(size_t)(lds0 + (dh * 10 + m * 2 + 1) * 1024 + lane * 16) = acc[m][1]; }
  WL0();
  BAR(); MEMB();
  auto rO = RSRC(out, 2048 * 1152);
  const bool sw2 = g & 1;
#pragma unroll
  for (int m = 0; m < 5; ++m)
    if ((m >= 3) == KH) {
      const f32x4 a = acc[m][0] + *(LAS f32x4*)(size_t)(lds0 + (dh * 10 + m * 2) * 1024 + lane * 16), b = acc[m][1] + *(LAS f32x4*)(size_t)(lds0 + (dh * 10 + m * 2 + 1) * 1024 + lane * 16);
      const unsigned a0 = cvt_pk(a[0], a[1]), a1 = cvt_pk(a[2], a[3]), b0 = cvt_pk(b[0], b[1]), b1 = cvt_pk(b[2], b[3]);
      const u32x4 v = {sw2 ? b0 : a0, sw2 ? b1 : a1, sw2 ? a0 : b0, sw2 ? a1 : b1};
      __builtin_amdgcn_raw_buffer_store_b128(v, rO, (tt * 80 + m * 16 + (lane & 15)) * 1152 + dd * 128 + dh * 64 + g * 16, 0, OAUX);
    }
}

template <int WF>
DEV void front(unsigned lds0, int wid, int wt, int lane, int ti, int fj, char* ws) {
  const int r16 = lane & 15, g = lane >> 4, qr = (lane >> 2) & 3, p = lane & 3;
  TAddr A;
  A.w[0] = lds0 + (64 * wt + r16) * 128 + ((g ^ (r16 >> 1)) << 4); A.w[1] = A.w[0] ^ 64;
#pragma unroll
  for (int u = 0; u < 2; ++u) {
    const unsigned b = lds0 + 16384 + (16 * u + 4 * g + qr) * 288 + p * 8;
    A.z[u][0] = b + WF * 128; A.z[u][1] = b + WF * 128 + 32; A.s[u] = b + 256;
  }
  asm volatile("" : "+v"(A.w[0]), "+v"(A.w[1]), "+v"(A.s[0]), "+v"(A.s[1]), "+v"(A.z[0][0]), "+v"(A.z[0][1]), "+v"(A.z[1][0]), "+v"(A.z[1][1]));
  f32x4 acc[4][5];
  in_pass<WF, false>(lds0, wt, lane, A, acc, []() {});
  STAMP(2);
  u32x2 ua[4][4], ub[2];
  {
    f32x2 s1[4], s2[4];
#pragma unroll
    for (int m = 0; m < 4; ++m) {
      s1[m] = (f32x2){0.f, 0.f}; s2[m] = (f32x2){0.f, 0.f};
#pragma unroll
      for (int n = 0; n < 4; ++n) ua[m][n] = gelu_pack_sum(acc[m][n], s1[m], s2[m]);
      if ((m >> 1) == WF) ub[m & 1] = gelu_pack_sum(acc[m][4], s1[m], s2[m]);
    }
#pragma unroll
    for (int m = 0; m < 4; ++m) *(LAS f32x2*)(size_t)(lds0 + L_T + (64 * wt + 16 * m + r16) * 64 + (WF * 4 + g) * 8) = (f32x2){s1[m].x + s1[m].y, s2[m].x + s2[m].y};
  }
#pragma unroll
  for (int m = 0; m < 4; ++m) asm volatile("" : "+v"(ua[m][0]), "+v"(ua[m][1]), "+v"(ua[m][2]), "+v"(ua[m][3]));
  asm volatile("" : "+v"(ub[0]), "+v"(ub[1]));
  STAMP(3);
  in_pass<WF, true>(lds0, wt, lane, A, acc, [&]() {
    STAMP(4);
#pragma unroll
  for (int m = 0; m < 4; ++m) asm volatile("" : "+v"(ua[m][0]), "+v"(ua[m][1]), "+v"(ua[m][2]), "+v"(ua[m][3]));
  asm volatile("" : "+v"(ub[0]), "+v"(ub[1]));
  int lz = lane; asm volatile("" : "+v"(lz));
  {
    const int r16 = lz & 15, g = lz >> 4;
    auto rZ = RSRC(ws + WS_Z2, FRAG_BYTES);
    const unsigned sl = g * 16 + (r16 ^ (4 * (g >> 1)));
    const bool sw = g & 1;
#pragma unroll
    for (int m = 0; m < 4; ++m) {
      const f32x4 q4 = *(LAS f32x4*)(size_t)(lds0 + L_R + (64 * wt + 16 * m + r16) * 32) + *(LAS f32x4*)(size_t)(lds0 + L_R + (64 * wt + 16 * m + r16) * 32 + 16);
      const float mean = (q4[0] + q4[2]) * (1.0f / 2304.0f), var = fmaxf((q4[1] + q4[3]) * (1.0f / 2304.0f) - mean * mean, 0.f);
      const float rs = __builtin_amdgcn_rsqf(var + 1e-5f), sh = -mean * rs;
      auto norm = [&](unsigned v) { const f32x2 y = (f32x2){bflo(v), bfhi(v)} * rs + sh; return cvt_pk(y.x, y.y); };
      const unsigned dst = (fj * 128 + ti * 8 + wt * 4 + m) * CH;
#pragma unroll
      for (int pp = 0; pp < 2; ++pp) {
        const u32x2 a = sw ? ua[m][2 * pp + 1] : ua[m][2 * pp], b = sw ? ua[m][2 * pp] : ua[m][2 * pp + 1];
        const u32x4 v = {norm(a.x), norm(a.y), norm(b.x), norm(b.y)};
        __builtin_amdgcn_raw_buffer_store_b128(v, rZ, dst + sl * 16 + WF * 2048 + pp * 1024, 0, 16);
        ua[m][2 * pp] = (u32x2){v.x, v.y}; ua[m][2 * pp + 1] = (u32x2){v.z, v.w};
      }
      if ((m >> 1) == WF) {
        const u32x2 v = {norm(ub[m & 1].x), norm(ub[m & 1].y)};
        __builtin_amdgcn_raw_buffer_store_b64(v, rZ, dst + sl * 8 + 4096, 0, 16);
        ub[m & 1] = v;
      }
    }
  }
    STAMP(5);
  });
#pragma unroll
  for (int m = 0; m < 4; ++m) asm volatile("" : "+v"(acc[m][0]), "+v"(acc[m][1]), "+v"(acc[m][2]), "+v"(acc[m][3]), "+v"(acc[m][4]));
  const u32x2 tg = *(LAS u32x2*)(size_t)(lds0 + L_TAG);
  const u64 tag = ((u64)(unsigned)__builtin_amdgcn_readfirstlane(tg.y) << 32) | (unsigned)__builtin_amdgcn_readfirstlane(tg.x);
  {
    const unsigned ld = lds0 + wt * STG + 16384, sl = g * 16 + (r16 ^ (4 * (g >> 1)));
#pragma unroll
    for (int m = 0; m < 4; ++m) {
#pragma unroll
      for (int pp = 0; pp < 2; ++pp) *(LAS u32x4*)(size_t)(ld + m * CH + WF * 2048 + pp * 1024 + sl * 16) = (u32x4){ua[m][2 * pp].x, ua[m][2 * pp].y, ua[m][2 * pp + 1].x, ua[m][2 * pp + 1].y};
      if ((m >> 1) == WF) *(LAS u32x2*)(size_t)(ld + m * CH + 4096 + sl * 8) = ub[m & 1];
    }
  }
  STAMP(7);
  SB();
  u32x2 za[4][4], zb[2];
#pragma unroll
  for (int m = 0; m < 4; ++m) {
#pragma unroll
    for (int n = 0; n < 4; ++n) za[m][n] = gelu_pack(acc[m][n]);
    if ((m >> 1) == WF) zb[m & 1] = gelu_pack(acc[m][4]);
    asm volatile("" : "+v"(za[m][0]), "+v"(za[m][1]), "+v"(za[m][2]), "+v"(za[m][3]));
    if (m == FLAGM) {
      SB();
      asm volatile("s_waitcnt vmcnt(0) lgkmcnt(0)" ::: "memory");
      if (lane == 0) __hip_atomic_store((u64*)(ws + WS_F1 + (fj * 16 + ti) * 128) + wid, tag, RLX_AGENT);
      BAR();
      SB();
    }
  }
  asm volatile("" : "+v"(zb[0]), "+v"(zb[1]));
  STAMP(6);
#pragma unroll
  for (int u = 0; u < 2; ++u) {
    const unsigned sl = p * 16 + 8 * (g & 1) + 4 * (u ^ (p >> 1)) + qr;
#pragma unroll
    for (int h = 0; h < 2; ++h) A.z[u][h] = lds0 + 16384 + (g >> 1) * CH + WF * 2048 + sl * 16 + ((h ^ (p & 1)) * 8);
    A.s[u] = lds0 + 16384 + (g >> 1) * CH + 4096 + sl * 8;
  }
  asm volatile("" : "+v"(A.s[0]), "+v"(A.s[1]), "+v"(A.z[0][0]), "+v"(A.z[0][1]), "+v"(A.z[1][0]), "+v"(A.z[1][1]));
#pragma unroll
  for (int m = 0; m < 4; ++m) {
    const float b = __uint_as_float((unsigned)*(LAS u16*)(size_t)(lds0 + L_BIAS + (wt * 64 + m * 16 + r16) * 2) << 16);
#pragma unroll
    for (int n = 0; n < 5; ++n) { const float b0 = (n < 4 || (m >> 1) == WF) ? b : 0.f; acc[m][n] = (f32x4){b0, b0, b0, b0}; }
  }
  TFrag F;
  int dlt = 2 * STG;
  WAITL0();
  t_step<WF, 1024, 0, true, false>(A, dlt, acc, F);
  t_step<WF, 1024, STG, false, false>(A, dlt, acc, F);
  STAMP(8);
  for (int k = 0; k < 15; ++k) {
    dlt = -dlt;
    t_step<WF, 1024, 0, false>(A, dlt, acc, F);
    t_step<WF, 1024, STG, false>(A, dlt, acc, F);
  }
  t_finish<WF>(lds0, wt, lane, acc, F);
  STAMP(10);
  auto rP = RSRC(ws + WS_P, FRAG_BYTES);
  auto gate = [&](f32x4 a, u32x2 z) {
    const f32x2 x = (f32x2){a[0], a[1]} * (f32x2){bflo(z.x), bfhi(z.x)}, y = (f32x2){a[2], a[3]} * (f32x2){bflo(z.y), bfhi(z.y)};
    return (u32x2){cvt_pk(x.x, x.y), cvt_pk(y.x, y.y)};
  };
#pragma unroll
  for (int m = 0; m < 4; ++m) {
    const unsigned dst = ((ti * 8 + wt * 4 + m) * 16 + fj) * CH;
#pragma unroll
    for (int pp = 0; pp < 2; ++pp) {
      const u32x2 a = gate(acc[m][2 * pp], za[m][2 * pp]), b = gate(acc[m][2 * pp + 1], za[m][2 * pp + 1]);
      __builtin_amdgcn_raw_buffer_store_b128((u32x4){a.x, a.y, b.x, b.y}, rP, dst + lane * 16 + (2 * WF + pp) * 1024, 0, 16);
    }
    if ((m >> 1) == WF) __builtin_amdgcn_raw_buffer_store_b64(gate(acc[m][4], zb[m & 1]), rP, dst + lane * 8 + 4096, 0, 16);
  }
  STAMP(11);
  WV(0);
  if (lane == 0) __hip_atomic_store((u64*)(ws + WS_F2 + (ti * 16 + fj) * 128) + wid, tag, RLX_AGENT);
  BAR();
  STAMP(12);
}

DEV u64 poll16(const char* base, u64 tag, int lane) {
  const u64 v = __hip_atomic_load((const u64*)(base + (lane >> 2) * 128) + (lane & 3), RLX_AGENT);
  return __builtin_amdgcn_ballot_w64(v == tag);
}
DEV bool got(u64 mask, int who) { return ((unsigned)(mask >> (4 * who)) & 15u) == 15u; }

__global__ __launch_bounds__(512, 2) void gmlp_fused(const char* __restrict__ X, const char* __restrict__ Win, const char* __restrict__ Wsp, const u16* __restrict__ bias,
                                                     const char* __restrict__ Wo, char* __restrict__ out, char* __restrict__ ws) {
  extern __shared__ __attribute__((aligned(16))) char smem[];
  u64 pc0; asm volatile("s_getpc_b64 %0" : "=s"(pc0));
  const int tid = threadIdx.x, wid = __builtin_amdgcn_readfirstlane(tid >> 6), lane = tid & 63;
  const int bid = blockIdx.x, xg = bid & 7, rk = bid >> 3, il = rk & 7, jl = rk >> 3, ti = (xg & 1) * 8 + il, fj = (xg >> 1) * 4 + jl;
  const int L3 = xg * 30 + rk, tt = L3 / 9, dd = L3 - tt * 9;
  const bool has3 = rk < 30 && L3 < 234;
  const unsigned lds0 = (unsigned)(size_t)(LAS char*)smem;
  STAMP(0);
  if (__builtin_expect(wid >= 4, 1)) {
    __builtin_amdgcn_s_setprio(3);
    const int lw = wid - 4;
    auto rX = RSRC(X, 2048 * 1152); auto rWi = RSRC(Win, 576 * 9216);
    unsigned vX[4], vW[5];
    const unsigned zp0 = lw < 2 ? lw * 5 : 2 + lw * 4;
    const unsigned sX = (ti * 128 + lw * 32) * 1152, sW = fj * 288;
    const int rot = ROT1 == 0 ? 0 : ROT1 == 1 ? il + 2 * jl : 3 * ((il + jl) % 3);
    auto k1 = [&](int k) { int kk = k + rot; kk = kk >= 9 ? kk - 9 : kk; kk = kk >= 9 ? kk - 9 : kk; return kk; };
    auto issue1 = [&](int half, int k) {
      const int kk = k1(k);
      const unsigned d = lds0 + (k & 3) * STG, dz = d + 16384 + zp0 * 1024, sw = sW + half * 4608 + kk * 64 * 9216;
      GL4(rX, d + lw * 4096, vX[0], vX[1], vX[2], vX[3], sX + kk * 128);
      GL4(rWi, dz, vW[0], vW[1], vW[2], vW[3], sw);
      GL(rWi, lw < 2 ? dz + 4096 : lds0 + L_DUMMY, vW[4], sw, 0);
    };
    {
#pragma unroll
      for (int q = 0; q < 4; ++q) { const int row = 8 * q + (lane >> 3); vX[q] = row * 1152 + (((lane & 7) ^ ((row >> 1) & 7)) << 4) - q * 1024; }
      GL4(rX, lds0 + lw * 4096, vX[0], vX[1], vX[2], vX[3], sX + k1(0) * 128);
      SB();
#pragma unroll
      for (int q = 0; q < 5; ++q) {
        const unsigned c16 = (zp0 + q) * 64 + lane, R = (c16 * 3641u) >> 16, c = (c16 - R * 18) * 16, k = (R & 32) + 8 * ((R >> 2) & 3) + 4 * ((R >> 4) & 1) + (R & 3);
        vW[q] = k * 9216 + c - (q & 3) * 1024;
      }
      if (lw >= 2) vW[4] = OOB;
      const unsigned dz = lds0 + 16384 + zp0 * 1024, sw = sW + 4608 + k1(0) * 64 * 9216;
      GL4(rWi, dz, vW[0], vW[1], vW[2], vW[3], sw);
      GL(rWi, lw < 2 ? dz + 4096 : lds0 + L_DUMMY, vW[4], sw, 0);
    }
    const unsigned vL = lane * 16;
    auto rWs = RSRC(Wsp, 2048 * 4096); auto rWo = RSRC(Wo, 2304 * 1152);
    issue1(1, 1);
    WV(9); BAR();
    STAMP(1);
    issue1(1, 2);
    WV(9);
    for (int k = 1; k < 5; ++k) { BAR(); issue1(1, k + 2); WV(9); }
    BAR(); issue1(1, 7); issue1(1, 8); WV(18);
    BAR(); issue1(0, 1); WV(18);
    BAR(); issue1(0, 2); WV(18);
    BAR(); issue1(0, 3);
    const u64 ep = __hip_atomic_load((const u64*)(ws + WS_EP), RLX_AGENT);
    if (lw == 0) __builtin_amdgcn_raw_ptr_buffer_load_lds(RSRC(bias, 4096), (LAS void*)(size_t)(lds0 + L_BIAS), 4, lane * 4, ti * 256, 0, 0);
    BAR();
    issue1(0, 0);
#pragma unroll
    for (int q = 0; q < 8; ++q) __builtin_amdgcn_raw_ptr_buffer_load_lds(rWs, (LAS void*)(size_t)(lds0 + L_DUMMY), 16, vL, bid * 32768 + lw * 8192 + q * 1024, 0, WAUX);
    GL(rWo, lds0 + L_DUMMY, vL, bid * 10368 + lw * 2592, 0); GL(rWo, lds0 + L_DUMMY, vL, bid * 10368 + lw * 2592 + 1024, 0); GL(rWo, lds0 + L_DUMMY, vL, bid * 10368 + lw * 2592 + 1568, 0);
    const unsigned tagA = __builtin_amdgcn_readfirstlane((unsigned)ep) + 0x9E3779B9u, tagB = __builtin_amdgcn_readfirstlane((unsigned)(ep >> 32)) + 0x85EBCA6Bu;
    const u64 tag = ((u64)tagB << 32) | tagA;
    if (lw == 0) { const u32x2 tg = {tagA, tagB}; asm volatile("ds_write_b64 %0, %1\n\ts_waitcnt lgkmcnt(0)" :: "v"(lds0 + L_TAG), "v"(tg) : "memory"); }
    unsigned vS[4];
#pragma unroll
    for (int q = 0; q < 4; ++q) { const int row = 8 * q + (lane >> 3); vS[q] = row * 4096 + (((lane & 7) ^ ((row >> 1) & 7)) << 4) - q * 1024; }
    const unsigned sWs = (ti * 128 + lw * 32) * 4096;
    auto kpos = [&](int n) { return n < 2 ? 2 * ti + n : (n - 2 < 2 * ti ? n - 2 : n); };
    auto issueW = [&](int n) { GL4(rWs, lds0 + (n & 3) * STG + lw * 4096, vS[0], vS[1], vS[2], vS[3], sWs + kpos(n) * 128); };
    WV(11);
    BAR();
    STAMP(2);
    auto rS = RSRC(ws + WS_ST, 16 * 16 * 128 * 16);
    if (lane < 32) {
      const int row = lw * 32 + lane;
      f32x4 v0, v1, v2, v3;
      asm volatile("ds_read_b128 %0, %4\n\tds_read_b128 %1, %4 offset:16\n\tds_read_b128 %2, %4 offset:32\n\tds_read_b128 %3, %4 offset:48\n\ts_waitcnt lgkmcnt(0)"
                   : "=&v"(v0), "=&v"(v1), "=&v"(v2), "=&v"(v3) : "v"(lds0 + L_T + row * 64) : "memory");
      const f32x4 s = (v0 + v1) + (v2 + v3);
      u32x4 gq; gq.x = tagA; gq.y = __float_as_uint(s[0] + s[2]); gq.z = tagB; gq.w = __float_as_uint(s[1] + s[3]);
      __builtin_amdgcn_raw_buffer_store_b128(gq, rS, ((ti * 16 + fj) * 128 + row) * 16, 0, 16);
    }
    u32x4 sv[8];
    auto sweep = [&]() {
      MEMB();
#pragma unroll
      for (int p = 0; p < 8; ++p) sv[p] = __builtin_amdgcn_raw_buffer_load_b128(rS, lane * 16 + (p & 1) * 1024, (ti * 16 + lw * 4 + (p >> 1)) * 2048, 16);
      SB();
    };
    BAR(); issue1(0, 4);
    BAR(); issue1(0, 5);
    BAR(); issue1(0, 6); WV(18);
    BAR(); sweep(); issue1(0, 7); WV(9 + 8 + 9);
    BAR(); issue1(0, 8); WV(18);
    BAR();
    {
      f32x4 s;
      for (unsigned spins = 0;; ++spins) {
        bool ok = true;
        s = (f32x4){0.f, 0.f, 0.f, 0.f};
#pragma unroll
        for (int p = 0; p < 8; ++p) { ok &= (sv[p].x == tagA) & (sv[p].z == tagB); s[(p & 1) * 2] += __uint_as_float(sv[p].y); s[(p & 1) * 2 + 1] += __uint_as_float(sv[p].w); }
        if (__all(ok)) break;
        if (spins > SPIN_CAP) { s[0] = s[2] = __builtin_nanf(""); break; }
        sweep();
      }
      const f32x2 o0 = {s[0], s[1]}, o1 = {s[2], s[3]};
      asm volatile("ds_write_b64 %0, %1\n\tds_write_b64 %0, %2 offset:2048\n\ts_waitcnt lgkmcnt(0)" :: "v"(lds0 + L_R + lane * 32 + lw * 8), "v"(o0), "v"(o1) : "memory");
    }
    STAMP(3);
    issueW(1); WV(9 + 4);
    BAR(); WV(4);
    BAR();
    BAR();
    issueW(0);
    WV(0);
    BAR();
    auto rZ = RSRC(ws + WS_Z2, FRAG_BYTES);
    const unsigned sZ = fj * 128 * CH + zp0 * 1024, vZ5 = lw < 2 ? vL : OOB;
    u64 ready = 0;
    auto issueZ = [&](int n) {
      const int kp = kpos(n);
      if (__builtin_expect(!got(ready, kp >> 1), 0)) { for (unsigned spins = 0; !got(ready, kp >> 1) && spins < SPIN_CAP; ++spins) ready = poll16(ws + WS_F1 + fj * 16 * 128, tag, lane); }
      MEMB();
      const unsigned dz = lds0 + (n & 3) * STG + 16384 + zp0 * 1024, so = sZ + kp * 18432;
      GS4(rZ, dz, vL, so);
      GS(rZ, lw < 2 ? dz + 4096 : lds0 + L_DUMMY, vZ5, so + 4096, 0);
    };
    issueW(2); issueW(3);
    issueZ(2); issueZ(3);
    STAMP(4);
    WV(5);
    BAR();
    STAMP(5);
    for (int n = 4; n < 31; ++n) {
      issueW(n); issueZ(n);
      WV(9);
      BAR();
    }
    issueW(31); issueZ(31);
    constexpr int TAIL = 9;
    unsigned vO[2], vOh;
#pragma unroll
    for (int q = 0; q < 2; ++q) { const int row = 36 * lw + 8 * q + (lane >> 3); vO[q] = row * 1152 + (((lane & 7) ^ (((row >> 1) & 1) | (row & 4))) << 4); }
    { const int row = 36 * lw + 32 + ((lane >> 3) & 3); vOh = row * 1152 + (((lane & 7) ^ (((row >> 1) & 1) | (row & 4))) << 4); }
    const unsigned vO1 = vO[1] - 1024, vO2 = vO[0] + 16 * 1152 - 2048, vO3 = vO[1] + 16 * 1152 - 3072;
    const unsigned sO = min(dd, 8) * 128;
    auto issueO = [&](int n, int slot) {
      const unsigned d = lds0 + L_O + slot * STGO + lw * 4608, s0 = sO + n * 144 * 1152;
      GL4(rWo, d, vO[0], vO1, vO2, vO3, s0);
      if (lane < 32) GL(rWo, d + 4096, vOh, s0, 0);
    };
    issueO(0, 0);
    WV(9 + 5); BAR();
    issueO(1, 1); issueO(2, 2);
    WV(15); BAR();
    BAR(); BAR();
    if (!has3) { WV(0); return; }
    issueO(3, 3); issueO(4, 4);
    auto rP = RSRC(ws + WS_P, FRAG_BYTES);
    const unsigned vPs = lw < 3 ? (lane >> 5) * 16 * CH + (lane & 31) * 16 + 4096 : OOB;
    const int limS = lw == 2 ? 32 : 64;
    const unsigned sP = tt * 5 * 16 * CH;
    const int pa = (tt * 80) >> 7, pb = min((tt * 80 + 79) >> 7, 15);
    u64 rdy = 0;
    auto issueP = [&](int j, int slot) {
      if (__builtin_expect(!got(rdy, j), 0)) { for (unsigned spins = 0; !got(rdy, j) && spins < SPIN_CAP; ++spins) rdy = poll16(ws + WS_F2 + pa * 16 * 128, tag, lane) & poll16(ws + WS_F2 + pb * 16 * 128, tag, lane); }
      MEMB();
      const unsigned d = lds0 + L_P + slot * STGP, sp = sP + j * CH;
      GS4(rP, d + lw * 4096, vL, sp + lw * 16 * CH);
      GS(rP, d + 16384 + lw * 1024, vL, sp + 64 * CH + lw * 1024, 0);
      if (lane < limS) GS(rP, lw < 3 ? d + 20480 + lw * 1024 : lds0 + L_DUMMY, vPs, sp + lw * 32 * CH, 0);
    };
    issueP(0, 0);
    STAMP(6);
    issueP(1, 1); issueP(2, 2);
    WV(12);
    BAR();
    STAMP(7);
    WV(6);
    BAR(); issueP(3, 0); issueO(5, 0); WV(11);
    int sp3 = 1, so5 = 1;
    for (int n = 2; n < 12; ++n) {
      BAR();
      issueP(n + 2, sp3); issueO(n + 4, so5);
      sp3 = sp3 == 2 ? 0 : sp3 + 1; so5 = so5 == 4 ? 0 : so5 + 1;
      WV(16);
    }
    BAR(); issueP(14, 2); WV(6);
    BAR(); issueP(15, 0); WV(6);
    BAR(); WV(0);
    BAR();
    BAR(); BAR();
    STAMP(8);
    DUMP();
    if (bid == 0 && tid == 256) __hip_atomic_store((u64*)(ws + WS_EP), tag, RLX_AGENT);
  } else {
    const int wt = wid >> 1, wf = wid & 1;
    if (wid == 0) {
      auto rC = RSRC((const char*)(pc0 & ~(u64)255), CODE_KB * 1024);
      GL(rC, lds0 + L_DUMMY, lane * 16, rk * 1024, 0); GL(rC, lds0 + L_DUMMY, lane * 16, rk * 1024 + 32768, 0);
      WV(0);
    }
    if (wf == 0) front<0>(lds0, wid, wt, lane, ti, fj, ws); else front<1>(lds0, wid, wt, lane, ti, fj, ws);
    if (!has3) return;
    if (wt == 0) g3_compute<0>(lds0, wf, lane, out, tt, dd); else g3_compute<1>(lds0, wf, lane, out, tt, dd);
    STAMP(13);
    DUMP();
  }
}

extern "C" void kernel_launch(void* const* d_in, const int* in_sizes, int n_in, void* d_out, int out_size, void* d_ws, size_t ws_size, hipStream_t stream) {
  (void)hipFuncSetAttribute((const void*)gmlp_fused, hipFuncAttributeMaxDynamicSharedMemorySize, LDS_BYTES);
  gmlp_fused<<<256, 512, LDS_BYTES, stream>>>((const char*)d_in[0], (const char*)d_in[1], (const char*)d_in[2], (const u16*)d_in[3], (const char*)d_in[4], (char*)d_out, (char*)d_ws);
}
```

```cpp
#include <hip/hip_runtime.h>
#include <hip/hip_bf16.h>
typedef unsigned short u16;
typedef unsigned long long u64;
typedef short s16x4 __attribute__((ext_vector_type(4)));
typedef short bf16x8 __attribute__((ext_vector_type(8)));
typedef float f32x2 __attribute__((ext_vector_type(2)));
typedef float f32x4 __attribute__((ext_vector_type(4)));
typedef unsigned u32x4 __attribute__((ext_vector_type(4)));
typedef unsigned u32x2 __attribute__((ext_vector_type(2)));
#define LAS __attribute__((address_space(3)))
#define DEV __device__ __forceinline__
#define BAR() __builtin_amdgcn_s_barrier()
#define SB() __builtin_amdgcn_sched_barrier(0)
#define MEMB() asm volatile("" ::: "memory")
#define WAITL0() __builtin_amdgcn_s_waitcnt(0xC07F)
#define WL0() asm volatile("s_waitcnt lgkmcnt(0)" ::: "memory")
#define WV(N) asm volatile("s_waitcnt vmcnt(%0)" ::"i"(N) : "memory")
#define MFMA(a, b, c) __builtin_amdgcn_mfma_f32_16x16x32_bf16(a, b, c, 0, 0, 0)
#define MFMA16(a, b, c) __builtin_amdgcn_mfma_f32_16x16x16bf16_1k(a, b, c, 0, 0, 0)
#define GL(R, D, V, S, I) __builtin_amdgcn_raw_ptr_buffer_load_lds(R, (LAS void*)(size_t)(D), 16, V, S, I, 0)
#define GL4(R, D, V0, V1, V2, V3, S) do { GL(R, D, V0, S, 0); GL(R, D, V1, S, 1024); GL(R, D, V2, S, 2048); GL(R, D, V3, S, 3072); } while (0)
#define GS(R, D, V, S, I) __builtin_amdgcn_raw_ptr_buffer_load_lds(R, (LAS void*)(size_t)(D), 16, V, S, I, 16)
#define GS4(R, D, V, S) do { GS(R, D, V, S, 0); GS(R, D, V, S, 1024); GS(R, D, V, S, 2048); GS(R, D, V, S, 3072); } while (0)
#define RSRC(p, n) __builtin_amdgcn_make_buffer_rsrc((void*)(p), 0, (n), 0x00020000)
#define RLX_AGENT __ATOMIC_RELAXED, __HIP_MEMORY_SCOPE_AGENT
#define ROT1 2
#define BSH 1
#define LS0 1
#define OAUX 16
#define WAUX 0
#define FLAGM 0
#define LA2 2
#define CODE_KB 53

constexpr int CH = 4608;
constexpr int FRAG_BYTES = 16 * 128 * CH;
constexpr size_t WS_Z2 = 0, WS_P = FRAG_BYTES, WS_ST = 2 * (size_t)FRAG_BYTES, WS_F1 = WS_ST + 16 * 16 * 128 * 16, WS_F2 = WS_F1 + 256 * 128, WS_EP = WS_F2 + 256 * 128;
constexpr int STG = 34816, STGP = 23040, STGO = 18432, L_O = 0, L_P = 5 * STGO, LDS_BYTES = 163840;
constexpr int L_T = 139264, L_R = L_T + 8192, L_X = 151552, L_BIAS = 159744, L_TAG = L_BIAS + 256, L_DUMMY = 161280;
constexpr unsigned SPIN_CAP = 1u << 16, OOB = 0x80000000u;
#define STAMP(I)
#define DUMP()

DEV unsigned cvt_pk(float lo, float hi) { unsigned r; asm("v_cvt_pk_bf16_f32 %0, %1, %2" : "=v"(r) : "v"(lo), "v"(hi)); return r; }
DEV bf16x8 ld128(unsigned a) { return *(LAS bf16x8*)(size_t)a; }
DEV s16x4 ld64(unsigned a) { return *(LAS s16x4*)(size_t)a; }
DEV s16x4 ldtr(unsigned a) { return __builtin_amdgcn_ds_read_tr16_b64_v4i16((LAS s16x4*)(size_t)a); }
DEV bf16x8 cat(s16x4 a, s16x4 b) { return __builtin_shufflevector(a, b, 0, 1, 2, 3, 4, 5, 6, 7); }
DEV float bflo(unsigned v) { return __uint_as_float(v << 16); }
DEV float bfhi(unsigned v) { return __uint_as_float(v & 0xffff0000u); }
DEV f32x2 gelu2(f32x2 x) {
  const float k1 = -2.0f * 0.7978845608028654f * 1.4426950408889634f, k2 = k1 * 0.044715f;
  f32x2 t = x * (x * x * k2 + k1), e;
  e.x = __builtin_amdgcn_exp2f(t.x); e.y = __builtin_amdgcn_exp2f(t.y);
  e = e + 1.0f;
  e.x = __builtin_amdgcn_rcpf(e.x); e.y = __builtin_amdgcn_rcpf(e.y);
  return x * e;
}
DEV u32x2 gelu_pack(f32x4 v) { const f32x2 a = gelu2((f32x2){v[0], v[1]}), b = gelu2((f32x2){v[2], v[3]}); return (u32x2){cvt_pk(a.x, a.y), cvt_pk(b.x, b.y)}; }
DEV u32x2 gelu_pack_sum(f32x4 v, f32x2& s1, f32x2& s2) {
  const f32x2 a = gelu2((f32x2){v[0], v[1]}), b = gelu2((f32x2){v[2], v[3]});
  s1 += a + b; s2 += a * a; s2 += b * b;
  return (u32x2){cvt_pk(a.x, a.y), cvt_pk(b.x, b.y)};
}

struct TAddr { unsigned w[2], z[2][2], s[2]; };
struct TFrag { bf16x8 wX[4], wY[4]; s16x4 tX[5][2], tY[4][2]; };
template <int WF, int PS, int OFF, bool FIRST, bool SYNC = true>
DEV void t_step(TAddr& A, int dlt, f32x4 (&acc)[4][5], TFrag& F) {
  constexpr int KX = WF, KY = 1 - WF;
  SB(); if (SYNC) BAR(); MEMB(); SB();
  auto rdz = [&](s16x4 (&t)[2], int n, int u, int K) { t[u] = n < 4 ? ldtr(A.z[u][n & 1] + OFF + K * 9216 + (n >> 1) * PS) : ldtr(A.s[u] + OFF + K * 9216); };
#pragma unroll
  for (int i = 0; i < 16; ++i) {
    const int m = i >> 2, n = i & 3;
    if (!FIRST) acc[m][n] = MFMA(cat(F.tY[n][0], F.tY[n][1]), F.wY[m], acc[m][n]);
    SB();
    if (i == 0) F.wX[0] = ld128(A.w[KX] + OFF);
    else if (i < 11) rdz(F.tX[(i - 1) >> 1], (i - 1) >> 1, (i - 1) & 1, KX);
    else if (i < 14) F.wX[i - 10] = ld128(A.w[KX] + OFF + (i - 10) * 2048);
    SB();
  }
#pragma unroll
  for (int i = 0; i < 20; ++i) {
    const int m = i / 5, n = i % 5;
    acc[m][n] = MFMA(cat(F.tX[n][0], F.tX[n][1]), F.wX[m], acc[m][n]);
    SB();
    if (i == 0) F.wY[0] = ld128(A.w[KY] + OFF);
    else if (i < 9) rdz(F.tY[(i - 1) >> 1], (i - 1) >> 1, (i - 1) & 1, KY);
    else if (i < 12) F.wY[i - 8] = ld128(A.w[KY] + OFF + (i - 8) * 2048);
    else if (OFF != 0 && i < 16) {
      const int u = i & 1;
      if (i < 14) { A.w[u] += dlt; A.s[u] += dlt; asm volatile("" : "+v"(A.w[u]), "+v"(A.s[u])); }
      else { A.z[u][0] += dlt; A.z[u][1] += dlt; asm volatile("" : "+v"(A.z[u][0]), "+v"(A.z[u][1])); }
    }
    SB();
  }
  WAITL0();
}
template <int WF>
DEV void t_finish(unsigned lds0, int wt, int lane, f32x4 (&acc)[4][5], TFrag& F) {
#pragma unroll
  for (int h = 0; h < 2; ++h) *(LAS f32x4*)(size_t)(lds0 + L_X + ((wt * 2 + WF) * 2 + h) * 1024 + lane * 16) = acc[2 * (1 - WF) + h][4];
#pragma unroll
  for (int n = 0; n < 4; ++n)
#pragma unroll
    for (int m = 0; m < 4; ++m) acc[m][n] = MFMA(cat(F.tY[n][0], F.tY[n][1]), F.wY[m], acc[m][n]);
  WL0();
  BAR(); MEMB();
#pragma unroll
  for (int h = 0; h < 2; ++h) acc[2 * WF + h][4] += *(LAS f32x4*)(size_t)(lds0 + L_X + ((wt * 2 + 1 - WF) * 2 + h) * 1024 + lane * 16);
}
template <int WF, bool MID, class Mid>
DEV void in_pass(unsigned lds0, int wt, int lane, TAddr& A, f32x4 (&acc)[4][5], Mid mid) {
  TFrag F;
#pragma unroll
  for (int m = 0; m < 4; ++m)
#pragma unroll
    for (int n = 0; n < 5; ++n) acc[m][n] = (f32x4){0, 0, 0, 0};
  int dlt = 2 * STG;
  WAITL0();
  t_step<WF, 64, 0, true>(A, dlt, acc, F);
  for (int k = 0; k < (MID ? 3 : 4); ++k) {
    t_step<WF, 64, STG, false>(A, dlt, acc, F);
    dlt = -dlt;
    t_step<WF, 64, 0, false>(A, dlt, acc, F);
  }
  if (MID) {
    t_step<WF, 64, STG, false>(A, -2 * STG, acc, F);
    SB(); mid(); SB();
    t_step<WF, 64, 0, false>(A, 0, acc, F);
  }
  t_finish<WF>(lds0, wt, lane, acc, F);
}

struct G3P { bf16x8 p[2][5]; s16x4 ps[3]; };
struct G3W { s16x4 w[2][2][2], ws[2]; };
template <int KH>
DEV void g3_readw(G3W& n, unsigned a0, unsigned a1) {
#pragma unroll
  for (int q = 0; q < 8; ++q) n.w[q >> 2][(q >> 1) & 1][q & 1] = ldtr((((q >> 1) & 1) ? a1 : a0) + (2 * KH + (q >> 2)) * 4096 + (q & 1) * 2048);
  n.ws[0] = ldtr(a0 + 16384); n.ws[1] = ldtr(a1 + 16384);
}
template <int KH, bool MM, bool RDW>
DEV void g3_stage(f32x4 (&acc)[5][2], const G3P& cp, const G3W& cw, G3P& np, G3W& nw, unsigned aP, unsigned aS, unsigned a0, unsigned a1) {
  constexpr int M0 = KH ? 3 : 0, NS = KH ? 2 : 3, NM = 20 + 2 * NS, NR = 20 + NS;
  SB(); BAR(); MEMB(); SB();
#pragma unroll
  for (int i = 0; i < NM; ++i) {
    if (MM) {
      if (i >= 10 && i < 10 + 2 * NS) acc[M0 + ((i - 10) >> 1)][i & 1] = MFMA16(cw.ws[i & 1], cp.ps[(i - 10) >> 1], acc[M0 + ((i - 10) >> 1)][i & 1]);
      else { const int ii = i < 10 ? i : i - 2 * NS, P = ii / 10, mm = (ii % 10) >> 1, m = (KH != P) ? (mm + 3) % 5 : mm, cc = ii & 1; acc[m][cc] = MFMA(cat(cw.w[P][cc][0], cw.w[P][cc][1]), cp.p[P][m], acc[m][cc]); }
    }
    SB();
#pragma unroll
    for (int r = (i * 3) / 2; r < ((i + 1) * 3) / 2; ++r) {
      if (r < 18) { const int P = r / 9, q = r % 9;
        if (q < 4) { if (RDW) nw.w[P][q >> 1][q & 1] = ldtr(((q >> 1) ? a1 : a0) + (2 * KH + P) * 4096 + (q & 1) * 2048); }
        else np.p[P][q - 4] = ld128(aP + P * 1024 + (q - 4) * 4096); }
      else if (r < 20) { if (RDW) nw.ws[r - 18] = ldtr(((r - 18) ? a1 : a0) + 16384); }
      else if (r < NR) np.ps[r - 20] = ld64(aS + (M0 + r - 20) * 512);
    }
    SB();
  }
  WAITL0();
}
template <int KH>
DEV void g3_compute(unsigned lds0, int dh, int lane, char* out, int tt, int dd) {
  const int g = lane >> 4, qr = (lane >> 2) & 3, p = lane & 3;
  const int rr = 4 * g + qr;
  unsigned bW[2];
#pragma unroll
  for (int c = 0; c < 2; ++c) bW[c] = lds0 + L_O + rr * 128 + (((4 * dh + p) ^ (((rr >> 1) & 1) | (rr & 4))) << 4) + ((c ^ (p & 1)) * 8);
  const unsigned bP = lds0 + L_P + lane * 16 + KH * 2048, bS = lds0 + L_P + 20480 + lane * 8;
  G3W a0, a1, a2;
  g3_readw<KH>(a0, bW[0], bW[1]); g3_readw<KH>(a1, bW[0] + STGO, bW[1] + STGO); g3_readw<KH>(a2, bW[0] + 2 * STGO, bW[1] + 2 * STGO);
  f32x4 acc[5][2];
#pragma unroll
  for (int m = 0; m < 5; ++m) { acc[m][0] = (f32x4){0, 0, 0, 0}; acc[m][1] = (f32x4){0, 0, 0, 0}; }
  WAITL0();
  SB(); BAR(); SB();
  G3P p0, p1; G3W w0, w1;
  g3_stage<KH, false, false>(acc, p1, a0, p0, w0, bP, bS, 0, 0);
  g3_stage<KH, true, false>(acc, p0, a0, p1, w0, bP + STGP, bS + STGP, 0, 0);
  g3_stage<KH, true, false>(acc, p1, a1, p0, w0, bP + 2 * STGP, bS + 2 * STGP, 0, 0);
  g3_stage<KH, true, true>(acc, p0, a2, p1, w1, bP, bS, bW[0] + 3 * STGO, bW[1] + 3 * STGO);
  unsigned sp = STGP, sw = 4 * STGO;
  for (int j = 4; j < 16; j += 2) {
    g3_stage<KH, true, true>(acc, p1, w1, p0, w0, bP + sp, bS + sp, bW[0] + sw, bW[1] + sw);
    sp = sp == 2 * STGP ? 0 : sp + STGP; sw = sw == 4 * STGO ? 0 : sw + STGO;
    g3_stage<KH, true, true>(acc, p0, w0, p1, w1, bP + sp, bS + sp, bW[0] + sw, bW[1] + sw);
    sp = sp == 2 * STGP ? 0 : sp + STGP; sw = sw == 4 * STGO ? 0 : sw + STGO;
  }
  {
    constexpr int M0 = KH ? 3 : 0, NS = KH ? 2 : 3;
    SB(); BAR(); SB();
#pragma unroll
    for (int i = 0; i < 2 * NS; ++i) { acc[M0 + (i >> 1)][i & 1] = MFMA16(w1.ws[i & 1], p1.ps[i >> 1], acc[M0 + (i >> 1)][i & 1]); SB(); }
#pragma unroll
    for (int i = 0; i < 20; ++i) { const int P = (i >> 1) & 1, m = KH ? i >> 2 : 4 - (i >> 2), cc = i & 1; acc[m][cc] = MFMA(cat(w1.w[P][cc][0], w1.w[P][cc][1]), p1.p[P][m], acc[m][cc]); SB(); }
  }
#pragma unroll
  for (int m = 0; m < 5; ++m)
    if ((m >= 3) != KH) { *(LAS f32x4*)(size_t)(lds0 + (dh * 10 + m * 2) * 1024 + lane * 16) = acc[m][0]; *(LAS f32x4*)(size_t)(lds0 + (dh * 10 + m * 2 + 1) * 1024 + lane * 16) = acc[m][1]; }
  WL0();
  BAR(); MEMB();
  auto rO = RSRC(out, 2048 * 1152);
  const bool sw2 = g & 1;
#pragma unroll
  for (int m = 0; m < 5; ++m)
    if ((m >= 3) == KH) {
      const f32x4 a = acc[m][0] + *(LAS f32x4*)(size_t)(lds0 + (dh * 10 + m * 2) * 1024 + lane * 16), b = acc[m][1] + *(LAS f32x4*)(size_t)(lds0 + (dh * 10 + m * 2 + 1) * 1024 + lane * 16);
      const unsigned a0 = cvt_pk(a[0], a[1]), a1 = cvt_pk(a[2], a[3]), b0 = cvt_pk(b[0], b[1]), b1 = cvt_pk(b[2], b[3]);
      const u32x4 v = {sw2 ? b0 : a0, sw2 ? b1 : a1, sw2 ? a0 : b0, sw2 ? a1 : b1};
      __builtin_amdgcn_raw_buffer_store_b128(v, rO, (tt * 80 + m * 16 + (lane & 15)) * 1152 + dd * 128 + dh * 64 + g * 16, 0, OAUX);
    }
}

template <int WF>
DEV void front(unsigned lds0, int wid, int wt, int lane, int ti, int fj, char* ws) {
  const int r16 = lane & 15, g = lane >> 4, qr = (lane >> 2) & 3, p = lane & 3;
  TAddr A;
  A.w[0] = lds0 + (64 * wt + r16) * 128 + ((g ^ (r16 >> 1)) << 4); A.w[1] = A.w[0] ^ 64;
#pragma unroll
  for (int u = 0; u < 2; ++u) {
    const unsigned b = lds0 + 16384 + (16 * u + 4 * g + qr) * 288 + p * 8;
    A.z[u][0] = b + WF * 128 + (p & 1) * 32; A.z[u][1] = b + WF * 128 + 32 - (p & 1) * 32; A.s[u] = b + 256;
  }
  asm volatile("" : "+v"(A.w[0]), "+v"(A.w[1]), "+v"(A.s[0]), "+v"(A.s[1]), "+v"(A.z[0][0]), "+v"(A.z[0][1]), "+v"(A.z[1][0]), "+v"(A.z[1][1]));
  f32x4 acc[4][5];
  in_pass<WF, false>(lds0, wt, lane, A, acc, []() {});
  STAMP(2);
  u32x2 ua[4][4], ub[2];
  {
    f32x2 s1[4], s2[4];
#pragma unroll
    for (int m = 0; m < 4; ++m) {
      s1[m] = (f32x2){0.f, 0.f}; s2[m] = (f32x2){0.f, 0.f};
#pragma unroll
      for (int n = 0; n < 4; ++n) ua[m][n] = gelu_pack_sum(acc[m][n], s1[m], s2[m]);
      if ((m >> 1) == WF) ub[m & 1] = gelu_pack_sum(acc[m][4], s1[m], s2[m]);
    }
#pragma unroll
    for (int m = 0; m < 4; ++m) *(LAS f32x2*)(size_t)(lds0 + L_T + (64 * wt + 16 * m + r16) * 64 + (WF * 4 + g) * 8) = (f32x2){s1[m].x + s1[m].y, s2[m].x + s2[m].y};
  }
#pragma unroll
  for (int m = 0; m < 4; ++m) asm volatile("" : "+v"(ua[m][0]), "+v"(ua[m][1]), "+v"(ua[m][2]), "+v"(ua[m][3]));
  asm volatile("" : "+v"(ub[0]), "+v"(ub[1]));
#pragma unroll
  for (int u = 0; u < 2; ++u) { A.z[u][0] -= (p & 1) * 32; A.z[u][1] += (p & 1) * 32; }
  asm volatile("" : "+v"(A.z[0][0]), "+v"(A.z[0][1]), "+v"(A.z[1][0]), "+v"(A.z[1][1]));
  STAMP(3);
  in_pass<WF, true>(lds0, wt, lane, A, acc, [&]() {
    STAMP(4);
#pragma unroll
  for (int m = 0; m < 4; ++m) asm volatile("" : "+v"(ua[m][0]), "+v"(ua[m][1]), "+v"(ua[m][2]), "+v"(ua[m][3]));
  asm volatile("" : "+v"(ub[0]), "+v"(ub[1]));
  int lz = lane; asm volatile("" : "+v"(lz));
  {
    const int r16 = lz & 15, g = lz >> 4;
    auto rZ = RSRC(ws + WS_Z2, FRAG_BYTES);
    const unsigned sl = g * 16 + (r16 ^ (4 * (g >> 1)));
#pragma unroll
    for (int m = 0; m < 4; ++m) {
      const f32x4 q4 = *(LAS f32x4*)(size_t)(lds0 + L_R + (64 * wt + 16 * m + r16) * 32) + *(LAS f32x4*)(size_t)(lds0 + L_R + (64 * wt + 16 * m + r16) * 32 + 16);
      const float mean = (q4[0] + q4[2]) * (1.0f / 2304.0f), var = fmaxf((q4[1] + q4[3]) * (1.0f / 2304.0f) - mean * mean, 0.f);
      const float rs = __builtin_amdgcn_rsqf(var + 1e-5f), sh = -mean * rs;
      auto norm = [&](unsigned v) { const f32x2 y = (f32x2){bflo(v), bfhi(v)} * rs + sh; return cvt_pk(y.x, y.y); };
      const unsigned dst = (fj * 128 + ti * 8 + wt * 4 + m) * CH;
#pragma unroll
      for (int pp = 0; pp < 2; ++pp) {
        const u32x2 a = ua[m][2 * pp], b = ua[m][2 * pp + 1];
        const u32x4 v = {norm(a.x), norm(a.y), norm(b.x), norm(b.y)};
        __builtin_amdgcn_raw_buffer_store_b128(v, rZ, dst + sl * 16 + WF * 2048 + pp * 1024, 0, 16);
        ua[m][2 * pp] = (u32x2){v.x, v.y}; ua[m][2 * pp + 1] = (u32x2){v.z, v.w};
      }
      if ((m >> 1) == WF) {
        const u32x2 v = {norm(ub[m & 1].x), norm(ub[m & 1].y)};
        __builtin_amdgcn_raw_buffer_store_b64(v, rZ, dst + sl * 8 + 4096, 0, 16);
        ub[m & 1] = v;
      }
    }
  }
    STAMP(5);
  });
#pragma unroll
  for (int m = 0; m < 4; ++m) asm volatile("" : "+v"(acc[m][0]), "+v"(acc[m][1]), "+v"(acc[m][2]), "+v"(acc[m][3]), "+v"(acc[m][4]));
  const u32x2 tg = *(LAS u32x2*)(size_t)(lds0 + L_TAG);
  const u64 tag = ((u64)(unsigned)__builtin_amdgcn_readfirstlane(tg.y) << 32) | (unsigned)__builtin_amdgcn_readfirstlane(tg.x);
  {
    const unsigned ld = lds0 + wt * STG + 16384, sl = g * 16 + (r16 ^ (4 * (g >> 1)));
#pragma unroll
    for (int m = 0; m < 4; ++m) {
#pragma unroll
      for (int pp = 0; pp < 2; ++pp) *(LAS u32x4*)(size_t)(ld + m * CH + WF * 2048 + pp * 1024 + sl * 16) = (u32x4){ua[m][2 * pp].x, ua[m][2 * pp].y, ua[m][2 * pp + 1].x, ua[m][2 * pp + 1].y};
      if ((m >> 1) == WF) *(LAS u32x2*)(size_t)(ld + m * CH + 4096 + sl * 8) = ub[m & 1];
    }
  }
  STAMP(7);
  SB();
  u32x2 za[4][4], zb[2];
#pragma unroll
  for (int m = 0; m < 4; ++m) {
#pragma unroll
    for (int n = 0; n < 4; ++n) za[m][n] = gelu_pack(acc[m][n]);
    if ((m >> 1) == WF) zb[m & 1] = gelu_pack(acc[m][4]);
    asm volatile("" : "+v"(za[m][0]), "+v"(za[m][1]), "+v"(za[m][2]), "+v"(za[m][3]));
    if (m == FLAGM) {
      SB();
      asm volatile("s_waitcnt vmcnt(0) lgkmcnt(0)" ::: "memory");
      if (lane == 0) __hip_atomic_store((u64*)(ws + WS_F1 + (fj * 16 + ti) * 128) + wid, tag, RLX_AGENT);
      BAR();
      SB();
    }
  }
  asm volatile("" : "+v"(zb[0]), "+v"(zb[1]));
  STAMP(6);
#pragma unroll
  for (int u = 0; u < 2; ++u) {
    const unsigned sl = p * 16 + 8 * (g & 1) + 4 * (u ^ (p >> 1)) + qr;
#pragma unroll
    for (int h = 0; h < 2; ++h) A.z[u][h] = lds0 + 16384 + (g >> 1) * CH + WF * 2048 + sl * 16 + ((h ^ (p & 1)) * 8);
    A.s[u] = lds0 + 16384 + (g >> 1) * CH + 4096 + sl * 8;
  }
  asm volatile("" : "+v"(A.s[0]), "+v"(A.s[1]), "+v"(A.z[0][0]), "+v"(A.z[0][1]), "+v"(A.z[1][0]), "+v"(A.z[1][1]));
#pragma unroll
  for (int m = 0; m < 4; ++m) {
    const float b = __uint_as_float((unsigned)*(LAS u16*)(size_t)(lds0 + L_BIAS + (wt * 64 + m * 16 + r16) * 2) << 16);
#pragma unroll
    for (int n = 0; n < 5; ++n) { const float b0 = (n < 4 || (m >> 1) == WF) ? b : 0.f; acc[m][n] = (f32x4){b0, b0, b0, b0}; }
  }
  TFrag F;
  int dlt = 2 * STG;
  WAITL0();
  t_step<WF, 1024, 0, true, false>(A, dlt, acc, F);
  t_step<WF, 1024, STG, false, false>(A, dlt, acc, F);
  STAMP(8);
  for (int k = 0; k < 15; ++k) {
    dlt = -dlt;
    t_step<WF, 1024, 0, false>(A, dlt, acc, F);
    t_step<WF, 1024, STG, false>(A, dlt, acc, F);
  }
  auto rP = RSRC(ws + WS_P, FRAG_BYTES);
  auto gate = [&](f32x4 a, u32x2 z) {
    const f32x2 x = (f32x2){a[0], a[1]} * (f32x2){bflo(z.x), bfhi(z.x)}, y = (f32x2){a[2], a[3]} * (f32x2){bflo(z.y), bfhi(z.y)};
    return (u32x2){cvt_pk(x.x, x.y), cvt_pk(y.x, y.y)};
  };
#pragma unroll
  for (int h = 0; h < 2; ++h) *(LAS f32x4*)(size_t)(lds0 + L_X + ((wt * 2 + WF) * 2 + h) * 1024 + lane * 16) = acc[2 * (1 - WF) + h][4];
  const unsigned dst0 = (ti * 8 + wt * 4) * 16 * CH + fj * CH;
#pragma unroll
  for (int m = 0; m < 4; ++m)
#pragma unroll
    for (int n = 0; n < 4; ++n) acc[m][n] = MFMA(cat(F.tY[n][0], F.tY[n][1]), F.wY[m], acc[m][n]);
  WL0();
  BAR(); MEMB();
  STAMP(10);
  f32x4 o8[2];
#pragma unroll
  for (int h = 0; h < 2; ++h) o8[h] = *(LAS f32x4*)(size_t)(lds0 + L_X + ((wt * 2 + 1 - WF) * 2 + h) * 1024 + lane * 16);
#pragma unroll
  for (int m = 0; m < 4; ++m)
#pragma unroll
    for (int pp = 0; pp < 2; ++pp) {
      const u32x2 a = gate(acc[m][2 * pp], za[m][2 * pp]), b = gate(acc[m][2 * pp + 1], za[m][2 * pp + 1]);
      __builtin_amdgcn_raw_buffer_store_b128((u32x4){a.x, a.y, b.x, b.y}, rP, dst0 + m * 16 * CH + lane * 16 + (2 * WF + pp) * 1024, 0, 16);
    }
#pragma unroll
  for (int h = 0; h < 2; ++h) __builtin_amdgcn_raw_buffer_store_b64(gate(acc[2 * WF + h][4] + o8[h], zb[h]), rP, dst0 + (2 * WF + h) * 16 * CH + lane * 8 + 4096, 0, 16);
  STAMP(11);
  WV(0);
  if (lane == 0) __hip_atomic_store((u64*)(ws + WS_F2 + (ti * 16 + fj) * 128) + wid, tag, RLX_AGENT);
  SB(); BAR(); MEMB(); SB();
  STAMP(12);
}

DEV u64 poll16(const char* base, u64 tag, int lane) {
  const u64 v = __hip_atomic_load((const u64*)(base + (lane >> 2) * 128) + (lane & 3), RLX_AGENT);
  return __builtin_amdgcn_ballot_w64(v == tag);
}
DEV bool got(u64 mask, int who) { return ((unsigned)(mask >> (4 * who)) & 15u) == 15u; }

__global__ __launch_bounds__(512, 2) void gmlp_fused(const char* __restrict__ X, const char* __restrict__ Win, const char* __restrict__ Wsp, const u16* __restrict__ bias,
                                                     const char* __restrict__ Wo, char* __restrict__ out, char* __restrict__ ws) {
  extern __shared__ __attribute__((aligned(16))) char smem[];
  u64 pc0; asm volatile("s_getpc_b64 %0" : "=s"(pc0));
  const int tid = threadIdx.x, wid = __builtin_amdgcn_readfirstlane(tid >> 6), lane = tid & 63;
  const int bid = blockIdx.x, xg = bid & 7, rk = bid >> 3, il = rk & 7, jl = rk >> 3, ti = (xg & 1) * 8 + il, fj = (xg >> 1) * 4 + jl;
  const int L3 = xg * 30 + rk, tt = L3 / 9, dd = L3 - tt * 9;
  const bool has3 = rk < 30 && L3 < 234;
  const unsigned lds0 = (unsigned)(size_t)(LAS char*)smem;
  STAMP(0);
  if (__builtin_expect(wid >= 4, 1)) {
    __builtin_amdgcn_s_setprio(3);
    const int lw = wid - 4;
    auto rX = RSRC(X, 2048 * 1152); auto rWi = RSRC(Win, 576 * 9216);
    unsigned vX[4], vW[5];
    const unsigned zp0 = lw < 2 ? lw * 5 : 2 + lw * 4;
    const unsigned sX = (ti * 128 + lw * 32) * 1152, sW = fj * 288;
    const int rot = ROT1 == 0 ? 0 : ROT1 == 1 ? il + 2 * jl : 3 * ((il + jl) % 3);
    const int rot8 = ROT1 == 1 ? (il + 2 * jl) & 7 : ROT1 == 2 ? (((il + jl) % 3) * 8) / 3 : 0;
    auto k1 = [&](int k) { return k == 0 ? 0 : 1 + ((k - 1 + rot8) & 7); };
    auto issue1 = [&](int half, int k) {
      const int kk = k1(k);
      const unsigned d = lds0 + (k & 3) * STG, dz = d + 16384 + zp0 * 1024, sw = sW + half * 4608 + kk * 64 * 9216;
      GL4(rX, d + lw * 4096, vX[0], vX[1], vX[2], vX[3], sX + kk * 128);
      GL4(rWi, dz, vW[0], vW[1], vW[2], vW[3], sw);
      GL(rWi, lw < 2 ? dz + 4096 : lds0 + L_DUMMY, vW[4], sw, 0);
    };
    {
#pragma unroll
      for (int q = 0; q < 4; ++q) { const int row = 8 * q + (lane >> 3); vX[q] = row * 1152 + (((lane & 7) ^ ((row >> 1) & 7)) << 4) - q * 1024; }
      GL4(rX, lds0 + lw * 4096, vX[0], vX[1], vX[2], vX[3], sX + k1(0) * 128);
      SB();
#pragma unroll
      for (int q = 0; q < 5; ++q) {
        const unsigned c16 = (zp0 + q) * 64 + lane, R = (c16 * 3641u) >> 16, c = (c16 - R * 18) * 16, k = (R & 32) + 8 * ((R >> 2) & 3) + 4 * ((R >> 4) & 1) + (R & 3);
        vW[q] = k * 9216 + c - (q & 3) * 1024;
      }
      if (lw >= 2) vW[4] = OOB;
      const unsigned dz = lds0 + 16384 + zp0 * 1024, sw = sW + 4608 + k1(0) * 64 * 9216;
      GL4(rWi, dz, vW[0], vW[1], vW[2], vW[3], sw);
      GL(rWi, lw < 2 ? dz + 4096 : lds0 + L_DUMMY, vW[4], sw, 0);
    }
    const unsigned vL = lane * 16;
    auto rWs = RSRC(Wsp, 2048 * 4096); auto rWo = RSRC(Wo, 2304 * 1152);
    issue1(1, 1);
    WV(9); BAR();
    STAMP(1);
    issue1(1, 2);
    WV(9);
    for (int k = 1; k < 5; ++k) { BAR(); issue1(1, k + 2); WV(9); }
    BAR(); issue1(1, 7); issue1(1, 8); WV(18);
    BAR(); issue1(0, 1); WV(18);
    BAR(); issue1(0, 2); WV(18);
    BAR(); issue1(0, 3);
    const u64 ep = __hip_atomic_load((const u64*)(ws + WS_EP), RLX_AGENT);
    if (lw == 0) __builtin_amdgcn_raw_ptr_buffer_load_lds(RSRC(bias, 4096), (LAS void*)(size_t)(lds0 + L_BIAS), 4, lane * 4, ti * 256, 0, 0);
    BAR();
    issue1(0, 0);
#pragma unroll
    for (int q = 0; q < 8; ++q) __builtin_amdgcn_raw_ptr_buffer_load_lds(rWs, (LAS void*)(size_t)(lds0 + L_DUMMY), 16, vL, bid * 32768 + lw * 8192 + q * 1024, 0, WAUX);
    GL(rWo, lds0 + L_DUMMY, vL, bid * 10368 + lw * 2592, 0); GL(rWo, lds0 + L_DUMMY, vL, bid * 10368 + lw * 2592 + 1024, 0); GL(rWo, lds0 + L_DUMMY, vL, bid * 10368 + lw * 2592 + 1568, 0);
    const unsigned tagA = __builtin_amdgcn_readfirstlane((unsigned)ep) + 0x9E3779B9u, tagB = __builtin_amdgcn_readfirstlane((unsigned)(ep >> 32)) + 0x85EBCA6Bu;
    const u64 tag = ((u64)tagB << 32) | tagA;
    if (lw == 0) { const u32x2 tg = {tagA, tagB}; asm volatile("ds_write_b64 %0, %1\n\ts_waitcnt lgkmcnt(0)" :: "v"(lds0 + L_TAG), "v"(tg) : "memory"); }
    unsigned vS[4];
#pragma unroll
    for (int q = 0; q < 4; ++q) { const int row = 8 * q + (lane >> 3); vS[q] = row * 4096 + (((lane & 7) ^ ((row >> 1) & 7)) << 4) - q * 1024; }
    const unsigned sWs = (ti * 128 + lw * 32) * 4096;
    auto kpos = [&](int n) { return n < 2 ? 2 * ti + n : (n - 2 < 2 * ti ? n - 2 : n); };
    auto issueW = [&](int n) { GL4(rWs, lds0 + (n & 3) * STG + lw * 4096, vS[0], vS[1], vS[2], vS[3], sWs + kpos(n) * 128); };
    WV(11);
    BAR();
    STAMP(2);
    auto rS = RSRC(ws + WS_ST, 16 * 16 * 128 * 16);
    if (lane < 32) {
      const int row = lw * 32 + lane;
      f32x4 v0, v1, v2, v3;
      asm volatile("ds_read_b128 %0, %4\n\tds_read_b128 %1, %4 offset:16\n\tds_read_b128 %2, %4 offset:32\n\tds_read_b128 %3, %4 offset:48\n\ts_waitcnt lgkmcnt(0)"
                   : "=&v"(v0), "=&v"(v1), "=&v"(v2), "=&v"(v3) : "v"(lds0 + L_T + row * 64) : "memory");
      const f32x4 s = (v0 + v1) + (v2 + v3);
      u32x4 gq; gq.x = tagA; gq.y = __float_as_uint(s[0] + s[2]); gq.z = tagB; gq.w = __float_as_uint(s[1] + s[3]);
      __builtin_amdgcn_raw_buffer_store_b128(gq, rS, ((ti * 16 + fj) * 128 + row) * 16, 0, 16);
    }
    u32x4 sv[8];
    auto sweep = [&]() {
      MEMB();
#pragma unroll
      for (int p = 0; p < 8; ++p) sv[p] = __builtin_amdgcn_raw_buffer_load_b128(rS, lane * 16 + (p & 1) * 1024, (ti * 16 + lw * 4 + (p >> 1)) * 2048, 16);
      SB();
    };
    BAR(); issue1(0, 4);
    BAR(); issue1(0, 5);
    BAR(); issue1(0, 6); WV(18);
    BAR(); sweep(); issue1(0, 7); WV(9 + 8 + 9);
    BAR(); issue1(0, 8); WV(18);
    BAR();
    {
      f32x4 s;
      for (unsigned spins = 0;; ++spins) {
        bool ok = true;
        s = (f32x4){0.f, 0.f, 0.f, 0.f};
#pragma unroll
        for (int p = 0; p < 8; ++p) { ok &= (sv[p].x == tagA) & (sv[p].z == tagB); s[(p & 1) * 2] += __uint_as_float(sv[p].y); s[(p & 1) * 2 + 1] += __uint_as_float(sv[p].w); }
        if (__all(ok)) break;
        if (spins > SPIN_CAP) { s[0] = s[2] = __builtin_nanf(""); break; }
        sweep();
      }
      const f32x2 o0 = {s[0], s[1]}, o1 = {s[2], s[3]};
      asm volatile("ds_write_b64 %0, %1\n\tds_write_b64 %0, %2 offset:2048\n\ts_waitcnt lgkmcnt(0)" :: "v"(lds0 + L_R + lane * 32 + lw * 8), "v"(o0), "v"(o1) : "memory");
    }
    STAMP(3);
    issueW(1); WV(9 + 4);
    BAR(); WV(4);
    BAR();
    BAR();
    issueW(0);
    WV(0);
    BAR();
    auto rZ = RSRC(ws + WS_Z2, FRAG_BYTES);
    const unsigned sZ = fj * 128 * CH + zp0 * 1024, vZ5 = lw < 2 ? vL : OOB;
    u64 ready = 0;
    auto issueZ = [&](int n) {
      const int kp = kpos(n);
      if (__builtin_expect(!got(ready, kp >> 1), 0)) { for (unsigned spins = 0; !got(ready, kp >> 1) && spins < SPIN_CAP; ++spins) ready = poll16(ws + WS_F1 + fj * 16 * 128, tag, lane); }
      MEMB();
      const unsigned dz = lds0 + (n & 3) * STG + 16384 + zp0 * 1024, so = sZ + kp * 18432;
      GS4(rZ, dz, vL, so);
      GS(rZ, lw < 2 ? dz + 4096 : lds0 + L_DUMMY, vZ5, so + 4096, 0);
    };
    issueW(2); issueW(3);
    issueZ(2); issueZ(3);
    STAMP(4);
    WV(5);
    BAR();
    STAMP(5);
    for (int n = 4; n < 31; ++n) {
      issueW(n); issueZ(n);
      WV(9);
      BAR();
    }
    issueW(31); issueZ(31);
    constexpr int TAIL = 9;
    unsigned vO[2], vOh;
#pragma unroll
    for (int q = 0; q < 2; ++q) { const int row = 36 * lw + 8 * q + (lane >> 3); vO[q] = row * 1152 + (((lane & 7) ^ (((row >> 1) & 1) | (row & 4))) << 4); }
    { const int row = 36 * lw + 32 + ((lane >> 3) & 3); vOh = row * 1152 + (((lane & 7) ^ (((row >> 1) & 1) | (row & 4))) << 4); }
    const unsigned vO1 = vO[1] - 1024, vO2 = vO[0] + 16 * 1152 - 2048, vO3 = vO[1] + 16 * 1152 - 3072;
    const unsigned sO = min(dd, 8) * 128;
    auto issueO = [&](int n, int slot) {
      const unsigned d = lds0 + L_O + slot * STGO + lw * 4608, s0 = sO + n * 144 * 1152;
      GL4(rWo, d, vO[0], vO1, vO2, vO3, s0);
      if (lane < 32) GL(rWo, d + 4096, vOh, s0, 0);
    };
    issueO(0, 0);
    WV(9 + 5); BAR();
    issueO(1, 1); issueO(2, 2);
    WV(15); BAR();
    BAR();
    WV(0);
    BAR();
    if (!has3) return;
    issueO(3, 3); issueO(4, 4);
    BAR();
    issueO(5, 0); issueO(6, 1); issueO(7, 2);
    auto rP = RSRC(ws + WS_P, FRAG_BYTES);
    const unsigned vPs = lw < 3 ? (lane >> 5) * 16 * CH + (lane & 31) * 16 + 4096 : OOB;
    const int limS = lw == 2 ? 32 : 64;
    const unsigned sP = tt * 5 * 16 * CH;
    const int pa = (tt * 80) >> 7, pb = min((tt * 80 + 79) >> 7, 15);
    u64 rdy = 0;
    auto issueP = [&](int j, int slot) {
      if (__builtin_expect(!got(rdy, j), 0)) { for (unsigned spins = 0; !got(rdy, j) && spins < SPIN_CAP; ++spins) rdy = poll16(ws + WS_F2 + pa * 16 * 128, tag, lane) & poll16(ws + WS_F2 + pb * 16 * 128, tag, lane); }
      MEMB();
      const unsigned d = lds0 + L_P + slot * STGP, sp = sP + j * CH;
      GS4(rP, d + lw * 4096, vL, sp + lw * 16 * CH);
      GS(rP, d + 16384 + lw * 1024, vL, sp + 64 * CH + lw * 1024, 0);
      if (lane < limS) GS(rP, lw < 3 ? d + 20480 + lw * 1024 : lds0 + L_DUMMY, vPs, sp + lw * 32 * CH, 0);
    };
    issueP(0, 0);
    STAMP(6);
    issueP(1, 1); issueP(2, 2);
    WV(12);
    BAR();
    STAMP(7);
    WV(6);
    BAR(); issueP(3, 0); WV(6);
    BAR(); issueP(4, 1); WV(6);
    BAR(); issueP(5, 2); WV(6);
    BAR(); issueP(6, 0); issueO(8, 3); WV(11);
    int sp3 = 1, so5 = 4;
    for (int n = 5; n < 12; ++n) {
      BAR();
      issueP(n + 2, sp3); issueO(n + 4, so5);
      sp3 = sp3 == 2 ? 0 : sp3 + 1; so5 = so5 == 4 ? 0 : so5 + 1;
      WV(16);
    }
    BAR(); issueP(14, 2); WV(6);
    BAR(); issueP(15, 0); WV(6);
    BAR(); WV(0);
    BAR();
    BAR(); BAR();
    STAMP(8);
    DUMP();
    if (bid == 0 && tid == 256) __hip_atomic_store((u64*)(ws + WS_EP), tag, RLX_AGENT);
  } else {
    const int wt = wid >> 1, wf = wid & 1;
    if (wid == 0) {
      auto rC = RSRC((const char*)(pc0 & ~(u64)255), CODE_KB * 1024);
      GL(rC, lds0 + L_DUMMY, lane * 16, rk * 1024, 0); GL(rC, lds0 + L_DUMMY, lane * 16, rk * 1024 + 32768, 0);
      WV(0);
    }
    if (wf == 0) front<0>(lds0, wid, wt, lane, ti, fj, ws); else front<1>(lds0, wid, wt, lane, ti, fj, ws);
    if (!has3) return;
    if (wt == 0) g3_compute<0>(lds0, wf, lane, out, tt, dd); else g3_compute<1>(lds0, wf, lane, out, tt, dd);
    STAMP(13);
    DUMP();
  }
}

extern "C" void kernel_launch(void* const* d_in, const int* in_sizes, int n_in, void* d_out, int out_size, void* d_ws, size_t ws_size, hipStream_t stream) {
  (void)hipFuncSetAttribute((const void*)gmlp_fused, hipFuncAttributeMaxDynamicSharedMemorySize, LDS_BYTES);
  gmlp_fused<<<256, 512, LDS_BYTES, stream>>>((const char*)d_in[0], (const char*)d_in[1], (const char*)d_in[2], (const u16*)d_in[3], (const char*)d_in[4], (char*)d_out, (char*)d_ws);
}
```

```cpp
#include <hip/hip_runtime.h>
#include <hip/hip_bf16.h>
typedef unsigned short u16;
typedef unsigned long long u64;
typedef short s16x4 __attribute__((ext_vector_type(4)));
typedef short bf16x8 __attribute__((ext_vector_type(8)));
typedef float f32x2 __attribute__((ext_vector_type(2)));
typedef float f32x4 __attribute__((ext_vector_type(4)));
typedef unsigned u32x4 __attribute__((ext_vector_type(4)));
typedef unsigned u32x2 __attribute__((ext_vector_type(2)));
#define LAS __attribute__((address_space(3)))
#define DEV __device__ __forceinline__
#define BAR() __builtin_amdgcn_s_barrier()
#define SB() __builtin_amdgcn_sched_barrier(0)
#define MEMB() asm volatile("" ::: "memory")
#define WAITL0() __builtin_amdgcn_s_waitcnt(0xC07F)
#define WL0() asm volatile("s_waitcnt lgkmcnt(0)" ::: "memory")
#define WV(N) asm volatile("s_waitcnt vmcnt(%0)" ::"i"(N) : "memory")
#define MFMA(a, b, c) __builtin_amdgcn_mfma_f32_16x16x32_bf16(a, b, c, 0, 0, 0)
#define MFMA16(a, b, c) __builtin_amdgcn_mfma_f32_16x16x16bf16_1k(a, b, c, 0, 0, 0)
#define GL(R, D, V, S, I) __builtin_amdgcn_raw_ptr_buffer_load_lds(R, (LAS void*)(size_t)(D), 16, V, S, I, 0)
#define GL4(R, D, V0, V1, V2, V3, S) do { GL(R, D, V0, S, 0); GL(R, D, V1, S, 1024); GL(R, D, V2, S, 2048); GL(R, D, V3, S, 3072); } while (0)
#define GS(R, D, V, S, I) __builtin_amdgcn_raw_ptr_buffer_load_lds(R, (LAS void*)(size_t)(D), 16, V, S, I, 16)
#define GS4(R, D, V, S) do { GS(R, D, V, S, 0); GS(R, D, V, S, 1024); GS(R, D, V, S, 2048); GS(R, D, V, S, 3072); } while (0)
#define RSRC(p, n) __builtin_amdgcn_make_buffer_rsrc((void*)(p), 0, (n), 0x00020000)
#define RLX_AGENT __ATOMIC_RELAXED, __HIP_MEMORY_SCOPE_AGENT
#define ROT1 2
#define Z1W 1
#define BSH 1
#define LS0 1
#define OAUX 16
#define WAUX 0
#define FLAGM 0
#define LA2 2
#define CODE_KB 54

constexpr int CH = 4608;
constexpr int FRAG_BYTES = 16 * 128 * CH;
constexpr size_t WS_Z2 = 0, WS_P = FRAG_BYTES, WS_ST = 2 * (size_t)FRAG_BYTES, WS_F1 = WS_ST + 16 * 16 * 128 * 16, WS_F2 = WS_F1 + 256 * 128, WS_EP = WS_F2 + 256 * 128;
constexpr int STG = 34816, STGP = 23040, STGO = 18432, L_O = 0, L_P = 5 * STGO, LDS_BYTES = 163840;
constexpr int L_T = 139264, L_R = L_T + 8192, L_X = 151552, L_BIAS = 159744, L_TAG = L_BIAS + 256, L_DUMMY = 161280;
constexpr unsigned SPIN_CAP = 1u << 16, OOB = 0x80000000u;
#define STAMP(I)
#define DUMP()

DEV unsigned cvt_pk(float lo, float hi) { unsigned r; asm("v_cvt_pk_bf16_f32 %0, %1, %2" : "=v"(r) : "v"(lo), "v"(hi)); return r; }
DEV bf16x8 ld128(unsigned a) { return *(LAS bf16x8*)(size_t)a; }
DEV s16x4 ld64(unsigned a) { return *(LAS s16x4*)(size_t)a; }
DEV s16x4 ldtr(unsigned a) { return __builtin_amdgcn_ds_read_tr16_b64_v4i16((LAS s16x4*)(size_t)a); }
DEV bf16x8 cat(s16x4 a, s16x4 b) { return __builtin_shufflevector(a, b, 0, 1, 2, 3, 4, 5, 6, 7); }
DEV float bflo(unsigned v) { return __uint_as_float(v << 16); }
DEV float bfhi(unsigned v) { return __uint_as_float(v & 0xffff0000u); }
DEV f32x2 gelu2(f32x2 x) {
  const float k1 = -2.0f * 0.7978845608028654f * 1.4426950408889634f, k2 = k1 * 0.044715f;
  f32x2 t = x * (x * x * k2 + k1), e;
  e.x = __builtin_amdgcn_exp2f(t.x); e.y = __builtin_amdgcn_exp2f(t.y);
  e = e + 1.0f;
  e.x = __builtin_amdgcn_rcpf(e.x); e.y = __builtin_amdgcn_rcpf(e.y);
  return x * e;
}
DEV u32x2 gelu_pack(f32x4 v) { const f32x2 a = gelu2((f32x2){v[0], v[1]}), b = gelu2((f32x2){v[2], v[3]}); return (u32x2){cvt_pk(a.x, a.y), cvt_pk(b.x, b.y)}; }
DEV u32x2 gelu_pack_sum(f32x4 v, f32x2& s1, f32x2& s2) {
  const f32x2 a = gelu2((f32x2){v[0], v[1]}), b = gelu2((f32x2){v[2], v[3]});
  s1 += a + b; s2 += a * a; s2 += b * b;
  return (u32x2){cvt_pk(a.x, a.y), cvt_pk(b.x, b.y)};
}

struct TAddr { unsigned w[2], z[2][2], s[2]; };
struct TFrag { bf16x8 wX[4], wY[4]; s16x4 tX[5][2], tY[4][2]; };
template <int WF, int PS, int OFF, bool FIRST, bool SYNC = true>
DEV void t_step(TAddr& A, int dlt, f32x4 (&acc)[4][5], TFrag& F) {
  constexpr int KX = WF, KY = 1 - WF;
  SB(); if (SYNC) BAR(); MEMB(); SB();
  auto rdz = [&](s16x4 (&t)[2], int n, int u, int K) { t[u] = n < 4 ? ldtr(A.z[u][n & 1] + OFF + K * 9216 + (n >> 1) * PS) : ldtr(A.s[u] + OFF + K * 9216); };
#pragma unroll
  for (int i = 0; i < 16; ++i) {
    const int m = i >> 2, n = i & 3;
    if (!FIRST) acc[m][n] = MFMA(cat(F.tY[n][0], F.tY[n][1]), F.wY[m], acc[m][n]);
    SB();
    if (i == 0) F.wX[0] = ld128(A.w[KX] + OFF);
    else if (i < 11) rdz(F.tX[(i - 1) >> 1], (i - 1) >> 1, (i - 1) & 1, KX);
    else if (i < 14) F.wX[i - 10] = ld128(A.w[KX] + OFF + (i - 10) * 2048);
    SB();
  }
#pragma unroll
  for (int i = 0; i < 20; ++i) {
    const int m = i / 5, n = i % 5;
    acc[m][n] = MFMA(cat(F.tX[n][0], F.tX[n][1]), F.wX[m], acc[m][n]);
    SB();
    if (i == 0) F.wY[0] = ld128(A.w[KY] + OFF);
    else if (i < 9) rdz(F.tY[(i - 1) >> 1], (i - 1) >> 1, (i - 1) & 1, KY);
    else if (i < 12) F.wY[i - 8] = ld128(A.w[KY] + OFF + (i - 8) * 2048);
    else if (OFF != 0 && i < 16) {
      const int u = i & 1;
      if (i < 14) { A.w[u] += dlt; A.s[u] += dlt; asm volatile("" : "+v"(A.w[u]), "+v"(A.s[u])); }
      else { A.z[u][0] += dlt; A.z[u][1] += dlt; asm volatile("" : "+v"(A.z[u][0]), "+v"(A.z[u][1])); }
    }
    SB();
  }
  WAITL0();
}
template <int WF>
DEV void t_finish(unsigned lds0, int wt, int lane, f32x4 (&acc)[4][5], TFrag& F) {
#pragma unroll
  for (int h = 0; h < 2; ++h) *(LAS f32x4*)(size_t)(lds0 + L_X + ((wt * 2 + WF) * 2 + h) * 1024 + lane * 16) = acc[2 * (1 - WF) + h][4];
#pragma unroll
  for (int n = 0; n < 4; ++n)
#pragma unroll
    for (int m = 0; m < 4; ++m) acc[m][n] = MFMA(cat(F.tY[n][0], F.tY[n][1]), F.wY[m], acc[m][n]);
  WL0();
  BAR(); MEMB();
#pragma unroll
  for (int h = 0; h < 2; ++h) acc[2 * WF + h][4] += *(LAS f32x4*)(size_t)(lds0 + L_X + ((wt * 2 + 1 - WF) * 2 + h) * 1024 + lane * 16);
}
template <int WF, bool MID, class Mid>
DEV void in_pass(unsigned lds0, int wt, int lane, TAddr& A, f32x4 (&acc)[4][5], Mid mid) {
  TFrag F;
#pragma unroll
  for (int m = 0; m < 4; ++m)
#pragma unroll
    for (int n = 0; n < 5; ++n) acc[m][n] = (f32x4){0, 0, 0, 0};
  int dlt = 2 * STG;
  WAITL0();
  t_step<WF, 64, 0, true>(A, dlt, acc, F);
  for (int k = 0; k < (MID ? 3 : 4); ++k) {
    t_step<WF, 64, STG, false>(A, dlt, acc, F);
    dlt = -dlt;
    t_step<WF, 64, 0, false>(A, dlt, acc, F);
  }
  if (MID) {
    t_step<WF, 64, STG, false>(A, -2 * STG, acc, F);
    SB(); BAR(); MEMB(); mid(); SB();
    t_step<WF, 64, 0, false>(A, 0, acc, F);
  }
  t_finish<WF>(lds0, wt, lane, acc, F);
}

struct G3P { bf16x8 p[2][5]; s16x4 ps[3]; };
struct G3W { s16x4 w[2][2][2], ws[2]; };
template <int KH>
DEV void g3_readw(G3W& n, unsigned a0, unsigned a1) {
#pragma unroll
  for (int q = 0; q < 8; ++q) n.w[q >> 2][(q >> 1) & 1][q & 1] = ldtr((((q >> 1) & 1) ? a1 : a0) + (2 * KH + (q >> 2)) * 4096 + (q & 1) * 2048);
  n.ws[0] = ldtr(a0 + 16384); n.ws[1] = ldtr(a1 + 16384);
}
template <int KH, bool MM, bool RDW>
DEV void g3_stage(f32x4 (&acc)[5][2], const G3P& cp, const G3W& cw, G3P& np, G3W& nw, unsigned aP, unsigned aS, unsigned a0, unsigned a1) {
  constexpr int M0 = KH ? 3 : 0, NS = KH ? 2 : 3, NM = 20 + 2 * NS, NR = 20 + NS;
  SB(); BAR(); MEMB(); SB();
#pragma unroll
  for (int i = 0; i < NM; ++i) {
    if (MM) {
      if (i >= 10 && i < 10 + 2 * NS) acc[M0 + ((i - 10) >> 1)][i & 1] = MFMA16(cw.ws[i & 1], cp.ps[(i - 10) >> 1], acc[M0 + ((i - 10) >> 1)][i & 1]);
      else { const int ii = i < 10 ? i : i - 2 * NS, P = ii / 10, mm = (ii % 10) >> 1, m = (KH != P) ? (mm + 3) % 5 : mm, cc = ii & 1; acc[m][cc] = MFMA(cat(cw.w[P][cc][0], cw.w[P][cc][1]), cp.p[P][m], acc[m][cc]); }
    }
    SB();
#pragma unroll
    for (int r = (i * 3) / 2; r < ((i + 1) * 3) / 2; ++r) {
      if (r < 18) { const int P = r / 9, q = r % 9;
        if (q < 4) { if (RDW) nw.w[P][q >> 1][q & 1] = ldtr(((q >> 1) ? a1 : a0) + (2 * KH + P) * 4096 + (q & 1) * 2048); }
        else np.p[P][q - 4] = ld128(aP + P * 1024 + (q - 4) * 4096); }
      else if (r < 20) { if (RDW) nw.ws[r - 18] = ldtr(((r - 18) ? a1 : a0) + 16384); }
      else if (r < NR) np.ps[r - 20] = ld64(aS + (M0 + r - 20) * 512);
    }
    SB();
  }
  WAITL0();
}
template <int KH>
DEV void g3_compute(unsigned lds0, int dh, int lane, char* out, int tt, int dd) {
  const int g = lane >> 4, qr = (lane >> 2) & 3, p = lane & 3;
  const int rr = 4 * g + qr;
  unsigned bW[2];
#pragma unroll
  for (int c = 0; c < 2; ++c) bW[c] = lds0 + L_O + rr * 128 + (((4 * dh + p) ^ (((rr >> 1) & 1) | (rr & 4))) << 4) + ((c ^ (p & 1)) * 8);
  const unsigned bP = lds0 + L_P + lane * 16 + KH * 2048, bS = lds0 + L_P + 20480 + lane * 8;
  G3W a0, a1, a2;
  g3_readw<KH>(a0, bW[0], bW[1]); g3_readw<KH>(a1, bW[0] + STGO, bW[1] + STGO); g3_readw<KH>(a2, bW[0] + 2 * STGO, bW[1] + 2 * STGO);
  f32x4 acc[5][2];
#pragma unroll
  for (int m = 0; m < 5; ++m) { acc[m][0] = (f32x4){0, 0, 0, 0}; acc[m][1] = (f32x4){0, 0, 0, 0}; }
  WAITL0();
  SB(); BAR(); SB();
  G3P p0, p1; G3W w0, w1;
  g3_stage<KH, false, false>(acc, p1, a0, p0, w0, bP, bS, 0, 0);
  g3_stage<KH, true, false>(acc, p0, a0, p1, w0, bP + STGP, bS + STGP, 0, 0);
  g3_stage<KH, true, false>(acc, p1, a1, p0, w0, bP + 2 * STGP, bS + 2 * STGP, 0, 0);
  g3_stage<KH, true, true>(acc, p0, a2, p1, w1, bP, bS, bW[0] + 3 * STGO, bW[1] + 3 * STGO);
  unsigned sp = STGP, sw = 4 * STGO;
  for (int j = 4; j < 16; j += 2) {
    g3_stage<KH, true, true>(acc, p1, w1, p0, w0, bP + sp, bS + sp, bW[0] + sw, bW[1] + sw);
    sp = sp == 2 * STGP ? 0 : sp + STGP; sw = sw == 4 * STGO ? 0 : sw + STGO;
    g3_stage<KH, true, true>(acc, p0, w0, p1, w1, bP + sp, bS + sp, bW[0] + sw, bW[1] + sw);
    sp = sp == 2 * STGP ? 0 : sp + STGP; sw = sw == 4 * STGO ? 0 : sw + STGO;
  }
  {
    constexpr int M0 = KH ? 3 : 0, NS = KH ? 2 : 3;
    SB(); BAR(); SB();
#pragma unroll
    for (int i = 0; i < 2 * NS; ++i) { acc[M0 + (i >> 1)][i & 1] = MFMA16(w1.ws[i & 1], p1.ps[i >> 1], acc[M0 + (i >> 1)][i & 1]); SB(); }
#pragma unroll
    for (int i = 0; i < 20; ++i) { const int P = (i >> 1) & 1, m = KH ? i >> 2 : 4 - (i >> 2), cc = i & 1; acc[m][cc] = MFMA(cat(w1.w[P][cc][0], w1.w[P][cc][1]), p1.p[P][m], acc[m][cc]); SB(); }
  }
#pragma unroll
  for (int m = 0; m < 5; ++m)
    if ((m >= 3) != KH) { *(LAS f32x4*)(size_t)(lds0 + (dh * 10 + m * 2) * 1024 + lane * 16) = acc[m][0]; *(LAS f32x4*)(size_t)(lds0 + (dh * 10 + m * 2 + 1) * 1024 + lane * 16) = acc[m][1]; }
  WL0();
  BAR(); MEMB();
  auto rO = RSRC(out, 2048 * 1152);
  const bool sw2 = g & 1;
#pragma unroll
  for (int m = 0; m < 5; ++m)
    if ((m >= 3) == KH) {
      const f32x4 a = acc[m][0] + *(LAS f32x4*)(size_t)(lds0 + (dh * 10 + m * 2) * 1024 + lane * 16), b = acc[m][1] + *(LAS f32x4*)(size_t)(lds0 + (dh * 10 + m * 2 + 1) * 1024 + lane * 16);
      const unsigned a0 = cvt_pk(a[0], a[1]), a1 = cvt_pk(a[2], a[3]), b0 = cvt_pk(b[0], b[1]), b1 = cvt_pk(b[2], b[3]);
      const u32x4 v = {sw2 ? b0 : a0, sw2 ? b1 : a1, sw2 ? a0 : b0, sw2 ? a1 : b1};
      __builtin_amdgcn_raw_buffer_store_b128(v, rO, (tt * 80 + m * 16 + (lane & 15)) * 1152 + dd * 128 + dh * 64 + g * 16, 0, OAUX);
    }
}

template <int WF>
DEV void front(unsigned lds0, int wid, int wt, int lane, int ti, int fj, char* ws) {
  const int r16 = lane & 15, g = lane >> 4, qr = (lane >> 2) & 3, p = lane & 3;
  TAddr A;
  A.w[0] = lds0 + (64 * wt + r16) * 128 + ((g ^ (r16 >> 1)) << 4); A.w[1] = A.w[0] ^ 64;
#pragma unroll
  for (int u = 0; u < 2; ++u) {
    const unsigned b = lds0 + 16384 + (16 * u + 4 * g + qr) * 288 + p * 8;
    A.z[u][0] = b + WF * 128 + (p & 1) * 32; A.z[u][1] = b + WF * 128 + 32 - (p & 1) * 32; A.s[u] = b + 256;
  }
  asm volatile("" : "+v"(A.w[0]), "+v"(A.w[1]), "+v"(A.s[0]), "+v"(A.s[1]), "+v"(A.z[0][0]), "+v"(A.z[0][1]), "+v"(A.z[1][0]), "+v"(A.z[1][1]));
  f32x4 acc[4][5];
  in_pass<WF, false>(lds0, wt, lane, A, acc, []() {});
  STAMP(2);
  u32x2 ua[4][4], ub[2];
  {
    f32x2 s1[4], s2[4];
#pragma unroll
    for (int m = 0; m < 4; ++m) {
      s1[m] = (f32x2){0.f, 0.f}; s2[m] = (f32x2){0.f, 0.f};
#pragma unroll
      for (int n = 0; n < 4; ++n) ua[m][n] = gelu_pack_sum(acc[m][n], s1[m], s2[m]);
      if ((m >> 1) == WF) ub[m & 1] = gelu_pack_sum(acc[m][4], s1[m], s2[m]);
    }
#pragma unroll
    for (int m = 0; m < 4; ++m) *(LAS f32x2*)(size_t)(lds0 + L_T + (64 * wt + 16 * m + r16) * 64 + (WF * 4 + g) * 8) = (f32x2){s1[m].x + s1[m].y, s2[m].x + s2[m].y};
  }
#pragma unroll
  for (int m = 0; m < 4; ++m) asm volatile("" : "+v"(ua[m][0]), "+v"(ua[m][1]), "+v"(ua[m][2]), "+v"(ua[m][3]));
  asm volatile("" : "+v"(ub[0]), "+v"(ub[1]));
#pragma unroll
  for (int u = 0; u < 2; ++u) { A.z[u][0] -= (p & 1) * 32; A.z[u][1] += (p & 1) * 32; }
  asm volatile("" : "+v"(A.z[0][0]), "+v"(A.z[0][1]), "+v"(A.z[1][0]), "+v"(A.z[1][1]));
  STAMP(3);
  in_pass<WF, true>(lds0, wt, lane, A, acc, [&]() {
    STAMP(4);
#pragma unroll
  for (int m = 0; m < 4; ++m) asm volatile("" : "+v"(ua[m][0]), "+v"(ua[m][1]), "+v"(ua[m][2]), "+v"(ua[m][3]));
  asm volatile("" : "+v"(ub[0]), "+v"(ub[1]));
  int lz = lane; asm volatile("" : "+v"(lz));
  {
    const int r16 = lz & 15, g = lz >> 4;
    auto rZ = RSRC(ws + WS_Z2, FRAG_BYTES);
    const unsigned sl = g * 16 + (r16 ^ (4 * (g >> 1)));
#pragma unroll
    for (int m = 0; m < 4; ++m) {
      const f32x4 q4 = *(LAS f32x4*)(size_t)(lds0 + L_R + (64 * wt + 16 * m + r16) * 32) + *(LAS f32x4*)(size_t)(lds0 + L_R + (64 * wt + 16 * m + r16) * 32 + 16);
      const float mean = (q4[0] + q4[2]) * (1.0f / 2304.0f), var = fmaxf((q4[1] + q4[3]) * (1.0f / 2304.0f) - mean * mean, 0.f);
      const float rs = __builtin_amdgcn_rsqf(var + 1e-5f), sh = -mean * rs;
      auto norm = [&](unsigned v) { const f32x2 y = (f32x2){bflo(v), bfhi(v)} * rs + sh; return cvt_pk(y.x, y.y); };
      const unsigned dst = (fj * 128 + ti * 8 + wt * 4 + m) * CH;
#pragma unroll
      for (int pp = 0; pp < 2; ++pp) {
        const u32x2 a = ua[m][2 * pp], b = ua[m][2 * pp + 1];
        const u32x4 v = {norm(a.x), norm(a.y), norm(b.x), norm(b.y)};
        __builtin_amdgcn_raw_buffer_store_b128(v, rZ, dst + sl * 16 + WF * 2048 + pp * 1024, 0, 16);
        ua[m][2 * pp] = (u32x2){v.x, v.y}; ua[m][2 * pp + 1] = (u32x2){v.z, v.w};
      }
      if ((m >> 1) == WF) {
        const u32x2 v = {norm(ub[m & 1].x), norm(ub[m & 1].y)};
        __builtin_amdgcn_raw_buffer_store_b64(v, rZ, dst + sl * 8 + 4096, 0, 16);
        ub[m & 1] = v;
      }
    }
  }
    STAMP(5);
  });
#pragma unroll
  for (int m = 0; m < 4; ++m) asm volatile("" : "+v"(acc[m][0]), "+v"(acc[m][1]), "+v"(acc[m][2]), "+v"(acc[m][3]), "+v"(acc[m][4]));
  const u32x2 tg = *(LAS u32x2*)(size_t)(lds0 + L_TAG);
  const u64 tag = ((u64)(unsigned)__builtin_amdgcn_readfirstlane(tg.y) << 32) | (unsigned)__builtin_amdgcn_readfirstlane(tg.x);
  {
    const unsigned ld = lds0 + wt * STG + 16384, sl = g * 16 + (r16 ^ (4 * (g >> 1)));
#pragma unroll
    for (int m = 0; m < 4; ++m) {
#pragma unroll
      for (int pp = 0; pp < 2; ++pp) *(LAS u32x4*)(size_t)(ld + m * CH + WF * 2048 + pp * 1024 + sl * 16) = (u32x4){ua[m][2 * pp].x, ua[m][2 * pp].y, ua[m][2 * pp + 1].x, ua[m][2 * pp + 1].y};
      if ((m >> 1) == WF) *(LAS u32x2*)(size_t)(ld + m * CH + 4096 + sl * 8) = ub[m & 1];
    }
  }
  STAMP(7);
  SB();
  u32x2 za[4][4], zb[2];
#pragma unroll
  for (int m = 0; m < 4; ++m) {
#pragma unroll
    for (int n = 0; n < 4; ++n) za[m][n] = gelu_pack(acc[m][n]);
    if ((m >> 1) == WF) zb[m & 1] = gelu_pack(acc[m][4]);
    asm volatile("" : "+v"(za[m][0]), "+v"(za[m][1]), "+v"(za[m][2]), "+v"(za[m][3]));
    if (m == FLAGM) {
      SB();
      asm volatile("s_waitcnt vmcnt(0) lgkmcnt(0)" ::: "memory");
      if (lane == 0) __hip_atomic_store((u64*)(ws + WS_F1 + (fj * 16 + ti) * 128) + wid, tag, RLX_AGENT);
      BAR();
      SB();
    }
  }
  asm volatile("" : "+v"(zb[0]), "+v"(zb[1]));
  STAMP(6);
#pragma unroll
  for (int u = 0; u < 2; ++u) {
    const unsigned sl = p * 16 + 8 * (g & 1) + 4 * (u ^ (p >> 1)) + qr;
#pragma unroll
    for (int h = 0; h < 2; ++h) A.z[u][h] = lds0 + 16384 + (g >> 1) * CH + WF * 2048 + sl * 16 + ((h ^ (p & 1)) * 8);
    A.s[u] = lds0 + 16384 + (g >> 1) * CH + 4096 + sl * 8;
  }
  asm volatile("" : "+v"(A.s[0]), "+v"(A.s[1]), "+v"(A.z[0][0]), "+v"(A.z[0][1]), "+v"(A.z[1][0]), "+v"(A.z[1][1]));
#pragma unroll
  for (int m = 0; m < 4; ++m) {
    const float b = __uint_as_float((unsigned)*(LAS u16*)(size_t)(lds0 + L_BIAS + (wt * 64 + m * 16 + r16) * 2) << 16);
#pragma unroll
    for (int n = 0; n < 5; ++n) { const float b0 = (n < 4 || (m >> 1) == WF) ? b : 0.f; acc[m][n] = (f32x4){b0, b0, b0, b0}; }
  }
  TFrag F;
  int dlt = 2 * STG;
  WAITL0();
  t_step<WF, 1024, 0, true, false>(A, dlt, acc, F);
  t_step<WF, 1024, STG, false, false>(A, dlt, acc, F);
  STAMP(8);
  for (int k = 0; k < 15; ++k) {
    dlt = -dlt;
    t_step<WF, 1024, 0, false>(A, dlt, acc, F);
    t_step<WF, 1024, STG, false>(A, dlt, acc, F);
  }
  auto rP = RSRC(ws + WS_P, FRAG_BYTES);
  auto gate = [&](f32x4 a, u32x2 z) {
    const f32x2 x = (f32x2){a[0], a[1]} * (f32x2){bflo(z.x), bfhi(z.x)}, y = (f32x2){a[2], a[3]} * (f32x2){bflo(z.y), bfhi(z.y)};
    return (u32x2){cvt_pk(x.x, x.y), cvt_pk(y.x, y.y)};
  };
#pragma unroll
  for (int h = 0; h < 2; ++h) *(LAS f32x4*)(size_t)(lds0 + L_X + ((wt * 2 + WF) * 2 + h) * 1024 + lane * 16) = acc[2 * (1 - WF) + h][4];
  const unsigned dst0 = (ti * 8 + wt * 4) * 16 * CH + fj * CH;
#pragma unroll
  for (int m = 0; m < 4; ++m)
#pragma unroll
    for (int n = 0; n < 4; ++n) acc[m][n] = MFMA(cat(F.tY[n][0], F.tY[n][1]), F.wY[m], acc[m][n]);
  WL0();
  BAR(); MEMB();
  STAMP(10);
  f32x4 o8[2];
#pragma unroll
  for (int h = 0; h < 2; ++h) o8[h] = *(LAS f32x4*)(size_t)(lds0 + L_X + ((wt * 2 + 1 - WF) * 2 + h) * 1024 + lane * 16);
#pragma unroll
  for (int m = 0; m < 4; ++m)
#pragma unroll
    for (int pp = 0; pp < 2; ++pp) {
      const u32x2 a = gate(acc[m][2 * pp], za[m][2 * pp]), b = gate(acc[m][2 * pp + 1], za[m][2 * pp + 1]);
      __builtin_amdgcn_raw_buffer_store_b128((u32x4){a.x, a.y, b.x, b.y}, rP, dst0 + m * 16 * CH + lane * 16 + (2 * WF + pp) * 1024, 0, 16);
    }
#pragma unroll
  for (int h = 0; h < 2; ++h) __builtin_amdgcn_raw_buffer_store_b64(gate(acc[2 * WF + h][4] + o8[h], zb[h]), rP, dst0 + (2 * WF + h) * 16 * CH + lane * 8 + 4096, 0, 16);
  STAMP(11);
  WV(0);
  if (lane == 0) __hip_atomic_store((u64*)(ws + WS_F2 + (ti * 16 + fj) * 128) + wid, tag, RLX_AGENT);
  SB(); BAR(); MEMB(); SB();
  STAMP(12);
}

DEV u64 poll16(const char* base, u64 tag, int lane) {
  const u64 v = __hip_atomic_load((const u64*)(base + (lane >> 2) * 128) + (lane & 3), RLX_AGENT);
  return __builtin_amdgcn_ballot_w64(v == tag);
}
DEV bool got(u64 mask, int who) { return ((unsigned)(mask >> (4 * who)) & 15u) == 15u; }

__global__ __launch_bounds__(512, 2) void gmlp_fused(const char* __restrict__ X, const char* __restrict__ Win, const char* __restrict__ Wsp, const u16* __restrict__ bias,
                                                     const char* __restrict__ Wo, char* __restrict__ out, char* __restrict__ ws) {
  extern __shared__ __attribute__((aligned(16))) char smem[];
  u64 pc0; asm volatile("s_getpc_b64 %0" : "=s"(pc0));
  const int tid = threadIdx.x, wid = __builtin_amdgcn_readfirstlane(tid >> 6), lane = tid & 63;
  const int bid = blockIdx.x, xg = bid & 7, rk = bid >> 3, il = rk & 7, jl = rk >> 3, ti = (xg & 1) * 8 + il, fj = (xg >> 1) * 4 + jl;
  const int L3 = xg * 30 + rk, tt = L3 / 9, dd = L3 - tt * 9;
  const bool has3 = rk < 30 && L3 < 234;
  const unsigned lds0 = (unsigned)(size_t)(LAS char*)smem;
  STAMP(0);
  if (__builtin_expect(wid >= 4, 1)) {
    __builtin_amdgcn_s_setprio(3);
    const int lw = wid - 4;
    const bool big = wid < 6;
#define WVS(STAGES, EXTRA) do { if (big) WV(9 * (STAGES) + (EXTRA)); else WV(8 * (STAGES) + (EXTRA)); } while (0)
    auto rX = RSRC(X, 2048 * 1152); auto rWi = RSRC(Win, 576 * 9216);
    unsigned vX[4], vW[5];
    const unsigned zp0 = lw < 2 ? lw * 5 : 2 + lw * 4;
    const unsigned sX = (ti * 128 + lw * 32) * 1152, sW = fj * 288;
    const int rot = ROT1 == 0 ? 0 : ROT1 == 1 ? il + 2 * jl : 3 * ((il + jl) % 3);
    const int rot8 = ROT1 == 1 ? (il + 2 * jl) & 7 : ROT1 == 2 ? (((il + jl) % 3) * 8) / 3 : 0;
    auto k1 = [&](int k) { return k == 0 ? 0 : 1 + ((k - 1 + rot8) & 7); };
    auto issue1 = [&](int half, int k) {
      const int kk = k1(k);
      const unsigned d = lds0 + (k & 3) * STG, dz = d + 16384 + zp0 * 1024, sw = sW + half * 4608 + kk * 64 * 9216;
      GL4(rX, d + lw * 4096, vX[0], vX[1], vX[2], vX[3], sX + kk * 128);
      GL4(rWi, dz, vW[0], vW[1], vW[2], vW[3], sw);
      if (big) GL(rWi, dz + 4096, vW[4], sw, 0);
    };
    {
#pragma unroll
      for (int q = 0; q < 4; ++q) { const int row = 8 * q + (lane >> 3); vX[q] = row * 1152 + (((lane & 7) ^ ((row >> 1) & 7)) << 4) - q * 1024; }
      GL4(rX, lds0 + lw * 4096, vX[0], vX[1], vX[2], vX[3], sX + k1(0) * 128);
      SB();
#pragma unroll
      for (int q = 0; q < 5; ++q) {
        const unsigned c16 = (zp0 + q) * 64 + lane, R = (c16 * 3641u) >> 16, c = (c16 - R * 18) * 16, k = (R & 32) + 8 * ((R >> 2) & 3) + 4 * ((R >> 4) & 1) + (R & 3);
        vW[q] = k * 9216 + c - (q & 3) * 1024;
      }
      const unsigned dz = lds0 + 16384 + zp0 * 1024, sw = sW + 4608 + k1(0) * 64 * 9216;
      GL4(rWi, dz, vW[0], vW[1], vW[2], vW[3], sw);
      if (big) GL(rWi, dz + 4096, vW[4], sw, 0);
    }
    const unsigned vL = lane * 16;
    auto rWs = RSRC(Wsp, 2048 * 4096); auto rWo = RSRC(Wo, 2304 * 1152);
    issue1(1, 1);
    WVS(1, 0); BAR();
    STAMP(1);
    issue1(1, 2);
    WVS(1, 0);
    BAR(); issue1(1, 3); WVS(1, 0);
    BAR(); issue1(1, 4);
    {
      const unsigned s0 = (bid >> 2) * 9 * 9216 + (bid & 3) * 1152, c = lw * 162 + lane;
#pragma unroll
      for (int q = 0; q < 3; ++q) { const unsigned e = c + (q < 2 ? q * 64 : 98), r = (e * 3641u) >> 18; GL(rWi, lds0 + L_DUMMY, r * 9216 + (e - r * 72) * 16, s0, 0); }
    }
    WVS(1, 3);
    BAR(); issue1(1, 5); WVS(1, 3);
    BAR(); issue1(1, 6); WVS(1, 0);
    BAR(); issue1(1, 7); issue1(1, 8); WVS(2, 0);
    BAR(); issue1(0, 1); WVS(2, 0);
    BAR(); issue1(0, 2); WVS(2, 0);
    BAR(); issue1(0, 3);
    const u64 ep = __hip_atomic_load((const u64*)(ws + WS_EP), RLX_AGENT);
    if (lw == 0) __builtin_amdgcn_raw_ptr_buffer_load_lds(RSRC(bias, 4096), (LAS void*)(size_t)(lds0 + L_BIAS), 4, lane * 4, ti * 256, 0, 0);
    BAR();
    issue1(0, 0);
    auto warm = [&]() {
#pragma unroll
      for (int q = 0; q < 8; ++q) __builtin_amdgcn_raw_ptr_buffer_load_lds(rWs, (LAS void*)(size_t)(lds0 + L_DUMMY), 16, vL, bid * 32768 + lw * 8192 + q * 1024, 0, WAUX);
#pragma unroll
      for (int q = 0; q < 3; ++q) __builtin_amdgcn_raw_ptr_buffer_load_lds(rWo, (LAS void*)(size_t)(lds0 + L_DUMMY), 16, vL, bid * 10368 + lw * 2592 + (q < 2 ? q * 1024 : 1568), 0, WAUX);
    };
    warm();
    const unsigned tagA = __builtin_amdgcn_readfirstlane((unsigned)ep) + 0x9E3779B9u, tagB = __builtin_amdgcn_readfirstlane((unsigned)(ep >> 32)) + 0x85EBCA6Bu;
    const u64 tag = ((u64)tagB << 32) | tagA;
    if (lw == 0) { const u32x2 tg = {tagA, tagB}; asm volatile("ds_write_b64 %0, %1\n\ts_waitcnt lgkmcnt(0)" :: "v"(lds0 + L_TAG), "v"(tg) : "memory"); }
    unsigned vS[4];
#pragma unroll
    for (int q = 0; q < 4; ++q) { const int row = 8 * q + (lane >> 3); vS[q] = row * 4096 + (((lane & 7) ^ ((row >> 1) & 7)) << 4) - q * 1024; }
    const unsigned sWs = (ti * 128 + lw * 32) * 4096;
    auto kpos = [&](int n) { return n < 2 ? 2 * ti + n : (n - 2 < 2 * ti ? n - 2 : n); };
    auto issueW = [&](int n) { GL4(rWs, lds0 + (n & 3) * STG + lw * 4096, vS[0], vS[1], vS[2], vS[3], sWs + kpos(n) * 128); };
    WV(11);
    BAR();
    STAMP(2);
    auto rS = RSRC(ws + WS_ST, 16 * 16 * 128 * 16);
    if (lane < 32) {
      const int row = lw * 32 + lane;
      f32x4 v0, v1, v2, v3;
      asm volatile("ds_read_b128 %0, %4\n\tds_read_b128 %1, %4 offset:16\n\tds_read_b128 %2, %4 offset:32\n\tds_read_b128 %3, %4 offset:48\n\ts_waitcnt lgkmcnt(0)"
                   : "=&v"(v0), "=&v"(v1), "=&v"(v2), "=&v"(v3) : "v"(lds0 + L_T + row * 64) : "memory");
      const f32x4 s = (v0 + v1) + (v2 + v3);
      u32x4 gq; gq.x = tagA; gq.y = __float_as_uint(s[0] + s[2]); gq.z = tagB; gq.w = __float_as_uint(s[1] + s[3]);
      __builtin_amdgcn_raw_buffer_store_b128(gq, rS, ((ti * 16 + fj) * 128 + row) * 16, 0, 16);
    }
    u32x4 sv[8];
    auto sweep = [&]() {
      MEMB();
#pragma unroll
      for (int p = 0; p < 8; ++p) sv[p] = __builtin_amdgcn_raw_buffer_load_b128(rS, lane * 16 + (p & 1) * 1024, (ti * 16 + lw * 4 + (p >> 1)) * 2048, 16);
      SB();
    };
    BAR(); issue1(0, 4);
    BAR(); issue1(0, 5);
    BAR(); issue1(0, 6); WVS(2, 0);
    BAR(); issue1(0, 7); WVS(2, 0);
    BAR(); issue1(0, 8); WVS(2, 0);
    BAR(); issueW(1); sweep(); WVS(1, 12);
    BAR();
    {
      f32x4 s;
      for (unsigned spins = 0;; ++spins) {
        bool ok = true;
        s = (f32x4){0.f, 0.f, 0.f, 0.f};
#pragma unroll
        for (int p = 0; p < 8; ++p) { ok &= (sv[p].x == tagA) & (sv[p].z == tagB); s[(p & 1) * 2] += __uint_as_float(sv[p].y); s[(p & 1) * 2 + 1] += __uint_as_float(sv[p].w); }
        if (__all(ok)) break;
        if (spins > SPIN_CAP) { s[0] = s[2] = __builtin_nanf(""); break; }
        sweep();
      }
      const f32x2 o0 = {s[0], s[1]}, o1 = {s[2], s[3]};
      asm volatile("ds_write_b64 %0, %1\n\tds_write_b64 %0, %2 offset:2048\n\ts_waitcnt lgkmcnt(0)" :: "v"(lds0 + L_R + lane * 32 + lw * 8), "v"(o0), "v"(o1) : "memory");
    }
    STAMP(3);
    WV(4);
    BAR();
    BAR();
    BAR();
    issueW(0);
    WV(0);
    BAR();
    auto rZ = RSRC(ws + WS_Z2, FRAG_BYTES);
    const unsigned sZ = fj * 128 * CH + zp0 * 1024;
    u64 ready = 0;
    auto issueZ = [&](int n) {
      const int kp = kpos(n);
      if (__builtin_expect(!got(ready, kp >> 1), 0)) { for (unsigned spins = 0; !got(ready, kp >> 1) && spins < SPIN_CAP; ++spins) ready = poll16(ws + WS_F1 + fj * 16 * 128, tag, lane); }
      MEMB();
      const unsigned dz = lds0 + (n & 3) * STG + 16384 + zp0 * 1024, so = sZ + kp * 18432;
      GS4(rZ, dz, vL, so);
      if (big) GS(rZ, dz + 4096, vL, so + 4096, 0);
    };
    issueW(2); issueW(3);
    issueZ(2); issueZ(3);
    STAMP(4);
    WVS(1, -4);
    BAR();
    STAMP(5);
    for (int n = 4; n < 31; ++n) {
      issueW(n); issueZ(n);
      WVS(1, 0);
      BAR();
    }
    issueW(31); issueZ(31);
    constexpr int TAIL = 9;
    unsigned vO[2], vOh;
#pragma unroll
    for (int q = 0; q < 2; ++q) { const int row = 36 * lw + 8 * q + (lane >> 3); vO[q] = row * 1152 + (((lane & 7) ^ (((row >> 1) & 1) | (row & 4))) << 4); }
    { const int row = 36 * lw + 32 + ((lane >> 3) & 3); vOh = row * 1152 + (((lane & 7) ^ (((row >> 1) & 1) | (row & 4))) << 4); }
    const unsigned vO1 = vO[1] - 1024, vO2 = vO[0] + 16 * 1152 - 2048, vO3 = vO[1] + 16 * 1152 - 3072;
    const unsigned sO = min(dd, 8) * 128;
    auto issueO = [&](int n, int slot) {
      const unsigned d = lds0 + L_O + slot * STGO + lw * 4608, s0 = sO + n * 144 * 1152;
      GL4(rWo, d, vO[0], vO1, vO2, vO3, s0);
      if (lane < 32) GL(rWo, d + 4096, vOh, s0, 0);
    };
    issueO(0, 0);
    WVS(1, 5); BAR();
    issueO(1, 1); issueO(2, 2);
    WV(15); BAR();
    BAR();
    WV(0);
    BAR();
    if (!has3) return;
    issueO(3, 3); issueO(4, 4);
    BAR();
    issueO(5, 0); issueO(6, 1); issueO(7, 2);
    auto rP = RSRC(ws + WS_P, FRAG_BYTES);
    const unsigned vPs = lw < 3 ? (lane >> 5) * 16 * CH + (lane & 31) * 16 + 4096 : OOB;
    const int limS = lw == 2 ? 32 : 64;
    const unsigned sP = tt * 5 * 16 * CH;
    const int pa = (tt * 80) >> 7, pb = min((tt * 80 + 79) >> 7, 15);
    u64 rdy = 0;
    auto issueP = [&](int j, int slot) {
      if (__builtin_expect(!got(rdy, j), 0)) { for (unsigned spins = 0; !got(rdy, j) && spins < SPIN_CAP; ++spins) rdy = poll16(ws + WS_F2 + pa * 16 * 128, tag, lane) & poll16(ws + WS_F2 + pb * 16 * 128, tag, lane); }
      MEMB();
      const unsigned d = lds0 + L_P + slot * STGP, sp = sP + j * CH;
      GS4(rP, d + lw * 4096, vL, sp + lw * 16 * CH);
      GS(rP, d + 16384 + lw * 1024, vL, sp + 64 * CH + lw * 1024, 0);
      if (lane < limS) GS(rP, lw < 3 ? d + 20480 + lw * 1024 : lds0 + L_DUMMY, vPs, sp + lw * 32 * CH, 0);
    };
    issueP(0, 0);
    STAMP(6);
    issueP(1, 1); issueP(2, 2);
    WV(12);
    BAR();
    STAMP(7);
    WV(6);
    BAR(); issueP(3, 0); WV(6);
    BAR(); issueP(4, 1); WV(6);
    BAR(); issueP(5, 2); WV(6);
    BAR(); issueP(6, 0); issueO(8, 3); WV(11);
    int sp3 = 1, so5 = 4;
    for (int n = 5; n < 12; ++n) {
      BAR();
      issueP(n + 2, sp3); issueO(n + 4, so5);
      sp3 = sp3 == 2 ? 0 : sp3 + 1; so5 = so5 == 4 ? 0 : so5 + 1;
      WV(16);
    }
    BAR(); issueP(14, 2); WV(6);
    BAR(); issueP(15, 0); WV(6);
    BAR(); WV(0);
    BAR();
    BAR(); BAR();
    STAMP(8);
    DUMP();
    if (bid == 0 && tid == 256) __hip_atomic_store((u64*)(ws + WS_EP), tag, RLX_AGENT);
  } else {
    const int wt = wid >> 1, wf = wid & 1;
    if (wid == 0) {
      auto rC = RSRC((const char*)(pc0 & ~(u64)255), CODE_KB * 1024);
      GL(rC, lds0 + L_DUMMY, lane * 16, rk * 1024, 0); GL(rC, lds0 + L_DUMMY, lane * 16, rk * 1024 + 32768, 0);
      WV(0);
    }
    if (wf == 0) front<0>(lds0, wid, wt, lane, ti, fj, ws); else front<1>(lds0, wid, wt, lane, ti, fj, ws);
    if (!has3) return;
    if (wt == 0) g3_compute<0>(lds0, wf, lane, out, tt, dd); else g3_compute<1>(lds0, wf, lane, out, tt, dd);
    STAMP(13);
    DUMP();
  }
}

extern "C" void kernel_launch(void* const* d_in, const int* in_sizes, int n_in, void* d_out, int out_size, void* d_ws, size_t ws_size, hipStream_t stream) {
  (void)hipFuncSetAttribute((const void*)gmlp_fused, hipFuncAttributeMaxDynamicSharedMemorySize, LDS_BYTES);
  gmlp_fused<<<256, 512, LDS_BYTES, stream>>>((const char*)d_in[0], (const char*)d_in[1], (const char*)d_in[2], (const u16*)d_in[3], (const char*)d_in[4], (char*)d_out, (char*)d_ws);
}
```

```cpp
#include <hip/hip_runtime.h>
#include <hip/hip_bf16.h>
typedef unsigned short u16;
typedef unsigned long long u64;
typedef short s16x4 __attribute__((ext_vector_type(4)));
typedef short bf16x8 __attribute__((ext_vector_type(8)));
typedef float f32x2 __attribute__((ext_vector_type(2)));
typedef float f32x4 __attribute__((ext_vector_type(4)));
typedef unsigned u32x4 __attribute__((ext_vector_type(4)));
typedef unsigned u32x2 __attribute__((ext_vector_type(2)));
#define LAS __attribute__((address_space(3)))
#define DEV __device__ __forceinline__
#define BAR() __builtin_amdgcn_s_barrier()
#define SB() __builtin_amdgcn_sched_barrier(0)
#define MEMB() asm volatile("" ::: "memory")
#define WAITL0() __builtin_amdgcn_s_waitcnt(0xC07F)
#define WL0() asm volatile("s_waitcnt lgkmcnt(0)" ::: "memory")
#define WV(N) asm volatile("s_waitcnt vmcnt(%0)" ::"i"(N) : "memory")
#define MFMA(a, b, c) __builtin_amdgcn_mfma_f32_16x16x32_bf16(a, b, c, 0, 0, 0)
#define MFMA16(a, b, c) __builtin_amdgcn_mfma_f32_16x16x16bf16_1k(a, b, c, 0, 0, 0)
#define GL(R, D, V, S, I) __builtin_amdgcn_raw_ptr_buffer_load_lds(R, (LAS void*)(size_t)(D), 16, V, S, I, 0)
#define GL4(R, D, V0, V1, V2, V3, S) do { GL(R, D, V0, S, 0); GL(R, D, V1, S, 1024); GL(R, D, V2, S, 2048); GL(R, D, V3, S, 3072); } while (0)
#define GS(R, D, V, S, I) __builtin_amdgcn_raw_ptr_buffer_load_lds(R, (LAS void*)(size_t)(D), 16, V, S, I, 16)
#define GS4(R, D, V, S) do { GS(R, D, V, S, 0); GS(R, D, V, S, 1024); GS(R, D, V, S, 2048); GS(R, D, V, S, 3072); } while (0)
#define TOUCH(R, V, S) __builtin_amdgcn_raw_ptr_buffer_load_lds(R, (LAS void*)(size_t)(lds0 + L_DUMMY), 4, V, S, 0, 0)
#define RSRC(p, n) __builtin_amdgcn_make_buffer_rsrc((void*)(p), 0, (n), 0x00020000)
#define RLX_AGENT __ATOMIC_RELAXED, __HIP_MEMORY_SCOPE_AGENT
#define ROT1 2
#define TW 1
#define NPRE3 5
#define Z1W 1
#define LS0 1
#define OAUX 16
#define WAUX 0
#define FLAGM -1
#define LA2 2
#define CODE_KB 54

constexpr int CH = 4608;
constexpr int FRAG_BYTES = 16 * 128 * CH;
constexpr size_t WS_Z2 = 0, WS_P = FRAG_BYTES, WS_ST = 2 * (size_t)FRAG_BYTES, WS_F1 = WS_ST + 16 * 16 * 128 * 16, WS_F2 = WS_F1 + 256 * 128, WS_EP = WS_F2 + 256 * 128;
constexpr int STG = 34816, STGP = 23040, STGO = 18432, L_O = 0, L_P = 5 * STGO, LDS_BYTES = 163840;
constexpr int L_T = 139264, L_R = L_T + 8192, L_X = 151552, L_BIAS = 159744, L_TAG = L_BIAS + 256, L_DUMMY = 161280;
constexpr unsigned SPIN_CAP = 1u << 16, OOB = 0x80000000u;
#define LBAR() BAR()

DEV unsigned cvt_pk(float lo, float hi) { unsigned r; asm("v_cvt_pk_bf16_f32 %0, %1, %2" : "=v"(r) : "v"(lo), "v"(hi)); return r; }
DEV bf16x8 ld128(unsigned a) { return *(LAS bf16x8*)(size_t)a; }
DEV s16x4 ld64(unsigned a) { return *(LAS s16x4*)(size_t)a; }
DEV s16x4 ldtr(unsigned a) { return __builtin_amdgcn_ds_read_tr16_b64_v4i16((LAS s16x4*)(size_t)a); }
DEV bf16x8 cat(s16x4 a, s16x4 b) { return __builtin_shufflevector(a, b, 0, 1, 2, 3, 4, 5, 6, 7); }
DEV float bflo(unsigned v) { return __uint_as_float(v << 16); }
DEV float bfhi(unsigned v) { return __uint_as_float(v & 0xffff0000u); }
template <int NB, bool SUM>
DEV void gelu_blocks(const f32x4* const (&v)[NB], u32x2* const (&o)[NB], f32x2& s1, f32x2& s2) {
  const float k1 = -2.0f * 0.7978845608028654f * 1.4426950408889634f, k2 = k1 * 0.044715f;
  f32x2 x[2 * NB], t[2 * NB];
#pragma unroll
  for (int i = 0; i < 2 * NB; ++i) { x[i] = (f32x2){(*v[i >> 1])[(i & 1) * 2], (*v[i >> 1])[(i & 1) * 2 + 1]}; t[i] = x[i] * x[i]; }
  SB();
#pragma unroll
  for (int i = 0; i < 2 * NB; ++i) t[i] = t[i] * k2 + k1;
  SB();
#pragma unroll
  for (int i = 0; i < 2 * NB; ++i) t[i] = t[i] * x[i];
  SB();
#pragma unroll
  for (int i = 0; i < 2 * NB; ++i) { t[i].x = __builtin_amdgcn_exp2f(t[i].x); t[i].y = __builtin_amdgcn_exp2f(t[i].y); }
  SB();
#pragma unroll
  for (int i = 0; i < 2 * NB; ++i) t[i] = t[i] + 1.0f;
  SB();
#pragma unroll
  for (int i = 0; i < 2 * NB; ++i) { t[i].x = __builtin_amdgcn_rcpf(t[i].x); t[i].y = __builtin_amdgcn_rcpf(t[i].y); }
  SB();
#pragma unroll
  for (int i = 0; i < 2 * NB; ++i) t[i] = t[i] * x[i];
  SB();
  f32x2 a1 = s1, a2 = s2, b1 = {0.f, 0.f}, b2 = {0.f, 0.f};
#pragma unroll
  for (int i = 0; i < NB; ++i) {
    o[i]->x = cvt_pk(t[2 * i].x, t[2 * i].y);
    if (SUM) { a1 += t[2 * i]; a2 += t[2 * i] * t[2 * i]; }
    o[i]->y = cvt_pk(t[2 * i + 1].x, t[2 * i + 1].y);
    if (SUM) { if (i == 0) { b1 = t[1]; b2 = t[1] * t[1]; } else { b1 += t[2 * i + 1]; b2 += t[2 * i + 1] * t[2 * i + 1]; } }
  }
  if (SUM) { s1 = a1 + b1; s2 = a2 + b2; }
  SB();
}
template <int WF, bool SUM>
DEV void gelu_row(int m, const f32x4 (&acc)[4][5], u32x2 (&oa)[4][4], u32x2 (&ob)[2], f32x2& s1, f32x2& s2) {
  if ((m >> 1) == WF) { const f32x4* const v[5] = {&acc[m][0], &acc[m][1], &acc[m][2], &acc[m][3], &acc[m][4]}; u32x2* const o[5] = {&oa[m][0], &oa[m][1], &oa[m][2], &oa[m][3], &ob[m & 1]}; gelu_blocks<5, SUM>(v, o, s1, s2); }
  else { const f32x4* const v[4] = {&acc[m][0], &acc[m][1], &acc[m][2], &acc[m][3]}; u32x2* const o[4] = {&oa[m][0], &oa[m][1], &oa[m][2], &oa[m][3]}; gelu_blocks<4, SUM>(v, o, s1, s2); }
}

struct TAddr { unsigned w[2], z[2][2], s[2]; };
struct TFrag { bf16x8 wX[4], wY[4]; s16x4 tX[5][2], tY[4][2]; };
template <int WF, int PS, int OFF, bool FIRST, bool SYNC = true>
DEV void t_step(TAddr& A, int dlt, f32x4 (&acc)[4][5], TFrag& F, const f32x4* init = nullptr) {
  constexpr int KX = WF, KY = 1 - WF;
  SB(); if (SYNC) BAR(); MEMB(); SB();
  auto rdz = [&](s16x4 (&t)[2], int n, int u, int K) { t[u] = n < 4 ? ldtr(A.z[u][n & 1] + OFF + K * 9216 + (n >> 1) * PS) : ldtr(A.s[u] + OFF + K * 9216); };
#pragma unroll
  for (int i = 0; i < 16; ++i) {
    const int m = i >> 2, n = i & 3;
    if (!FIRST) acc[m][n] = MFMA(cat(F.tY[n][0], F.tY[n][1]), F.wY[m], acc[m][n]);
    SB();
    if (i == 0) F.wX[0] = ld128(A.w[KX] + OFF);
    else if (i < 11) rdz(F.tX[(i - 1) >> 1], (i - 1) >> 1, (i - 1) & 1, KX);
    else if (i < 14) F.wX[i - 10] = ld128(A.w[KX] + OFF + (i - 10) * 2048);
    SB();
  }
#pragma unroll
  for (int i = 0; i < 20; ++i) {
    const int m = i / 5, n = i % 5;
    const f32x4 zero4 = {0, 0, 0, 0};
    acc[m][n] = MFMA(cat(F.tX[n][0], F.tX[n][1]), F.wX[m], !FIRST ? acc[m][n] : (PS == 1024 && (n < 4 || (m >> 1) == WF)) ? init[m] : zero4);
    SB();
    if (i == 0) F.wY[0] = ld128(A.w[KY] + OFF);
    else if (i < 9) rdz(F.tY[(i - 1) >> 1], (i - 1) >> 1, (i - 1) & 1, KY);
    else if (i < 12) F.wY[i - 8] = ld128(A.w[KY] + OFF + (i - 8) * 2048);
    else if (OFF != 0 && i < 16) {
      const int u = i & 1;
      if (i < 14) { A.w[u] += dlt; A.s[u] += dlt; asm volatile("" : "+v"(A.w[u]), "+v"(A.s[u])); }
      else { A.z[u][0] += dlt; A.z[u][1] += dlt; asm volatile("" : "+v"(A.z[u][0]), "+v"(A.z[u][1])); }
    }
    SB();
  }
  WAITL0();
}
template <int WF>
DEV void t_finish(unsigned lds0, int wt, int lane, f32x4 (&acc)[4][5], TFrag& F) {
#pragma unroll
  for (int h = 0; h < 2; ++h) *(LAS f32x4*)(size_t)(lds0 + L_X + ((wt * 2 + WF) * 2 + h) * 1024 + lane * 16) = acc[2 * (1 - WF) + h][4];
#pragma unroll
  for (int n = 0; n < 4; ++n)
#pragma unroll
    for (int m = 0; m < 4; ++m) acc[m][n] = MFMA(cat(F.tY[n][0], F.tY[n][1]), F.wY[m], acc[m][n]);
  WL0();
  BAR(); MEMB();
#pragma unroll
  for (int h = 0; h < 2; ++h) acc[2 * WF + h][4] += *(LAS f32x4*)(size_t)(lds0 + L_X + ((wt * 2 + 1 - WF) * 2 + h) * 1024 + lane * 16);
}
template <int WF, bool MID, class Mid>
DEV void in_pass(unsigned lds0, int wt, int lane, TAddr& A, f32x4 (&acc)[4][5], Mid mid) {
  TFrag F;
  int dlt = 2 * STG;
  WAITL0();
  t_step<WF, 64, 0, true>(A, dlt, acc, F);
  for (int k = 0; k < (MID ? 3 : 4); ++k) {
    t_step<WF, 64, STG, false>(A, dlt, acc, F);
    dlt = -dlt;
    t_step<WF, 64, 0, false>(A, dlt, acc, F);
  }
  if (MID) {
    SB(); BAR(); MEMB(); mid(); SB();
    t_step<WF, 64, STG, false>(A, -2 * STG, acc, F);
    t_step<WF, 64, 0, false>(A, 0, acc, F);
  }
  t_finish<WF>(lds0, wt, lane, acc, F);
}

struct G3P { bf16x8 p[2][5]; s16x4 ps[3]; };
struct G3W { s16x4 w[2][2][2], ws[2]; };
template <int KH>
DEV void g3_readw(G3W& n, unsigned a0, unsigned a1) {
#pragma unroll
  for (int q = 0; q < 8; ++q) n.w[q >> 2][(q >> 1) & 1][q & 1] = ldtr((((q >> 1) & 1) ? a1 : a0) + (2 * KH + (q >> 2)) * 4096 + (q & 1) * 2048);
  n.ws[0] = ldtr(a0 + 16384); n.ws[1] = ldtr(a1 + 16384);
}
template <int KH, bool MM, bool RDW>
DEV void g3_stage(f32x4 (&acc)[5][2], const G3P& cp, const G3W& cw, G3P& np, G3W& nw, unsigned aP, unsigned aS, unsigned a0, unsigned a1) {
  constexpr int M0 = KH ? 3 : 0, NS = KH ? 2 : 3, NM = 20 + 2 * NS, NR = 20 + NS;
  SB(); BAR(); MEMB(); SB();
#pragma unroll
  for (int i = 0; i < NM; ++i) {
    if (MM) {
      if (i >= 10 && i < 10 + 2 * NS) acc[M0 + ((i - 10) >> 1)][i & 1] = MFMA16(cw.ws[i & 1], cp.ps[(i - 10) >> 1], acc[M0 + ((i - 10) >> 1)][i & 1]);
      else { const int ii = i < 10 ? i : i - 2 * NS, P = ii / 10, mm = (ii % 10) >> 1, m = (KH != P) ? (mm + 3) % 5 : mm, cc = ii & 1; acc[m][cc] = MFMA(cat(cw.w[P][cc][0], cw.w[P][cc][1]), cp.p[P][m], acc[m][cc]); }
    }
    SB();
#pragma unroll
    for (int r = (i * 3) / 2; r < ((i + 1) * 3) / 2; ++r) {
      if (r < 18) { const int P = r / 9, q = r % 9;
        if (q < 4) { if (RDW) nw.w[P][q >> 1][q & 1] = ldtr(((q >> 1) ? a1 : a0) + (2 * KH + P) * 4096 + (q & 1) * 2048); }
        else np.p[P][q - 4] = ld128(aP + P * 1024 + (q - 4) * 4096); }
      else if (r < 20) { if (RDW) nw.ws[r - 18] = ldtr(((r - 18) ? a1 : a0) + 16384); }
      else if (r < NR) np.ps[r - 20] = ld64(aS + (M0 + r - 20) * 512);
    }
    SB();
  }
  WAITL0();
}
template <int KH>
DEV void g3_compute(unsigned lds0, int dh, int lane, char* out, int tt, int dd) {
  const int g = lane >> 4, qr = (lane >> 2) & 3, p = lane & 3;
  const int rr = 4 * g + qr;
  unsigned bW[2];
#pragma unroll
  for (int c = 0; c < 2; ++c) bW[c] = lds0 + L_O + rr * 128 + (((4 * dh + p) ^ (((rr >> 1) & 1) | (rr & 4))) << 4) + ((c ^ (p & 1)) * 8);
  const unsigned bP = lds0 + L_P + lane * 16 + KH * 2048, bS = lds0 + L_P + 20480 + lane * 8;
  G3W a0, a1, a2;
  g3_readw<KH>(a0, bW[0], bW[1]); g3_readw<KH>(a1, bW[0] + STGO, bW[1] + STGO); g3_readw<KH>(a2, bW[0] + 2 * STGO, bW[1] + 2 * STGO);
  f32x4 acc[5][2];
#pragma unroll
  for (int m = 0; m < 5; ++m) { acc[m][0] = (f32x4){0, 0, 0, 0}; acc[m][1] = (f32x4){0, 0, 0, 0}; }
  WAITL0();
  SB(); BAR(); SB();
  G3P p0, p1; G3W w0, w1;
  g3_stage<KH, false, false>(acc, p1, a0, p0, w0, bP, bS, 0, 0);
  g3_stage<KH, true, false>(acc, p0, a0, p1, w0, bP + STGP, bS + STGP, 0, 0);
  g3_stage<KH, true, false>(acc, p1, a1, p0, w0, bP + 2 * STGP, bS + 2 * STGP, 0, 0);
  g3_stage<KH, true, true>(acc, p0, a2, p1, w1, bP, bS, bW[0] + 3 * STGO, bW[1] + 3 * STGO);
  unsigned sp = STGP, sw = 4 * STGO;
  for (int j = 4; j < 16; j += 2) {
    g3_stage<KH, true, true>(acc, p1, w1, p0, w0, bP + sp, bS + sp, bW[0] + sw, bW[1] + sw);
    sp = sp == 2 * STGP ? 0 : sp + STGP; sw = sw == 4 * STGO ? 0 : sw + STGO;
    g3_stage<KH, true, true>(acc, p0, w0, p1, w1, bP + sp, bS + sp, bW[0] + sw, bW[1] + sw);
    sp = sp == 2 * STGP ? 0 : sp + STGP; sw = sw == 4 * STGO ? 0 : sw + STGO;
  }
  {
    constexpr int M0 = KH ? 3 : 0, NS = KH ? 2 : 3;
    SB(); BAR(); SB();
#pragma unroll
    for (int i = 0; i < 2 * NS; ++i) { acc[M0 + (i >> 1)][i & 1] = MFMA16(w1.ws[i & 1], p1.ps[i >> 1], acc[M0 + (i >> 1)][i & 1]); SB(); }
#pragma unroll
    for (int i = 0; i < 20; ++i) { const int P = (i >> 1) & 1, m = KH ? i >> 2 : 4 - (i >> 2), cc = i & 1; acc[m][cc] = MFMA(cat(w1.w[P][cc][0], w1.w[P][cc][1]), p1.p[P][m], acc[m][cc]); SB(); }
  }
#pragma unroll
  for (int m = 0; m < 5; ++m)
    if ((m >= 3) != KH) { *(LAS f32x4*)(size_t)(lds0 + (dh * 10 + m * 2) * 1024 + lane * 16) = acc[m][0]; *(LAS f32x4*)(size_t)(lds0 + (dh * 10 + m * 2 + 1) * 1024 + lane * 16) = acc[m][1]; }
  WL0();
  BAR(); MEMB();
  auto rO = RSRC(out, 2048 * 1152);
  const bool sw2 = g & 1;
#pragma unroll
  for (int m = 0; m < 5; ++m)
    if ((m >= 3) == KH) {
      const f32x4 a = acc[m][0] + *(LAS f32x4*)(size_t)(lds0 + (dh * 10 + m * 2) * 1024 + lane * 16), b = acc[m][1] + *(LAS f32x4*)(size_t)(lds0 + (dh * 10 + m * 2 + 1) * 1024 + lane * 16);
      const unsigned a0 = cvt_pk(a[0], a[1]), a1 = cvt_pk(a[2], a[3]), b0 = cvt_pk(b[0], b[1]), b1 = cvt_pk(b[2], b[3]);
      const u32x4 v = {sw2 ? b0 : a0, sw2 ? b1 : a1, sw2 ? a0 : b0, sw2 ? a1 : b1};
      __builtin_amdgcn_raw_buffer_store_b128(v, rO, (tt * 80 + m * 16 + (lane & 15)) * 1152 + dd * 128 + dh * 64 + g * 16, 0, OAUX);
    }
}

template <int WF>
DEV void front(unsigned lds0, int wid, int wt, int lane, int ti, int fj, char* ws) {
  const int r16 = lane & 15, g = lane >> 4, qr = (lane >> 2) & 3, p = lane & 3;
  TAddr A;
  A.w[0] = lds0 + (64 * wt + r16) * 128 + ((g ^ (r16 >> 1)) << 4); A.w[1] = A.w[0] ^ 64;
#pragma unroll
  for (int u = 0; u < 2; ++u) {
    const unsigned b = lds0 + 16384 + (16 * u + 4 * g + qr) * 288 + p * 8;
    A.z[u][0] = b + WF * 128 + (p & 1) * 32; A.z[u][1] = b + WF * 128 + 32 - (p & 1) * 32; A.s[u] = b + 256;
  }
  asm volatile("" : "+v"(A.w[0]), "+v"(A.w[1]), "+v"(A.s[0]), "+v"(A.s[1]), "+v"(A.z[0][0]), "+v"(A.z[0][1]), "+v"(A.z[1][0]), "+v"(A.z[1][1]));
  f32x4 acc[4][5];
  in_pass<WF, false>(lds0, wt, lane, A, acc, []() {});
  u32x2 ua[4][4], ub[2];
  {
    f32x2 s1[4], s2[4];
#pragma unroll
    for (int mm = 0; mm < 4; ++mm) {
      const int m = mm ^ (2 * (1 - WF));
      s1[m] = (f32x2){0.f, 0.f}; s2[m] = (f32x2){0.f, 0.f};
      gelu_row<WF, true>(m, acc, ua, ub, s1[m], s2[m]);
    }
#pragma unroll
    for (int m = 0; m < 4; ++m) *(LAS f32x2*)(size_t)(lds0 + L_T + (64 * wt + 16 * m + r16) * 64 + (WF * 4 + g) * 8) = (f32x2){s1[m].x + s1[m].y, s2[m].x + s2[m].y};
  }
#pragma unroll
  for (int m = 0; m < 4; ++m) asm volatile("" : "+v"(ua[m][0]), "+v"(ua[m][1]), "+v"(ua[m][2]), "+v"(ua[m][3]));
  asm volatile("" : "+v"(ub[0]), "+v"(ub[1]));
#pragma unroll
  for (int u = 0; u < 2; ++u) { A.z[u][0] -= (p & 1) * 32; A.z[u][1] += (p & 1) * 32; }
  asm volatile("" : "+v"(A.z[0][0]), "+v"(A.z[0][1]), "+v"(A.z[1][0]), "+v"(A.z[1][1]));
  in_pass<WF, true>(lds0, wt, lane, A, acc, [&]() {
#pragma unroll
  for (int m = 0; m < 4; ++m) asm volatile("" : "+v"(ua[m][0]), "+v"(ua[m][1]), "+v"(ua[m][2]), "+v"(ua[m][3]));
  asm volatile("" : "+v"(ub[0]), "+v"(ub[1]));
  int lz = lane; asm volatile("" : "+v"(lz));
  {
    const int r16 = lz & 15, g = lz >> 4;
    auto rZ = RSRC(ws + WS_Z2, FRAG_BYTES);
    const unsigned sl = g * 16 + (r16 ^ (4 * (g >> 1)));
#pragma unroll
    for (int m = 0; m < 4; ++m) {
      const f32x4 q4 = *(LAS f32x4*)(size_t)(lds0 + L_R + (64 * wt + 16 * m + r16) * 32) + *(LAS f32x4*)(size_t)(lds0 + L_R + (64 * wt + 16 * m + r16) * 32 + 16);
      const float mean = (q4[0] + q4[2]) * (1.0f / 2304.0f), var = fmaxf((q4[1] + q4[3]) * (1.0f / 2304.0f) - mean * mean, 0.f);
      const float rs = __builtin_amdgcn_rsqf(var + 1e-5f), sh = -mean * rs;
      auto norm = [&](unsigned v) { const f32x2 y = (f32x2){bflo(v), bfhi(v)} * rs + sh; return cvt_pk(y.x, y.y); };
      const unsigned dst = (fj * 128 + ti * 8 + wt * 4 + m) * CH;
#pragma unroll
      for (int pp = 0; pp < 2; ++pp) {
        const u32x2 a = ua[m][2 * pp], b = ua[m][2 * pp + 1];
        const u32x4 v = {norm(a.x), norm(a.y), norm(b.x), norm(b.y)};
        __builtin_amdgcn_raw_buffer_store_b128(v, rZ, dst + sl * 16 + WF * 2048 + pp * 1024, 0, 16);
        ua[m][2 * pp] = (u32x2){v.x, v.y}; ua[m][2 * pp + 1] = (u32x2){v.z, v.w};
      }
      if ((m >> 1) == WF) {
        const u32x2 v = {norm(ub[m & 1].x), norm(ub[m & 1].y)};
        __builtin_amdgcn_raw_buffer_store_b64(v, rZ, dst + sl * 8 + 4096, 0, 16);
        ub[m & 1] = v;
      }
    }
  }
  });
#pragma unroll
  for (int m = 0; m < 4; ++m) asm volatile("" : "+v"(acc[m][0]), "+v"(acc[m][1]), "+v"(acc[m][2]), "+v"(acc[m][3]), "+v"(acc[m][4]));
  const u32x2 tg = *(LAS u32x2*)(size_t)(lds0 + L_TAG);
  const u64 tag = ((u64)(unsigned)__builtin_amdgcn_readfirstlane(tg.y) << 32) | (unsigned)__builtin_amdgcn_readfirstlane(tg.x);
  if (FLAGM < 0) {
    WV(0);
    if (lane == 0) __hip_atomic_store((u64*)(ws + WS_F1 + (fj * 16 + ti) * 128) + wid, tag, RLX_AGENT);
  }
  {
    const unsigned ld = lds0 + wt * STG + 16384, sl = g * 16 + (r16 ^ (4 * (g >> 1)));
#pragma unroll
    for (int m = 0; m < 4; ++m) {
#pragma unroll
      for (int pp = 0; pp < 2; ++pp) *(LAS u32x4*)(size_t)(ld + m * CH + WF * 2048 + pp * 1024 + sl * 16) = (u32x4){ua[m][2 * pp].x, ua[m][2 * pp].y, ua[m][2 * pp + 1].x, ua[m][2 * pp + 1].y};
      if ((m >> 1) == WF) *(LAS u32x2*)(size_t)(ld + m * CH + 4096 + sl * 8) = ub[m & 1];
    }
  }
  SB();
  u32x2 za[4][4], zb[2];
#pragma unroll
  for (int m = 0; m < 4; ++m) {
    f32x2 d1, d2;
    gelu_row<WF, false>(m, acc, za, zb, d1, d2);
    asm volatile("" : "+v"(za[m][0]), "+v"(za[m][1]), "+v"(za[m][2]), "+v"(za[m][3]));
    if (m == (FLAGM < 0 ? 0 : FLAGM)) {
      SB();
      asm volatile("s_waitcnt vmcnt(1) lgkmcnt(0)" ::: "memory");
      if (FLAGM >= 0) WV(0);
      if (FLAGM >= 0 && lane == 0) __hip_atomic_store((u64*)(ws + WS_F1 + (fj * 16 + ti) * 128) + wid, tag, RLX_AGENT);
      BAR();
      SB();
    }
  }
  asm volatile("" : "+v"(zb[0]), "+v"(zb[1]));
#pragma unroll
  for (int u = 0; u < 2; ++u) {
    const unsigned sl = p * 16 + 8 * (g & 1) + 4 * (u ^ (p >> 1)) + qr;
#pragma unroll
    for (int h = 0; h < 2; ++h) A.z[u][h] = lds0 + 16384 + (g >> 1) * CH + WF * 2048 + sl * 16 + ((h ^ (p & 1)) * 8);
    A.s[u] = lds0 + 16384 + (g >> 1) * CH + 4096 + sl * 8;
  }
  asm volatile("" : "+v"(A.s[0]), "+v"(A.s[1]), "+v"(A.z[0][0]), "+v"(A.z[0][1]), "+v"(A.z[1][0]), "+v"(A.z[1][1]));
  f32x4 bias4[4];
#pragma unroll
  for (int m = 0; m < 4; ++m) { const float b = __uint_as_float((unsigned)*(LAS u16*)(size_t)(lds0 + L_BIAS + (wt * 64 + m * 16 + r16) * 2) << 16); bias4[m] = (f32x4){b, b, b, b}; }
  TFrag F;
  int dlt = 2 * STG;
  WAITL0();
  t_step<WF, 1024, 0, true, false>(A, dlt, acc, F, bias4);
  t_step<WF, 1024, STG, false, false>(A, dlt, acc, F);
  for (int k = 0; k < 15; ++k) {
    dlt = -dlt;
    t_step<WF, 1024, 0, false>(A, dlt, acc, F);
    t_step<WF, 1024, STG, false>(A, dlt, acc, F);
  }
  auto rP = RSRC(ws + WS_P, FRAG_BYTES);
  auto gate = [&](f32x4 a, u32x2 z) {
    const f32x2 x = (f32x2){a[0], a[1]} * (f32x2){bflo(z.x), bfhi(z.x)}, y = (f32x2){a[2], a[3]} * (f32x2){bflo(z.y), bfhi(z.y)};
    return (u32x2){cvt_pk(x.x, x.y), cvt_pk(y.x, y.y)};
  };
#pragma unroll
  for (int h = 0; h < 2; ++h) *(LAS f32x4*)(size_t)(lds0 + L_X + ((wt * 2 + WF) * 2 + h) * 1024 + lane * 16) = acc[2 * (1 - WF) + h][4];
  const unsigned dst0 = (ti * 8 + wt * 4) * 16 * CH + fj * CH;
#pragma unroll
  for (int m = 0; m < 4; ++m)
#pragma unroll
    for (int n = 0; n < 4; ++n) acc[m][n] = MFMA(cat(F.tY[n][0], F.tY[n][1]), F.wY[m], acc[m][n]);
  WL0();
  BAR(); MEMB();
  f32x4 o8[2];
#pragma unroll
  for (int h = 0; h < 2; ++h) o8[h] = *(LAS f32x4*)(size_t)(lds0 + L_X + ((wt * 2 + 1 - WF) * 2 + h) * 1024 + lane * 16);
#pragma unroll
  for (int m = 0; m < 4; ++m)
#pragma unroll
    for (int pp = 0; pp < 2; ++pp) {
      const u32x2 a = gate(acc[m][2 * pp], za[m][2 * pp]), b = gate(acc[m][2 * pp + 1], za[m][2 * pp + 1]);
      __builtin_amdgcn_raw_buffer_store_b128((u32x4){a.x, a.y, b.x, b.y}, rP, dst0 + m * 16 * CH + lane * 16 + (2 * WF + pp) * 1024, 0, 16);
    }
#pragma unroll
  for (int h = 0; h < 2; ++h) __builtin_amdgcn_raw_buffer_store_b64(gate(acc[2 * WF + h][4] + o8[h], zb[h]), rP, dst0 + (2 * WF + h) * 16 * CH + lane * 8 + 4096, 0, 16);
  WV(0);
  if (lane == 0) __hip_atomic_store((u64*)(ws + WS_F2 + (ti * 16 + fj) * 128) + wid, tag, RLX_AGENT);
  SB(); BAR(); MEMB(); SB();
}

DEV u64 poll16(const char* base, u64 tag, int lane) {
  const u64 v = __hip_atomic_load((const u64*)(base + (lane >> 2) * 128) + (lane & 3), RLX_AGENT);
  return __builtin_amdgcn_ballot_w64(v == tag);
}
DEV bool got(u64 mask, int who) { return ((unsigned)(mask >> (4 * who)) & 15u) == 15u; }

__global__ __launch_bounds__(512, 2) void gmlp_fused(const char* __restrict__ X, const char* __restrict__ Win, const char* __restrict__ Wsp, const u16* __restrict__ bias,
                                                     const char* __restrict__ Wo, char* __restrict__ out, char* __restrict__ ws) {
  extern __shared__ __attribute__((aligned(16))) char smem[];
  u64 pc0; asm volatile("s_getpc_b64 %0" : "=s"(pc0));
  const int tid = threadIdx.x, wid = __builtin_amdgcn_readfirstlane(tid >> 6), lane = tid & 63;
  const int bid = blockIdx.x, xg = bid & 7, rk = bid >> 3, il = rk & 7, jl = rk >> 3, ti = (xg & 1) * 8 + il, fj = (xg >> 1) * 4 + jl;
  const int L3 = xg * 30 + rk, tt = L3 / 9, dd = L3 - tt * 9;
  const bool has3 = rk < 30 && L3 < 234;
  const unsigned lds0 = (unsigned)(size_t)(LAS char*)smem;
  if (__builtin_expect(wid >= 4, 1)) {
    __builtin_amdgcn_s_setprio(3);
    const int lw = wid - 4;
    const bool big = wid < 6;
#define WVS(STAGES, EXTRA) do { if (big) WV(9 * (STAGES) + (EXTRA)); else WV(8 * (STAGES) + (EXTRA)); } while (0)
    auto rX = RSRC(X, 2048 * 1152); auto rWi = RSRC(Win, 576 * 9216);
    unsigned vX[4], vW[5];
    const unsigned zp0 = lw < 2 ? lw * 5 : 2 + lw * 4;
    const unsigned sX = (ti * 128 + lw * 32) * 1152, sW = fj * 288;
    const int rot = ROT1 == 0 ? 0 : ROT1 == 1 ? il + 2 * jl : 3 * ((il + jl) % 3);
    const int rot8 = ROT1 == 1 ? (il + 2 * jl) & 7 : ROT1 == 2 ? (((il + jl) % 3) * 8) / 3 : 0;
    auto k1 = [&](int k) { return k == 0 ? 0 : 1 + ((k - 1 + rot8) & 7); };
    auto issue1 = [&](int half, int k) {
      const int kk = k1(k);
      const unsigned d = lds0 + (k & 3) * STG, dz = d + 16384 + zp0 * 1024, sw = sW + half * 4608 + kk * 64 * 9216;
      GL4(rX, d + lw * 4096, vX[0], vX[1], vX[2], vX[3], sX + kk * 128);
      GL4(rWi, dz, vW[0], vW[1], vW[2], vW[3], sw);
      if (big) GL(rWi, dz + 4096, vW[4], sw, 0);
    };
    {
#pragma unroll
      for (int q = 0; q < 4; ++q) { const int row = 8 * q + (lane >> 3); vX[q] = row * 1152 + (((lane & 7) ^ ((row >> 1) & 7)) << 4) - q * 1024; }
      GL4(rX, lds0 + lw * 4096, vX[0], vX[1], vX[2], vX[3], sX + k1(0) * 128);
      SB();
#pragma unroll
      for (int q = 0; q < 5; ++q) {
        const unsigned c16 = (zp0 + q) * 64 + lane, R = (c16 * 3641u) >> 16, c = (c16 - R * 18) * 16, k = (R & 32) + 8 * ((R >> 2) & 3) + 4 * ((R >> 4) & 1) + (R & 3);
        vW[q] = k * 9216 + c - (q & 3) * 1024;
      }
      const unsigned dz = lds0 + 16384 + zp0 * 1024, sw = sW + 4608 + k1(0) * 64 * 9216;
      GL4(rWi, dz, vW[0], vW[1], vW[2], vW[3], sw);
      if (big) GL(rWi, dz + 4096, vW[4], sw, 0);
    }
    const unsigned vL = lane * 16;
    auto rWs = RSRC(Wsp, 2048 * 4096); auto rWo = RSRC(Wo, 2304 * 1152);
    issue1(1, 1);
    WVS(1, 0); LBAR();
    issue1(1, 2);
    WVS(1, 0);
    LBAR(); issue1(1, 3); WVS(1, 0);
    LBAR(); issue1(1, 4);
    {
      const unsigned e = lw * 21 + lane, r = (e * 7282u) >> 16;
      TOUCH(rWi, lane < 21 && e < 81 ? r * 9216 + (e - r * 9) * 128 : OOB, (bid >> 2) * 9 * 9216 + (bid & 3) * 1152);
    }
    WVS(1, 1);
    LBAR(); issue1(1, 5); WVS(1, 1);
    LBAR(); issue1(1, 6); WVS(1, 0);
    LBAR(); issue1(1, 7); WVS(1, 0);
    LBAR(); issue1(1, 8); WVS(1, 0);
    LBAR(); issue1(0, 1); WVS(1, 0);
    LBAR(); issue1(0, 2);
    const u64 ep = __hip_atomic_load((const u64*)(ws + WS_EP), RLX_AGENT);
    if (lw == 0) __builtin_amdgcn_raw_ptr_buffer_load_lds(RSRC(bias, 4096), (LAS void*)(size_t)(lds0 + L_BIAS), 4, lane * 4, ti * 256, 0, 0);
    LBAR();
    issue1(0, 0); issue1(0, 3);
    TOUCH(rWs, lane * 128, bid * 32768 + lw * 8192);
    TOUCH(rWo, lane < 21 ? lane * 128 : OOB, bid * 10368 + lw * 2688);
    constexpr int NWARM = 2;
    const unsigned tagA = __builtin_amdgcn_readfirstlane((unsigned)ep) + 0x9E3779B9u, tagB = __builtin_amdgcn_readfirstlane((unsigned)(ep >> 32)) + 0x85EBCA6Bu;
    const u64 tag = ((u64)tagB << 32) | tagA;
    if (lw == 0) { const u32x2 tg = {tagA, tagB}; asm volatile("ds_write_b64 %0, %1\n\ts_waitcnt lgkmcnt(0)" :: "v"(lds0 + L_TAG), "v"(tg) : "memory"); }
    unsigned vS[4];
#pragma unroll
    for (int q = 0; q < 4; ++q) { const int row = 8 * q + (lane >> 3); vS[q] = row * 4096 + (((lane & 7) ^ ((row >> 1) & 7)) << 4) - q * 1024; }
    const unsigned sWs = (ti * 128 + lw * 32) * 4096;
    auto kpos = [&](int n) { return n < 2 ? 2 * ti + n : (n - 2 < 2 * ti ? n - 2 : n); };
    auto issueW = [&](int n) { GL4(rWs, lds0 + (n & 3) * STG + lw * 4096, vS[0], vS[1], vS[2], vS[3], sWs + kpos(n) * 128); };
    WVS(1, NWARM);
    LBAR();
    auto rS = RSRC(ws + WS_ST, 16 * 16 * 128 * 16);
    if (lane < 32) {
      const int row = lw * 32 + lane;
      f32x4 v0, v1, v2, v3;
      asm volatile("ds_read_b128 %0, %4\n\tds_read_b128 %1, %4 offset:16\n\tds_read_b128 %2, %4 offset:32\n\tds_read_b128 %3, %4 offset:48\n\ts_waitcnt lgkmcnt(0)"
                   : "=&v"(v0), "=&v"(v1), "=&v"(v2), "=&v"(v3) : "v"(lds0 + L_T + row * 64) : "memory");
      const f32x4 s = (v0 + v1) + (v2 + v3);
      u32x4 gq; gq.x = tagA; gq.y = __float_as_uint(s[0] + s[2]); gq.z = tagB; gq.w = __float_as_uint(s[1] + s[3]);
      __builtin_amdgcn_raw_buffer_store_b128(gq, rS, ((ti * 16 + fj) * 128 + row) * 16, 0, 16);
    }
    u32x4 sv[8];
    const u32x4 srd = {(unsigned)(size_t)(ws + WS_ST), (unsigned)((size_t)(ws + WS_ST) >> 32) & 0xffffu, 16 * 16 * 128 * 16, 0x00020000u};
    auto sweep = [&]() {
      const unsigned s0 = (ti * 16 + lw * 4) * 2048, v1 = vL + 4096;
      asm volatile("buffer_load_dwordx4 %0, %8, %10, %11 offen sc1\n\tbuffer_load_dwordx4 %1, %8, %10, %11 offen offset:1024 sc1\n\tbuffer_load_dwordx4 %2, %8, %10, %11 offen offset:2048 sc1\n\t"
                   "buffer_load_dwordx4 %3, %8, %10, %11 offen offset:3072 sc1\n\tbuffer_load_dwordx4 %4, %9, %10, %11 offen sc1\n\tbuffer_load_dwordx4 %5, %9, %10, %11 offen offset:1024 sc1\n\t"
                   "buffer_load_dwordx4 %6, %9, %10, %11 offen offset:2048 sc1\n\tbuffer_load_dwordx4 %7, %9, %10, %11 offen offset:3072 sc1"
                   : "=&v"(sv[0]), "=&v"(sv[1]), "=&v"(sv[2]), "=&v"(sv[3]), "=&v"(sv[4]), "=&v"(sv[5]), "=&v"(sv[6]), "=&v"(sv[7]) : "v"(vL), "v"(v1), "s"(srd), "s"(s0) : "memory");
    };
    LBAR(); issue1(0, 4);
    LBAR(); issue1(0, 5); WVS(2, 0);
    LBAR(); issue1(0, 6); WVS(2, 0);
    LBAR(); issue1(0, 7); WVS(2, 0);
    LBAR(); sweep(); WVS(1, 8);
    LBAR(); issue1(0, 8);
    {
      f32x4 s;
      auto total = [&](const u32x4 (&v)[8]) {
        bool ok = true;
        s = (f32x4){0.f, 0.f, 0.f, 0.f};
#pragma unroll
        for (int p = 0; p < 8; ++p) { ok &= (v[p].x == tagA) & (v[p].z == tagB); s[(p & 1) * 2] += __uint_as_float(v[p].y); s[(p & 1) * 2 + 1] += __uint_as_float(v[p].w); }
        return (bool)__all(ok);
      };
      WVS(1, 0);
      asm volatile("" : "+v"(sv[0]), "+v"(sv[1]), "+v"(sv[2]), "+v"(sv[3]), "+v"(sv[4]), "+v"(sv[5]), "+v"(sv[6]), "+v"(sv[7]) :: "memory");
      if (__builtin_expect(!total(sv), 0)) {
#pragma unroll 1
        for (unsigned spins = 0;; ++spins) {
          u32x4 rv[8];
          MEMB();
#pragma unroll
          for (int p = 0; p < 8; ++p) rv[p] = __builtin_amdgcn_raw_buffer_load_b128(rS, vL + (p & 1) * 1024, (ti * 16 + lw * 4 + (p >> 1)) * 2048, 16);
          if (total(rv)) break;
          if (spins > SPIN_CAP) { s[0] = s[2] = __builtin_nanf(""); break; }
        }
      }
      const f32x2 o0 = {s[0], s[1]}, o1 = {s[2], s[3]};
      asm volatile("ds_write_b64 %0, %1\n\tds_write_b64 %0, %2 offset:2048\n\ts_waitcnt lgkmcnt(0)" :: "v"(lds0 + L_R + lane * 32 + lw * 8), "v"(o0), "v"(o1) : "memory");
    }
    LBAR();
    LBAR();
    issueW(1);
    WV(4);
    LBAR();
    issueW(2);
    LBAR();
    issueW(0); issueW(3);
    WV(4);
    LBAR();
    auto rZ = RSRC(ws + WS_Z2, FRAG_BYTES);
    const unsigned sZ = fj * 128 * CH + zp0 * 1024;
    u64 ready = 0;
    auto issueZ = [&](int n) {
      const int kp = kpos(n);
      if (__builtin_expect(!got(ready, kp >> 1), 0)) { for (unsigned spins = 0; !got(ready, kp >> 1) && spins < SPIN_CAP; ++spins) ready = poll16(ws + WS_F1 + fj * 16 * 128, tag, lane); }
      MEMB();
      const unsigned dz = lds0 + (n & 3) * STG + 16384 + zp0 * 1024, so = sZ + kp * 18432;
      GS4(rZ, dz, vL, so);
      if (big) GS(rZ, dz + 4096, vL, so + 4096, 0);
    };
    issueZ(2); issueZ(3);
    WVS(1, -4);
    LBAR();
    for (int n = 4; n < 31; ++n) {
      issueW(n); issueZ(n);
      WVS(1, 0);
      LBAR();
    }
    issueW(31); issueZ(31);
    constexpr int TAIL = 9;
    unsigned vO[2], vOh;
#pragma unroll
    for (int q = 0; q < 2; ++q) { const int row = 36 * lw + 8 * q + (lane >> 3); vO[q] = row * 1152 + (((lane & 7) ^ (((row >> 1) & 1) | (row & 4))) << 4); }
    { const int row = 36 * lw + 32 + ((lane >> 3) & 3); vOh = row * 1152 + (((lane & 7) ^ (((row >> 1) & 1) | (row & 4))) << 4); }
    const unsigned vO1 = vO[1] - 1024, vO2 = vO[0] + 16 * 1152 - 2048, vO3 = vO[1] + 16 * 1152 - 3072;
    const unsigned sO = min(dd, 8) * 128;
    auto issueO = [&](int n, int slot) {
      const unsigned d = lds0 + L_O + slot * STGO + lw * 4608, s0 = sO + n * 144 * 1152;
      GL4(rWo, d, vO[0], vO1, vO2, vO3, s0);
      if (lane < 32) GL(rWo, d + 4096, vOh, s0, 0);
    };
    issueO(0, 0);
    WVS(1, 5); LBAR();
    issueO(1, 1); issueO(2, 2);
    WV(15); LBAR();
    LBAR();
    WV(0);
    LBAR();
    if (!has3) return;
    issueO(3, 3); issueO(4, 4);
    LBAR();
    issueO(5, 0);
    if (NPRE3 == 5) { issueO(6, 1); issueO(7, 2); }
    auto rP = RSRC(ws + WS_P, FRAG_BYTES);
    const unsigned vPs = lw < 3 ? (lane >> 5) * 16 * CH + (lane & 31) * 16 + 4096 : OOB;
    const int limS = lw == 2 ? 32 : 64;
    const unsigned sP = tt * 5 * 16 * CH;
    const int pa = (tt * 80) >> 7, pb = min((tt * 80 + 79) >> 7, 15);
    u64 rdy = 0;
    auto issueP = [&](int j, int slot) {
      if (__builtin_expect(!got(rdy, j), 0)) { for (unsigned spins = 0; !got(rdy, j) && spins < SPIN_CAP; ++spins) rdy = poll16(ws + WS_F2 + pa * 16 * 128, tag, lane) & poll16(ws + WS_F2 + pb * 16 * 128, tag, lane); }
      MEMB();
      const unsigned d = lds0 + L_P + slot * STGP, sp = sP + j * CH;
      GS4(rP, d + lw * 4096, vL, sp + lw * 16 * CH);
      GS(rP, d + 16384 + lw * 1024, vL, sp + 64 * CH + lw * 1024, 0);
      if (lane < limS) GS(rP, lw < 3 ? d + 20480 + lw * 1024 : lds0 + L_DUMMY, vPs, sp + lw * 32 * CH, 0);
    };
    issueP(0, 0);
    issueP(1, 1); issueP(2, 2);
    WV(12);
    LBAR();
    WV(6);
    if (NPRE3 == 5) {
      LBAR(); issueP(3, 0); WV(6);
      LBAR(); issueP(4, 1); WV(6);
      LBAR(); issueP(5, 2); WV(6);
    } else {
      LBAR(); issueP(3, 0); issueO(6, 1); WV(11);
      LBAR(); issueP(4, 1); issueO(7, 2); WV(16);
      LBAR(); issueP(5, 2); WV(11);
    }
    LBAR(); issueP(6, 0); issueO(8, 3); WV(11);
    int sp3 = 1, so5 = 4;
    for (int n = 5; n < 12; ++n) {
      LBAR();
      issueP(n + 2, sp3); issueO(n + 4, so5);
      sp3 = sp3 == 2 ? 0 : sp3 + 1; so5 = so5 == 4 ? 0 : so5 + 1;
      WV(16);
    }
    LBAR(); issueP(14, 2); WV(6);
    LBAR(); issueP(15, 0); WV(6);
    LBAR(); WV(0);
    LBAR();
    LBAR(); LBAR();
    if (bid == 0 && tid == 256) __hip_atomic_store((u64*)(ws + WS_EP), tag, RLX_AGENT);
  } else {
    const int wt = wid >> 1, wf = wid & 1;
    if (wid == 0) {
      auto rC = RSRC((const char*)(pc0 & ~(u64)255), CODE_KB * 1024);
      GL(rC, lds0 + L_DUMMY, lane * 16, rk * 1024, 0); GL(rC, lds0 + L_DUMMY, lane * 16, rk * 1024 + 32768, 0);
      WV(0);
    }
    if (wf == 0) front<0>(lds0, wid, wt, lane, ti, fj, ws); else front<1>(lds0, wid, wt, lane, ti, fj, ws);
    if (!has3) return;
    if (wt == 0) g3_compute<0>(lds0, wf, lane, out, tt, dd); else g3_compute<1>(lds0, wf, lane, out, tt, dd);
  }
}

extern "C" void kernel_launch(void* const* d_in, const int* in_sizes, int n_in, void* d_out, int out_size, void* d_ws, size_t ws_size, hipStream_t stream) {
  (void)hipFuncSetAttribute((const void*)gmlp_fused, hipFuncAttributeMaxDynamicSharedMemorySize, LDS_BYTES);
  gmlp_fused<<<256, 512, LDS_BYTES, stream>>>((const char*)d_in[0], (const char*)d_in[1], (const char*)d_in[2], (const u16*)d_in[3], (const char*)d_in[4], (char*)d_out, (char*)d_ws);
}
```

```cpp
#define PROBE 1
#include <hip/hip_runtime.h>
#include <hip/hip_bf16.h>
typedef unsigned short u16;
typedef unsigned long long u64;
typedef short s16x4 __attribute__((ext_vector_type(4)));
typedef short bf16x8 __attribute__((ext_vector_type(8)));
typedef float f32x2 __attribute__((ext_vector_type(2)));
typedef float f32x4 __attribute__((ext_vector_type(4)));
typedef unsigned u32x4 __attribute__((ext_vector_type(4)));
typedef unsigned u32x2 __attribute__((ext_vector_type(2)));
#define LAS __attribute__((address_space(3)))
#define DEV __device__ __forceinline__
#define BAR() __builtin_amdgcn_s_barrier()
#define SB() __builtin_amdgcn_sched_barrier(0)
#define MEMB() asm volatile("" ::: "memory")
#define WAITL0() __builtin_amdgcn_s_waitcnt(0xC07F)
#define WL0() asm volatile("s_waitcnt lgkmcnt(0)" ::: "memory")
#define WV(N) asm volatile("s_waitcnt vmcnt(%0)" ::"i"(N) : "memory")
#define MFMA(a, b, c) __builtin_amdgcn_mfma_f32_16x16x32_bf16(a, b, c, 0, 0, 0)
#define MFMA16(a, b, c) __builtin_amdgcn_mfma_f32_16x16x16bf16_1k(a, b, c, 0, 0, 0)
#define GL(R, D, V, S, I) __builtin_amdgcn_raw_ptr_buffer_load_lds(R, (LAS void*)(size_t)(D), 16, V, S, I, 0)
#define GL4(R, D, V0, V1, V2, V3, S) do { GL(R, D, V0, S, 0); GL(R, D, V1, S, 1024); GL(R, D, V2, S, 2048); GL(R, D, V3, S, 3072); } while (0)
#define GS(R, D, V, S, I) __builtin_amdgcn_raw_ptr_buffer_load_lds(R, (LAS void*)(size_t)(D), 16, V, S, I, 16)
#define GS4(R, D, V, S) do { GS(R, D, V, S, 0); GS(R, D, V, S, 1024); GS(R, D, V, S, 2048); GS(R, D, V, S, 3072); } while (0)
#define TOUCH(R, V, S) __builtin_amdgcn_raw_ptr_buffer_load_lds(R, (LAS void*)(size_t)(lds0 + L_DUMMY), 4, V, S, 0, 0)
#define RSRC(p, n) __builtin_amdgcn_make_buffer_rsrc((void*)(p), 0, (n), 0x00020000)
#define RLX_AGENT __ATOMIC_RELAXED, __HIP_MEMORY_SCOPE_AGENT
#define ROT1 2
#define TW 1
#define NPRE3 3
#define Z1W 1
#define LS0 1
#define OAUX 16
#define WAUX 0
#define MIDP 7
#define PDELAY 7
#define PSLEEP 0
#define LA2 2
#define CODE_KB 54

constexpr int CH = 4608;
constexpr int FRAG_BYTES = 16 * 128 * CH;
constexpr size_t WS_Z2 = 0, WS_P = FRAG_BYTES, WS_ST = 2 * (size_t)FRAG_BYTES, WS_F1 = WS_ST + 16 * 16 * 128 * 16, WS_F2 = WS_F1 + 256 * 128, WS_EP = WS_F2 + 256 * 128;
constexpr int STG = 34816, STGP = 23040, STGO = 18432, L_O = 0, L_P = 5 * STGO, LDS_BYTES = 163840;
constexpr int L_T = 139264, L_R = L_T + 8192, L_X = 151552, L_BIAS = 159744, L_TAG = L_BIAS + 256, L_DUMMY = 161280;
constexpr unsigned SPIN_CAP = 1u << 16, OOB = 0x80000000u;
#define LBAR() BAR()
#define CST(I)
#define LST(I, X)

DEV unsigned cvt_pk(float lo, float hi) { unsigned r; asm("v_cvt_pk_bf16_f32 %0, %1, %2" : "=v"(r) : "v"(lo), "v"(hi)); return r; }
DEV bf16x8 ld128(unsigned a) { return *(LAS bf16x8*)(size_t)a; }
DEV s16x4 ld64(unsigned a) { return *(LAS s16x4*)(size_t)a; }
DEV s16x4 ldtr(unsigned a) { return __builtin_amdgcn_ds_read_tr16_b64_v4i16((LAS s16x4*)(size_t)a); }
DEV bf16x8 cat(s16x4 a, s16x4 b) { return __builtin_shufflevector(a, b, 0, 1, 2, 3, 4, 5, 6, 7); }
DEV float bflo(unsigned v) { return __uint_as_float(v << 16); }
DEV float bfhi(unsigned v) { return __uint_as_float(v & 0xffff0000u); }
template <int NB, bool SUM>
DEV void gelu_blocks(const f32x4* const (&v)[NB], u32x2* const (&o)[NB], f32x2& s1, f32x2& s2) {
  const float k1 = -2.0f * 0.7978845608028654f * 1.4426950408889634f, k2 = k1 * 0.044715f;
  f32x2 x[2 * NB], t[2 * NB];
#pragma unroll
  for (int i = 0; i < 2 * NB; ++i) { x[i] = (f32x2){(*v[i >> 1])[(i & 1) * 2], (*v[i >> 1])[(i & 1) * 2 + 1]}; t[i] = x[i] * x[i]; }
  SB();
#pragma unroll
  for (int i = 0; i < 2 * NB; ++i) t[i] = t[i] * k2 + k1;
  SB();
#pragma unroll
  for (int i = 0; i < 2 * NB; ++i) t[i] = t[i] * x[i];
  SB();
#pragma unroll
  for (int i = 0; i < 2 * NB; ++i) { t[i].x = __builtin_amdgcn_exp2f(t[i].x); t[i].y = __builtin_amdgcn_exp2f(t[i].y); }
  SB();
#pragma unroll
  for (int i = 0; i < 2 * NB; ++i) t[i] = t[i] + 1.0f;
  SB();
#pragma unroll
  for (int i = 0; i < 2 * NB; ++i) { t[i].x = __builtin_amdgcn_rcpf(t[i].x); t[i].y = __builtin_amdgcn_rcpf(t[i].y); }
  SB();
#pragma unroll
  for (int i = 0; i < 2 * NB; ++i) t[i] = t[i] * x[i];
  SB();
  f32x2 a1 = s1, a2 = s2, b1 = {0.f, 0.f}, b2 = {0.f, 0.f};
#pragma unroll
  for (int i = 0; i < NB; ++i) {
    o[i]->x = cvt_pk(t[2 * i].x, t[2 * i].y);
    if (SUM) { a1 += t[2 * i]; a2 += t[2 * i] * t[2 * i]; }
    o[i]->y = cvt_pk(t[2 * i + 1].x, t[2 * i + 1].y);
    if (SUM) { if (i == 0) { b1 = t[1]; b2 = t[1] * t[1]; } else { b1 += t[2 * i + 1]; b2 += t[2 * i + 1] * t[2 * i + 1]; } }
  }
  if (SUM) { s1 = a1 + b1; s2 = a2 + b2; }
  SB();
}
template <int WF, bool SUM>
DEV void gelu_row(int m, const f32x4 (&acc)[4][5], u32x2 (&oa)[4][4], u32x2 (&ob)[2], f32x2& s1, f32x2& s2) {
  if ((m >> 1) == WF) { const f32x4* const v[5] = {&acc[m][0], &acc[m][1], &acc[m][2], &acc[m][3], &acc[m][4]}; u32x2* const o[5] = {&oa[m][0], &oa[m][1], &oa[m][2], &oa[m][3], &ob[m & 1]}; gelu_blocks<5, SUM>(v, o, s1, s2); }
  else { const f32x4* const v[4] = {&acc[m][0], &acc[m][1], &acc[m][2], &acc[m][3]}; u32x2* const o[4] = {&oa[m][0], &oa[m][1], &oa[m][2], &oa[m][3]}; gelu_blocks<4, SUM>(v, o, s1, s2); }
}

struct TAddr { unsigned w[2], z[2][2], s[2]; };
struct TFrag { bf16x8 wX[4], wY[4]; s16x4 tX[5][2], tY[4][2]; };
template <int WF, int PS, int OFF, bool FIRST, bool SYNC = true>
DEV void t_step(TAddr& A, int dlt, f32x4 (&acc)[4][5], TFrag& F, const f32x4* init = nullptr) {
  constexpr int KX = WF, KY = 1 - WF;
  SB(); if (SYNC) BAR(); MEMB(); SB();
  auto rdz = [&](s16x4 (&t)[2], int n, int u, int K) { t[u] = n < 4 ? ldtr(A.z[u][n & 1] + OFF + K * 9216 + (n >> 1) * PS) : ldtr(A.s[u] + OFF + K * 9216); };
#pragma unroll
  for (int i = 0; i < 16; ++i) {
    const int m = i >> 2, n = i & 3;
    if (!FIRST) acc[m][n] = MFMA(cat(F.tY[n][0], F.tY[n][1]), F.wY[m], acc[m][n]);
    SB();
    if (i == 0) F.wX[0] = ld128(A.w[KX] + OFF);
    else if (i < 11) rdz(F.tX[(i - 1) >> 1], (i - 1) >> 1, (i - 1) & 1, KX);
    else if (i < 14) F.wX[i - 10] = ld128(A.w[KX] + OFF + (i - 10) * 2048);
    SB();
  }
#pragma unroll
  for (int i = 0; i < 20; ++i) {
    const int m = i / 5, n = i % 5;
    const f32x4 zero4 = {0, 0, 0, 0};
    acc[m][n] = MFMA(cat(F.tX[n][0], F.tX[n][1]), F.wX[m], !FIRST ? acc[m][n] : (PS == 1024 && (n < 4 || (m >> 1) == WF)) ? init[m] : zero4);
    SB();
    if (i == 0) F.wY[0] = ld128(A.w[KY] + OFF);
    else if (i < 9) rdz(F.tY[(i - 1) >> 1], (i - 1) >> 1, (i - 1) & 1, KY);
    else if (i < 12) F.wY[i - 8] = ld128(A.w[KY] + OFF + (i - 8) * 2048);
    else if (OFF != 0 && i < 16) {
      const int u = i & 1;
      if (i < 14) { A.w[u] += dlt; A.s[u] += dlt; asm volatile("" : "+v"(A.w[u]), "+v"(A.s[u])); }
      else { A.z[u][0] += dlt; A.z[u][1] += dlt; asm volatile("" : "+v"(A.z[u][0]), "+v"(A.z[u][1])); }
    }
    SB();
  }
  WAITL0();
}
template <int WF>
DEV void t_finish(unsigned lds0, int wt, int lane, f32x4 (&acc)[4][5], TFrag& F) {
#pragma unroll
  for (int h = 0; h < 2; ++h) *(LAS f32x4*)(size_t)(lds0 + L_X + ((wt * 2 + WF) * 2 + h) * 1024 + lane * 16) = acc[2 * (1 - WF) + h][4];
  SB();
#pragma unroll
  for (int m = 0; m < 4; ++m)
#pragma unroll
    for (int n = 0; n < 4; ++n) acc[m][n] = MFMA(cat(F.tY[n][0], F.tY[n][1]), F.wY[m], acc[m][n]);
  SB();
  WL0();
  BAR(); MEMB();
#pragma unroll
  for (int h = 0; h < 2; ++h) acc[2 * WF + h][4] += *(LAS f32x4*)(size_t)(lds0 + L_X + ((wt * 2 + 1 - WF) * 2 + h) * 1024 + lane * 16);
}
template <int WF, bool MID, class Mid>
DEV void in_pass(unsigned lds0, int wt, int lane, TAddr& A, f32x4 (&acc)[4][5], Mid mid) {
  TFrag F;
  int dlt = 2 * STG;
  WAITL0();
  t_step<WF, 64, 0, true>(A, dlt, acc, F);
  for (int k = 0; k < (MID ? 3 : 4); ++k) {
    t_step<WF, 64, STG, false>(A, dlt, acc, F);
    dlt = -dlt;
    t_step<WF, 64, 0, false>(A, dlt, acc, F);
  }
  if (MID) {
    t_step<WF, 64, STG, false>(A, -2 * STG, acc, F);
    SB(); BAR(); MEMB(); mid(); SB();
    t_step<WF, 64, 0, false>(A, 0, acc, F);
  }
  t_finish<WF>(lds0, wt, lane, acc, F);
}

struct G3P { bf16x8 p[2][5]; s16x4 ps[3]; };
struct G3W { s16x4 w[2][2][2], ws[2]; };
template <int KH>
DEV void g3_readw(G3W& n, unsigned a0, unsigned a1) {
#pragma unroll
  for (int q = 0; q < 8; ++q) n.w[q >> 2][(q >> 1) & 1][q & 1] = ldtr((((q >> 1) & 1) ? a1 : a0) + (2 * KH + (q >> 2)) * 4096 + (q & 1) * 2048);
  n.ws[0] = ldtr(a0 + 16384); n.ws[1] = ldtr(a1 + 16384);
}
template <int KH, bool MM, bool RDW>
DEV void g3_stage(f32x4 (&acc)[5][2], const G3P& cp, const G3W& cw, G3P& np, G3W& nw, unsigned aP, unsigned aS, unsigned a0, unsigned a1) {
  constexpr int M0 = KH ? 3 : 0, NS = KH ? 2 : 3, NM = 20 + 2 * NS, NR = 20 + NS;
  SB(); BAR(); MEMB(); SB();
#pragma unroll
  for (int i = 0; i < NM; ++i) {
    if (MM) {
      if (i >= 10 && i < 10 + 2 * NS) acc[M0 + ((i - 10) >> 1)][i & 1] = MFMA16(cw.ws[i & 1], cp.ps[(i - 10) >> 1], acc[M0 + ((i - 10) >> 1)][i & 1]);
      else { const int ii = i < 10 ? i : i - 2 * NS, P = ii / 10, mm = (ii % 10) >> 1, m = (KH != P) ? (mm + 3) % 5 : mm, cc = ii & 1; acc[m][cc] = MFMA(cat(cw.w[P][cc][0], cw.w[P][cc][1]), cp.p[P][m], acc[m][cc]); }
    }
    SB();
#pragma unroll
    for (int r = (i * 3) / 2; r < ((i + 1) * 3) / 2; ++r) {
      if (r < 18) { const int P = r / 9, q = r % 9;
        if (q < 4) { if (RDW) nw.w[P][q >> 1][q & 1] = ldtr(((q >> 1) ? a1 : a0) + (2 * KH + P) * 4096 + (q & 1) * 2048); }
        else np.p[P][q - 4] = ld128(aP + P * 1024 + (q - 4) * 4096); }
      else if (r < 20) { if (RDW) nw.ws[r - 18] = ldtr(((r - 18) ? a1 : a0) + 16384); }
      else if (r < NR) np.ps[r - 20] = ld64(aS + (M0 + r - 20) * 512);
    }
    SB();
  }
  WAITL0();
}
template <int KH>
DEV void g3_compute(unsigned lds0, int dh, int lane, char* out, int tt, int dd) {
  const int g = lane >> 4, qr = (lane >> 2) & 3, p = lane & 3;
  const int rr = 4 * g + qr;
  unsigned bW[2];
#pragma unroll
  for (int c = 0; c < 2; ++c) bW[c] = lds0 + L_O + rr * 128 + (((4 * dh + p) ^ (((rr >> 1) & 1) | (rr & 4))) << 4) + ((c ^ (p & 1)) * 8);
  const unsigned bP = lds0 + L_P + lane * 16 + KH * 2048, bS = lds0 + L_P + 20480 + lane * 8;
  G3W a0, a1, a2;
  g3_readw<KH>(a0, bW[0], bW[1]); g3_readw<KH>(a1, bW[0] + STGO, bW[1] + STGO); g3_readw<KH>(a2, bW[0] + 2 * STGO, bW[1] + 2 * STGO);
  f32x4 acc[5][2];
#pragma unroll
  for (int m = 0; m < 5; ++m) { acc[m][0] = (f32x4){0, 0, 0, 0}; acc[m][1] = (f32x4){0, 0, 0, 0}; }
  WAITL0();
  SB(); BAR(); SB();
  G3P p0, p1; G3W w0, w1;
  g3_stage<KH, false, false>(acc, p1, a0, p0, w0, bP, bS, 0, 0);
  g3_stage<KH, true, false>(acc, p0, a0, p1, w0, bP + STGP, bS + STGP, 0, 0);
  g3_stage<KH, true, false>(acc, p1, a1, p0, w0, bP + 2 * STGP, bS + 2 * STGP, 0, 0);
  g3_stage<KH, true, true>(acc, p0, a2, p1, w1, bP, bS, bW[0] + 3 * STGO, bW[1] + 3 * STGO);
  unsigned sp = STGP, sw = 4 * STGO;
  for (int j = 4; j < 16; j += 2) {
    g3_stage<KH, true, true>(acc, p1, w1, p0, w0, bP + sp, bS + sp, bW[0] + sw, bW[1] + sw);
    sp = sp == 2 * STGP ? 0 : sp + STGP; sw = sw == 4 * STGO ? 0 : sw + STGO;
    g3_stage<KH, true, true>(acc, p0, w0, p1, w1, bP + sp, bS + sp, bW[0] + sw, bW[1] + sw);
    sp = sp == 2 * STGP ? 0 : sp + STGP; sw = sw == 4 * STGO ? 0 : sw + STGO;
  }
  {
    constexpr int M0 = KH ? 3 : 0, NS = KH ? 2 : 3;
    SB(); BAR(); SB();
#pragma unroll
    for (int i = 0; i < 2 * NS; ++i) { acc[M0 + (i >> 1)][i & 1] = MFMA16(w1.ws[i & 1], p1.ps[i >> 1], acc[M0 + (i >> 1)][i & 1]); SB(); }
#pragma unroll
    for (int i = 0; i < 20; ++i) { const int P = (i >> 1) & 1, m = KH ? i >> 2 : 4 - (i >> 2), cc = i & 1; acc[m][cc] = MFMA(cat(w1.w[P][cc][0], w1.w[P][cc][1]), p1.p[P][m], acc[m][cc]); SB(); }
  }
#pragma unroll
  for (int m = 0; m < 5; ++m)
    if ((m >= 3) != KH) { *(LAS f32x4*)(size_t)(lds0 + (dh * 10 + m * 2) * 1024 + lane * 16) = acc[m][0]; *(LAS f32x4*)(size_t)(lds0 + (dh * 10 + m * 2 + 1) * 1024 + lane * 16) = acc[m][1]; }
  WL0();
  BAR(); MEMB();
  auto rO = RSRC(out, 2048 * 1152);
  const bool sw2 = g & 1;
#pragma unroll
  for (int m = 0; m < 5; ++m)
    if ((m >= 3) == KH) {
      const f32x4 a = acc[m][0] + *(LAS f32x4*)(size_t)(lds0 + (dh * 10 + m * 2) * 1024 + lane * 16), b = acc[m][1] + *(LAS f32x4*)(size_t)(lds0 + (dh * 10 + m * 2 + 1) * 1024 + lane * 16);
      const unsigned a0 = cvt_pk(a[0], a[1]), a1 = cvt_pk(a[2], a[3]), b0 = cvt_pk(b[0], b[1]), b1 = cvt_pk(b[2], b[3]);
      const u32x4 v = {sw2 ? b0 : a0, sw2 ? b1 : a1, sw2 ? a0 : b0, sw2 ? a1 : b1};
      __builtin_amdgcn_raw_buffer_store_b128(v, rO, (tt * 80 + m * 16 + (lane & 15)) * 1152 + dd * 128 + dh * 64 + g * 16, 0, OAUX);
    }
}

template <int WF>
DEV void front(unsigned lds0, int wid, int wt, int lane, int ti, int fj, char* ws) {
  const int r16 = lane & 15, g = lane >> 4, qr = (lane >> 2) & 3, p = lane & 3;
  TAddr A;
  A.w[0] = lds0 + (64 * wt + r16) * 128 + ((g ^ (r16 >> 1)) << 4); A.w[1] = A.w[0] ^ 64;
#pragma unroll
  for (int u = 0; u < 2; ++u) {
    const unsigned b = lds0 + 16384 + (16 * u + 4 * g + qr) * 288 + p * 8;
    A.z[u][0] = b + WF * 128 + (p & 1) * 32; A.z[u][1] = b + WF * 128 + 32 - (p & 1) * 32; A.s[u] = b + 256;
  }
  asm volatile("" : "+v"(A.w[0]), "+v"(A.w[1]), "+v"(A.s[0]), "+v"(A.s[1]), "+v"(A.z[0][0]), "+v"(A.z[0][1]), "+v"(A.z[1][0]), "+v"(A.z[1][1]));
  f32x4 acc[4][5];
  in_pass<WF, false>(lds0, wt, lane, A, acc, []() {});
  CST(0);
  u32x2 ua[4][4], ub[2];
  {
    f32x2 s1[4], s2[4];
#pragma unroll
    for (int mm = 0; mm < 4; ++mm) {
      const int m = mm ^ (2 * (1 - WF));
      s1[m] = (f32x2){0.f, 0.f}; s2[m] = (f32x2){0.f, 0.f};
      gelu_row<WF, true>(m, acc, ua, ub, s1[m], s2[m]);
    }
#pragma unroll
    for (int m = 0; m < 4; ++m) *(LAS f32x2*)(size_t)(lds0 + L_T + (64 * wt + 16 * m + r16) * 64 + (WF * 4 + g) * 8) = (f32x2){s1[m].x + s1[m].y, s2[m].x + s2[m].y};
  }
#pragma unroll
  for (int m = 0; m < 4; ++m) asm volatile("" : "+v"(ua[m][0]), "+v"(ua[m][1]), "+v"(ua[m][2]), "+v"(ua[m][3]));
  asm volatile("" : "+v"(ub[0]), "+v"(ub[1]));
#pragma unroll
  for (int u = 0; u < 2; ++u) { A.z[u][0] -= (p & 1) * 32; A.z[u][1] += (p & 1) * 32; }
  asm volatile("" : "+v"(A.z[0][0]), "+v"(A.z[0][1]), "+v"(A.z[1][0]), "+v"(A.z[1][1]));
  CST(1);
  in_pass<WF, true>(lds0, wt, lane, A, acc, [&]() {
    CST(2);
#pragma unroll
    for (int m = 0; m < 4; ++m) asm volatile("" : "+v"(ua[m][0]), "+v"(ua[m][1]), "+v"(ua[m][2]), "+v"(ua[m][3]));
    asm volatile("" : "+v"(ub[0]), "+v"(ub[1]));
    int lz = lane; asm volatile("" : "+v"(lz));
    auto rZ = RSRC(ws + WS_Z2, FRAG_BYTES);
    const unsigned sl = (lz >> 4) * 16 + ((lz & 15) ^ (4 * (lz >> 5))), dst = (fj * 128 + ti * 8 + wt * 4) * CH;
#pragma unroll
    for (int m = 0; m < 4; ++m) {
      const unsigned ar = lds0 + L_R + (64 * wt + 16 * m + (lz & 15)) * 32;
      const f32x4 q4 = *(LAS f32x4*)(size_t)ar + *(LAS f32x4*)(size_t)(ar + 16);
      const float mean = (q4[0] + q4[2]) * (1.0f / 2304.0f), var = fmaxf((q4[1] + q4[3]) * (1.0f / 2304.0f) - mean * mean, 0.f);
      const float rs = __builtin_amdgcn_rsqf(var + 1e-5f), sh = -mean * rs;
      auto norm = [&](unsigned v) { const f32x2 y = (f32x2){bflo(v), bfhi(v)} * rs + sh; return cvt_pk(y.x, y.y); };
#pragma unroll
      for (int pp = 0; pp < 2; ++pp)
        __builtin_amdgcn_raw_buffer_store_b128((u32x4){norm(ua[m][2 * pp].x), norm(ua[m][2 * pp].y), norm(ua[m][2 * pp + 1].x), norm(ua[m][2 * pp + 1].y)}, rZ, dst + m * CH + sl * 16 + WF * 2048 + pp * 1024, 0, 16);
      if ((m >> 1) == WF) __builtin_amdgcn_raw_buffer_store_b64((u32x2){norm(ub[m & 1].x), norm(ub[m & 1].y)}, rZ, dst + m * CH + sl * 8 + 4096, 0, 16);
    }
    CST(3);
  });
  CST(4);
#pragma unroll
  for (int m = 0; m < 4; ++m) asm volatile("" : "+v"(acc[m][0]), "+v"(acc[m][1]), "+v"(acc[m][2]), "+v"(acc[m][3]), "+v"(acc[m][4]));
  const u32x2 tg = *(LAS u32x2*)(size_t)(lds0 + L_TAG);
  const u64 tag = ((u64)(unsigned)__builtin_amdgcn_readfirstlane(tg.y) << 32) | (unsigned)__builtin_amdgcn_readfirstlane(tg.x);
  WV(0);
  if (lane == 0) __hip_atomic_store((u64*)(ws + WS_F1 + (fj * 16 + ti) * 128) + wid, tag, RLX_AGENT);
  CST(5);
  SB();
  u32x2 za[4][4], zb[2];
#pragma unroll
  for (int m = 0; m < 4; ++m) {
    f32x2 d1, d2;
    gelu_row<WF, false>(m, acc, za, zb, d1, d2);
    asm volatile("" : "+v"(za[m][0]), "+v"(za[m][1]), "+v"(za[m][2]), "+v"(za[m][3]));
  }
  asm volatile("" : "+v"(zb[0]), "+v"(zb[1]));
  CST(6);
#pragma unroll
  for (int u = 0; u < 2; ++u) {
    const unsigned sl = p * 16 + 8 * (g & 1) + 4 * (u ^ (p >> 1)) + qr;
#pragma unroll
    for (int h = 0; h < 2; ++h) A.z[u][h] = lds0 + 16384 + (g >> 1) * CH + WF * 2048 + sl * 16 + ((h ^ (p & 1)) * 8);
    A.s[u] = lds0 + 16384 + (g >> 1) * CH + 4096 + sl * 8;
  }
  asm volatile("" : "+v"(A.s[0]), "+v"(A.s[1]), "+v"(A.z[0][0]), "+v"(A.z[0][1]), "+v"(A.z[1][0]), "+v"(A.z[1][1]));
  f32x4 bias4[4];
#pragma unroll
  for (int m = 0; m < 4; ++m) { const float b = __uint_as_float((unsigned)*(LAS u16*)(size_t)(lds0 + L_BIAS + (wt * 64 + m * 16 + r16) * 2) << 16); bias4[m] = (f32x4){b, b, b, b}; }
  TFrag F;
  int dlt = 2 * STG;
  WAITL0();
  CST(7);
  t_step<WF, 1024, 0, true, true>(A, dlt, acc, F, bias4);
  CST(8);
  t_step<WF, 1024, STG, false, true>(A, dlt, acc, F);

  for (int k = 0; k < 15; ++k) {
    dlt = -dlt;
    t_step<WF, 1024, 0, false>(A, dlt, acc, F);
    t_step<WF, 1024, STG, false>(A, dlt, acc, F);
  }
  auto rP = RSRC(ws + WS_P, FRAG_BYTES);
  auto gate = [&](f32x4 a, u32x2 z) {
    const f32x2 x = (f32x2){a[0], a[1]} * (f32x2){bflo(z.x), bfhi(z.x)}, y = (f32x2){a[2], a[3]} * (f32x2){bflo(z.y), bfhi(z.y)};
    return (u32x2){cvt_pk(x.x, x.y), cvt_pk(y.x, y.y)};
  };
#pragma unroll
  for (int h = 0; h < 2; ++h) *(LAS f32x4*)(size_t)(lds0 + L_X + ((wt * 2 + WF) * 2 + h) * 1024 + lane * 16) = acc[2 * (1 - WF) + h][4];
  const unsigned dst0 = (ti * 8 + wt * 4) * 16 * CH + fj * CH;
  CST(9);
#pragma unroll
  for (int m = 0; m < 4; ++m)
#pragma unroll
    for (int n = 0; n < 4; ++n) acc[m][n] = MFMA(cat(F.tY[n][0], F.tY[n][1]), F.wY[m], acc[m][n]);
#pragma unroll
  for (int m = 0; m < 4; ++m)
#pragma unroll
    for (int pp = 0; pp < 2; ++pp) {
      const u32x2 a = gate(acc[m][2 * pp], za[m][2 * pp]), b = gate(acc[m][2 * pp + 1], za[m][2 * pp + 1]);
      __builtin_amdgcn_raw_buffer_store_b128((u32x4){a.x, a.y, b.x, b.y}, rP, dst0 + m * 16 * CH + lane * 16 + (2 * WF + pp) * 1024, 0, 16);
    }
  SB();
  WL0();
  BAR(); MEMB();
#pragma unroll
  for (int h = 0; h < 2; ++h) {
    const f32x4 o8 = *(LAS f32x4*)(size_t)(lds0 + L_X + ((wt * 2 + 1 - WF) * 2 + h) * 1024 + lane * 16);
    __builtin_amdgcn_raw_buffer_store_b64(gate(acc[2 * WF + h][4] + o8, zb[h]), rP, dst0 + (2 * WF + h) * 16 * CH + lane * 8 + 4096, 0, 16);
  }
  CST(10);
  WV(0);
  CST(11);
  if (lane == 0) __hip_atomic_store((u64*)(ws + WS_F2 + (ti * 16 + fj) * 128) + wid, tag, RLX_AGENT);
  SB(); BAR(); MEMB(); SB();
}

DEV u64 poll16(const char* base, u64 tag, int lane) {
  const u64 v = __hip_atomic_load((const u64*)(base + (lane >> 2) * 128) + (lane & 3), RLX_AGENT);
  return __builtin_amdgcn_ballot_w64(v == tag);
}
template <int NP> DEV u64 pollN(const char* base, u64 tag, int lane) {
  const u64 v = __hip_atomic_load((const u64*)(base + min(lane >> 2, NP - 1) * 128) + (lane & 3), RLX_AGENT);
  return __builtin_amdgcn_ballot_w64(v == tag) & ((1ull << (4 * NP)) - 1);
}
DEV bool got(u64 mask, int who) { return ((unsigned)(mask >> (4 * who)) & 15u) == 15u; }

__global__ __launch_bounds__(512, 2) void gmlp_fused(const char* __restrict__ X, const char* __restrict__ Win, const char* __restrict__ Wsp, const u16* __restrict__ bias,
                                                     const char* __restrict__ Wo, char* __restrict__ out, char* __restrict__ ws) {
  extern __shared__ __attribute__((aligned(16))) char smem[];
  u64 pc0; asm volatile("s_getpc_b64 %0" : "=s"(pc0));
  const int tid = threadIdx.x, wid = __builtin_amdgcn_readfirstlane(tid >> 6), lane = tid & 63;
  const int bid = blockIdx.x, xg = bid & 7, rk = bid >> 3, il = rk & 7, jl = rk >> 3, ti = (xg & 1) * 8 + il, fj = (xg >> 1) * 4 + jl;
  const int L3 = xg * 30 + rk, tt = L3 / 9, dd = L3 - tt * 9;
  const bool has3 = rk < 30 && L3 < 234;
  const unsigned lds0 = (unsigned)(size_t)(LAS char*)smem;
  if (__builtin_expect(wid >= 4, 1)) {
    __builtin_amdgcn_s_setprio(3);
    const u64 rt0p = __builtin_amdgcn_s_memrealtime(); u64 rt1 = rt0p; WAITL0();
#define MARK(I) do { if (PROBE == (I)) { rt1 = __builtin_amdgcn_s_memrealtime(); WAITL0(); } } while (0)
    const int lw = wid - 4;
    const bool big = wid < 6;
#define WVS(STAGES, EXTRA) do { if (big) WV(9 * (STAGES) + (EXTRA)); else WV(8 * (STAGES) + (EXTRA)); } while (0)
    auto rX = RSRC(X, 2048 * 1152); auto rWi = RSRC(Win, 576 * 9216);
    unsigned vX[4], vW[5];
    const unsigned zp0 = lw < 2 ? lw * 5 : 2 + lw * 4;
    const unsigned sX = (ti * 128 + lw * 32) * 1152, sW = fj * 288;
    const int rot = ROT1 == 0 ? 0 : ROT1 == 1 ? il + 2 * jl : 3 * ((il + jl) % 3);
    const int rot8 = ROT1 == 1 ? (il + 2 * jl) & 7 : ROT1 == 2 ? (((il + jl) % 3) * 8) / 3 : 0;
    auto k1 = [&](int k) { return k == 0 ? 0 : 1 + ((k - 1 + rot8) & 7); };
    auto issue1 = [&](int half, int k) {
      const int kk = k1(k);
      const unsigned d = lds0 + (k & 3) * STG, dz = d + 16384 + zp0 * 1024, sw = sW + half * 4608 + kk * 64 * 9216;
      GL4(rX, d + lw * 4096, vX[0], vX[1], vX[2], vX[3], sX + kk * 128);
      GL4(rWi, dz, vW[0], vW[1], vW[2], vW[3], sw);
      if (big) GL(rWi, dz + 4096, vW[4], sw, 0);
    };
    {
#pragma unroll
      for (int q = 0; q < 4; ++q) { const int row = 8 * q + (lane >> 3); vX[q] = row * 1152 + (((lane & 7) ^ ((row >> 1) & 7)) << 4) - q * 1024; }
      GL4(rX, lds0 + lw * 4096, vX[0], vX[1], vX[2], vX[3], sX + k1(0) * 128);
      SB();
#pragma unroll
      for (int q = 0; q < 5; ++q) {
        const unsigned c16 = (zp0 + q) * 64 + lane, R = (c16 * 3641u) >> 16, c = (c16 - R * 18) * 16, k = (R & 32) + 8 * ((R >> 2) & 3) + 4 * ((R >> 4) & 1) + (R & 3);
        vW[q] = k * 9216 + c - (q & 3) * 1024;
      }
      const unsigned dz = lds0 + 16384 + zp0 * 1024, sw = sW + 4608 + k1(0) * 64 * 9216;
      GL4(rWi, dz, vW[0], vW[1], vW[2], vW[3], sw);
      if (big) GL(rWi, dz + 4096, vW[4], sw, 0);
    }
    const unsigned vL = lane * 16;
    auto rWs = RSRC(Wsp, 2048 * 4096); auto rWo = RSRC(Wo, 2304 * 1152);
    issue1(1, 1);
    WVS(1, 0); LBAR();
    issue1(1, 2);
    WVS(1, 0);
    LBAR(); issue1(1, 3); WVS(1, 0);
    LBAR(); issue1(1, 4);
    {
      const unsigned e = lw * 21 + lane, r = (e * 7282u) >> 16;
      TOUCH(rWi, lane < 21 && e < 81 ? r * 9216 + (e - r * 9) * 128 : OOB, (bid >> 2) * 9 * 9216 + (bid & 3) * 1152);
    }
    WVS(1, 1);
    LBAR(); issue1(1, 5); WVS(1, 1);
    LBAR(); issue1(1, 6); WVS(1, 0);
    LBAR(); issue1(1, 7); WVS(1, 0);
    LBAR(); issue1(1, 8); WVS(1, 0);
    LBAR(); issue1(0, 1); WVS(1, 0);
    LBAR(); issue1(0, 2);
    const u64 ep = __hip_atomic_load((const u64*)(ws + WS_EP), RLX_AGENT);
    if (lw == 0) __builtin_amdgcn_raw_ptr_buffer_load_lds(RSRC(bias, 4096), (LAS void*)(size_t)(lds0 + L_BIAS), 4, lane * 4, ti * 256, 0, 0);
    LBAR();
    MARK(1);
    issue1(0, 0); issue1(0, 3);
    TOUCH(rWs, lane * 128, bid * 32768 + lw * 8192);
    TOUCH(rWo, lane < 21 ? lane * 128 : OOB, bid * 10368 + lw * 2688);
    constexpr int NWARM = 2;
    const unsigned tagA = __builtin_amdgcn_readfirstlane((unsigned)ep) + 0x9E3779B9u, tagB = __builtin_amdgcn_readfirstlane((unsigned)(ep >> 32)) + 0x85EBCA6Bu;
    const u64 tag = ((u64)tagB << 32) | tagA;
    if (lw == 0) { const u32x2 tg = {tagA, tagB}; asm volatile("ds_write_b64 %0, %1\n\ts_waitcnt lgkmcnt(0)" :: "v"(lds0 + L_TAG), "v"(tg) : "memory"); }
    unsigned vS[4];
#pragma unroll
    for (int q = 0; q < 4; ++q) { const int row = 8 * q + (lane >> 3); vS[q] = row * 4096 + (((lane & 7) ^ ((row >> 1) & 7)) << 4) - q * 1024; }
    const unsigned sWs = (ti * 128 + lw * 32) * 4096;
    auto kpos = [&](int n) { return n; };
    auto issueW = [&](int n) { GL4(rWs, lds0 + (n & 3) * STG + lw * 4096, vS[0], vS[1], vS[2], vS[3], sWs + kpos(n) * 128); };
    WVS(1, NWARM);
    LBAR();
    auto rS = RSRC(ws + WS_ST, 16 * 16 * 128 * 16);
    if (lane < 32) {
      const int row = lw * 32 + lane;
      f32x4 v0, v1, v2, v3;
      asm volatile("ds_read_b128 %0, %4\n\tds_read_b128 %1, %4 offset:16\n\tds_read_b128 %2, %4 offset:32\n\tds_read_b128 %3, %4 offset:48\n\ts_waitcnt lgkmcnt(0)"
                   : "=&v"(v0), "=&v"(v1), "=&v"(v2), "=&v"(v3) : "v"(lds0 + L_T + row * 64) : "memory");
      const f32x4 s = (v0 + v1) + (v2 + v3);
      u32x4 gq; gq.x = tagA; gq.y = __float_as_uint(s[0] + s[2]); gq.z = tagB; gq.w = __float_as_uint(s[1] + s[3]);
      __builtin_amdgcn_raw_buffer_store_b128(gq, rS, ((ti * 16 + fj) * 128 + row) * 16, 0, 16);
    }
    u32x4 sv[8];
    const u32x4 srd = {(unsigned)(size_t)(ws + WS_ST), (unsigned)((size_t)(ws + WS_ST) >> 32) & 0xffffu, 16 * 16 * 128 * 16, 0x00020000u};
    auto sweep = [&]() {
      const unsigned s0 = (ti * 16 + lw * 4) * 2048, v1 = vL + 4096;
      asm volatile("buffer_load_dwordx4 %0, %8, %10, %11 offen sc1\n\tbuffer_load_dwordx4 %1, %8, %10, %11 offen offset:1024 sc1\n\tbuffer_load_dwordx4 %2, %8, %10, %11 offen offset:2048 sc1\n\t"
                   "buffer_load_dwordx4 %3, %8, %10, %11 offen offset:3072 sc1\n\tbuffer_load_dwordx4 %4, %9, %10, %11 offen sc1\n\tbuffer_load_dwordx4 %5, %9, %10, %11 offen offset:1024 sc1\n\t"
                   "buffer_load_dwordx4 %6, %9, %10, %11 offen offset:2048 sc1\n\tbuffer_load_dwordx4 %7, %9, %10, %11 offen offset:3072 sc1"
                   : "=&v"(sv[0]), "=&v"(sv[1]), "=&v"(sv[2]), "=&v"(sv[3]), "=&v"(sv[4]), "=&v"(sv[5]), "=&v"(sv[6]), "=&v"(sv[7]) : "v"(vL), "v"(v1), "s"(srd), "s"(s0) : "memory");
    };
    LBAR(); issue1(0, 4);
    LBAR(); issue1(0, 5); WVS(2, 0);
    LBAR(); issue1(0, 6); WVS(2, 0);
    LBAR(); issue1(0, 7); WVS(2, 0);
    LBAR(); sweep(); WVS(1, 8);
    LBAR(); issue1(0, 8);
    WVS(1, 8);
    LBAR();
    {
      f32x4 s;
      auto total = [&](const u32x4 (&v)[8]) {
        unsigned bad = 0;
        s = (f32x4){0.f, 0.f, 0.f, 0.f};
#pragma unroll
        for (int p = 0; p < 8; ++p) { bad |= (v[p].x ^ tagA) | (v[p].z ^ tagB); s[(p & 1) * 2] += __uint_as_float(v[p].y); s[(p & 1) * 2 + 1] += __uint_as_float(v[p].w); }
        return __builtin_amdgcn_ballot_w64(bad != 0) == 0;
      };
      WVS(1, 0);
      asm volatile("" : "+v"(sv[0]), "+v"(sv[1]), "+v"(sv[2]), "+v"(sv[3]), "+v"(sv[4]), "+v"(sv[5]), "+v"(sv[6]), "+v"(sv[7]) :: "memory");
      if (__builtin_expect(!total(sv), 0)) {
#pragma unroll 1
        for (unsigned spins = 0;; ++spins) {
          u32x4 rv[8];
          MEMB();
#pragma unroll
          for (int p = 0; p < 8; ++p) rv[p] = __builtin_amdgcn_raw_buffer_load_b128(rS, vL + (p & 1) * 1024, (ti * 16 + lw * 4 + (p >> 1)) * 2048, 16);
          if (total(rv)) break;
          if (spins > SPIN_CAP) { s[0] = s[2] = __builtin_nanf(""); break; }
        }
      }
      const f32x2 o0 = {s[0], s[1]}, o1 = {s[2], s[3]};
      asm volatile("ds_write_b64 %0, %1\n\tds_write_b64 %0, %2 offset:2048\n\ts_waitcnt lgkmcnt(0)" :: "v"(lds0 + L_R + lane * 32 + lw * 8), "v"(o0), "v"(o1) : "memory");
    }
    LBAR();
    WV(0);
    LBAR();
    issueW(1); issueW(2);
    LBAR();
    MARK(2);
    issueW(0); issueW(3);
    auto rZ = RSRC(ws + WS_Z2, FRAG_BYTES);
    const unsigned sZ = fj * 128 * CH + zp0 * 1024;
    u64 ready = 0;
    auto issueZ = [&](int n) {
      const int kp = kpos(n);
      if (__builtin_expect(!got(ready, kp >> 1), 0)) { for (unsigned spins = 0; !got(ready, kp >> 1) && spins < SPIN_CAP; ++spins) ready = poll16(ws + WS_F1 + fj * 16 * 128, tag, lane); }
      MEMB();
      const unsigned dz = lds0 + (n & 3) * STG + 16384 + zp0 * 1024, so = sZ + kp * 18432;
      GS4(rZ, dz, vL, so);
      if (big) GS(rZ, dz + 4096, vL, so + 4096, 0);
    };
    const char* f1 = ws + WS_F1 + fj * 16 * 128;
    for (int q = 0; q < PDELAY; ++q) __builtin_amdgcn_s_sleep(4);
    { unsigned np = 0; for (; ready != 0xff && np < SPIN_CAP; ++np) ready = pollN<2>(f1, tag, lane); LST(0, np); }
    issueZ(0); issueZ(1);
    WVS(1, -4);
    LBAR();
    MARK(3);
    u64 fv;
    asm volatile("global_load_dwordx2 %0, %1, %2 sc1" : "=&v"(fv) : "v"((lane >> 2) * 128 + (lane & 3) * 8), "s"(f1) : "memory");
    issueZ(2);
    WVS(1, -3);
    LBAR();
    issueZ(3);
    WVS(1, -4);
    asm volatile("" : "+v"(fv));
    ready |= __builtin_amdgcn_ballot_w64(fv == tag);
    LBAR();
    for (int n = 4; n < 31; ++n) {
      issueW(n); issueZ(n);
      WVS(1, 0);
      LBAR();
    }
    issueW(31); issueZ(31);
    constexpr int TAIL = 9;
    unsigned vO[2], vOh;
#pragma unroll
    for (int q = 0; q < 2; ++q) { const int row = 36 * lw + 8 * q + (lane >> 3); vO[q] = row * 1152 + (((lane & 7) ^ (((row >> 1) & 1) | (row & 4))) << 4); }
    { const int row = 36 * lw + 32 + ((lane >> 3) & 3); vOh = row * 1152 + (((lane & 7) ^ (((row >> 1) & 1) | (row & 4))) << 4); }
    const unsigned vO1 = vO[1] - 1024, vO2 = vO[0] + 16 * 1152 - 2048, vO3 = vO[1] + 16 * 1152 - 3072;
    const unsigned sO = min(dd, 8) * 128;
    auto issueO = [&](int n, int slot) {
      const unsigned d = lds0 + L_O + slot * STGO + lw * 4608, s0 = sO + n * 144 * 1152;
      GL4(rWo, d, vO[0], vO1, vO2, vO3, s0);
      if (lane < 32) GL(rWo, d + 4096, vOh, s0, 0);
    };
    issueO(0, 0);
    WVS(1, 5); LBAR();
    issueO(1, 1); issueO(2, 2);
    WV(15); LBAR();
    LBAR();
    MARK(4);
    WV(0);
    LBAR();
    if (!has3) return;
    issueO(3, 3); issueO(4, 4);
    LBAR();
    issueO(5, 0);
    if (NPRE3 == 5) { issueO(6, 1); issueO(7, 2); }
    auto rP = RSRC(ws + WS_P, FRAG_BYTES);
    const unsigned vPs = lw < 3 ? (lane >> 5) * 16 * CH + (lane & 31) * 16 + 4096 : OOB;
    const int limS = lw == 2 ? 32 : 64;
    const unsigned sP = tt * 5 * 16 * CH;
    const int pa = (tt * 80) >> 7, pb = min((tt * 80 + 79) >> 7, 15);
    u64 rdy = 0;
    auto issueP = [&](int j, int slot) {
      if (__builtin_expect(!got(rdy, j), 0)) { for (unsigned spins = 0; !got(rdy, j) && spins < SPIN_CAP; ++spins) rdy = poll16(ws + WS_F2 + pa * 16 * 128, tag, lane) & poll16(ws + WS_F2 + pb * 16 * 128, tag, lane); }
      MEMB();
      const unsigned d = lds0 + L_P + slot * STGP, sp = sP + j * CH;
      GS4(rP, d + lw * 4096, vL, sp + lw * 16 * CH);
      GS(rP, d + 16384 + lw * 1024, vL, sp + 64 * CH + lw * 1024, 0);
      if (lane < limS) GS(rP, lw < 3 ? d + 20480 + lw * 1024 : lds0 + L_DUMMY, vPs, sp + lw * 32 * CH, 0);
    };
    const char *fa = ws + WS_F2 + pa * 16 * 128, *fb = ws + WS_F2 + pb * 16 * 128;
    { unsigned np = 0; LST(2, 0); for (; rdy != 0xfff && np < SPIN_CAP; ++np) rdy = pollN<3>(fa, tag, lane) & pollN<3>(fb, tag, lane); LST(1, np); }
    u64 fva, fvb;
    asm volatile("global_load_dwordx2 %0, %2, %3 sc1\n\tglobal_load_dwordx2 %1, %2, %4 sc1" : "=&v"(fva), "=&v"(fvb) : "v"((lane >> 2) * 128 + (lane & 3) * 8), "s"(fa), "s"(fb) : "memory");
    issueP(0, 0); issueP(1, 1); issueP(2, 2);
    WV(12);
    asm volatile("" : "+v"(fva), "+v"(fvb));
    rdy |= __builtin_amdgcn_ballot_w64(fva == tag) & __builtin_amdgcn_ballot_w64(fvb == tag);
    LBAR();
    MARK(5);
    WV(6);
    if (NPRE3 == 5) {
      LBAR(); issueP(3, 0); WV(6);
      LBAR(); issueP(4, 1); WV(6);
      LBAR(); issueP(5, 2); WV(6);
    } else {
      LBAR(); issueP(3, 0); issueO(6, 1); WV(11);
      LBAR(); issueP(4, 1); issueO(7, 2); WV(16);
      LBAR(); issueP(5, 2); WV(11);
    }
    LBAR(); issueP(6, 0); issueO(8, 3); WV(11);
    int sp3 = 1, so5 = 4;
    for (int n = 5; n < 12; ++n) {
      LBAR();
      issueP(n + 2, sp3); issueO(n + 4, so5);
      sp3 = sp3 == 2 ? 0 : sp3 + 1; so5 = so5 == 4 ? 0 : so5 + 1;
      WV(16);
    }
    LBAR(); issueP(14, 2); WV(6);
    LBAR(); issueP(15, 0); WV(6);
    LBAR(); WV(0);
    LBAR();
    LBAR(); LBAR();
    MARK(6);
    { const u64 e = __builtin_amdgcn_s_memrealtime() + 2 * (rt1 - rt0p); while (__builtin_amdgcn_s_memrealtime() < e) __builtin_amdgcn_s_sleep(4); }
    if (bid == 0 && tid == 256) __hip_atomic_store((u64*)(ws + WS_EP), tag, RLX_AGENT);
  } else {
    const int wt = wid >> 1, wf = wid & 1;
    if (wid == 0) {
      auto rC = RSRC((const char*)(pc0 & ~(u64)255), CODE_KB * 1024);
      GL(rC, lds0 + L_DUMMY, lane * 16, rk * 1024, 0); GL(rC, lds0 + L_DUMMY, lane * 16, rk * 1024 + 32768, 0);
      WV(0);
    }
    if (wf == 0) front<0>(lds0, wid, wt, lane, ti, fj, ws); else front<1>(lds0, wid, wt, lane, ti, fj, ws);
    if (!has3) return;
    if (wt == 0) g3_compute<0>(lds0, wf, lane, out, tt, dd); else g3_compute<1>(lds0, wf, lane, out, tt, dd);
  }
}

extern "C" void kernel_launch(void* const* d_in, const int* in_sizes, int n_in, void* d_out, int out_size, void* d_ws, size_t ws_size, hipStream_t stream) {
  (void)hipFuncSetAttribute((const void*)gmlp_fused, hipFuncAttributeMaxDynamicSharedMemorySize, LDS_BYTES);
  gmlp_fused<<<256, 512, LDS_BYTES, stream>>>((const char*)d_in[0], (const char*)d_in[1], (const char*)d_in[2], (const u16*)d_in[3], (const char*)d_in[4], (char*)d_out, (char*)d_ws);
}
```

```cpp
#include <hip/hip_runtime.h>
#include <hip/hip_bf16.h>
typedef unsigned short u16;
typedef unsigned long long u64;
typedef short s16x4 __attribute__((ext_vector_type(4)));
typedef short bf16x8 __attribute__((ext_vector_type(8)));
typedef float f32x2 __attribute__((ext_vector_type(2)));
typedef float f32x4 __attribute__((ext_vector_type(4)));
typedef unsigned u32x4 __attribute__((ext_vector_type(4)));
typedef unsigned u32x2 __attribute__((ext_vector_type(2)));
#define LAS __attribute__((address_space(3)))
#define DEV __device__ __forceinline__
#define BAR() __builtin_amdgcn_s_barrier()
#define SB() __builtin_amdgcn_sched_barrier(0)
#define MEMB() asm volatile("" ::: "memory")
#define WAITL0() __builtin_amdgcn_s_waitcnt(0xC07F)
#define WL0() asm volatile("s_waitcnt lgkmcnt(0)" ::: "memory")
#define WV(N) asm volatile("s_waitcnt vmcnt(%0)" ::"i"(N) : "memory")
#define MFMA(a, b, c) __builtin_amdgcn_mfma_f32_16x16x32_bf16(a, b, c, 0, 0, 0)
#define MFMA16(a, b, c) __builtin_amdgcn_mfma_f32_16x16x16bf16_1k(a, b, c, 0, 0, 0)
#define GL(R, D, V, S, I) __builtin_amdgcn_raw_ptr_buffer_load_lds(R, (LAS void*)(size_t)(D), 16, V, S, I, 0)
#define GL4(R, D, V0, V1, V2, V3, S) do { GL(R, D, V0, S, 0); GL(R, D, V1, S, 1024); GL(R, D, V2, S, 2048); GL(R, D, V3, S, 3072); } while (0)
#define GS(R, D, V, S, I) __builtin_amdgcn_raw_ptr_buffer_load_lds(R, (LAS void*)(size_t)(D), 16, V, S, I, 16)
#define GS4(R, D, V, S) do { GS(R, D, V, S, 0); GS(R, D, V, S, 1024); GS(R, D, V, S, 2048); GS(R, D, V, S, 3072); } while (0)
#define TOUCH(R, V, S) __builtin_amdgcn_raw_ptr_buffer_load_lds(R, (LAS void*)(size_t)(lds0 + L_DUMMY), 4, V, S, 0, 0)
#define RSRC(p, n) __builtin_amdgcn_make_buffer_rsrc((void*)(p), 0, (n), 0x00020000)
#define RLX_AGENT __ATOMIC_RELAXED, __HIP_MEMORY_SCOPE_AGENT
#define ROT1 2
#define NPRE3 3
#define Z1W 1
#define LS0 1
#define OAUX 16
#define WAUX 0
#define T0W 0
#define PDELAY 7
#define PSLEEP 0
#define LA2 2
#define CODE_KB 54

constexpr int CH = 4608;
constexpr int FRAG_BYTES = 16 * 128 * CH;
constexpr size_t WS_Z2 = 0, WS_P = FRAG_BYTES, WS_ST = 2 * (size_t)FRAG_BYTES, WS_F1 = WS_ST + 16 * 16 * 128 * 16, WS_F2 = WS_F1 + 256 * 128, WS_EP = WS_F2 + 256 * 128;
constexpr int STG = 34816, STGP = 23040, STGO = 18432, L_O = 0, L_P = 5 * STGO, LDS_BYTES = 163840;
constexpr int L_T = 139264, L_R = L_T + 8192, L_X = 151552, L_BIAS = 159744, L_TAG = L_BIAS + 256, L_DUMMY = 161280;
constexpr unsigned SPIN_CAP = 1u << 16, OOB = 0x80000000u;
#define LBAR() BAR()
#define CST(I)
#define LST(I, X)

DEV unsigned cvt_pk(float lo, float hi) { unsigned r; asm("v_cvt_pk_bf16_f32 %0, %1, %2" : "=v"(r) : "v"(lo), "v"(hi)); return r; }
DEV bf16x8 ld128(unsigned a) { return *(LAS bf16x8*)(size_t)a; }
DEV s16x4 ld64(unsigned a) { return *(LAS s16x4*)(size_t)a; }
DEV s16x4 ldtr(unsigned a) { return __builtin_amdgcn_ds_read_tr16_b64_v4i16((LAS s16x4*)(size_t)a); }
DEV bf16x8 cat(s16x4 a, s16x4 b) { return __builtin_shufflevector(a, b, 0, 1, 2, 3, 4, 5, 6, 7); }
DEV float bflo(unsigned v) { return __uint_as_float(v << 16); }
DEV float bfhi(unsigned v) { return __uint_as_float(v & 0xffff0000u); }
template <int NB, bool SUM>
DEV void gelu_blocks(const f32x4* const (&v)[NB], u32x2* const (&o)[NB], f32x2& s1, f32x2& s2) {
  const float k1 = -2.0f * 0.7978845608028654f * 1.4426950408889634f, k2 = k1 * 0.044715f;
  f32x2 x[2 * NB], t[2 * NB];
#pragma unroll
  for (int i = 0; i < 2 * NB; ++i) { x[i] = (f32x2){(*v[i >> 1])[(i & 1) * 2], (*v[i >> 1])[(i & 1) * 2 + 1]}; t[i] = x[i] * x[i]; }
  SB();
#pragma unroll
  for (int i = 0; i < 2 * NB; ++i) t[i] = t[i] * k2 + k1;
  SB();
#pragma unroll
  for (int i = 0; i < 2 * NB; ++i) t[i] = t[i] * x[i];
  SB();
#pragma unroll
  for (int i = 0; i < 2 * NB; ++i) { t[i].x = __builtin_amdgcn_exp2f(t[i].x); t[i].y = __builtin_amdgcn_exp2f(t[i].y); }
  SB();
#pragma unroll
  for (int i = 0; i < 2 * NB; ++i) t[i] = t[i] + 1.0f;
  SB();
#pragma unroll
  for (int i = 0; i < 2 * NB; ++i) { t[i].x = __builtin_amdgcn_rcpf(t[i].x); t[i].y = __builtin_amdgcn_rcpf(t[i].y); }
  SB();
#pragma unroll
  for (int i = 0; i < 2 * NB; ++i) t[i] = t[i] * x[i];
  SB();
  f32x2 a1 = s1, a2 = s2, b1 = {0.f, 0.f}, b2 = {0.f, 0.f};
#pragma unroll
  for (int i = 0; i < NB; ++i) {
    o[i]->x = cvt_pk(t[2 * i].x, t[2 * i].y);
    if (SUM) { a1 += t[2 * i]; a2 += t[2 * i] * t[2 * i]; }
    o[i]->y = cvt_pk(t[2 * i + 1].x, t[2 * i + 1].y);
    if (SUM) { if (i == 0) { b1 = t[1]; b2 = t[1] * t[1]; } else { b1 += t[2 * i + 1]; b2 += t[2 * i + 1] * t[2 * i + 1]; } }
  }
  if (SUM) { s1 = a1 + b1; s2 = a2 + b2; }
  SB();
}
template <int WF, bool SUM>
DEV void gelu_row(int m, const f32x4 (&acc)[4][5], u32x2 (&oa)[4][4], u32x2 (&ob)[2], f32x2& s1, f32x2& s2) {
  if ((m >> 1) == WF) { const f32x4* const v[5] = {&acc[m][0], &acc[m][1], &acc[m][2], &acc[m][3], &acc[m][4]}; u32x2* const o[5] = {&oa[m][0], &oa[m][1], &oa[m][2], &oa[m][3], &ob[m & 1]}; gelu_blocks<5, SUM>(v, o, s1, s2); }
  else { const f32x4* const v[4] = {&acc[m][0], &acc[m][1], &acc[m][2], &acc[m][3]}; u32x2* const o[4] = {&oa[m][0], &oa[m][1], &oa[m][2], &oa[m][3]}; gelu_blocks<4, SUM>(v, o, s1, s2); }
}

struct TAddr { unsigned w[2], z[2][2], s[2]; };
struct TFrag { bf16x8 wX[4], wY[4]; s16x4 tX[5][2], tY[4][2]; };
template <int WF, int PS, int OFF, bool FIRST, bool SYNC = true>
DEV void t_step(TAddr& A, int dlt, f32x4 (&acc)[4][5], TFrag& F, const f32x4* init = nullptr) {
  constexpr int KX = WF, KY = 1 - WF;
  SB(); if (SYNC) BAR(); MEMB(); SB();
  auto rdz = [&](s16x4 (&t)[2], int n, int u, int K) { t[u] = n < 4 ? ldtr(A.z[u][n & 1] + OFF + K * 9216 + (n >> 1) * PS) : ldtr(A.s[u] + OFF + K * 9216); };
#pragma unroll
  for (int i = 0; i < 16; ++i) {
    const int m = i >> 2, n = i & 3;
    if (!FIRST) acc[m][n] = MFMA(cat(F.tY[n][0], F.tY[n][1]), F.wY[m], acc[m][n]);
    SB();
    if (i == 0) F.wX[0] = ld128(A.w[KX] + OFF);
    else if (i < 11) rdz(F.tX[(i - 1) >> 1], (i - 1) >> 1, (i - 1) & 1, KX);
    else if (i < 14) F.wX[i - 10] = ld128(A.w[KX] + OFF + (i - 10) * 2048);
    SB();
  }
#pragma unroll
  for (int i = 0; i < 20; ++i) {
    const int m = i / 5, n = i % 5;
    const f32x4 zero4 = {0, 0, 0, 0};
    acc[m][n] = MFMA(cat(F.tX[n][0], F.tX[n][1]), F.wX[m], !FIRST ? acc[m][n] : (PS == 1024 && (n < 4 || (m >> 1) == WF)) ? init[m] : zero4);
    SB();
    if (i == 0) F.wY[0] = ld128(A.w[KY] + OFF);
    else if (i < 9) rdz(F.tY[(i - 1) >> 1], (i - 1) >> 1, (i - 1) & 1, KY);
    else if (i < 12) F.wY[i - 8] = ld128(A.w[KY] + OFF + (i - 8) * 2048);
    else if (OFF != 0 && i < 16) {
      const int u = i & 1;
      if (i < 14) { A.w[u] += dlt; A.s[u] += dlt; asm volatile("" : "+v"(A.w[u]), "+v"(A.s[u])); }
      else { A.z[u][0] += dlt; A.z[u][1] += dlt; asm volatile("" : "+v"(A.z[u][0]), "+v"(A.z[u][1])); }
    }
    SB();
  }
  WAITL0();
}
template <int WF>
DEV void t_finish(unsigned lds0, int wt, int lane, f32x4 (&acc)[4][5], TFrag& F) {
#pragma unroll
  for (int h = 0; h < 2; ++h) *(LAS f32x4*)(size_t)(lds0 + L_X + ((wt * 2 + WF) * 2 + h) * 1024 + lane * 16) = acc[2 * (1 - WF) + h][4];
  SB();
#pragma unroll
  for (int m = 0; m < 4; ++m)
#pragma unroll
    for (int n = 0; n < 4; ++n) acc[m][n] = MFMA(cat(F.tY[n][0], F.tY[n][1]), F.wY[m], acc[m][n]);
  SB();
  WL0();
  BAR(); MEMB();
#pragma unroll
  for (int h = 0; h < 2; ++h) acc[2 * WF + h][4] += *(LAS f32x4*)(size_t)(lds0 + L_X + ((wt * 2 + 1 - WF) * 2 + h) * 1024 + lane * 16);
}
template <int WF, bool MID, class Mid>
DEV void in_pass(unsigned lds0, int wt, int lane, TAddr& A, f32x4 (&acc)[4][5], Mid mid) {
  TFrag F;
  int dlt = 2 * STG;
  WAITL0();
  if (!MID) {
    t_step<WF, 64, 0, true>(A, dlt, acc, F);
    for (int k = 0; k < 4; ++k) {
      t_step<WF, 64, STG, false>(A, dlt, acc, F);
      dlt = -dlt;
      t_step<WF, 64, 0, false>(A, dlt, acc, F);
    }
  } else {
    t_step<WF, 64, STG, true>(A, dlt, acc, F);
    for (int k = 0; k < 3; ++k) {
      dlt = -dlt;
      t_step<WF, 64, 0, false>(A, dlt, acc, F);
      t_step<WF, 64, STG, false>(A, dlt, acc, F);
    }
    t_step<WF, 64, 0, false>(A, 0, acc, F);
    SB(); BAR(); MEMB(); mid(); SB();
    t_step<WF, 64, STG, false>(A, 0, acc, F);
  }
  t_finish<WF>(lds0, wt, lane, acc, F);
}

struct G3P { bf16x8 p[2][5]; s16x4 ps[3]; };
struct G3W { s16x4 w[2][2][2], ws[2]; };
template <int KH>
DEV void g3_readw(G3W& n, unsigned a0, unsigned a1) {
#pragma unroll
  for (int q = 0; q < 8; ++q) n.w[q >> 2][(q >> 1) & 1][q & 1] = ldtr((((q >> 1) & 1) ? a1 : a0) + (2 * KH + (q >> 2)) * 4096 + (q & 1) * 2048);
  n.ws[0] = ldtr(a0 + 16384); n.ws[1] = ldtr(a1 + 16384);
}
template <int KH, bool MM, bool RDW>
DEV void g3_stage(f32x4 (&acc)[5][2], const G3P& cp, const G3W& cw, G3P& np, G3W& nw, unsigned aP, unsigned aS, unsigned a0, unsigned a1) {
  constexpr int M0 = KH ? 3 : 0, NS = KH ? 2 : 3, NM = 20 + 2 * NS, NR = 20 + NS;
  SB(); BAR(); MEMB(); SB();
#pragma unroll
  for (int i = 0; i < NM; ++i) {
    if (MM) {
      if (i >= 10 && i < 10 + 2 * NS) acc[M0 + ((i - 10) >> 1)][i & 1] = MFMA16(cw.ws[i & 1], cp.ps[(i - 10) >> 1], acc[M0 + ((i - 10) >> 1)][i & 1]);
      else { const int ii = i < 10 ? i : i - 2 * NS, P = ii / 10, mm = (ii % 10) >> 1, m = (KH != P) ? (mm + 3) % 5 : mm, cc = ii & 1; acc[m][cc] = MFMA(cat(cw.w[P][cc][0], cw.w[P][cc][1]), cp.p[P][m], acc[m][cc]); }
    }
    SB();
#pragma unroll
    for (int r = (i * 3) / 2; r < ((i + 1) * 3) / 2; ++r) {
      if (r < 18) { const int P = r / 9, q = r % 9;
        if (q < 4) { if (RDW) nw.w[P][q >> 1][q & 1] = ldtr(((q >> 1) ? a1 : a0) + (2 * KH + P) * 4096 + (q & 1) * 2048); }
        else np.p[P][q - 4] = ld128(aP + P * 1024 + (q - 4) * 4096); }
      else if (r < 20) { if (RDW) nw.ws[r - 18] = ldtr(((r - 18) ? a1 : a0) + 16384); }
      else if (r < NR) np.ps[r - 20] = ld64(aS + (M0 + r - 20) * 512);
    }
    SB();
  }
  WAITL0();
}
template <int KH>
DEV void g3_compute(unsigned lds0, int dh, int lane, char* out, int tt, int dd) {
  const int g = lane >> 4, qr = (lane >> 2) & 3, p = lane & 3;
  const int rr = 4 * g + qr;
  unsigned bW[2];
#pragma unroll
  for (int c = 0; c < 2; ++c) bW[c] = lds0 + L_O + rr * 128 + (((4 * dh + p) ^ (((rr >> 1) & 1) | (rr & 4))) << 4) + ((c ^ (p & 1)) * 8);
  const unsigned bP = lds0 + L_P + lane * 16 + KH * 2048, bS = lds0 + L_P + 20480 + lane * 8;
  G3W a0, a1, a2;
  g3_readw<KH>(a0, bW[0], bW[1]); g3_readw<KH>(a1, bW[0] + STGO, bW[1] + STGO); g3_readw<KH>(a2, bW[0] + 2 * STGO, bW[1] + 2 * STGO);
  f32x4 acc[5][2];
#pragma unroll
  for (int m = 0; m < 5; ++m) { acc[m][0] = (f32x4){0, 0, 0, 0}; acc[m][1] = (f32x4){0, 0, 0, 0}; }
  WAITL0();
  SB(); BAR(); SB();
  G3P p0, p1; G3W w0, w1;
  g3_stage<KH, false, false>(acc, p1, a0, p0, w0, bP, bS, 0, 0);
  g3_stage<KH, true, false>(acc, p0, a0, p1, w0, bP + STGP, bS + STGP, 0, 0);
  g3_stage<KH, true, false>(acc, p1, a1, p0, w0, bP + 2 * STGP, bS + 2 * STGP, 0, 0);
  g3_stage<KH, true, true>(acc, p0, a2, p1, w1, bP, bS, bW[0] + 3 * STGO, bW[1] + 3 * STGO);
  unsigned sp = STGP, sw = 4 * STGO;
  for (int j = 4; j < 16; j += 2) {
    g3_stage<KH, true, true>(acc, p1, w1, p0, w0, bP + sp, bS + sp, bW[0] + sw, bW[1] + sw);
    sp = sp == 2 * STGP ? 0 : sp + STGP; sw = sw == 4 * STGO ? 0 : sw + STGO;
    g3_stage<KH, true, true>(acc, p0, w0, p1, w1, bP + sp, bS + sp, bW[0] + sw, bW[1] + sw);
    sp = sp == 2 * STGP ? 0 : sp + STGP; sw = sw == 4 * STGO ? 0 : sw + STGO;
  }
  {
    constexpr int M0 = KH ? 3 : 0, NS = KH ? 2 : 3;
    SB(); BAR(); SB();
#pragma unroll
    for (int i = 0; i < 2 * NS; ++i) { acc[M0 + (i >> 1)][i & 1] = MFMA16(w1.ws[i & 1], p1.ps[i >> 1], acc[M0 + (i >> 1)][i & 1]); SB(); }
#pragma unroll
    for (int i = 0; i < 20; ++i) { const int P = (i >> 1) & 1, m = KH ? i >> 2 : 4 - (i >> 2), cc = i & 1; acc[m][cc] = MFMA(cat(w1.w[P][cc][0], w1.w[P][cc][1]), p1.p[P][m], acc[m][cc]); SB(); }
  }
#pragma unroll
  for (int m = 0; m < 5; ++m)
    if ((m >= 3) != KH) { *(LAS f32x4*)(size_t)(lds0 + (dh * 10 + m * 2) * 1024 + lane * 16) = acc[m][0]; *(LAS f32x4*)(size_t)(lds0 + (dh * 10 + m * 2 + 1) * 1024 + lane * 16) = acc[m][1]; }
  WL0();
  BAR(); MEMB();
  auto rO = RSRC(out, 2048 * 1152);
  const bool sw2 = g & 1;
#pragma unroll
  for (int m = 0; m < 5; ++m)
    if ((m >= 3) == KH) {
      const f32x4 a = acc[m][0] + *(LAS f32x4*)(size_t)(lds0 + (dh * 10 + m * 2) * 1024 + lane * 16), b = acc[m][1] + *(LAS f32x4*)(size_t)(lds0 + (dh * 10 + m * 2 + 1) * 1024 + lane * 16);
      const unsigned a0 = cvt_pk(a[0], a[1]), a1 = cvt_pk(a[2], a[3]), b0 = cvt_pk(b[0], b[1]), b1 = cvt_pk(b[2], b[3]);
      const u32x4 v = {sw2 ? b0 : a0, sw2 ? b1 : a1, sw2 ? a0 : b0, sw2 ? a1 : b1};
      __builtin_amdgcn_raw_buffer_store_b128(v, rO, (tt * 80 + m * 16 + (lane & 15)) * 1152 + dd * 128 + dh * 64 + g * 16, 0, OAUX);
    }
}

template <int WF>
DEV void front(unsigned lds0, int wid, int wt, int lane, int ti, int fj, char* ws) {
  const int r16 = lane & 15, g = lane >> 4, qr = (lane >> 2) & 3, p = lane & 3;
  TAddr A;
  A.w[0] = lds0 + (64 * wt + r16) * 128 + ((g ^ (r16 >> 1)) << 4); A.w[1] = A.w[0] ^ 64;
#pragma unroll
  for (int u = 0; u < 2; ++u) {
    const unsigned b = lds0 + 16384 + (16 * u + 4 * g + qr) * 288 + p * 8;
    A.z[u][0] = b + WF * 128 + (p & 1) * 32; A.z[u][1] = b + WF * 128 + 32 - (p & 1) * 32; A.s[u] = b + 256;
  }
  asm volatile("" : "+v"(A.w[0]), "+v"(A.w[1]), "+v"(A.s[0]), "+v"(A.s[1]), "+v"(A.z[0][0]), "+v"(A.z[0][1]), "+v"(A.z[1][0]), "+v"(A.z[1][1]));
  f32x4 acc[4][5];
  in_pass<WF, false>(lds0, wt, lane, A, acc, []() {});
  CST(0);
  u32x2 ua[4][4], ub[2];
  {
    f32x2 s1[4], s2[4];
#pragma unroll
    for (int mm = 0; mm < 4; ++mm) {
      const int m = mm ^ (2 * (1 - WF));
      s1[m] = (f32x2){0.f, 0.f}; s2[m] = (f32x2){0.f, 0.f};
      gelu_row<WF, true>(m, acc, ua, ub, s1[m], s2[m]);
    }
#pragma unroll
    for (int m = 0; m < 4; ++m) *(LAS f32x2*)(size_t)(lds0 + L_T + (64 * wt + 16 * m + r16) * 64 + (WF * 4 + g) * 8) = (f32x2){s1[m].x + s1[m].y, s2[m].x + s2[m].y};
  }
#pragma unroll
  for (int m = 0; m < 4; ++m) asm volatile("" : "+v"(ua[m][0]), "+v"(ua[m][1]), "+v"(ua[m][2]), "+v"(ua[m][3]));
  asm volatile("" : "+v"(ub[0]), "+v"(ub[1]));
#pragma unroll
  for (int u = 0; u < 2; ++u) { A.z[u][0] -= (p & 1) * 32; A.z[u][1] += (p & 1) * 32; }
  asm volatile("" : "+v"(A.z[0][0]), "+v"(A.z[0][1]), "+v"(A.z[1][0]), "+v"(A.z[1][1]));
  CST(1);
  in_pass<WF, true>(lds0, wt, lane, A, acc, [&]() {
    CST(2);
#pragma unroll
    for (int m = 0; m < 4; ++m) asm volatile("" : "+v"(ua[m][0]), "+v"(ua[m][1]), "+v"(ua[m][2]), "+v"(ua[m][3]));
    asm volatile("" : "+v"(ub[0]), "+v"(ub[1]));
    int lz = lane; asm volatile("" : "+v"(lz));
    auto rZ = RSRC(ws + WS_Z2, FRAG_BYTES);
    const unsigned sl = (lz >> 4) * 16 + ((lz & 15) ^ (4 * (lz >> 5))), dst = (fj * 128 + ti * 8 + wt * 4) * CH;
#pragma unroll
    for (int m = 0; m < 4; ++m) {
      const unsigned ar = lds0 + L_R + (64 * wt + 16 * m + (lz & 15)) * 32;
      const f32x4 q4 = *(LAS f32x4*)(size_t)ar + *(LAS f32x4*)(size_t)(ar + 16);
      const float mean = (q4[0] + q4[2]) * (1.0f / 2304.0f), var = fmaxf((q4[1] + q4[3]) * (1.0f / 2304.0f) - mean * mean, 0.f);
      const float rs = __builtin_amdgcn_rsqf(var + 1e-5f), sh = -mean * rs;
      auto norm = [&](unsigned v) { const f32x2 y = (f32x2){bflo(v), bfhi(v)} * rs + sh; return cvt_pk(y.x, y.y); };
#pragma unroll
      for (int pp = 0; pp < 2; ++pp)
        __builtin_amdgcn_raw_buffer_store_b128((u32x4){norm(ua[m][2 * pp].x), norm(ua[m][2 * pp].y), norm(ua[m][2 * pp + 1].x), norm(ua[m][2 * pp + 1].y)}, rZ, dst + m * CH + sl * 16 + WF * 2048 + pp * 1024, 0, 16);
      if ((m >> 1) == WF) __builtin_amdgcn_raw_buffer_store_b64((u32x2){norm(ub[m & 1].x), norm(ub[m & 1].y)}, rZ, dst + m * CH + sl * 8 + 4096, 0, 16);
    }
    CST(3);
  });
  CST(4);
#pragma unroll
  for (int m = 0; m < 4; ++m) asm volatile("" : "+v"(acc[m][0]), "+v"(acc[m][1]), "+v"(acc[m][2]), "+v"(acc[m][3]), "+v"(acc[m][4]));
  const u32x2 tg = *(LAS u32x2*)(size_t)(lds0 + L_TAG);
  const u64 tag = ((u64)(unsigned)__builtin_amdgcn_readfirstlane(tg.y) << 32) | (unsigned)__builtin_amdgcn_readfirstlane(tg.x);
  WV(0);
  if (lane == 0) __hip_atomic_store((u64*)(ws + WS_F1 + (fj * 16 + ti) * 128) + wid, tag, RLX_AGENT);
  CST(5);
  SB();
  u32x2 za[4][4], zb[2];
#pragma unroll
  for (int m = 0; m < 4; ++m) {
    f32x2 d1, d2;
    gelu_row<WF, false>(m, acc, za, zb, d1, d2);
    asm volatile("" : "+v"(za[m][0]), "+v"(za[m][1]), "+v"(za[m][2]), "+v"(za[m][3]));
  }
  asm volatile("" : "+v"(zb[0]), "+v"(zb[1]));
  CST(6);
#pragma unroll
  for (int u = 0; u < 2; ++u) {
    const unsigned sl = p * 16 + 8 * (g & 1) + 4 * (u ^ (p >> 1)) + qr;
#pragma unroll
    for (int h = 0; h < 2; ++h) A.z[u][h] = lds0 + 16384 + (g >> 1) * CH + WF * 2048 + sl * 16 + ((h ^ (p & 1)) * 8);
    A.s[u] = lds0 + 16384 + (g >> 1) * CH + 4096 + sl * 8;
  }
  asm volatile("" : "+v"(A.s[0]), "+v"(A.s[1]), "+v"(A.z[0][0]), "+v"(A.z[0][1]), "+v"(A.z[1][0]), "+v"(A.z[1][1]));
  f32x4 bias4[4];
#pragma unroll
  for (int m = 0; m < 4; ++m) { const float b = __uint_as_float((unsigned)*(LAS u16*)(size_t)(lds0 + L_BIAS + (wt * 64 + m * 16 + r16) * 2) << 16); bias4[m] = (f32x4){b, b, b, b}; }
  TFrag F;
  int dlt = 2 * STG;
  WAITL0();
  CST(7);
  t_step<WF, 1024, 0, true, true>(A, dlt, acc, F, bias4);
  CST(8);
  t_step<WF, 1024, STG, false, true>(A, dlt, acc, F);

  for (int k = 0; k < 15; ++k) {
    dlt = -dlt;
    t_step<WF, 1024, 0, false>(A, dlt, acc, F);
    t_step<WF, 1024, STG, false>(A, dlt, acc, F);
  }
  auto rP = RSRC(ws + WS_P, FRAG_BYTES);
  auto gate = [&](f32x4 a, u32x2 z) {
    const f32x2 x = (f32x2){a[0], a[1]} * (f32x2){bflo(z.x), bfhi(z.x)}, y = (f32x2){a[2], a[3]} * (f32x2){bflo(z.y), bfhi(z.y)};
    return (u32x2){cvt_pk(x.x, x.y), cvt_pk(y.x, y.y)};
  };
#pragma unroll
  for (int h = 0; h < 2; ++h) *(LAS f32x4*)(size_t)(lds0 + L_X + ((wt * 2 + WF) * 2 + h) * 1024 + lane * 16) = acc[2 * (1 - WF) + h][4];
  const unsigned dst0 = (ti * 8 + wt * 4) * 16 * CH + fj * CH;
  CST(9);
#pragma unroll
  for (int m = 0; m < 4; ++m)
#pragma unroll
    for (int n = 0; n < 4; ++n) acc[m][n] = MFMA(cat(F.tY[n][0], F.tY[n][1]), F.wY[m], acc[m][n]);
#pragma unroll
  for (int m = 0; m < 4; ++m)
#pragma unroll
    for (int pp = 0; pp < 2; ++pp) {
      const u32x2 a = gate(acc[m][2 * pp], za[m][2 * pp]), b = gate(acc[m][2 * pp + 1], za[m][2 * pp + 1]);
      __builtin_amdgcn_raw_buffer_store_b128((u32x4){a.x, a.y, b.x, b.y}, rP, dst0 + m * 16 * CH + lane * 16 + (2 * WF + pp) * 1024, 0, 16);
    }
  SB();
  WL0();
  BAR(); MEMB();
#pragma unroll
  for (int h = 0; h < 2; ++h) {
    const f32x4 o8 = *(LAS f32x4*)(size_t)(lds0 + L_X + ((wt * 2 + 1 - WF) * 2 + h) * 1024 + lane * 16);
    __builtin_amdgcn_raw_buffer_store_b64(gate(acc[2 * WF + h][4] + o8, zb[h]), rP, dst0 + (2 * WF + h) * 16 * CH + lane * 8 + 4096, 0, 16);
  }
  CST(10);
  WV(0);
  CST(11);
  if (lane == 0) __hip_atomic_store((u64*)(ws + WS_F2 + (ti * 16 + fj) * 128) + wid, tag, RLX_AGENT);
  SB(); BAR(); MEMB(); SB();
}

DEV u64 poll16(const char* base, u64 tag, int lane) {
  const u64 v = __hip_atomic_load((const u64*)(base + (lane >> 2) * 128) + (lane & 3), RLX_AGENT);
  return __builtin_amdgcn_ballot_w64(v == tag);
}
template <int NP> DEV u64 pollN(const char* base, u64 tag, int lane) {
  const u64 v = __hip_atomic_load((const u64*)(base + min(lane >> 2, NP - 1) * 128) + (lane & 3), RLX_AGENT);
  return __builtin_amdgcn_ballot_w64(v == tag) & ((1ull << (4 * NP)) - 1);
}
DEV bool got(u64 mask, int who) { return ((unsigned)(mask >> (4 * who)) & 15u) == 15u; }

__global__ __launch_bounds__(512, 2) void gmlp_fused(const char* __restrict__ X, const char* __restrict__ Win, const char* __restrict__ Wsp, const u16* __restrict__ bias,
                                                     const char* __restrict__ Wo, char* __restrict__ out, char* __restrict__ ws) {
  extern __shared__ __attribute__((aligned(16))) char smem[];
  u64 pc0; asm volatile("s_getpc_b64 %0" : "=s"(pc0));
  const int tid = threadIdx.x, wid = __builtin_amdgcn_readfirstlane(tid >> 6), lane = tid & 63;
  const int bid = blockIdx.x, xg = bid & 7, rk = bid >> 3, il = rk & 7, jl = rk >> 3, ti = (xg & 1) * 8 + il, fj = (xg >> 1) * 4 + jl;
  const int L3 = xg * 30 + rk, tt = L3 / 9, dd = L3 - tt * 9;
  const bool has3 = rk < 30 && L3 < 234;
  const unsigned lds0 = (unsigned)(size_t)(LAS char*)smem;
  if (__builtin_expect(wid >= 4, 1)) {
    __builtin_amdgcn_s_setprio(3);
    const int lw = wid - 4;
    const bool big = wid < 6;
#define WVS(STAGES, EXTRA) do { if (big) WV(9 * (STAGES) + (EXTRA)); else WV(8 * (STAGES) + (EXTRA)); } while (0)
    auto rX = RSRC(X, 2048 * 1152); auto rWi = RSRC(Win, 576 * 9216);
    unsigned vX[4], vW[5];
    const unsigned zp0 = lw < 2 ? lw * 5 : 2 + lw * 4;
    const unsigned sX = (ti * 128 + lw * 32) * 1152, sW = fj * 288;
    const int rot = ROT1 == 0 ? 0 : ROT1 == 1 ? il + 2 * jl : 3 * ((il + jl) % 3);
    const int rot8 = ROT1 == 1 ? (il + 2 * jl) & 7 : ROT1 == 2 ? (((il + jl) % 3) * 8) / 3 : 0;
    auto k1 = [&](int k) { return k == 0 ? 0 : 1 + ((k - 1 + rot8) & 7); };
    auto issue1 = [&](int half, int k) {
      const int kk = k1(k);
      const unsigned d = lds0 + ((k + 1 - half) & 3) * STG, dz = d + 16384 + zp0 * 1024, sw = sW + half * 4608 + kk * 64 * 9216;
      GL4(rX, d + lw * 4096, vX[0], vX[1], vX[2], vX[3], sX + kk * 128);
      GL4(rWi, dz, vW[0], vW[1], vW[2], vW[3], sw);
      if (big) GL(rWi, dz + 4096, vW[4], sw, 0);
    };
    {
#pragma unroll
      for (int q = 0; q < 4; ++q) { const int row = 8 * q + (lane >> 3); vX[q] = row * 1152 + (((lane & 7) ^ ((row >> 1) & 7)) << 4) - q * 1024; }
      GL4(rX, lds0 + lw * 4096, vX[0], vX[1], vX[2], vX[3], sX + k1(0) * 128);
      SB();
#pragma unroll
      for (int q = 0; q < 5; ++q) {
        const unsigned c16 = (zp0 + q) * 64 + lane, R = (c16 * 3641u) >> 16, c = (c16 - R * 18) * 16, k = (R & 32) + 8 * ((R >> 2) & 3) + 4 * ((R >> 4) & 1) + (R & 3);
        vW[q] = k * 9216 + c - (q & 3) * 1024;
      }
      const unsigned dz = lds0 + 16384 + zp0 * 1024, sw = sW + 4608 + k1(0) * 64 * 9216;
      GL4(rWi, dz, vW[0], vW[1], vW[2], vW[3], sw);
      if (big) GL(rWi, dz + 4096, vW[4], sw, 0);
    }
    const unsigned vL = lane * 16;
    auto rWs = RSRC(Wsp, 2048 * 4096); auto rWo = RSRC(Wo, 2304 * 1152);
    const unsigned e9 = lw * 21 + lane, r9 = (e9 * 7282u) >> 16, v81 = lane < 21 && e9 < 81 ? r9 * 9216 + (e9 - r9 * 9) * 128 : OOB, s81 = (bid >> 2) * 9 * 9216 + (bid & 3) * 1152;
    issue1(1, 1);
    WVS(1, T0W ? 2 : 0); LBAR();
    issue1(1, 2);
    WVS(1, 0);
    LBAR(); issue1(1, 3); WVS(1, 0);
    LBAR(); issue1(1, 4);
    TOUCH(rWi, v81, s81);
    WVS(1, 1);
    LBAR(); issue1(1, 5); WVS(1, 1);
    LBAR(); issue1(1, 6); WVS(1, 0);
    LBAR(); issue1(1, 7); WVS(1, 0);
    LBAR(); issue1(1, 8); WVS(1, 0);
    LBAR(); issue1(0, 0); WVS(1, 0);
    LBAR(); issue1(0, 1);
    const u64 ep = __hip_atomic_load((const u64*)(ws + WS_EP), RLX_AGENT);
    if (lw == 0) __builtin_amdgcn_raw_ptr_buffer_load_lds(RSRC(bias, 4096), (LAS void*)(size_t)(lds0 + L_BIAS), 4, lane * 4, ti * 256, 0, 0);
    TOUCH(rWs, lane * 128, bid * 32768 + lw * 8192);
    LBAR();
    issue1(0, 2); issue1(0, 3);
    const unsigned tagA = __builtin_amdgcn_readfirstlane((unsigned)ep) + 0x9E3779B9u, tagB = __builtin_amdgcn_readfirstlane((unsigned)(ep >> 32)) + 0x85EBCA6Bu;
    const u64 tag = ((u64)tagB << 32) | tagA;
    if (lw == 0) { const u32x2 tg = {tagA, tagB}; asm volatile("ds_write_b64 %0, %1\n\ts_waitcnt lgkmcnt(0)" :: "v"(lds0 + L_TAG), "v"(tg) : "memory"); }
    unsigned vS[4];
#pragma unroll
    for (int q = 0; q < 4; ++q) { const int row = 8 * q + (lane >> 3); vS[q] = row * 4096 + (((lane & 7) ^ ((row >> 1) & 7)) << 4) - q * 1024; }
    const unsigned sWs = (ti * 128 + lw * 32) * 4096;
    auto kpos = [&](int n) { return n; };
    auto issueW = [&](int n) { GL4(rWs, lds0 + (n & 3) * STG + lw * 4096, vS[0], vS[1], vS[2], vS[3], sWs + kpos(n) * 128); };
    WVS(2, 1);
    LBAR();
    auto rS = RSRC(ws + WS_ST, 16 * 16 * 128 * 16);
    if (lane < 32) {
      const int row = lw * 32 + lane;
      f32x4 v0, v1, v2, v3;
      asm volatile("ds_read_b128 %0, %4\n\tds_read_b128 %1, %4 offset:16\n\tds_read_b128 %2, %4 offset:32\n\tds_read_b128 %3, %4 offset:48\n\ts_waitcnt lgkmcnt(0)"
                   : "=&v"(v0), "=&v"(v1), "=&v"(v2), "=&v"(v3) : "v"(lds0 + L_T + row * 64) : "memory");
      const f32x4 s = (v0 + v1) + (v2 + v3);
      u32x4 gq; gq.x = tagA; gq.y = __float_as_uint(s[0] + s[2]); gq.z = tagB; gq.w = __float_as_uint(s[1] + s[3]);
      __builtin_amdgcn_raw_buffer_store_b128(gq, rS, ((ti * 16 + fj) * 128 + row) * 16, 0, 16);
    }
    u32x4 sv[8];
    const u32x4 srd = {(unsigned)(size_t)(ws + WS_ST), (unsigned)((size_t)(ws + WS_ST) >> 32) & 0xffffu, 16 * 16 * 128 * 16, 0x00020000u};
    auto sweep = [&]() {
      const unsigned s0 = (ti * 16 + lw * 4) * 2048, v1 = vL + 4096;
      asm volatile("buffer_load_dwordx4 %0, %8, %10, %11 offen sc1\n\tbuffer_load_dwordx4 %1, %8, %10, %11 offen offset:1024 sc1\n\tbuffer_load_dwordx4 %2, %8, %10, %11 offen offset:2048 sc1\n\t"
                   "buffer_load_dwordx4 %3, %8, %10, %11 offen offset:3072 sc1\n\tbuffer_load_dwordx4 %4, %9, %10, %11 offen sc1\n\tbuffer_load_dwordx4 %5, %9, %10, %11 offen offset:1024 sc1\n\t"
                   "buffer_load_dwordx4 %6, %9, %10, %11 offen offset:2048 sc1\n\tbuffer_load_dwordx4 %7, %9, %10, %11 offen offset:3072 sc1"
                   : "=&v"(sv[0]), "=&v"(sv[1]), "=&v"(sv[2]), "=&v"(sv[3]), "=&v"(sv[4]), "=&v"(sv[5]), "=&v"(sv[6]), "=&v"(sv[7]) : "v"(vL), "v"(v1), "s"(srd), "s"(s0) : "memory");
    };
    LBAR(); issue1(0, 4); WVS(2, 1);
    LBAR(); issue1(0, 5); WVS(2, 0);
    LBAR(); issue1(0, 6); WVS(2, 0);
    LBAR(); issue1(0, 7); WVS(2, 0);
    LBAR(); sweep(); WVS(1, 8);
    LBAR(); issue1(0, 8);
    WVS(1, 8);
    LBAR();
    {
      f32x4 s;
      auto total = [&](const u32x4 (&v)[8]) {
        unsigned bad = 0;
        s = (f32x4){0.f, 0.f, 0.f, 0.f};
#pragma unroll
        for (int p = 0; p < 8; ++p) { bad |= (v[p].x ^ tagA) | (v[p].z ^ tagB); s[(p & 1) * 2] += __uint_as_float(v[p].y); s[(p & 1) * 2 + 1] += __uint_as_float(v[p].w); }
        return __builtin_amdgcn_ballot_w64(bad != 0) == 0;
      };
      WVS(1, 0);
      asm volatile("" : "+v"(sv[0]), "+v"(sv[1]), "+v"(sv[2]), "+v"(sv[3]), "+v"(sv[4]), "+v"(sv[5]), "+v"(sv[6]), "+v"(sv[7]) :: "memory");
      if (__builtin_expect(!total(sv), 0)) {
#pragma unroll 1
        for (unsigned spins = 0;; ++spins) {
          u32x4 rv[8];
          MEMB();
#pragma unroll
          for (int p = 0; p < 8; ++p) rv[p] = __builtin_amdgcn_raw_buffer_load_b128(rS, vL + (p & 1) * 1024, (ti * 16 + lw * 4 + (p >> 1)) * 2048, 16);
          if (total(rv)) break;
          if (spins > SPIN_CAP) { s[0] = s[2] = __builtin_nanf(""); break; }
        }
      }
      const f32x2 o0 = {s[0], s[1]}, o1 = {s[2], s[3]};
      asm volatile("ds_write_b64 %0, %1\n\tds_write_b64 %0, %2 offset:2048\n\ts_waitcnt lgkmcnt(0)" :: "v"(lds0 + L_R + lane * 32 + lw * 8), "v"(o0), "v"(o1) : "memory");
    }
    LBAR();
    WV(0);
    LBAR();
    issueW(0); issueW(2);
    LBAR();
    issueW(1); issueW(3);
    TOUCH(rWo, lane < 21 ? lane * 128 : OOB, bid * 10368 + lw * 2688);
    auto rZ = RSRC(ws + WS_Z2, FRAG_BYTES);
    const unsigned sZ = fj * 128 * CH + zp0 * 1024;
    u64 ready = 0;
    auto issueZ = [&](int n) {
      const int kp = kpos(n);
      if (__builtin_expect(!got(ready, kp >> 1), 0)) { for (unsigned spins = 0; !got(ready, kp >> 1) && spins < SPIN_CAP; ++spins) ready = poll16(ws + WS_F1 + fj * 16 * 128, tag, lane); }
      MEMB();
      const unsigned dz = lds0 + (n & 3) * STG + 16384 + zp0 * 1024, so = sZ + kp * 18432;
      GS4(rZ, dz, vL, so);
      if (big) GS(rZ, dz + 4096, vL, so + 4096, 0);
    };
    const char* f1 = ws + WS_F1 + fj * 16 * 128;
    for (int q = 0; q < PDELAY; ++q) __builtin_amdgcn_s_sleep(4);
    { unsigned np = 0; for (; ready != 0xff && np < SPIN_CAP; ++np) ready = pollN<2>(f1, tag, lane); LST(0, np); }
    issueZ(0); issueZ(1);
    WVS(1, -4);
    LBAR();
    u64 fv;
    asm volatile("global_load_dwordx2 %0, %1, %2 sc1" : "=&v"(fv) : "v"((lane >> 2) * 128 + (lane & 3) * 8), "s"(f1) : "memory");
    issueZ(2);
    WVS(1, -3);
    LBAR();
    issueZ(3);
    WVS(1, -4);
    asm volatile("" : "+v"(fv));
    ready |= __builtin_amdgcn_ballot_w64(fv == tag);
    LBAR();
    for (int n = 4; n < 31; ++n) {
      issueW(n); issueZ(n);
      WVS(1, 0);
      LBAR();
    }
    issueW(31); issueZ(31);
    constexpr int TAIL = 9;
    unsigned vO[2], vOh;
#pragma unroll
    for (int q = 0; q < 2; ++q) { const int row = 36 * lw + 8 * q + (lane >> 3); vO[q] = row * 1152 + (((lane & 7) ^ (((row >> 1) & 1) | (row & 4))) << 4); }
    { const int row = 36 * lw + 32 + ((lane >> 3) & 3); vOh = row * 1152 + (((lane & 7) ^ (((row >> 1) & 1) | (row & 4))) << 4); }
    const unsigned vO1 = vO[1] - 1024, vO2 = vO[0] + 16 * 1152 - 2048, vO3 = vO[1] + 16 * 1152 - 3072;
    const unsigned sO = min(dd, 8) * 128;
    auto issueO = [&](int n, int slot) {
      const unsigned d = lds0 + L_O + slot * STGO + lw * 4608, s0 = sO + n * 144 * 1152;
      GL4(rWo, d, vO[0], vO1, vO2, vO3, s0);
      if (lane < 32) GL(rWo, d + 4096, vOh, s0, 0);
    };
    issueO(0, 0);
    WVS(1, 5); LBAR();
    issueO(1, 1); issueO(2, 2);
    WV(15); LBAR();
    LBAR();
    WV(0);
    LBAR();
    if (!has3) return;
    issueO(3, 3); issueO(4, 4);
    LBAR();
    issueO(5, 0);
    if (NPRE3 == 5) { issueO(6, 1); issueO(7, 2); }
    auto rP = RSRC(ws + WS_P, FRAG_BYTES);
    const unsigned vPs = lw < 3 ? (lane >> 5) * 16 * CH + (lane & 31) * 16 + 4096 : OOB;
    const int limS = lw == 2 ? 32 : 64;
    const unsigned sP = tt * 5 * 16 * CH;
    const int pa = (tt * 80) >> 7, pb = min((tt * 80 + 79) >> 7, 15);
    u64 rdy = 0;
    auto issueP = [&](int j, int slot) {
      if (__builtin_expect(!got(rdy, j), 0)) { for (unsigned spins = 0; !got(rdy, j) && spins < SPIN_CAP; ++spins) rdy = poll16(ws + WS_F2 + pa * 16 * 128, tag, lane) & poll16(ws + WS_F2 + pb * 16 * 128, tag, lane); }
      MEMB();
      const unsigned d = lds0 + L_P + slot * STGP, sp = sP + j * CH;
      GS4(rP, d + lw * 4096, vL, sp + lw * 16 * CH);
      GS(rP, d + 16384 + lw * 1024, vL, sp + 64 * CH + lw * 1024, 0);
      if (lane < limS) GS(rP, lw < 3 ? d + 20480 + lw * 1024 : lds0 + L_DUMMY, vPs, sp + lw * 32 * CH, 0);
    };
    const char *fa = ws + WS_F2 + pa * 16 * 128, *fb = ws + WS_F2 + pb * 16 * 128;
    { unsigned np = 0; LST(2, 0); for (; rdy != 0xfff && np < SPIN_CAP; ++np) rdy = pollN<3>(fa, tag, lane) & pollN<3>(fb, tag, lane); LST(1, np); }
    u64 fva, fvb;
    asm volatile("global_load_dwordx2 %0, %2, %3 sc1\n\tglobal_load_dwordx2 %1, %2, %4 sc1" : "=&v"(fva), "=&v"(fvb) : "v"((lane >> 2) * 128 + (lane & 3) * 8), "s"(fa), "s"(fb) : "memory");
    issueP(0, 0); issueP(1, 1); issueP(2, 2);
    WV(12);
    asm volatile("" : "+v"(fva), "+v"(fvb));
    rdy |= __builtin_amdgcn_ballot_w64(fva == tag) & __builtin_amdgcn_ballot_w64(fvb == tag);
    LBAR();
    WV(6);
    if (NPRE3 == 5) {
      LBAR(); issueP(3, 0); WV(6);
      LBAR(); issueP(4, 1); WV(6);
      LBAR(); issueP(5, 2); WV(6);
    } else {
      LBAR(); issueP(3, 0); issueO(6, 1); WV(11);
      LBAR(); issueP(4, 1); issueO(7, 2); WV(16);
      LBAR(); issueP(5, 2); WV(11);
    }
    LBAR(); issueP(6, 0); issueO(8, 3); WV(11);
    int sp3 = 1, so5 = 4;
    for (int n = 5; n < 12; ++n) {
      LBAR();
      issueP(n + 2, sp3); issueO(n + 4, so5);
      sp3 = sp3 == 2 ? 0 : sp3 + 1; so5 = so5 == 4 ? 0 : so5 + 1;
      WV(16);
    }
    LBAR(); issueP(14, 2); WV(6);
    LBAR(); issueP(15, 0); WV(6);
    LBAR(); WV(0);
    LBAR();
    LBAR(); LBAR();
    if (bid == 0 && tid == 256) __hip_atomic_store((u64*)(ws + WS_EP), tag, RLX_AGENT);
  } else {
    const int wt = wid >> 1, wf = wid & 1;
    if (wid == 0) {
      auto rC = RSRC((const char*)(pc0 & ~(u64)255), CODE_KB * 1024);
      GL(rC, lds0 + L_DUMMY, lane * 16, rk * 1024, 0); GL(rC, lds0 + L_DUMMY, lane * 16, rk * 1024 + 32768, 0);
      WV(0);
    }
    if (wf == 0) front<0>(lds0, wid, wt, lane, ti, fj, ws); else front<1>(lds0, wid, wt, lane, ti, fj, ws);
    if (!has3) return;
    if (wt == 0) g3_compute<0>(lds0, wf, lane, out, tt, dd); else g3_compute<1>(lds0, wf, lane, out, tt, dd);
  }
}

extern "C" void kernel_launch(void* const* d_in, const int* in_sizes, int n_in, void* d_out, int out_size, void* d_ws, size_t ws_size, hipStream_t stream) {
  (void)hipFuncSetAttribute((const void*)gmlp_fused, hipFuncAttributeMaxDynamicSharedMemorySize, LDS_BYTES);
  gmlp_fused<<<256, 512, LDS_BYTES, stream>>>((const char*)d_in[0], (const char*)d_in[1], (const char*)d_in[2], (const u16*)d_in[3], (const char*)d_in[4], (char*)d_out, (char*)d_ws);
}
```

```cpp
#include <hip/hip_runtime.h>
#include <hip/hip_bf16.h>
typedef unsigned short u16;
typedef unsigned long long u64;
typedef short s16x4 __attribute__((ext_vector_type(4)));
typedef short bf16x8 __attribute__((ext_vector_type(8)));
typedef float f32x2 __attribute__((ext_vector_type(2)));
typedef float f32x4 __attribute__((ext_vector_type(4)));
typedef unsigned u32x4 __attribute__((ext_vector_type(4)));
typedef unsigned u32x2 __attribute__((ext_vector_type(2)));
#define LAS __attribute__((address_space(3)))
#define DEV __device__ __forceinline__
#define BAR() __builtin_amdgcn_s_barrier()
#define SB() __builtin_amdgcn_sched_barrier(0)
#define MEMB() asm volatile("" ::: "memory")
#define WAITL0() __builtin_amdgcn_s_waitcnt(0xC07F)
#define WL0() asm volatile("s_waitcnt lgkmcnt(0)" ::: "memory")
#define WV(N) asm volatile("s_waitcnt vmcnt(%0)" ::"i"(N) : "memory")
#define MFMA(a, b, c) __builtin_amdgcn_mfma_f32_16x16x32_bf16(a, b, c, 0, 0, 0)
#define MFMA16(a, b, c) __builtin_amdgcn_mfma_f32_16x16x16bf16_1k(a, b, c, 0, 0, 0)
#define GL(R, D, V, S, I) __builtin_amdgcn_raw_ptr_buffer_load_lds(R, (LAS void*)(size_t)(D), 16, V, S, I, 0)
#define GL4(R, D, V0, V1, V2, V3, S) do { GL(R, D, V0, S, 0); GL(R, D, V1, S, 1024); GL(R, D, V2, S, 2048); GL(R, D, V3, S, 3072); } while (0)
#define GS(R, D, V, S, I) __builtin_amdgcn_raw_ptr_buffer_load_lds(R, (LAS void*)(size_t)(D), 16, V, S, I, 16)
#define GS4(R, D, V, S) do { GS(R, D, V, S, 0); GS(R, D, V, S, 1024); GS(R, D, V, S, 2048); GS(R, D, V, S, 3072); } while (0)
#define TOUCH(R, V, S) __builtin_amdgcn_raw_ptr_buffer_load_lds(R, (LAS void*)(size_t)(lds0 + L_DUMMY), 4, V, S, 0, 0)
#define RSRC(p, n) __builtin_amdgcn_make_buffer_rsrc((void*)(p), 0, (n), 0x00020000)
#define RLX_AGENT __ATOMIC_RELAXED, __HIP_MEMORY_SCOPE_AGENT
#define ROT1 2
#define NPRE3 3
#define Z1W 0
#define LS0 1
#define OAUX 16
#define WAUX 0
#define T0W 0
#define PDELAY 7
#define PSLEEP 0
#define LA2 2
#define CODE_KB 54

constexpr int CH = 4608;
constexpr int FRAG_BYTES = 16 * 128 * CH;
constexpr size_t WS_Z2 = 0, WS_P = FRAG_BYTES, WS_ST = 2 * (size_t)FRAG_BYTES, WS_F1 = WS_ST + 16 * 16 * 128 * 16, WS_F2 = WS_F1 + 256 * 128, WS_EP = WS_F2 + 256 * 128;
constexpr int STG = 34816, STGP = 23040, STGO = 18432, L_O = 0, L_P = 5 * STGO, LDS_BYTES = 163840;
constexpr int L_T = 139264, L_R = L_T + 8192, L_X = 151552, L_BIAS = 159744, L_TAG = L_BIAS + 256, L_DUMMY = 161280;
constexpr unsigned SPIN_CAP = 1u << 16, OOB = 0x80000000u;
#define LBAR() BAR()
#define CST(I)
#define LST(I, X)

DEV unsigned cvt_pk(float lo, float hi) { unsigned r; asm("v_cvt_pk_bf16_f32 %0, %1, %2" : "=v"(r) : "v"(lo), "v"(hi)); return r; }
DEV bf16x8 ld128(unsigned a) { return *(LAS bf16x8*)(size_t)a; }
DEV s16x4 ld64(unsigned a) { return *(LAS s16x4*)(size_t)a; }
DEV s16x4 ldtr(unsigned a) { return __builtin_amdgcn_ds_read_tr16_b64_v4i16((LAS s16x4*)(size_t)a); }
DEV bf16x8 cat(s16x4 a, s16x4 b) { return __builtin_shufflevector(a, b, 0, 1, 2, 3, 4, 5, 6, 7); }
DEV float bflo(unsigned v) { return __uint_as_float(v << 16); }
DEV float bfhi(unsigned v) { return __uint_as_float(v & 0xffff0000u); }
template <int NB, bool SUM>
DEV void gelu_blocks(const f32x4* const (&v)[NB], u32x2* const (&o)[NB], f32x2& s1, f32x2& s2) {
  const float k1 = -2.0f * 0.7978845608028654f * 1.4426950408889634f, k2 = k1 * 0.044715f;
  f32x2 x[2 * NB], t[2 * NB];
#pragma unroll
  for (int i = 0; i < 2 * NB; ++i) { x[i] = (f32x2){(*v[i >> 1])[(i & 1) * 2], (*v[i >> 1])[(i & 1) * 2 + 1]}; t[i] = x[i] * x[i]; }
  SB();
#pragma unroll
  for (int i = 0; i < 2 * NB; ++i) t[i] = t[i] * k2 + k1;
  SB();
#pragma unroll
  for (int i = 0; i < 2 * NB; ++i) t[i] = t[i] * x[i];
  SB();
#pragma unroll
  for (int i = 0; i < 2 * NB; ++i) { t[i].x = __builtin_amdgcn_exp2f(t[i].x); t[i].y = __builtin_amdgcn_exp2f(t[i].y); }
  SB();
#pragma unroll
  for (int i = 0; i < 2 * NB; ++i) t[i] = t[i] + 1.0f;
  SB();
#pragma unroll
  for (int i = 0; i < 2 * NB; ++i) { t[i].x = __builtin_amdgcn_rcpf(t[i].x); t[i].y = __builtin_amdgcn_rcpf(t[i].y); }
  SB();
#pragma unroll
  for (int i = 0; i < 2 * NB; ++i) t[i] = t[i] * x[i];
  SB();
  f32x2 a1 = s1, a2 = s2, b1 = {0.f, 0.f}, b2 = {0.f, 0.f};
#pragma unroll
  for (int i = 0; i < NB; ++i) {
    o[i]->x = cvt_pk(t[2 * i].x, t[2 * i].y);
    if (SUM) { a1 += t[2 * i]; a2 += t[2 * i] * t[2 * i]; }
    o[i]->y = cvt_pk(t[2 * i + 1].x, t[2 * i + 1].y);
    if (SUM) { if (i == 0) { b1 = t[1]; b2 = t[1] * t[1]; } else { b1 += t[2 * i + 1]; b2 += t[2 * i + 1] * t[2 * i + 1]; } }
  }
  if (SUM) { s1 = a1 + b1; s2 = a2 + b2; }
  SB();
}
template <int WF, bool SUM>
DEV void gelu_row(int m, const f32x4 (&acc)[4][5], u32x2 (&oa)[4][4], u32x2 (&ob)[2], f32x2& s1, f32x2& s2) {
  if ((m >> 1) == WF) { const f32x4* const v[5] = {&acc[m][0], &acc[m][1], &acc[m][2], &acc[m][3], &acc[m][4]}; u32x2* const o[5] = {&oa[m][0], &oa[m][1], &oa[m][2], &oa[m][3], &ob[m & 1]}; gelu_blocks<5, SUM>(v, o, s1, s2); }
  else { const f32x4* const v[4] = {&acc[m][0], &acc[m][1], &acc[m][2], &acc[m][3]}; u32x2* const o[4] = {&oa[m][0], &oa[m][1], &oa[m][2], &oa[m][3]}; gelu_blocks<4, SUM>(v, o, s1, s2); }
}

struct TAddr { unsigned w[2], z[2][2], s[2]; };
struct TFrag { bf16x8 wX[4], wY[4]; s16x4 tX[5][2], tY[4][2]; };
template <int WF, int PS, int OFF, bool FIRST, bool SYNC = true>
DEV void t_step(TAddr& A, int dlt, f32x4 (&acc)[4][5], TFrag& F, const f32x4* init = nullptr) {
  constexpr int KX = WF, KY = 1 - WF;
  SB(); if (SYNC) BAR(); MEMB(); SB();
  auto rdz = [&](s16x4 (&t)[2], int n, int u, int K) { t[u] = n < 4 ? ldtr(A.z[u][n & 1] + OFF + K * 9216 + (n >> 1) * PS) : ldtr(A.s[u] + OFF + K * 9216); };
#pragma unroll
  for (int i = 0; i < 16; ++i) {
    const int m = i >> 2, n = i & 3;
    if (!FIRST) acc[m][n] = MFMA(cat(F.tY[n][0], F.tY[n][1]), F.wY[m], acc[m][n]);
    SB();
    if (i == 0) F.wX[0] = ld128(A.w[KX] + OFF);
    else if (i < 11) rdz(F.tX[(i - 1) >> 1], (i - 1) >> 1, (i - 1) & 1, KX);
    else if (i < 14) F.wX[i - 10] = ld128(A.w[KX] + OFF + (i - 10) * 2048);
    SB();
  }
#pragma unroll
  for (int i = 0; i < 20; ++i) {
    const int m = i / 5, n = i % 5;
    const f32x4 zero4 = {0, 0, 0, 0};
    acc[m][n] = MFMA(cat(F.tX[n][0], F.tX[n][1]), F.wX[m], !FIRST ? acc[m][n] : (PS == 1024 && (n < 4 || (m >> 1) == WF)) ? init[m] : zero4);
    SB();
    if (i == 0) F.wY[0] = ld128(A.w[KY] + OFF);
    else if (i < 9) rdz(F.tY[(i - 1) >> 1], (i - 1) >> 1, (i - 1) & 1, KY);
    else if (i < 12) F.wY[i - 8] = ld128(A.w[KY] + OFF + (i - 8) * 2048);
    else if (OFF != 0 && i < 16) {
      const int u = i & 1;
      if (i < 14) { A.w[u] += dlt; A.s[u] += dlt; asm volatile("" : "+v"(A.w[u]), "+v"(A.s[u])); }
      else { A.z[u][0] += dlt; A.z[u][1] += dlt; asm volatile("" : "+v"(A.z[u][0]), "+v"(A.z[u][1])); }
    }
    SB();
  }
  WAITL0();
}
template <int WF>
DEV void t_finish(unsigned lds0, int wt, int lane, f32x4 (&acc)[4][5], TFrag& F) {
#pragma unroll
  for (int h = 0; h < 2; ++h) *(LAS f32x4*)(size_t)(lds0 + L_X + ((wt * 2 + WF) * 2 + h) * 1024 + lane * 16) = acc[2 * (1 - WF) + h][4];
  SB();
#pragma unroll
  for (int m = 0; m < 4; ++m)
#pragma unroll
    for (int n = 0; n < 4; ++n) acc[m][n] = MFMA(cat(F.tY[n][0], F.tY[n][1]), F.wY[m], acc[m][n]);
  SB();
  WL0();
  BAR(); MEMB();
#pragma unroll
  for (int h = 0; h < 2; ++h) acc[2 * WF + h][4] += *(LAS f32x4*)(size_t)(lds0 + L_X + ((wt * 2 + 1 - WF) * 2 + h) * 1024 + lane * 16);
}
template <int WF, bool MID, class Mid>
DEV void in_pass(unsigned lds0, int wt, int lane, TAddr& A, f32x4 (&acc)[4][5], Mid mid) {
  TFrag F;
  int dlt = 2 * STG;
  WAITL0();
  if (!MID) {
    t_step<WF, 64, 0, true>(A, dlt, acc, F);
    for (int k = 0; k < 4; ++k) {
      t_step<WF, 64, STG, false>(A, dlt, acc, F);
      dlt = -dlt;
      t_step<WF, 64, 0, false>(A, dlt, acc, F);
    }
  } else {
    t_step<WF, 64, STG, true>(A, dlt, acc, F);
    for (int k = 0; k < 3; ++k) {
      dlt = -dlt;
      t_step<WF, 64, 0, false>(A, dlt, acc, F);
      t_step<WF, 64, STG, false>(A, dlt, acc, F);
    }
    t_step<WF, 64, 0, false>(A, 0, acc, F);
    SB(); BAR(); MEMB(); mid(); SB();
    t_step<WF, 64, STG, false>(A, 0, acc, F);
  }
  t_finish<WF>(lds0, wt, lane, acc, F);
}

struct G3P { bf16x8 p[2][5]; s16x4 ps[3]; };
struct G3W { s16x4 w[2][2][2], ws[2]; };
template <int KH>
DEV void g3_readw(G3W& n, unsigned a0, unsigned a1) {
#pragma unroll
  for (int q = 0; q < 8; ++q) n.w[q >> 2][(q >> 1) & 1][q & 1] = ldtr((((q >> 1) & 1) ? a1 : a0) + (2 * KH + (q >> 2)) * 4096 + (q & 1) * 2048);
  n.ws[0] = ldtr(a0 + 16384); n.ws[1] = ldtr(a1 + 16384);
}
template <int KH, bool MM, bool RDW>
DEV void g3_stage(f32x4 (&acc)[5][2], const G3P& cp, const G3W& cw, G3P& np, G3W& nw, unsigned aP, unsigned aS, unsigned a0, unsigned a1) {
  constexpr int M0 = KH ? 3 : 0, NS = KH ? 2 : 3, NM = 20 + 2 * NS, NR = 20 + NS;
  SB(); BAR(); MEMB(); SB();
#pragma unroll
  for (int i = 0; i < NM; ++i) {
    if (MM) {
      if (i >= 10 && i < 10 + 2 * NS) acc[M0 + ((i - 10) >> 1)][i & 1] = MFMA16(cw.ws[i & 1], cp.ps[(i - 10) >> 1], acc[M0 + ((i - 10) >> 1)][i & 1]);
      else { const int ii = i < 10 ? i : i - 2 * NS, P = ii / 10, mm = (ii % 10) >> 1, m = (KH != P) ? (mm + 3) % 5 : mm, cc = ii & 1; acc[m][cc] = MFMA(cat(cw.w[P][cc][0], cw.w[P][cc][1]), cp.p[P][m], acc[m][cc]); }
    }
    SB();
#pragma unroll
    for (int r = (i * 3) / 2; r < ((i + 1) * 3) / 2; ++r) {
      if (r < 18) { const int P = r / 9, q = r % 9;
        if (q < 4) { if (RDW) nw.w[P][q >> 1][q & 1] = ldtr(((q >> 1) ? a1 : a0) + (2 * KH + P) * 4096 + (q & 1) * 2048); }
        else np.p[P][q - 4] = ld128(aP + P * 1024 + (q - 4) * 4096); }
      else if (r < 20) { if (RDW) nw.ws[r - 18] = ldtr(((r - 18) ? a1 : a0) + 16384); }
      else if (r < NR) np.ps[r - 20] = ld64(aS + (M0 + r - 20) * 512);
    }
    SB();
  }
  WAITL0();
}
template <int KH>
DEV void g3_compute(unsigned lds0, int dh, int lane, char* out, int tt, int dd) {
  const int g = lane >> 4, qr = (lane >> 2) & 3, p = lane & 3;
  const int rr = 4 * g + qr;
  unsigned bW[2];
#pragma unroll
  for (int c = 0; c < 2; ++c) bW[c] = lds0 + L_O + rr * 128 + (((4 * dh + p) ^ (((rr >> 1) & 1) | (rr & 4))) << 4) + ((c ^ (p & 1)) * 8);
  const unsigned bP = lds0 + L_P + lane * 16 + KH * 2048, bS = lds0 + L_P + 20480 + lane * 8;
  G3W a0, a1, a2;
  g3_readw<KH>(a0, bW[0], bW[1]); g3_readw<KH>(a1, bW[0] + STGO, bW[1] + STGO); g3_readw<KH>(a2, bW[0] + 2 * STGO, bW[1] + 2 * STGO);
  f32x4 acc[5][2];
#pragma unroll
  for (int m = 0; m < 5; ++m) { acc[m][0] = (f32x4){0, 0, 0, 0}; acc[m][1] = (f32x4){0, 0, 0, 0}; }
  WAITL0();
  SB(); BAR(); SB();
  G3P p0, p1; G3W w0, w1;
  g3_stage<KH, false, false>(acc, p1, a0, p0, w0, bP, bS, 0, 0);
  g3_stage<KH, true, false>(acc, p0, a0, p1, w0, bP + STGP, bS + STGP, 0, 0);
  g3_stage<KH, true, false>(acc, p1, a1, p0, w0, bP + 2 * STGP, bS + 2 * STGP, 0, 0);
  g3_stage<KH, true, true>(acc, p0, a2, p1, w1, bP, bS, bW[0] + 3 * STGO, bW[1] + 3 * STGO);
  unsigned sp = STGP, sw = 4 * STGO;
  for (int j = 4; j < 16; j += 2) {
    g3_stage<KH, true, true>(acc, p1, w1, p0, w0, bP + sp, bS + sp, bW[0] + sw, bW[1] + sw);
    sp = sp == 2 * STGP ? 0 : sp + STGP; sw = sw == 4 * STGO ? 0 : sw + STGO;
    g3_stage<KH, true, true>(acc, p0, w0, p1, w1, bP + sp, bS + sp, bW[0] + sw, bW[1] + sw);
    sp = sp == 2 * STGP ? 0 : sp + STGP; sw = sw == 4 * STGO ? 0 : sw + STGO;
  }
  {
    constexpr int M0 = KH ? 3 : 0, NS = KH ? 2 : 3;
    SB(); BAR(); SB();
#pragma unroll
    for (int i = 0; i < 2 * NS; ++i) { acc[M0 + (i >> 1)][i & 1] = MFMA16(w1.ws[i & 1], p1.ps[i >> 1], acc[M0 + (i >> 1)][i & 1]); SB(); }
#pragma unroll
    for (int i = 0; i < 20; ++i) { const int P = (i >> 1) & 1, m = KH ? i >> 2 : 4 - (i >> 2), cc = i & 1; acc[m][cc] = MFMA(cat(w1.w[P][cc][0], w1.w[P][cc][1]), p1.p[P][m], acc[m][cc]); SB(); }
  }
#pragma unroll
  for (int m = 0; m < 5; ++m)
    if ((m >= 3) != KH) { *(LAS f32x4*)(size_t)(lds0 + (dh * 10 + m * 2) * 1024 + lane * 16) = acc[m][0]; *(LAS f32x4*)(size_t)(lds0 + (dh * 10 + m * 2 + 1) * 1024 + lane * 16) = acc[m][1]; }
  WL0();
  BAR(); MEMB();
  auto rO = RSRC(out, 2048 * 1152);
  const bool sw2 = g & 1;
#pragma unroll
  for (int m = 0; m < 5; ++m)
    if ((m >= 3) == KH) {
      const f32x4 a = acc[m][0] + *(LAS f32x4*)(size_t)(lds0 + (dh * 10 + m * 2) * 1024 + lane * 16), b = acc[m][1] + *(LAS f32x4*)(size_t)(lds0 + (dh * 10 + m * 2 + 1) * 1024 + lane * 16);
      const unsigned a0 = cvt_pk(a[0], a[1]), a1 = cvt_pk(a[2], a[3]), b0 = cvt_pk(b[0], b[1]), b1 = cvt_pk(b[2], b[3]);
      const u32x4 v = {sw2 ? b0 : a0, sw2 ? b1 : a1, sw2 ? a0 : b0, sw2 ? a1 : b1};
      __builtin_amdgcn_raw_buffer_store_b128(v, rO, (tt * 80 + m * 16 + (lane & 15)) * 1152 + dd * 128 + dh * 64 + g * 16, 0, OAUX);
    }
}

template <int WF>
DEV void front(unsigned lds0, int wid, int wt, int lane, int ti, int fj, char* ws) {
  const int r16 = lane & 15, g = lane >> 4, qr = (lane >> 2) & 3, p = lane & 3;
  TAddr A;
  A.w[0] = lds0 + (64 * wt + r16) * 128 + ((g ^ (r16 >> 1)) << 4); A.w[1] = A.w[0] ^ 64;
#pragma unroll
  for (int u = 0; u < 2; ++u) {
    const unsigned b = lds0 + 16384 + (16 * u + 4 * g + qr) * 288 + p * 8;
    A.z[u][0] = b + WF * 128 + (p & 1) * 32; A.z[u][1] = b + WF * 128 + 32 - (p & 1) * 32; A.s[u] = b + 256;
  }
  asm volatile("" : "+v"(A.w[0]), "+v"(A.w[1]), "+v"(A.s[0]), "+v"(A.s[1]), "+v"(A.z[0][0]), "+v"(A.z[0][1]), "+v"(A.z[1][0]), "+v"(A.z[1][1]));
  f32x4 acc[4][5];
  in_pass<WF, false>(lds0, wt, lane, A, acc, []() {});
  CST(0);
  u32x2 ua[4][4], ub[2];
  {
    f32x2 s1[4], s2[4];
#pragma unroll
    for (int mm = 0; mm < 4; ++mm) {
      const int m = mm ^ (2 * (1 - WF));
      s1[m] = (f32x2){0.f, 0.f}; s2[m] = (f32x2){0.f, 0.f};
      gelu_row<WF, true>(m, acc, ua, ub, s1[m], s2[m]);
    }
#pragma unroll
    for (int m = 0; m < 4; ++m) *(LAS f32x2*)(size_t)(lds0 + L_T + (64 * wt + 16 * m + r16) * 64 + (WF * 4 + g) * 8) = (f32x2){s1[m].x + s1[m].y, s2[m].x + s2[m].y};
  }
#pragma unroll
  for (int m = 0; m < 4; ++m) asm volatile("" : "+v"(ua[m][0]), "+v"(ua[m][1]), "+v"(ua[m][2]), "+v"(ua[m][3]));
  asm volatile("" : "+v"(ub[0]), "+v"(ub[1]));
#pragma unroll
  for (int u = 0; u < 2; ++u) { A.z[u][0] -= (p & 1) * 32; A.z[u][1] += (p & 1) * 32; }
  asm volatile("" : "+v"(A.z[0][0]), "+v"(A.z[0][1]), "+v"(A.z[1][0]), "+v"(A.z[1][1]));
  CST(1);
  in_pass<WF, true>(lds0, wt, lane, A, acc, [&]() {
    CST(2);
#pragma unroll
    for (int m = 0; m < 4; ++m) asm volatile("" : "+v"(ua[m][0]), "+v"(ua[m][1]), "+v"(ua[m][2]), "+v"(ua[m][3]));
    asm volatile("" : "+v"(ub[0]), "+v"(ub[1]));
    int lz = lane; asm volatile("" : "+v"(lz));
    auto rZ = RSRC(ws + WS_Z2, FRAG_BYTES);
    const unsigned sl = (lz >> 4) * 16 + ((lz & 15) ^ (4 * (lz >> 5))), dst = (fj * 128 + ti * 8 + wt * 4) * CH;
#pragma unroll
    for (int m = 0; m < 4; ++m) {
      const unsigned ar = lds0 + L_R + (64 * wt + 16 * m + (lz & 15)) * 32;
      const f32x4 q4 = *(LAS f32x4*)(size_t)ar + *(LAS f32x4*)(size_t)(ar + 16);
      const float mean = (q4[0] + q4[2]) * (1.0f / 2304.0f), var = fmaxf((q4[1] + q4[3]) * (1.0f / 2304.0f) - mean * mean, 0.f);
      const float rs = __builtin_amdgcn_rsqf(var + 1e-5f), sh = -mean * rs;
      auto norm = [&](unsigned v) { const f32x2 y = (f32x2){bflo(v), bfhi(v)} * rs + sh; return cvt_pk(y.x, y.y); };
#pragma unroll
      for (int pp = 0; pp < 2; ++pp)
        __builtin_amdgcn_raw_buffer_store_b128((u32x4){norm(ua[m][2 * pp].x), norm(ua[m][2 * pp].y), norm(ua[m][2 * pp + 1].x), norm(ua[m][2 * pp + 1].y)}, rZ, dst + m * CH + sl * 16 + WF * 2048 + pp * 1024, 0, 16);
      if ((m >> 1) == WF) __builtin_amdgcn_raw_buffer_store_b64((u32x2){norm(ub[m & 1].x), norm(ub[m & 1].y)}, rZ, dst + m * CH + sl * 8 + 4096, 0, 16);
    }
    CST(3);
  });
  CST(4);
#pragma unroll
  for (int m = 0; m < 4; ++m) asm volatile("" : "+v"(acc[m][0]), "+v"(acc[m][1]), "+v"(acc[m][2]), "+v"(acc[m][3]), "+v"(acc[m][4]));
  const u32x2 tg = *(LAS u32x2*)(size_t)(lds0 + L_TAG);
  const u64 tag = ((u64)(unsigned)__builtin_amdgcn_readfirstlane(tg.y) << 32) | (unsigned)__builtin_amdgcn_readfirstlane(tg.x);
  WV(0);
  if (lane == 0) __hip_atomic_store((u64*)(ws + WS_F1 + (fj * 16 + ti) * 128) + wid, tag, RLX_AGENT);
  CST(5);
  SB();
  u32x2 za[4][4], zb[2];
#pragma unroll
  for (int m = 0; m < 4; ++m) {
    f32x2 d1, d2;
    gelu_row<WF, false>(m, acc, za, zb, d1, d2);
    asm volatile("" : "+v"(za[m][0]), "+v"(za[m][1]), "+v"(za[m][2]), "+v"(za[m][3]));
  }
  asm volatile("" : "+v"(zb[0]), "+v"(zb[1]));
  CST(6);
#pragma unroll
  for (int u = 0; u < 2; ++u) {
    const unsigned sl = p * 16 + 8 * (g & 1) + 4 * (u ^ (p >> 1)) + qr;
#pragma unroll
    for (int h = 0; h < 2; ++h) A.z[u][h] = lds0 + 16384 + (g >> 1) * CH + WF * 2048 + sl * 16 + ((h ^ (p & 1)) * 8);
    A.s[u] = lds0 + 16384 + (g >> 1) * CH + 4096 + sl * 8;
  }
  asm volatile("" : "+v"(A.s[0]), "+v"(A.s[1]), "+v"(A.z[0][0]), "+v"(A.z[0][1]), "+v"(A.z[1][0]), "+v"(A.z[1][1]));
  f32x4 bias4[4];
#pragma unroll
  for (int m = 0; m < 4; ++m) { const float b = __uint_as_float((unsigned)*(LAS u16*)(size_t)(lds0 + L_BIAS + (wt * 64 + m * 16 + r16) * 2) << 16); bias4[m] = (f32x4){b, b, b, b}; }
  TFrag F;
  int dlt = 2 * STG;
  WAITL0();
  CST(7);
  t_step<WF, 1024, 0, true, true>(A, dlt, acc, F, bias4);
  CST(8);
  t_step<WF, 1024, STG, false, true>(A, dlt, acc, F);

  for (int k = 0; k < 15; ++k) {
    dlt = -dlt;
    t_step<WF, 1024, 0, false>(A, dlt, acc, F);
    t_step<WF, 1024, STG, false>(A, dlt, acc, F);
  }
  auto rP = RSRC(ws + WS_P, FRAG_BYTES);
  auto gate = [&](f32x4 a, u32x2 z) {
    const f32x2 x = (f32x2){a[0], a[1]} * (f32x2){bflo(z.x), bfhi(z.x)}, y = (f32x2){a[2], a[3]} * (f32x2){bflo(z.y), bfhi(z.y)};
    return (u32x2){cvt_pk(x.x, x.y), cvt_pk(y.x, y.y)};
  };
#pragma unroll
  for (int h = 0; h < 2; ++h) *(LAS f32x4*)(size_t)(lds0 + L_X + ((wt * 2 + WF) * 2 + h) * 1024 + lane * 16) = acc[2 * (1 - WF) + h][4];
  const unsigned dst0 = (ti * 8 + wt * 4) * 16 * CH + fj * CH;
  CST(9);
#pragma unroll
  for (int m = 0; m < 4; ++m)
#pragma unroll
    for (int n = 0; n < 4; ++n) acc[m][n] = MFMA(cat(F.tY[n][0], F.tY[n][1]), F.wY[m], acc[m][n]);
#pragma unroll
  for (int m = 0; m < 4; ++m)
#pragma unroll
    for (int pp = 0; pp < 2; ++pp) {
      const u32x2 a = gate(acc[m][2 * pp], za[m][2 * pp]), b = gate(acc[m][2 * pp + 1], za[m][2 * pp + 1]);
      __builtin_amdgcn_raw_buffer_store_b128((u32x4){a.x, a.y, b.x, b.y}, rP, dst0 + m * 16 * CH + lane * 16 + (2 * WF + pp) * 1024, 0, 16);
    }
  SB();
  WL0();
  BAR(); MEMB();
#pragma unroll
  for (int h = 0; h < 2; ++h) {
    const f32x4 o8 = *(LAS f32x4*)(size_t)(lds0 + L_X + ((wt * 2 + 1 - WF) * 2 + h) * 1024 + lane * 16);
    __builtin_amdgcn_raw_buffer_store_b64(gate(acc[2 * WF + h][4] + o8, zb[h]), rP, dst0 + (2 * WF + h) * 16 * CH + lane * 8 + 4096, 0, 16);
  }
  CST(10);
  WV(0);
  CST(11);
  if (lane == 0) __hip_atomic_store((u64*)(ws + WS_F2 + (ti * 16 + fj) * 128) + wid, tag, RLX_AGENT);
  SB(); BAR(); MEMB(); SB();
}

DEV u64 poll16(const char* base, u64 tag, int lane) {
  const u64 v = __hip_atomic_load((const u64*)(base + (lane >> 2) * 128) + (lane & 3), RLX_AGENT);
  return __builtin_amdgcn_ballot_w64(v == tag);
}
template <int NP> DEV u64 pollN(const char* base, u64 tag, int lane) {
  const u64 v = __hip_atomic_load((const u64*)(base + min(lane >> 2, NP - 1) * 128) + (lane & 3), RLX_AGENT);
  return __builtin_amdgcn_ballot_w64(v == tag) & ((1ull << (4 * NP)) - 1);
}
DEV bool got(u64 mask, int who) { return ((unsigned)(mask >> (4 * who)) & 15u) == 15u; }

__global__ __launch_bounds__(512, 2) void gmlp_fused(const char* __restrict__ X, const char* __restrict__ Win, const char* __restrict__ Wsp, const u16* __restrict__ bias,
                                                     const char* __restrict__ Wo, char* __restrict__ out, char* __restrict__ ws) {
  extern __shared__ __attribute__((aligned(16))) char smem[];
  u64 pc0; asm volatile("s_getpc_b64 %0" : "=s"(pc0));
  const int tid = threadIdx.x, wid = __builtin_amdgcn_readfirstlane(tid >> 6), lane = tid & 63;
  const int bid = blockIdx.x, xg = bid & 7, rk = bid >> 3, il = rk & 7, jl = rk >> 3, ti = (xg & 1) * 8 + il, fj = (xg >> 1) * 4 + jl;
  const int L3 = xg * 30 + rk, tt = L3 / 9, dd = L3 - tt * 9;
  const bool has3 = rk < 30 && L3 < 234;
  const unsigned lds0 = (unsigned)(size_t)(LAS char*)smem;
  if (__builtin_expect(wid >= 4, 1)) {
    __builtin_amdgcn_s_setprio(3);
    const int lw = wid - 4;
    const bool big = wid < 6;
#define WVS(STAGES, EXTRA) do { if (big) WV(9 * (STAGES) + (EXTRA)); else WV(8 * (STAGES) + (EXTRA)); } while (0)
    auto rX = RSRC(X, 2048 * 1152); auto rWi = RSRC(Win, 576 * 9216);
    unsigned vX[4], vW[5];
    const unsigned zp0 = lw < 2 ? lw * 5 : 2 + lw * 4;
    const unsigned sX = (ti * 128 + lw * 32) * 1152, sW = fj * 288;
    const int rot = ROT1 == 0 ? 0 : ROT1 == 1 ? il + 2 * jl : 3 * ((il + jl) % 3);
    const int rot8 = ROT1 == 1 ? (il + 2 * jl) & 7 : ROT1 == 2 ? (((il + jl) % 3) * 8) / 3 : 0;
    auto k1 = [&](int k) { return k == 0 ? 0 : 1 + ((k - 1 + rot8) & 7); };
    auto issue1 = [&](int half, int k) {
      const int kk = k1(k);
      const unsigned d = lds0 + ((k + 1 - half) & 3) * STG, dz = d + 16384 + zp0 * 1024, sw = sW + half * 4608 + kk * 64 * 9216;
      GL4(rX, d + lw * 4096, vX[0], vX[1], vX[2], vX[3], sX + kk * 128);
      GL4(rWi, dz, vW[0], vW[1], vW[2], vW[3], sw);
      if (big) GL(rWi, dz + 4096, vW[4], sw, 0);
    };
    {
#pragma unroll
      for (int q = 0; q < 4; ++q) { const int row = 8 * q + (lane >> 3); vX[q] = row * 1152 + (((lane & 7) ^ ((row >> 1) & 7)) << 4) - q * 1024; }
      GL4(rX, lds0 + lw * 4096, vX[0], vX[1], vX[2], vX[3], sX + k1(0) * 128);
      SB();
#pragma unroll
      for (int q = 0; q < 5; ++q) {
        const unsigned c16 = (zp0 + q) * 64 + lane, R = (c16 * 3641u) >> 16, c = (c16 - R * 18) * 16, k = (R & 32) + 8 * ((R >> 2) & 3) + 4 * ((R >> 4) & 1) + (R & 3);
        vW[q] = k * 9216 + c - (q & 3) * 1024;
      }
      const unsigned dz = lds0 + 16384 + zp0 * 1024, sw = sW + 4608 + k1(0) * 64 * 9216;
      GL4(rWi, dz, vW[0], vW[1], vW[2], vW[3], sw);
      if (big) GL(rWi, dz + 4096, vW[4], sw, 0);
    }
    const unsigned vL = lane * 16;
    auto rWs = RSRC(Wsp, 2048 * 4096); auto rWo = RSRC(Wo, 2304 * 1152);
    const unsigned e9 = lw * 21 + lane, r9 = (e9 * 7282u) >> 16, v81 = lane < 21 && e9 < 81 ? r9 * 9216 + (e9 - r9 * 9) * 128 : OOB, s81 = (bid >> 2) * 9 * 9216 + (bid & 3) * 1152;
    issue1(1, 1);
    WVS(1, T0W ? 2 : 0); LBAR();
    issue1(1, 2);
    WVS(1, 0);
    for (int k = 1; k < 5; ++k) { LBAR(); issue1(1, k + 2); WVS(1, 0); }
    LBAR(); issue1(1, 7); WVS(1, 0);
    LBAR(); issue1(1, 8); WVS(1, 0);
    LBAR(); issue1(0, 0); WVS(1, 0);
    LBAR(); issue1(0, 1);
    const u64 ep = __hip_atomic_load((const u64*)(ws + WS_EP), RLX_AGENT);
    if (lw == 0) __builtin_amdgcn_raw_ptr_buffer_load_lds(RSRC(bias, 4096), (LAS void*)(size_t)(lds0 + L_BIAS), 4, lane * 4, ti * 256, 0, 0);
    TOUCH(rWs, lane * 128, bid * 32768 + lw * 8192);
    LBAR();
    issue1(0, 2); issue1(0, 3);
    const unsigned tagA = __builtin_amdgcn_readfirstlane((unsigned)ep) + 0x9E3779B9u, tagB = __builtin_amdgcn_readfirstlane((unsigned)(ep >> 32)) + 0x85EBCA6Bu;
    const u64 tag = ((u64)tagB << 32) | tagA;
    if (lw == 0) { const u32x2 tg = {tagA, tagB}; asm volatile("ds_write_b64 %0, %1\n\ts_waitcnt lgkmcnt(0)" :: "v"(lds0 + L_TAG), "v"(tg) : "memory"); }
    unsigned vS[4];
#pragma unroll
    for (int q = 0; q < 4; ++q) { const int row = 8 * q + (lane >> 3); vS[q] = row * 4096 + (((lane & 7) ^ ((row >> 1) & 7)) << 4) - q * 1024; }
    const unsigned sWs = (ti * 128 + lw * 32) * 4096;
    auto kpos = [&](int n) { return n; };
    auto issueW = [&](int n) { GL4(rWs, lds0 + (n & 3) * STG + lw * 4096, vS[0], vS[1], vS[2], vS[3], sWs + kpos(n) * 128); };
    WVS(2, 1);
    LBAR();
    auto rS = RSRC(ws + WS_ST, 16 * 16 * 128 * 16);
    if (lane < 32) {
      const int row = lw * 32 + lane;
      f32x4 v0, v1, v2, v3;
      asm volatile("ds_read_b128 %0, %4\n\tds_read_b128 %1, %4 offset:16\n\tds_read_b128 %2, %4 offset:32\n\tds_read_b128 %3, %4 offset:48\n\ts_waitcnt lgkmcnt(0)"
                   : "=&v"(v0), "=&v"(v1), "=&v"(v2), "=&v"(v3) : "v"(lds0 + L_T + row * 64) : "memory");
      const f32x4 s = (v0 + v1) + (v2 + v3);
      u32x4 gq; gq.x = tagA; gq.y = __float_as_uint(s[0] + s[2]); gq.z = tagB; gq.w = __float_as_uint(s[1] + s[3]);
      __builtin_amdgcn_raw_buffer_store_b128(gq, rS, ((ti * 16 + fj) * 128 + row) * 16, 0, 16);
    }
    u32x4 sv[8];
    const u32x4 srd = {(unsigned)(size_t)(ws + WS_ST), (unsigned)((size_t)(ws + WS_ST) >> 32) & 0xffffu, 16 * 16 * 128 * 16, 0x00020000u};
    auto sweep = [&]() {
      const unsigned s0 = (ti * 16 + lw * 4) * 2048, v1 = vL + 4096;
      asm volatile("buffer_load_dwordx4 %0, %8, %10, %11 offen sc1\n\tbuffer_load_dwordx4 %1, %8, %10, %11 offen offset:1024 sc1\n\tbuffer_load_dwordx4 %2, %8, %10, %11 offen offset:2048 sc1\n\t"
                   "buffer_load_dwordx4 %3, %8, %10, %11 offen offset:3072 sc1\n\tbuffer_load_dwordx4 %4, %9, %10, %11 offen sc1\n\tbuffer_load_dwordx4 %5, %9, %10, %11 offen offset:1024 sc1\n\t"
                   "buffer_load_dwordx4 %6, %9, %10, %11 offen offset:2048 sc1\n\tbuffer_load_dwordx4 %7, %9, %10, %11 offen offset:3072 sc1"
                   : "=&v"(sv[0]), "=&v"(sv[1]), "=&v"(sv[2]), "=&v"(sv[3]), "=&v"(sv[4]), "=&v"(sv[5]), "=&v"(sv[6]), "=&v"(sv[7]) : "v"(vL), "v"(v1), "s"(srd), "s"(s0) : "memory");
    };
    LBAR(); issue1(0, 4); WVS(2, 1);
    LBAR(); issue1(0, 5); WVS(2, 0);
    LBAR(); issue1(0, 6); WVS(2, 0);
    LBAR(); issue1(0, 7); WVS(2, 0);
    LBAR(); sweep(); WVS(1, 8);
    LBAR(); issue1(0, 8);
    WVS(1, 8);
    LBAR();
    {
      f32x4 s;
      auto total = [&](const u32x4 (&v)[8]) {
        unsigned bad = 0;
        s = (f32x4){0.f, 0.f, 0.f, 0.f};
#pragma unroll
        for (int p = 0; p < 8; ++p) { bad |= (v[p].x ^ tagA) | (v[p].z ^ tagB); s[(p & 1) * 2] += __uint_as_float(v[p].y); s[(p & 1) * 2 + 1] += __uint_as_float(v[p].w); }
        return __builtin_amdgcn_ballot_w64(bad != 0) == 0;
      };
      WVS(1, 0);
      asm volatile("" : "+v"(sv[0]), "+v"(sv[1]), "+v"(sv[2]), "+v"(sv[3]), "+v"(sv[4]), "+v"(sv[5]), "+v"(sv[6]), "+v"(sv[7]) :: "memory");
      if (__builtin_expect(!total(sv), 0)) {
#pragma unroll 1
        for (unsigned spins = 0;; ++spins) {
          u32x4 rv[8];
          MEMB();
#pragma unroll
          for (int p = 0; p < 8; ++p) rv[p] = __builtin_amdgcn_raw_buffer_load_b128(rS, vL + (p & 1) * 1024, (ti * 16 + lw * 4 + (p >> 1)) * 2048, 16);
          if (total(rv)) break;
          if (spins > SPIN_CAP) { s[0] = s[2] = __builtin_nanf(""); break; }
        }
      }
      const f32x2 o0 = {s[0], s[1]}, o1 = {s[2], s[3]};
      asm volatile("ds_write_b64 %0, %1\n\tds_write_b64 %0, %2 offset:2048\n\ts_waitcnt lgkmcnt(0)" :: "v"(lds0 + L_R + lane * 32 + lw * 8), "v"(o0), "v"(o1) : "memory");
    }
    LBAR();
    WV(0);
    LBAR();
    issueW(0); issueW(2);
    LBAR();
    issueW(1); issueW(3);
    TOUCH(rWo, lane < 21 ? lane * 128 : OOB, bid * 10368 + lw * 2688);
    auto rZ = RSRC(ws + WS_Z2, FRAG_BYTES);
    const unsigned sZ = fj * 128 * CH + zp0 * 1024;
    u64 ready = 0;
    auto issueZ = [&](int n) {
      const int kp = kpos(n);
      if (__builtin_expect(!got(ready, kp >> 1), 0)) { for (unsigned spins = 0; !got(ready, kp >> 1) && spins < SPIN_CAP; ++spins) ready = poll16(ws + WS_F1 + fj * 16 * 128, tag, lane); }
      MEMB();
      const unsigned dz = lds0 + (n & 3) * STG + 16384 + zp0 * 1024, so = sZ + kp * 18432;
      GS4(rZ, dz, vL, so);
      if (big) GS(rZ, dz + 4096, vL, so + 4096, 0);
    };
    const char* f1 = ws + WS_F1 + fj * 16 * 128;
    for (int q = 0; q < PDELAY; ++q) __builtin_amdgcn_s_sleep(4);
    { unsigned np = 0; for (; ready != 0xff && np < SPIN_CAP; ++np) ready = pollN<2>(f1, tag, lane); LST(0, np); }
    issueZ(0); issueZ(1);
    WVS(1, -4);
    LBAR();
    u64 fv;
    asm volatile("global_load_dwordx2 %0, %1, %2 sc1" : "=&v"(fv) : "v"((lane >> 2) * 128 + (lane & 3) * 8), "s"(f1) : "memory");
    issueZ(2);
    WVS(1, -3);
    LBAR();
    issueZ(3);
    WVS(1, -4);
    asm volatile("" : "+v"(fv));
    ready |= __builtin_amdgcn_ballot_w64(fv == tag);
    LBAR();
    for (int n = 4; n < 31; ++n) {
      issueW(n); issueZ(n);
      WVS(1, 0);
      LBAR();
    }
    issueW(31); issueZ(31);
    constexpr int TAIL = 9;
    unsigned vO[2], vOh;
#pragma unroll
    for (int q = 0; q < 2; ++q) { const int row = 36 * lw + 8 * q + (lane >> 3); vO[q] = row * 1152 + (((lane & 7) ^ (((row >> 1) & 1) | (row & 4))) << 4); }
    { const int row = 36 * lw + 32 + ((lane >> 3) & 3); vOh = row * 1152 + (((lane & 7) ^ (((row >> 1) & 1) | (row & 4))) << 4); }
    const unsigned vO1 = vO[1] - 1024, vO2 = vO[0] + 16 * 1152 - 2048, vO3 = vO[1] + 16 * 1152 - 3072;
    const unsigned sO = min(dd, 8) * 128;
    auto issueO = [&](int n, int slot) {
      const unsigned d = lds0 + L_O + slot * STGO + lw * 4608, s0 = sO + n * 144 * 1152;
      GL4(rWo, d, vO[0], vO1, vO2, vO3, s0);
      if (lane < 32) GL(rWo, d + 4096, vOh, s0, 0);
    };
    issueO(0, 0);
    WVS(1, 5); LBAR();
    issueO(1, 1); issueO(2, 2);
    WV(15); LBAR();
    LBAR();
    WV(0);
    LBAR();
    if (!has3) return;
    issueO(3, 3); issueO(4, 4);
    LBAR();
    issueO(5, 0);
    if (NPRE3 == 5) { issueO(6, 1); issueO(7, 2); }
    auto rP = RSRC(ws + WS_P, FRAG_BYTES);
    const unsigned vPs = lw < 3 ? (lane >> 5) * 16 * CH + (lane & 31) * 16 + 4096 : OOB;
    const int limS = lw == 2 ? 32 : 64;
    const unsigned sP = tt * 5 * 16 * CH;
    const int pa = (tt * 80) >> 7, pb = min((tt * 80 + 79) >> 7, 15);
    u64 rdy = 0;
    auto issueP = [&](int j, int slot) {
      if (__builtin_expect(!got(rdy, j), 0)) { for (unsigned spins = 0; !got(rdy, j) && spins < SPIN_CAP; ++spins) rdy = poll16(ws + WS_F2 + pa * 16 * 128, tag, lane) & poll16(ws + WS_F2 + pb * 16 * 128, tag, lane); }
      MEMB();
      const unsigned d = lds0 + L_P + slot * STGP, sp = sP + j * CH;
      GS4(rP, d + lw * 4096, vL, sp + lw * 16 * CH);
      GS(rP, d + 16384 + lw * 1024, vL, sp + 64 * CH + lw * 1024, 0);
      if (lane < limS) GS(rP, lw < 3 ? d + 20480 + lw * 1024 : lds0 + L_DUMMY, vPs, sp + lw * 32 * CH, 0);
    };
    const char *fa = ws + WS_F2 + pa * 16 * 128, *fb = ws + WS_F2 + pb * 16 * 128;
    { unsigned np = 0; LST(2, 0); for (; rdy != 0xfff && np < SPIN_CAP; ++np) rdy = pollN<3>(fa, tag, lane) & pollN<3>(fb, tag, lane); LST(1, np); }
    u64 fva, fvb;
    asm volatile("global_load_dwordx2 %0, %2, %3 sc1\n\tglobal_load_dwordx2 %1, %2, %4 sc1" : "=&v"(fva), "=&v"(fvb) : "v"((lane >> 2) * 128 + (lane & 3) * 8), "s"(fa), "s"(fb) : "memory");
    issueP(0, 0); issueP(1, 1); issueP(2, 2);
    WV(12);
    asm volatile("" : "+v"(fva), "+v"(fvb));
    rdy |= __builtin_amdgcn_ballot_w64(fva == tag) & __builtin_amdgcn_ballot_w64(fvb == tag);
    LBAR();
    WV(6);
    if (NPRE3 == 5) {
      LBAR(); issueP(3, 0); WV(6);
      LBAR(); issueP(4, 1); WV(6);
      LBAR(); issueP(5, 2); WV(6);
    } else {
      LBAR(); issueP(3, 0); issueO(6, 1); WV(11);
      LBAR(); issueP(4, 1); issueO(7, 2); WV(16);
      LBAR(); issueP(5, 2); WV(11);
    }
    LBAR(); issueP(6, 0); issueO(8, 3); WV(11);
    int sp3 = 1, so5 = 4;
    for (int n = 5; n < 12; ++n) {
      LBAR();
      issueP(n + 2, sp3); issueO(n + 4, so5);
      sp3 = sp3 == 2 ? 0 : sp3 + 1; so5 = so5 == 4 ? 0 : so5 + 1;
      WV(16);
    }
    LBAR(); issueP(14, 2); WV(6);
    LBAR(); issueP(15, 0); WV(6);
    LBAR(); WV(0);
    LBAR();
    LBAR(); LBAR();
    if (bid == 0 && tid == 256) __hip_atomic_store((u64*)(ws + WS_EP), tag, RLX_AGENT);
  } else {
    const int wt = wid >> 1, wf = wid & 1;
    if (wid == 0) {
      auto rC = RSRC((const char*)(pc0 & ~(u64)255), CODE_KB * 1024);
      GL(rC, lds0 + L_DUMMY, lane * 16, rk * 1024, 0); GL(rC, lds0 + L_DUMMY, lane * 16, rk * 1024 + 32768, 0);
      WV(0);
    }
    if (wf == 0) front<0>(lds0, wid, wt, lane, ti, fj, ws); else front<1>(lds0, wid, wt, lane, ti, fj, ws);
    if (!has3) return;
    if (wt == 0) g3_compute<0>(lds0, wf, lane, out, tt, dd); else g3_compute<1>(lds0, wf, lane, out, tt, dd);
  }
}

extern "C" void kernel_launch(void* const* d_in, const int* in_sizes, int n_in, void* d_out, int out_size, void* d_ws, size_t ws_size, hipStream_t stream) {
  (void)hipFuncSetAttribute((const void*)gmlp_fused, hipFuncAttributeMaxDynamicSharedMemorySize, LDS_BYTES);
  gmlp_fused<<<256, 512, LDS_BYTES, stream>>>((const char*)d_in[0], (const char*)d_in[1], (const char*)d_in[2], (const u16*)d_in[3], (const char*)d_in[4], (char*)d_out, (char*)d_ws);
}
```

```cpp
#include <hip/hip_runtime.h>
#include <hip/hip_bf16.h>
typedef unsigned short u16;
typedef unsigned long long u64;
typedef short s16x4 __attribute__((ext_vector_type(4)));
typedef short bf16x8 __attribute__((ext_vector_type(8)));
typedef float f32x2 __attribute__((ext_vector_type(2)));
typedef float f32x4 __attribute__((ext_vector_type(4)));
typedef unsigned u32x4 __attribute__((ext_vector_type(4)));
typedef unsigned u32x2 __attribute__((ext_vector_type(2)));
#define LAS __attribute__((address_space(3)))
#define GAS __attribute__((address_space(1)))
#define G64(p) ((GAS u64*)(u64)(p))
#define DEV __device__ __forceinline__
#define BAR() __builtin_amdgcn_s_barrier()
#define SB() __builtin_amdgcn_sched_barrier(0)
#define MEMB() asm volatile("" ::: "memory")
#define WAITL0() __builtin_amdgcn_s_waitcnt(0xC07F)
#define WL0() asm volatile("s_waitcnt lgkmcnt(0)" ::: "memory")
#define WV(N) asm volatile("s_waitcnt vmcnt(%0)" ::"i"(N) : "memory")
#define MFMA(a, b, c) __builtin_amdgcn_mfma_f32_16x16x32_bf16(a, b, c, 0, 0, 0)
#define MFMA16(a, b, c) __builtin_amdgcn_mfma_f32_16x16x16bf16_1k(a, b, c, 0, 0, 0)
#define GL(R, D, V, S, I) __builtin_amdgcn_raw_ptr_buffer_load_lds(R, (LAS void*)(size_t)(D), 16, V, S, I, 0)
#define GL4(R, D, V0, V1, V2, V3, S) do { GL(R, D, V0, S, 0); GL(R, D, V1, S, 1024); GL(R, D, V2, S, 2048); GL(R, D, V3, S, 3072); } while (0)
#define GS(R, D, V, S, I) __builtin_amdgcn_raw_ptr_buffer_load_lds(R, (LAS void*)(size_t)(D), 16, V, S, I, 16)
#define GS4(R, D, V, S) do { GS(R, D, V, S, 0); GS(R, D, V, S, 1024); GS(R, D, V, S, 2048); GS(R, D, V, S, 3072); } while (0)
#define TOUCH(R, V, S) __builtin_amdgcn_raw_ptr_buffer_load_lds(R, (LAS void*)(size_t)(lds0 + L_DUMMY), 4, V, S, 0, 0)
#define RSRC(p, n) __builtin_amdgcn_make_buffer_rsrc((void*)(p), 0, (n), 0x00020000)
#define RLX_AGENT __ATOMIC_RELAXED, __HIP_MEMORY_SCOPE_AGENT
#define ROT1 2
#define NPRE3 3
#define Z1W 0
#define LS0 1
#define OAUX 16
#define WAUX 0
#define T0W 0
#define PDELAY 7
#define PSLEEP 0
#define LA2 2
#define CODE_KB 52

constexpr int CH = 4608;
constexpr int FRAG_BYTES = 16 * 128 * CH;
constexpr size_t WS_Z2 = 0, WS_P = FRAG_BYTES, WS_ST = 2 * (size_t)FRAG_BYTES, WS_F1 = WS_ST + 16 * 16 * 128 * 16, WS_F2 = WS_F1 + 256 * 128, WS_EP = WS_F2 + 256 * 128;
constexpr int STG = 34816, STGP = 23040, STGO = 18432, L_O = 0, L_P = 5 * STGO, LDS_BYTES = 163840;
constexpr int L_T = 139264, L_R = L_T + 8192, L_X = 151552, L_BIAS = 159744, L_TAG = L_BIAS + 256, L_DUMMY = 161280;
constexpr unsigned SPIN_CAP = 1u << 16, OOB = 0x80000000u;
#define LBAR() BAR()
#define CST(I)
#define LST(I, X)

typedef __bf16 bf16x2_t __attribute__((ext_vector_type(2)));
DEV unsigned cvt_pk(float lo, float hi) { return __builtin_bit_cast(unsigned, __builtin_convertvector((f32x2){lo, hi}, bf16x2_t)); }
DEV bf16x8 ld128(unsigned a) { return *(LAS bf16x8*)(size_t)a; }
DEV s16x4 ld64(unsigned a) { return *(LAS s16x4*)(size_t)a; }
DEV s16x4 ldtr(unsigned a) { return __builtin_amdgcn_ds_read_tr16_b64_v4i16((LAS s16x4*)(size_t)a); }
DEV bf16x8 cat(s16x4 a, s16x4 b) { return __builtin_shufflevector(a, b, 0, 1, 2, 3, 4, 5, 6, 7); }
DEV float bflo(unsigned v) { return __uint_as_float(v << 16); }
DEV float bfhi(unsigned v) { return __uint_as_float(v & 0xffff0000u); }
template <int NB, bool SUM>
DEV void gelu_blocks(const f32x4* const (&v)[NB], u32x2* const (&o)[NB], f32x2& s1, f32x2& s2) {
  const float k1 = -2.0f * 0.7978845608028654f * 1.4426950408889634f, k2 = k1 * 0.044715f;
  f32x2 x[2 * NB], t[2 * NB];
#pragma unroll
  for (int i = 0; i < 2 * NB; ++i) { x[i] = (f32x2){(*v[i >> 1])[(i & 1) * 2], (*v[i >> 1])[(i & 1) * 2 + 1]}; t[i] = x[i] * x[i]; }
  SB();
#pragma unroll
  for (int i = 0; i < 2 * NB; ++i) t[i] = t[i] * k2 + k1;
  SB();
#pragma unroll
  for (int i = 0; i < 2 * NB; ++i) t[i] = t[i] * x[i];
  SB();
#pragma unroll
  for (int i = 0; i < 2 * NB; ++i) { t[i].x = __builtin_amdgcn_exp2f(t[i].x); t[i].y = __builtin_amdgcn_exp2f(t[i].y); }
  SB();
#pragma unroll
  for (int i = 0; i < 2 * NB; ++i) t[i] = t[i] + 1.0f;
  SB();
#pragma unroll
  for (int i = 0; i < 2 * NB; ++i) { t[i].x = __builtin_amdgcn_rcpf(t[i].x); t[i].y = __builtin_amdgcn_rcpf(t[i].y); }
  SB();
#pragma unroll
  for (int i = 0; i < 2 * NB; ++i) t[i] = t[i] * x[i];
  SB();
  f32x2 a1 = s1, a2 = s2, b1 = {0.f, 0.f}, b2 = {0.f, 0.f};
#pragma unroll
  for (int i = 0; i < NB; ++i) {
    o[i]->x = cvt_pk(t[2 * i].x, t[2 * i].y);
    if (SUM) { a1 += t[2 * i]; a2 += t[2 * i] * t[2 * i]; }
    o[i]->y = cvt_pk(t[2 * i + 1].x, t[2 * i + 1].y);
    if (SUM) { if (i == 0) { b1 = t[1]; b2 = t[1] * t[1]; } else { b1 += t[2 * i + 1]; b2 += t[2 * i + 1] * t[2 * i + 1]; } }
  }
  if (SUM) { s1 = a1 + b1; s2 = a2 + b2; }
  SB();
}
template <int WF, bool SUM>
DEV void gelu_row(int m, const f32x4 (&acc)[4][5], u32x2 (&oa)[4][4], u32x2 (&ob)[2], f32x2& s1, f32x2& s2) {
  if ((m >> 1) == WF) { const f32x4* const v[5] = {&acc[m][0], &acc[m][1], &acc[m][2], &acc[m][3], &acc[m][4]}; u32x2* const o[5] = {&oa[m][0], &oa[m][1], &oa[m][2], &oa[m][3], &ob[m & 1]}; gelu_blocks<5, SUM>(v, o, s1, s2); }
  else { const f32x4* const v[4] = {&acc[m][0], &acc[m][1], &acc[m][2], &acc[m][3]}; u32x2* const o[4] = {&oa[m][0], &oa[m][1], &oa[m][2], &oa[m][3]}; gelu_blocks<4, SUM>(v, o, s1, s2); }
}

struct TAddr { unsigned w[2], z[2][2], s[2]; };
struct TFrag { bf16x8 wX[4], wY[4]; s16x4 tX[5][2], tY[4][2]; };
template <int WF, int PS, int OFF, bool FIRST, bool SYNC = true>
DEV void t_step(TAddr& A, int dlt, f32x4 (&acc)[4][5], TFrag& F, const f32x4* init = nullptr) {
  constexpr int KX = WF, KY = 1 - WF;
  SB(); if (SYNC) BAR(); MEMB(); SB();
  auto rdz = [&](s16x4 (&t)[2], int n, int u, int K) { t[u] = n < 4 ? ldtr(A.z[u][n & 1] + OFF + K * 9216 + (n >> 1) * PS) : ldtr(A.s[u] + OFF + K * 9216); };
#pragma unroll
  for (int i = 0; i < 16; ++i) {
    const int m = i >> 2, n = i & 3;
    if (!FIRST) acc[m][n] = MFMA(cat(F.tY[n][0], F.tY[n][1]), F.wY[m], acc[m][n]);
    SB();
    if (i == 0) F.wX[0] = ld128(A.w[KX] + OFF);
    else if (i < 11) rdz(F.tX[(i - 1) >> 1], (i - 1) >> 1, (i - 1) & 1, KX);
    else if (i < 14) F.wX[i - 10] = ld128(A.w[KX] + OFF + (i - 10) * 2048);
    SB();
  }
#pragma unroll
  for (int i = 0; i < 20; ++i) {
    const int m = i / 5, n = i % 5;
    const f32x4 zero4 = {0, 0, 0, 0};
    acc[m][n] = MFMA(cat(F.tX[n][0], F.tX[n][1]), F.wX[m], !FIRST ? acc[m][n] : (PS == 1024 && (n < 4 || (m >> 1) == WF)) ? init[m] : zero4);
    SB();
    if (i == 0) F.wY[0] = ld128(A.w[KY] + OFF);
    else if (i < 9) rdz(F.tY[(i - 1) >> 1], (i - 1) >> 1, (i - 1) & 1, KY);
    else if (i < 12) F.wY[i - 8] = ld128(A.w[KY] + OFF + (i - 8) * 2048);
    else if (OFF != 0 && i < 16) {
      const int u = i & 1;
      if (i < 14) { A.w[u] += dlt; A.s[u] += dlt; asm volatile("" : "+v"(A.w[u]), "+v"(A.s[u])); }
      else { A.z[u][0] += dlt; A.z[u][1] += dlt; asm volatile("" : "+v"(A.z[u][0]), "+v"(A.z[u][1])); }
    }
    SB();
  }
  WAITL0();
}
template <int WF>
DEV void t_finish(unsigned lds0, int wt, int lane, f32x4 (&acc)[4][5], TFrag& F) {
#pragma unroll
  for (int h = 0; h < 2; ++h) *(LAS f32x4*)(size_t)(lds0 + L_X + ((wt * 2 + WF) * 2 + h) * 1024 + lane * 16) = acc[2 * (1 - WF) + h][4];
  SB();
#pragma unroll
  for (int m = 0; m < 4; ++m)
#pragma unroll
    for (int n = 0; n < 4; ++n) acc[m][n] = MFMA(cat(F.tY[n][0], F.tY[n][1]), F.wY[m], acc[m][n]);
  SB();
  WL0();
  BAR(); MEMB();
#pragma unroll
  for (int h = 0; h < 2; ++h) acc[2 * WF + h][4] += *(LAS f32x4*)(size_t)(lds0 + L_X + ((wt * 2 + 1 - WF) * 2 + h) * 1024 + lane * 16);
}
template <int WF, bool MID, class Mid>
DEV void in_pass(unsigned lds0, int wt, int lane, TAddr& A, f32x4 (&acc)[4][5], Mid mid) {
  TFrag F;
  int dlt = 2 * STG;
  WAITL0();
  if (!MID) {
    t_step<WF, 64, 0, true>(A, dlt, acc, F);
    for (int k = 0; k < 4; ++k) {
      t_step<WF, 64, STG, false>(A, dlt, acc, F);
      dlt = -dlt;
      t_step<WF, 64, 0, false>(A, dlt, acc, F);
    }
  } else {
    t_step<WF, 64, STG, true>(A, dlt, acc, F);
    for (int k = 0; k < 3; ++k) {
      dlt = -dlt;
      t_step<WF, 64, 0, false>(A, dlt, acc, F);
      t_step<WF, 64, STG, false>(A, dlt, acc, F);
    }
    t_step<WF, 64, 0, false>(A, 0, acc, F);
    SB(); BAR(); MEMB(); mid(); SB();
    t_step<WF, 64, STG, false>(A, 0, acc, F);
  }
  t_finish<WF>(lds0, wt, lane, acc, F);
}

struct G3P { bf16x8 p[2][5]; s16x4 ps[3]; };
struct G3W { s16x4 w[2][2][2], ws[2]; };
template <int KH>
DEV void g3_readw(G3W& n, unsigned a0, unsigned a1) {
#pragma unroll
  for (int q = 0; q < 8; ++q) n.w[q >> 2][(q >> 1) & 1][q & 1] = ldtr((((q >> 1) & 1) ? a1 : a0) + (2 * KH + (q >> 2)) * 4096 + (q & 1) * 2048);
  n.ws[0] = ldtr(a0 + 16384); n.ws[1] = ldtr(a1 + 16384);
}
template <int KH, bool MM, bool RDW>
DEV void g3_stage(f32x4 (&acc)[5][2], const G3P& cp, const G3W& cw, G3P& np, G3W& nw, unsigned aP, unsigned aS, unsigned a0, unsigned a1) {
  constexpr int M0 = KH ? 3 : 0, NS = KH ? 2 : 3, NM = 20 + 2 * NS, NR = 20 + NS;
  SB(); BAR(); MEMB(); SB();
#pragma unroll
  for (int i = 0; i < NM; ++i) {
    if (MM) {
      if (i >= 10 && i < 10 + 2 * NS) acc[M0 + ((i - 10) >> 1)][i & 1] = MFMA16(cw.ws[i & 1], cp.ps[(i - 10) >> 1], acc[M0 + ((i - 10) >> 1)][i & 1]);
      else { const int ii = i < 10 ? i : i - 2 * NS, P = ii / 10, mm = (ii % 10) >> 1, m = (KH != P) ? (mm + 3) % 5 : mm, cc = ii & 1; acc[m][cc] = MFMA(cat(cw.w[P][cc][0], cw.w[P][cc][1]), cp.p[P][m], acc[m][cc]); }
    }
    SB();
#pragma unroll
    for (int r = (i * 3) / 2; r < ((i + 1) * 3) / 2; ++r) {
      if (r < 18) { const int P = r / 9, q = r % 9;
        if (q < 4) { if (RDW) nw.w[P][q >> 1][q & 1] = ldtr(((q >> 1) ? a1 : a0) + (2 * KH + P) * 4096 + (q & 1) * 2048); }
        else np.p[P][q - 4] = ld128(aP + P * 1024 + (q - 4) * 4096); }
      else if (r < 20) { if (RDW) nw.ws[r - 18] = ldtr(((r - 18) ? a1 : a0) + 16384); }
      else if (r < NR) np.ps[r - 20] = ld64(aS + (M0 + r - 20) * 512);
    }
    SB();
  }
  WAITL0();
}
template <int KH>
DEV void g3_compute(unsigned lds0, int dh, int lane, char* out, int tt, int dd) {
  const int g = lane >> 4, qr = (lane >> 2) & 3, p = lane & 3;
  const int rr = 4 * g + qr;
  unsigned bW[2];
#pragma unroll
  for (int c = 0; c < 2; ++c) bW[c] = lds0 + L_O + rr * 128 + (((4 * dh + p) ^ (((rr >> 1) & 1) | (rr & 4))) << 4) + ((c ^ (p & 1)) * 8);
  const unsigned bP = lds0 + L_P + lane * 16 + KH * 2048, bS = lds0 + L_P + 20480 + lane * 8;
  G3W a0, a1, a2;
  g3_readw<KH>(a0, bW[0], bW[1]); g3_readw<KH>(a1, bW[0] + STGO, bW[1] + STGO); g3_readw<KH>(a2, bW[0] + 2 * STGO, bW[1] + 2 * STGO);
  f32x4 acc[5][2];
#pragma unroll
  for (int m = 0; m < 5; ++m) { acc[m][0] = (f32x4){0, 0, 0, 0}; acc[m][1] = (f32x4){0, 0, 0, 0}; }
  WAITL0();
  SB(); BAR(); SB();
  G3P p0, p1; G3W w0, w1;
  g3_stage<KH, false, false>(acc, p1, a0, p0, w0, bP, bS, 0, 0);
  g3_stage<KH, true, false>(acc, p0, a0, p1, w0, bP + STGP, bS + STGP, 0, 0);
  g3_stage<KH, true, false>(acc, p1, a1, p0, w0, bP + 2 * STGP, bS + 2 * STGP, 0, 0);
  g3_stage<KH, true, true>(acc, p0, a2, p1, w1, bP, bS, bW[0] + 3 * STGO, bW[1] + 3 * STGO);
  unsigned sp = STGP, sw = 4 * STGO;
  for (int j = 4; j < 16; j += 2) {
    g3_stage<KH, true, true>(acc, p1, w1, p0, w0, bP + sp, bS + sp, bW[0] + sw, bW[1] + sw);
    sp = sp == 2 * STGP ? 0 : sp + STGP; sw = sw == 4 * STGO ? 0 : sw + STGO;
    g3_stage<KH, true, true>(acc, p0, w0, p1, w1, bP + sp, bS + sp, bW[0] + sw, bW[1] + sw);
    sp = sp == 2 * STGP ? 0 : sp + STGP; sw = sw == 4 * STGO ? 0 : sw + STGO;
  }
  {
    constexpr int M0 = KH ? 3 : 0, NS = KH ? 2 : 3;
    SB(); BAR(); SB();
#pragma unroll
    for (int i = 0; i < 2 * NS; ++i) { acc[M0 + (i >> 1)][i & 1] = MFMA16(w1.ws[i & 1], p1.ps[i >> 1], acc[M0 + (i >> 1)][i & 1]); SB(); }
#pragma unroll
    for (int i = 0; i < 20; ++i) { const int P = (i >> 1) & 1, m = KH ? i >> 2 : 4 - (i >> 2), cc = i & 1; acc[m][cc] = MFMA(cat(w1.w[P][cc][0], w1.w[P][cc][1]), p1.p[P][m], acc[m][cc]); SB(); }
  }
#pragma unroll
  for (int m = 0; m < 5; ++m)
    if ((m >= 3) != KH) { *(LAS f32x4*)(size_t)(lds0 + (dh * 10 + m * 2) * 1024 + lane * 16) = acc[m][0]; *(LAS f32x4*)(size_t)(lds0 + (dh * 10 + m * 2 + 1) * 1024 + lane * 16) = acc[m][1]; }
  WL0();
  BAR(); MEMB();
  auto rO = RSRC(out, 2048 * 1152);
  const bool sw2 = g & 1;
#pragma unroll
  for (int m = 0; m < 5; ++m)
    if ((m >= 3) == KH) {
      const f32x4 a = acc[m][0] + *(LAS f32x4*)(size_t)(lds0 + (dh * 10 + m * 2) * 1024 + lane * 16), b = acc[m][1] + *(LAS f32x4*)(size_t)(lds0 + (dh * 10 + m * 2 + 1) * 1024 + lane * 16);
      const unsigned a0 = cvt_pk(a[0], a[1]), a1 = cvt_pk(a[2], a[3]), b0 = cvt_pk(b[0], b[1]), b1 = cvt_pk(b[2], b[3]);
      const u32x4 v = {sw2 ? b0 : a0, sw2 ? b1 : a1, sw2 ? a0 : b0, sw2 ? a1 : b1};
      __builtin_amdgcn_raw_buffer_store_b128(v, rO, (tt * 80 + m * 16 + (lane & 15)) * 1152 + dd * 128 + dh * 64 + g * 16, 0, OAUX);
    }
}

template <int WF>
DEV void front(unsigned lds0, int wid, int wt, int lane, int ti, int fj, char* ws) {
  const int r16 = lane & 15, g = lane >> 4, qr = (lane >> 2) & 3, p = lane & 3;
  TAddr A;
  A.w[0] = lds0 + (64 * wt + r16) * 128 + ((g ^ (r16 >> 1)) << 4); A.w[1] = A.w[0] ^ 64;
#pragma unroll
  for (int u = 0; u < 2; ++u) {
    const unsigned b = lds0 + 16384 + (16 * u + 4 * g + qr) * 288 + p * 8;
    A.z[u][0] = b + WF * 128 + (p & 1) * 32; A.z[u][1] = b + WF * 128 + 32 - (p & 1) * 32; A.s[u] = b + 256;
  }
  asm volatile("" : "+v"(A.w[0]), "+v"(A.w[1]), "+v"(A.s[0]), "+v"(A.s[1]), "+v"(A.z[0][0]), "+v"(A.z[0][1]), "+v"(A.z[1][0]), "+v"(A.z[1][1]));
  f32x4 acc[4][5];
  in_pass<WF, false>(lds0, wt, lane, A, acc, []() {});
  CST(0);
  u32x2 ua[4][4], ub[2];
  {
    f32x2 s1[4], s2[4];
#pragma unroll
    for (int mm = 0; mm < 4; ++mm) {
      const int m = mm ^ (2 * (1 - WF));
      s1[m] = (f32x2){0.f, 0.f}; s2[m] = (f32x2){0.f, 0.f};
      gelu_row<WF, true>(m, acc, ua, ub, s1[m], s2[m]);
    }
#pragma unroll
    for (int m = 0; m < 4; ++m) *(LAS f32x2*)(size_t)(lds0 + L_T + (64 * wt + 16 * m + r16) * 64 + (WF * 4 + g) * 8) = (f32x2){s1[m].x + s1[m].y, s2[m].x + s2[m].y};
  }
#pragma unroll
  for (int m = 0; m < 4; ++m) asm volatile("" : "+v"(ua[m][0]), "+v"(ua[m][1]), "+v"(ua[m][2]), "+v"(ua[m][3]));
  asm volatile("" : "+v"(ub[0]), "+v"(ub[1]));
#pragma unroll
  for (int u = 0; u < 2; ++u) { A.z[u][0] -= (p & 1) * 32; A.z[u][1] += (p & 1) * 32; }
  asm volatile("" : "+v"(A.z[0][0]), "+v"(A.z[0][1]), "+v"(A.z[1][0]), "+v"(A.z[1][1]));
  CST(1);
  in_pass<WF, true>(lds0, wt, lane, A, acc, [&]() {
    CST(2);
#pragma unroll
    for (int m = 0; m < 4; ++m) asm volatile("" : "+v"(ua[m][0]), "+v"(ua[m][1]), "+v"(ua[m][2]), "+v"(ua[m][3]));
    asm volatile("" : "+v"(ub[0]), "+v"(ub[1]));
    int lz = lane; asm volatile("" : "+v"(lz));
    auto rZ = RSRC(ws + WS_Z2, FRAG_BYTES);
    const unsigned sl = (lz >> 4) * 16 + ((lz & 15) ^ (4 * (lz >> 5))), dst = (fj * 128 + ti * 8 + wt * 4) * CH;
#pragma unroll
    for (int m = 0; m < 4; ++m) {
      const unsigned ar = lds0 + L_R + (64 * wt + 16 * m + (lz & 15)) * 32;
      const f32x4 q4 = *(LAS f32x4*)(size_t)ar + *(LAS f32x4*)(size_t)(ar + 16);
      const float mean = (q4[0] + q4[2]) * (1.0f / 2304.0f), var = fmaxf((q4[1] + q4[3]) * (1.0f / 2304.0f) - mean * mean, 0.f);
      const float rs = __builtin_amdgcn_rsqf(var + 1e-5f), sh = -mean * rs;
      auto norm = [&](unsigned v) { const f32x2 y = (f32x2){bflo(v), bfhi(v)} * rs + sh; return cvt_pk(y.x, y.y); };
#pragma unroll
      for (int pp = 0; pp < 2; ++pp)
        __builtin_amdgcn_raw_buffer_store_b128((u32x4){norm(ua[m][2 * pp].x), norm(ua[m][2 * pp].y), norm(ua[m][2 * pp + 1].x), norm(ua[m][2 * pp + 1].y)}, rZ, dst + m * CH + sl * 16 + WF * 2048 + pp * 1024, 0, 16);
      if ((m >> 1) == WF) __builtin_amdgcn_raw_buffer_store_b64((u32x2){norm(ub[m & 1].x), norm(ub[m & 1].y)}, rZ, dst + m * CH + sl * 8 + 4096, 0, 16);
    }
    CST(3);
  });
  CST(4);
#pragma unroll
  for (int m = 0; m < 4; ++m) asm volatile("" : "+v"(acc[m][0]), "+v"(acc[m][1]), "+v"(acc[m][2]), "+v"(acc[m][3]), "+v"(acc[m][4]));
  const u32x2 tg = *(LAS u32x2*)(size_t)(lds0 + L_TAG);
  const u64 tag = ((u64)(unsigned)__builtin_amdgcn_readfirstlane(tg.y) << 32) | (unsigned)__builtin_amdgcn_readfirstlane(tg.x);
  WV(0);
  if (lane == 0) __hip_atomic_store(G64(ws + WS_F1 + (fj * 16 + ti) * 128) + wid, tag, RLX_AGENT);
  CST(5);
  SB();
  u32x2 za[4][4], zb[2];
#pragma unroll
  for (int m = 0; m < 4; ++m) {
    f32x2 d1, d2;
    gelu_row<WF, false>(m, acc, za, zb, d1, d2);
    asm volatile("" : "+v"(za[m][0]), "+v"(za[m][1]), "+v"(za[m][2]), "+v"(za[m][3]));
  }
  asm volatile("" : "+v"(zb[0]), "+v"(zb[1]));
  CST(6);
#pragma unroll
  for (int u = 0; u < 2; ++u) {
    const unsigned sl = p * 16 + 8 * (g & 1) + 4 * (u ^ (p >> 1)) + qr;
#pragma unroll
    for (int h = 0; h < 2; ++h) A.z[u][h] = lds0 + 16384 + (g >> 1) * CH + WF * 2048 + sl * 16 + ((h ^ (p & 1)) * 8);
    A.s[u] = lds0 + 16384 + (g >> 1) * CH + 4096 + sl * 8;
  }
  asm volatile("" : "+v"(A.s[0]), "+v"(A.s[1]), "+v"(A.z[0][0]), "+v"(A.z[0][1]), "+v"(A.z[1][0]), "+v"(A.z[1][1]));
  f32x4 bias4[4];
#pragma unroll
  for (int m = 0; m < 4; ++m) { const float b = __uint_as_float((unsigned)*(LAS u16*)(size_t)(lds0 + L_BIAS + (wt * 64 + m * 16 + r16) * 2) << 16); bias4[m] = (f32x4){b, b, b, b}; }
  TFrag F;
  int dlt = 2 * STG;
  WAITL0();
  CST(7);
  t_step<WF, 1024, 0, true, true>(A, dlt, acc, F, bias4);
  CST(8);
  t_step<WF, 1024, STG, false, true>(A, dlt, acc, F);

  for (int k = 0; k < 15; ++k) {
    dlt = -dlt;
    t_step<WF, 1024, 0, false>(A, dlt, acc, F);
    t_step<WF, 1024, STG, false>(A, dlt, acc, F);
  }
  auto rP = RSRC(ws + WS_P, FRAG_BYTES);
  auto gate = [&](f32x4 a, u32x2 z) {
    const f32x2 x = (f32x2){a[0], a[1]} * (f32x2){bflo(z.x), bfhi(z.x)}, y = (f32x2){a[2], a[3]} * (f32x2){bflo(z.y), bfhi(z.y)};
    return (u32x2){cvt_pk(x.x, x.y), cvt_pk(y.x, y.y)};
  };
#pragma unroll
  for (int h = 0; h < 2; ++h) *(LAS f32x4*)(size_t)(lds0 + L_X + ((wt * 2 + WF) * 2 + h) * 1024 + lane * 16) = acc[2 * (1 - WF) + h][4];
  const unsigned dst0 = (ti * 8 + wt * 4) * 16 * CH + fj * CH;
  CST(9);
#pragma unroll
  for (int m = 0; m < 4; ++m)
#pragma unroll
    for (int n = 0; n < 4; ++n) acc[m][n] = MFMA(cat(F.tY[n][0], F.tY[n][1]), F.wY[m], acc[m][n]);
#pragma unroll
  for (int m = 0; m < 4; ++m)
#pragma unroll
    for (int pp = 0; pp < 2; ++pp) {
      const u32x2 a = gate(acc[m][2 * pp], za[m][2 * pp]), b = gate(acc[m][2 * pp + 1], za[m][2 * pp + 1]);
      __builtin_amdgcn_raw_buffer_store_b128((u32x4){a.x, a.y, b.x, b.y}, rP, dst0 + m * 16 * CH + lane * 16 + (2 * WF + pp) * 1024, 0, 16);
    }
  SB();
  WL0();
  BAR(); MEMB();
#pragma unroll
  for (int h = 0; h < 2; ++h) {
    const f32x4 o8 = *(LAS f32x4*)(size_t)(lds0 + L_X + ((wt * 2 + 1 - WF) * 2 + h) * 1024 + lane * 16);
    __builtin_amdgcn_raw_buffer_store_b64(gate(acc[2 * WF + h][4] + o8, zb[h]), rP, dst0 + (2 * WF + h) * 16 * CH + lane * 8 + 4096, 0, 16);
  }
  CST(10);
  WV(0);
  CST(11);
  if (lane == 0) __hip_atomic_store(G64(ws + WS_F2 + (ti * 16 + fj) * 128) + wid, tag, RLX_AGENT);
  SB(); BAR(); MEMB(); SB();
}

DEV u64 poll16(const char* base, u64 tag, int lane) {
  const u64 v = __hip_atomic_load(G64(base) + (lane >> 2) * 16 + (lane & 3), RLX_AGENT);
  return __builtin_amdgcn_ballot_w64(v == tag);
}
template <int NP> DEV u64 pollN(const char* base, u64 tag, int lane) {
  const u64 v = __hip_atomic_load(G64(base) + min(lane >> 2, NP - 1) * 16 + (lane & 3), RLX_AGENT);
  return __builtin_amdgcn_ballot_w64(v == tag) & ((1ull << (4 * NP)) - 1);
}
DEV bool got(u64 mask, int who) { return ((unsigned)(mask >> (4 * who)) & 15u) == 15u; }

__global__ __launch_bounds__(512, 2) void gmlp_fused(const void* a0, const void* a1, const void* a2, const void* a3, const void* a4, void* a5, void* a6, void* a7) {
  extern __shared__ __attribute__((aligned(16))) char smem[];
  typedef unsigned u32x16 __attribute__((ext_vector_type(16)));
  u32x16 ka; u64 pc0;
  asm volatile("s_load_dwordx16 %0, %2, 0x0\n\ts_getpc_b64 %1" : "=&s"(ka), "=&s"(pc0) : "s"(__builtin_amdgcn_kernarg_segment_ptr()));
  const int tid = threadIdx.x, wid = __builtin_amdgcn_readfirstlane(tid >> 6), lane = tid & 63;
  const int bid = blockIdx.x, xg = bid & 7, rk = bid >> 3, il = rk & 7, jl = rk >> 3, ti = (xg & 1) * 8 + il, fj = (xg >> 1) * 4 + jl;
  const unsigned lds0 = (unsigned)(size_t)(LAS char*)smem;
  unsigned vX[4];
#pragma unroll
  for (int q = 0; q < 4; ++q) { const int row = 8 * q + (lane >> 3); vX[q] = row * 1152 + (((lane & 7) ^ ((row >> 1) & 7)) << 4) - q * 1024; }
  asm volatile("s_waitcnt lgkmcnt(0)" : "+s"(ka), "+v"(vX[0]), "+v"(vX[1]), "+v"(vX[2]), "+v"(vX[3]));
  auto arg = [&](int n) { return (char*)(((u64)ka[2 * n + 1] << 32) | ka[2 * n]); };
  const char *X = arg(0), *Win = arg(1), *Wsp = arg(2), *bias = arg(3), *Wo = arg(4);
  char *out = arg(5), *ws = arg(6);
  auto tile3 = [&](int& tt, int& dd) { int b = bid; asm volatile("" : "+s"(b)); const int L3 = (b & 7) * 30 + (b >> 3); tt = L3 / 9; dd = L3 - tt * 9; return (b >> 3) < 30 && L3 < 234; };
  if (__builtin_expect(wid >= 4, 1)) {
    __builtin_amdgcn_s_setprio(3);
    const int lw = wid - 4;
    const bool big = wid < 6;
#define WVS(STAGES, EXTRA) do { if (big) WV(9 * (STAGES) + (EXTRA)); else WV(8 * (STAGES) + (EXTRA)); } while (0)
    auto rX = RSRC(X, 2048 * 1152); auto rWi = RSRC(Win, 576 * 9216);
    unsigned vW[5];
    const unsigned zp0 = lw < 2 ? lw * 5 : 2 + lw * 4;
    const unsigned sX = (ti * 128 + lw * 32) * 1152, sW = fj * 288;
    const int rot = ROT1 == 0 ? 0 : ROT1 == 1 ? il + 2 * jl : 3 * ((il + jl) % 3);
    const int rot8 = ROT1 == 1 ? (il + 2 * jl) & 7 : ROT1 == 2 ? (((il + jl) % 3) * 8) / 3 : 0;
    auto k1 = [&](int k) { return k == 0 ? 0 : 1 + ((k - 1 + rot8) & 7); };
    auto issue1 = [&](int half, int k) {
      const int kk = k1(k);
      const unsigned d = lds0 + ((k + 1 - half) & 3) * STG, dz = d + 16384 + zp0 * 1024, sw = sW + half * 4608 + kk * 64 * 9216;
      GL4(rX, d + lw * 4096, vX[0], vX[1], vX[2], vX[3], sX + kk * 128);
      GL4(rWi, dz, vW[0], vW[1], vW[2], vW[3], sw);
      if (big) GL(rWi, dz + 4096, vW[4], sw, 0);
    };
    {
      GL4(rX, lds0 + lw * 4096, vX[0], vX[1], vX[2], vX[3], sX + k1(0) * 128);
      SB();
#pragma unroll
      for (int q = 0; q < 5; ++q) {
        const unsigned c16 = (zp0 + q) * 64 + lane, R = (c16 * 3641u) >> 16, c = (c16 - R * 18) * 16, k = (R & 32) + 8 * ((R >> 2) & 3) + 4 * ((R >> 4) & 1) + (R & 3);
        vW[q] = k * 9216 + c - (q & 3) * 1024;
      }
      const unsigned dz = lds0 + 16384 + zp0 * 1024, sw = sW + 4608 + k1(0) * 64 * 9216;
      GL4(rWi, dz, vW[0], vW[1], vW[2], vW[3], sw);
      if (big) GL(rWi, dz + 4096, vW[4], sw, 0);
    }
    const unsigned vL = lane * 16;
    auto rWs = RSRC(Wsp, 2048 * 4096); auto rWo = RSRC(Wo, 2304 * 1152);
    const unsigned e9 = lw * 21 + lane, r9 = (e9 * 7282u) >> 16, v81 = lane < 21 && e9 < 81 ? r9 * 9216 + (e9 - r9 * 9) * 128 : OOB, s81 = (bid >> 2) * 9 * 9216 + (bid & 3) * 1152;
    issue1(1, 1);
    WVS(1, T0W ? 2 : 0); LBAR();
    issue1(1, 2);
    WVS(1, 0);
    for (int k = 1; k < 5; ++k) { LBAR(); issue1(1, k + 2); WVS(1, 0); }
    LBAR(); issue1(1, 7); WVS(1, 0);
    LBAR(); issue1(1, 8); WVS(1, 0);
    LBAR(); issue1(0, 0); WVS(1, 0);
    LBAR(); issue1(0, 1);
    const u64 ep = __hip_atomic_load(G64(ws + WS_EP), RLX_AGENT);
    if (lw == 0) __builtin_amdgcn_raw_ptr_buffer_load_lds(RSRC(bias, 4096), (LAS void*)(size_t)(lds0 + L_BIAS), 4, lane * 4, ti * 256, 0, 0);
    TOUCH(rWs, lane * 128, bid * 32768 + lw * 8192);
    LBAR();
    issue1(0, 2); issue1(0, 3);
    const unsigned tagA = __builtin_amdgcn_readfirstlane((unsigned)ep) + 0x9E3779B9u, tagB = __builtin_amdgcn_readfirstlane((unsigned)(ep >> 32)) + 0x85EBCA6Bu;
    const u64 tag = ((u64)tagB << 32) | tagA;
    if (lw == 0) { const u32x2 tg = {tagA, tagB}; asm volatile("ds_write_b64 %0, %1\n\ts_waitcnt lgkmcnt(0)" :: "v"(lds0 + L_TAG), "v"(tg) : "memory"); }
    unsigned vS[4];
#pragma unroll
    for (int q = 0; q < 4; ++q) { const int row = 8 * q + (lane >> 3); vS[q] = row * 4096 + (((lane & 7) ^ ((row >> 1) & 7)) << 4) - q * 1024; }
    const unsigned sWs = (ti * 128 + lw * 32) * 4096;
    auto kpos = [&](int n) { return n; };
    auto issueW = [&](int n) { GL4(rWs, lds0 + (n & 3) * STG + lw * 4096, vS[0], vS[1], vS[2], vS[3], sWs + kpos(n) * 128); };
    WVS(2, 1);
    LBAR();
    auto rS = RSRC(ws + WS_ST, 16 * 16 * 128 * 16);
    if (lane < 32) {
      const int row = lw * 32 + lane;
      f32x4 v0, v1, v2, v3;
      asm volatile("ds_read_b128 %0, %4\n\tds_read_b128 %1, %4 offset:16\n\tds_read_b128 %2, %4 offset:32\n\tds_read_b128 %3, %4 offset:48\n\ts_waitcnt lgkmcnt(0)"
                   : "=&v"(v0), "=&v"(v1), "=&v"(v2), "=&v"(v3) : "v"(lds0 + L_T + row * 64) : "memory");
      const f32x4 s = (v0 + v1) + (v2 + v3);
      u32x4 gq; gq.x = tagA; gq.y = __float_as_uint(s[0] + s[2]); gq.z = tagB; gq.w = __float_as_uint(s[1] + s[3]);
      __builtin_amdgcn_raw_buffer_store_b128(gq, rS, ((ti * 16 + fj) * 128 + row) * 16, 0, 16);
    }
    u32x4 sv[8];
    const u32x4 srd = {(unsigned)(size_t)(ws + WS_ST), (unsigned)((size_t)(ws + WS_ST) >> 32) & 0xffffu, 16 * 16 * 128 * 16, 0x00020000u};
    auto sweep = [&]() {
      const unsigned s0 = (ti * 16 + lw * 4) * 2048, v1 = vL + 4096;
      asm volatile("buffer_load_dwordx4 %0, %8, %10, %11 offen sc1\n\tbuffer_load_dwordx4 %1, %8, %10, %11 offen offset:1024 sc1\n\tbuffer_load_dwordx4 %2, %8, %10, %11 offen offset:2048 sc1\n\t"
                   "buffer_load_dwordx4 %3, %8, %10, %11 offen offset:3072 sc1\n\tbuffer_load_dwordx4 %4, %9, %10, %11 offen sc1\n\tbuffer_load_dwordx4 %5, %9, %10, %11 offen offset:1024 sc1\n\t"
                   "buffer_load_dwordx4 %6, %9, %10, %11 offen offset:2048 sc1\n\tbuffer_load_dwordx4 %7, %9, %10, %11 offen offset:3072 sc1"
                   : "=&v"(sv[0]), "=&v"(sv[1]), "=&v"(sv[2]), "=&v"(sv[3]), "=&v"(sv[4]), "=&v"(sv[5]), "=&v"(sv[6]), "=&v"(sv[7]) : "v"(vL), "v"(v1), "s"(srd), "s"(s0) : "memory");
    };
    LBAR(); issue1(0, 4); WVS(2, 1);
    LBAR(); issue1(0, 5); WVS(2, 0);
    LBAR(); issue1(0, 6); WVS(2, 0);
    LBAR(); issue1(0, 7); WVS(2, 0);
    LBAR(); sweep(); WVS(1, 8);
    LBAR(); issue1(0, 8);
    WVS(1, 8);
    LBAR();
    {
      f32x4 s;
      auto total = [&](const u32x4 (&v)[8]) {
        unsigned bad = 0;
        s = (f32x4){0.f, 0.f, 0.f, 0.f};
#pragma unroll
        for (int p = 0; p < 8; ++p) { bad |= (v[p].x ^ tagA) | (v[p].z ^ tagB); s[(p & 1) * 2] += __uint_as_float(v[p].y); s[(p & 1) * 2 + 1] += __uint_as_float(v[p].w); }
        return __builtin_amdgcn_ballot_w64(bad != 0) == 0;
      };
      WVS(1, 0);
      asm volatile("" : "+v"(sv[0]), "+v"(sv[1]), "+v"(sv[2]), "+v"(sv[3]), "+v"(sv[4]), "+v"(sv[5]), "+v"(sv[6]), "+v"(sv[7]) :: "memory");
      if (__builtin_expect(!total(sv), 0)) {
#pragma unroll 1
        for (unsigned spins = 0;; ++spins) {
          u32x4 rv[8];
          MEMB();
#pragma unroll
          for (int p = 0; p < 8; ++p) rv[p] = __builtin_amdgcn_raw_buffer_load_b128(rS, vL + (p & 1) * 1024, (ti * 16 + lw * 4 + (p >> 1)) * 2048, 16);
          if (total(rv)) break;
          if (spins > SPIN_CAP) { s[0] = s[2] = __builtin_nanf(""); break; }
        }
      }
      const f32x2 o0 = {s[0], s[1]}, o1 = {s[2], s[3]};
      asm volatile("ds_write_b64 %0, %1\n\tds_write_b64 %0, %2 offset:2048\n\ts_waitcnt lgkmcnt(0)" :: "v"(lds0 + L_R + lane * 32 + lw * 8), "v"(o0), "v"(o1) : "memory");
    }
    LBAR();
    WV(0);
    LBAR();
    issueW(0); issueW(2);
    LBAR();
    issueW(1); issueW(3);
    TOUCH(rWo, lane < 21 ? lane * 128 : OOB, bid * 10368 + lw * 2688);
    auto rZ = RSRC(ws + WS_Z2, FRAG_BYTES);
    const unsigned sZ = fj * 128 * CH + zp0 * 1024;
    u64 ready = 0;
    auto issueZ = [&](int n) {
      const int kp = kpos(n);
      if (__builtin_expect(!got(ready, kp >> 1), 0)) { for (unsigned spins = 0; !got(ready, kp >> 1) && spins < SPIN_CAP; ++spins) ready = poll16(ws + WS_F1 + fj * 16 * 128, tag, lane); }
      MEMB();
      const unsigned dz = lds0 + (n & 3) * STG + 16384 + zp0 * 1024, so = sZ + kp * 18432;
      GS4(rZ, dz, vL, so);
      if (big) GS(rZ, dz + 4096, vL, so + 4096, 0);
    };
    const char* f1 = ws + WS_F1 + fj * 16 * 128;
    for (int q = 0; q < PDELAY; ++q) __builtin_amdgcn_s_sleep(4);
    { unsigned np = 0; for (; ready != 0xff && np < SPIN_CAP; ++np) ready = pollN<2>(f1, tag, lane); LST(0, np); }
    issueZ(0); issueZ(1);
    WVS(1, -4);
    LBAR();
    u64 fv;
    asm volatile("global_load_dwordx2 %0, %1, %2 sc1" : "=&v"(fv) : "v"((lane >> 2) * 128 + (lane & 3) * 8), "s"(f1) : "memory");
    issueZ(2);
    WVS(1, -3);
    LBAR();
    issueZ(3);
    WVS(1, -4);
    asm volatile("" : "+v"(fv));
    ready |= __builtin_amdgcn_ballot_w64(fv == tag);
    LBAR();
    for (int n = 4; n < 31; ++n) {
      issueW(n); issueZ(n);
      WVS(1, 0);
      LBAR();
    }
    issueW(31); issueZ(31);
    constexpr int TAIL = 9;
    int tt, dd; const bool has3 = tile3(tt, dd);
    unsigned vO[2], vOh;
#pragma unroll
    for (int q = 0; q < 2; ++q) { const int row = 36 * lw + 8 * q + (lane >> 3); vO[q] = row * 1152 + (((lane & 7) ^ (((row >> 1) & 1) | (row & 4))) << 4); }
    { const int row = 36 * lw + 32 + ((lane >> 3) & 3); vOh = row * 1152 + (((lane & 7) ^ (((row >> 1) & 1) | (row & 4))) << 4); }
    const unsigned vO1 = vO[1] - 1024, vO2 = vO[0] + 16 * 1152 - 2048, vO3 = vO[1] + 16 * 1152 - 3072;
    const unsigned sO = min(dd, 8) * 128;
    auto issueO = [&](int n, int slot) {
      const unsigned d = lds0 + L_O + slot * STGO + lw * 4608, s0 = sO + n * 144 * 1152;
      GL4(rWo, d, vO[0], vO1, vO2, vO3, s0);
      if (lane < 32) GL(rWo, d + 4096, vOh, s0, 0);
    };
    issueO(0, 0);
    WVS(1, 5); LBAR();
    issueO(1, 1); issueO(2, 2);
    WV(15); LBAR();
    LBAR();
    WV(0);
    LBAR();
    if (!has3) return;
    issueO(3, 3); issueO(4, 4);
    LBAR();
    issueO(5, 0);
    if (NPRE3 == 5) { issueO(6, 1); issueO(7, 2); }
    auto rP = RSRC(ws + WS_P, FRAG_BYTES);
    const unsigned vPs = lw < 3 ? (lane >> 5) * 16 * CH + (lane & 31) * 16 + 4096 : OOB;
    const int limS = lw == 2 ? 32 : 64;
    const unsigned sP = tt * 5 * 16 * CH;
    const int pa = (tt * 80) >> 7, pb = min((tt * 80 + 79) >> 7, 15);
    u64 rdy = 0;
    auto issueP = [&](int j, int slot) {
      if (__builtin_expect(!got(rdy, j), 0)) { for (unsigned spins = 0; !got(rdy, j) && spins < SPIN_CAP; ++spins) rdy = poll16(ws + WS_F2 + pa * 16 * 128, tag, lane) & poll16(ws + WS_F2 + pb * 16 * 128, tag, lane); }
      MEMB();
      const unsigned d = lds0 + L_P + slot * STGP, sp = sP + j * CH;
      GS4(rP, d + lw * 4096, vL, sp + lw * 16 * CH);
      GS(rP, d + 16384 + lw * 1024, vL, sp + 64 * CH + lw * 1024, 0);
      if (lane < limS) GS(rP, lw < 3 ? d + 20480 + lw * 1024 : lds0 + L_DUMMY, vPs, sp + lw * 32 * CH, 0);
    };
    const char *fa = ws + WS_F2 + pa * 16 * 128, *fb = ws + WS_F2 + pb * 16 * 128;
    { unsigned np = 0; LST(2, 0); for (; rdy != 0xfff && np < SPIN_CAP; ++np) rdy = pollN<3>(fa, tag, lane) & pollN<3>(fb, tag, lane); LST(1, np); }
    u64 fva, fvb;
    asm volatile("global_load_dwordx2 %0, %2, %3 sc1\n\tglobal_load_dwordx2 %1, %2, %4 sc1" : "=&v"(fva), "=&v"(fvb) : "v"((lane >> 2) * 128 + (lane & 3) * 8), "s"(fa), "s"(fb) : "memory");
    issueP(0, 0); issueP(1, 1); issueP(2, 2);
    WV(12);
    asm volatile("" : "+v"(fva), "+v"(fvb));
    rdy |= __builtin_amdgcn_ballot_w64(fva == tag) & __builtin_amdgcn_ballot_w64(fvb == tag);
    LBAR();
    WV(6);
    if (NPRE3 == 5) {
      LBAR(); issueP(3, 0); WV(6);
      LBAR(); issueP(4, 1); WV(6);
      LBAR(); issueP(5, 2); WV(6);
    } else {
      LBAR(); issueP(3, 0); issueO(6, 1); WV(11);
      LBAR(); issueP(4, 1); issueO(7, 2); WV(16);
      LBAR(); issueP(5, 2); WV(11);
    }
    LBAR(); issueP(6, 0); issueO(8, 3); WV(11);
    int sp3 = 1, so5 = 4;
    for (int n = 5; n < 12; ++n) {
      LBAR();
      issueP(n + 2, sp3); issueO(n + 4, so5);
      sp3 = sp3 == 2 ? 0 : sp3 + 1; so5 = so5 == 4 ? 0 : so5 + 1;
      WV(16);
    }
    LBAR(); issueP(14, 2); WV(6);
    LBAR(); issueP(15, 0); WV(6);
    LBAR(); WV(0);
    LBAR();
    LBAR(); LBAR();
    if (bid == 0 && tid == 256) __hip_atomic_store(G64(ws + WS_EP), tag, RLX_AGENT);
  } else {
    const int wt = wid >> 1, wf = wid & 1;
    if (wid == 0) {
      auto rC = RSRC((const char*)(pc0 & ~(u64)255), CODE_KB * 1024);
      GL(rC, lds0 + L_DUMMY, lane * 16, rk * 1024, 0); GL(rC, lds0 + L_DUMMY, lane * 16, rk * 1024 + 32768, 0);
      WV(0);
    }
    if (wf == 0) front<0>(lds0, wid, wt, lane, ti, fj, ws); else front<1>(lds0, wid, wt, lane, ti, fj, ws);
    int tt, dd;
    if (!tile3(tt, dd)) return;
    if (wt == 0) g3_compute<0>(lds0, wf, lane, out, tt, dd); else g3_compute<1>(lds0, wf, lane, out, tt, dd);
  }
}

extern "C" void kernel_launch(void* const* d_in, const int* in_sizes, int n_in, void* d_out, int out_size, void* d_ws, size_t ws_size, hipStream_t stream) {
  (void)hipFuncSetAttribute((const void*)gmlp_fused, hipFuncAttributeMaxDynamicSharedMemorySize, LDS_BYTES);
  gmlp_fused<<<256, 512, LDS_BYTES, stream>>>(d_in[0], d_in[1], d_in[2], d_in[3], d_in[4], d_out, d_ws, d_ws);
}
```

```cpp
#include <hip/hip_runtime.h>
#include <hip/hip_bf16.h>
typedef unsigned short u16;
typedef unsigned long long u64;
typedef short s16x4 __attribute__((ext_vector_type(4)));
typedef short bf16x8 __attribute__((ext_vector_type(8)));
typedef float f32x2 __attribute__((ext_vector_type(2)));
typedef float f32x4 __attribute__((ext_vector_type(4)));
typedef unsigned u32x4 __attribute__((ext_vector_type(4)));
typedef unsigned u32x2 __attribute__((ext_vector_type(2)));
#define LAS __attribute__((address_space(3)))
#define DEV __device__ __forceinline__
#define BAR() __builtin_amdgcn_s_barrier()
#define SB() __builtin_amdgcn_sched_barrier(0)
#define MEMB() asm volatile("" ::: "memory")
#define WAITL0() __builtin_amdgcn_s_waitcnt(0xC07F)
#define WL0() asm volatile("s_waitcnt lgkmcnt(0)" ::: "memory")
#define WV(N) asm volatile("s_waitcnt vmcnt(%0)" ::"i"(N) : "memory")
#define MFMA(a, b, c) __builtin_amdgcn_mfma_f32_16x16x32_bf16(a, b, c, 0, 0, 0)
#define MFMA16(a, b, c) __builtin_amdgcn_mfma_f32_16x16x16bf16_1k(a, b, c, 0, 0, 0)
#define GL(R, D, V, S, I) __builtin_amdgcn_raw_ptr_buffer_load_lds(R, (LAS void*)(size_t)(D), 16, V, S, I, 0)
#define GL4(R, D, V0, V1, V2, V3, S) do { GL(R, D, V0, S, 0); GL(R, D, V1, S, 1024); GL(R, D, V2, S, 2048); GL(R, D, V3, S, 3072); } while (0)
#define GS(R, D, V, S, I) __builtin_amdgcn_raw_ptr_buffer_load_lds(R, (LAS void*)(size_t)(D), 16, V, S, I, 16)
#define GS4(R, D, V, S) do { GS(R, D, V, S, 0); GS(R, D, V, S, 1024); GS(R, D, V, S, 2048); GS(R, D, V, S, 3072); } while (0)
#define TOUCH(R, V, S) __builtin_amdgcn_raw_ptr_buffer_load_lds(R, (LAS void*)(size_t)(lds0 + L_DUMMY), 4, V, S, 0, 0)
#define RSRC(p, n) __builtin_amdgcn_make_buffer_rsrc((void*)(p), 0, (n), 0x00020000)
#define RLX_AGENT __ATOMIC_RELAXED, __HIP_MEMORY_SCOPE_AGENT
#define ROT1 2
#define NPRE3 5
#define Z1W 0
#define LS0 1
#define OAUX 16
#define WAUX 0
#define T0W 0
#define PDELAY 7
#define PSLEEP 0
#define CODE_KB 54

constexpr int CH = 4608;
constexpr int FRAG_BYTES = 16 * 128 * CH;
constexpr size_t WS_Z2 = 0, WS_P = FRAG_BYTES, WS_ST = 2 * (size_t)FRAG_BYTES, WS_F1 = WS_ST + 16 * 16 * 128 * 16, WS_F2 = WS_F1 + 256 * 128, WS_EP = WS_F2 + 256 * 128;
constexpr int STG = 34816, STGP = 23040, STGO = 18432, L_O = 0, L_P = 5 * STGO, LDS_BYTES = 163840;
constexpr int L_T = 139264, L_R = L_T + 8192, L_X = 151552, L_BIAS = 159744, L_TAG = L_BIAS + 256, L_DUMMY = 161280;
constexpr unsigned SPIN_CAP = 1u << 16, OOB = 0x80000000u;
#define LBAR() BAR()
#define CST(I)
#define LST(I, X)

DEV unsigned cvt_pk(float lo, float hi) { unsigned r; asm("v_cvt_pk_bf16_f32 %0, %1, %2" : "=v"(r) : "v"(lo), "v"(hi)); return r; }
DEV bf16x8 ld128(unsigned a) { return *(LAS bf16x8*)(size_t)a; }
DEV s16x4 ld64(unsigned a) { return *(LAS s16x4*)(size_t)a; }
DEV s16x4 ldtr(unsigned a) { return __builtin_amdgcn_ds_read_tr16_b64_v4i16((LAS s16x4*)(size_t)a); }
DEV bf16x8 cat(s16x4 a, s16x4 b) { return __builtin_shufflevector(a, b, 0, 1, 2, 3, 4, 5, 6, 7); }
DEV float bflo(unsigned v) { return __uint_as_float(v << 16); }
DEV float bfhi(unsigned v) { return __uint_as_float(v & 0xffff0000u); }
template <int NB, bool SUM>
DEV void gelu_blocks(const f32x4* const (&v)[NB], u32x2* const (&o)[NB], f32x2& s1, f32x2& s2) {
  const float k1 = -2.0f * 0.7978845608028654f * 1.4426950408889634f, k2 = k1 * 0.044715f;
  f32x2 x[2 * NB], t[2 * NB];
#pragma unroll
  for (int i = 0; i < 2 * NB; ++i) { x[i] = (f32x2){(*v[i >> 1])[(i & 1) * 2], (*v[i >> 1])[(i & 1) * 2 + 1]}; t[i] = x[i] * x[i]; }
  SB();
#pragma unroll
  for (int i = 0; i < 2 * NB; ++i) t[i] = t[i] * k2 + k1;
  SB();
#pragma unroll
  for (int i = 0; i < 2 * NB; ++i) t[i] = t[i] * x[i];
  SB();
#pragma unroll
  for (int i = 0; i < 2 * NB; ++i) { t[i].x = __builtin_amdgcn_exp2f(t[i].x); t[i].y = __builtin_amdgcn_exp2f(t[i].y); }
  SB();
#pragma unroll
  for (int i = 0; i < 2 * NB; ++i) t[i] = t[i] + 1.0f;
  SB();
#pragma unroll
  for (int i = 0; i < 2 * NB; ++i) { t[i].x = __builtin_amdgcn_rcpf(t[i].x); t[i].y = __builtin_amdgcn_rcpf(t[i].y); }
  SB();
#pragma unroll
  for (int i = 0; i < 2 * NB; ++i) t[i] = t[i] * x[i];
  SB();
  f32x2 a1 = s1, a2 = s2, b1 = {0.f, 0.f}, b2 = {0.f, 0.f};
#pragma unroll
  for (int i = 0; i < NB; ++i) {
    o[i]->x = cvt_pk(t[2 * i].x, t[2 * i].y);
    if (SUM) { a1 += t[2 * i]; a2 += t[2 * i] * t[2 * i]; }
    o[i]->y = cvt_pk(t[2 * i + 1].x, t[2 * i + 1].y);
    if (SUM) { if (i == 0) { b1 = t[1]; b2 = t[1] * t[1]; } else { b1 += t[2 * i + 1]; b2 += t[2 * i + 1] * t[2 * i + 1]; } }
  }
  if (SUM) { s1 = a1 + b1; s2 = a2 + b2; }
  SB();
}
template <int WF, bool SUM>
DEV void gelu_row(int m, const f32x4 (&acc)[4][5], u32x2 (&oa)[4][4], u32x2 (&ob)[2], f32x2& s1, f32x2& s2) {
  if ((m >> 1) == WF) { const f32x4* const v[5] = {&acc[m][0], &acc[m][1], &acc[m][2], &acc[m][3], &acc[m][4]}; u32x2* const o[5] = {&oa[m][0], &oa[m][1], &oa[m][2], &oa[m][3], &ob[m & 1]}; gelu_blocks<5, SUM>(v, o, s1, s2); }
  else { const f32x4* const v[4] = {&acc[m][0], &acc[m][1], &acc[m][2], &acc[m][3]}; u32x2* const o[4] = {&oa[m][0], &oa[m][1], &oa[m][2], &oa[m][3]}; gelu_blocks<4, SUM>(v, o, s1, s2); }
}

struct TAddr { unsigned w[2], z[2][2], s[2]; };
struct TFrag { bf16x8 wX[4], wY[4]; s16x4 tX[5][2], tY[4][2]; };
template <int WF, int PS, int OFF, bool FIRST, bool SYNC = true>
DEV void t_step(TAddr& A, int dlt, f32x4 (&acc)[4][5], TFrag& F, const f32x4* init = nullptr) {
  constexpr int KX = WF, KY = 1 - WF;
  SB(); if (SYNC) BAR(); MEMB(); SB();
  auto rdz = [&](s16x4 (&t)[2], int n, int u, int K) { t[u] = n < 4 ? ldtr(A.z[u][n & 1] + OFF + K * 9216 + (n >> 1) * PS) : ldtr(A.s[u] + OFF + K * 9216); };
#pragma unroll
  for (int i = 0; i < 16; ++i) {
    const int m = i >> 2, n = i & 3;
    if (!FIRST) acc[m][n] = MFMA(cat(F.tY[n][0], F.tY[n][1]), F.wY[m], acc[m][n]);
    SB();
    if (i == 0) F.wX[0] = ld128(A.w[KX] + OFF);
    else if (i < 11) rdz(F.tX[(i - 1) >> 1], (i - 1) >> 1, (i - 1) & 1, KX);
    else if (i < 14) F.wX[i - 10] = ld128(A.w[KX] + OFF + (i - 10) * 2048);
    SB();
  }
#pragma unroll
  for (int i = 0; i < 20; ++i) {
    const int m = i / 5, n = i % 5;
    const f32x4 zero4 = {0, 0, 0, 0};
    acc[m][n] = MFMA(cat(F.tX[n][0], F.tX[n][1]), F.wX[m], !FIRST ? acc[m][n] : (PS == 1024 && (n < 4 || (m >> 1) == WF)) ? init[m] : zero4);
    SB();
    if (i == 0) F.wY[0] = ld128(A.w[KY] + OFF);
    else if (i < 9) rdz(F.tY[(i - 1) >> 1], (i - 1) >> 1, (i - 1) & 1, KY);
    else if (i < 12) F.wY[i - 8] = ld128(A.w[KY] + OFF + (i - 8) * 2048);
    else if (OFF != 0 && i < 16) {
      const int u = i & 1;
      if (i < 14) { A.w[u] += dlt; A.s[u] += dlt; asm volatile("" : "+v"(A.w[u]), "+v"(A.s[u])); }
      else { A.z[u][0] += dlt; A.z[u][1] += dlt; asm volatile("" : "+v"(A.z[u][0]), "+v"(A.z[u][1])); }
    }
    SB();
  }
  WAITL0();
}
template <int WF>
DEV void t_finish(unsigned lds0, int wt, int lane, f32x4 (&acc)[4][5], TFrag& F) {
#pragma unroll
  for (int h = 0; h < 2; ++h) *(LAS f32x4*)(size_t)(lds0 + L_X + ((wt * 2 + WF) * 2 + h) * 1024 + lane * 16) = acc[2 * (1 - WF) + h][4];
  SB();
#pragma unroll
  for (int m = 0; m < 4; ++m)
#pragma unroll
    for (int n = 0; n < 4; ++n) acc[m][n] = MFMA(cat(F.tY[n][0], F.tY[n][1]), F.wY[m], acc[m][n]);
  SB();
  WL0();
  BAR(); MEMB();
#pragma unroll
  for (int h = 0; h < 2; ++h) acc[2 * WF + h][4] += *(LAS f32x4*)(size_t)(lds0 + L_X + ((wt * 2 + 1 - WF) * 2 + h) * 1024 + lane * 16);
}
template <int WF, bool MID, class Mid>
DEV void in_pass(unsigned lds0, int wt, int lane, TAddr& A, f32x4 (&acc)[4][5], Mid mid) {
  TFrag F;
  int dlt = 2 * STG;
  WAITL0();
  if (!MID) {
    t_step<WF, 64, 0, true>(A, dlt, acc, F);
    for (int k = 0; k < 4; ++k) {
      t_step<WF, 64, STG, false>(A, dlt, acc, F);
      dlt = -dlt;
      t_step<WF, 64, 0, false>(A, dlt, acc, F);
    }
  } else {
    t_step<WF, 64, STG, true>(A, dlt, acc, F);
    for (int k = 0; k < 3; ++k) {
      dlt = -dlt;
      t_step<WF, 64, 0, false>(A, dlt, acc, F);
      t_step<WF, 64, STG, false>(A, dlt, acc, F);
    }
    t_step<WF, 64, 0, false>(A, 0, acc, F);
    SB(); BAR(); MEMB(); mid(); SB();
    t_step<WF, 64, STG, false>(A, 0, acc, F);
  }
  t_finish<WF>(lds0, wt, lane, acc, F);
}

struct G3P { bf16x8 p[2][5]; s16x4 ps[3]; };
struct G3W { s16x4 w[2][2][2], ws[2]; };
template <int KH>
DEV void g3_readw(G3W& n, unsigned a0, unsigned a1) {
#pragma unroll
  for (int q = 0; q < 8; ++q) n.w[q >> 2][(q >> 1) & 1][q & 1] = ldtr((((q >> 1) & 1) ? a1 : a0) + (2 * KH + (q >> 2)) * 4096 + (q & 1) * 2048);
  n.ws[0] = ldtr(a0 + 16384); n.ws[1] = ldtr(a1 + 16384);
}
template <int KH, bool MM, bool RDW>
DEV void g3_stage(f32x4 (&acc)[5][2], const G3P& cp, const G3W& cw, G3P& np, G3W& nw, unsigned aP, unsigned aS, unsigned a0, unsigned a1) {
  constexpr int M0 = KH ? 3 : 0, NS = KH ? 2 : 3, NM = 20 + 2 * NS, NR = 20 + NS;
  SB(); BAR(); MEMB(); SB();
#pragma unroll
  for (int i = 0; i < NM; ++i) {
    if (MM) {
      if (i >= 10 && i < 10 + 2 * NS) acc[M0 + ((i - 10) >> 1)][i & 1] = MFMA16(cw.ws[i & 1], cp.ps[(i - 10) >> 1], acc[M0 + ((i - 10) >> 1)][i & 1]);
      else { const int ii = i < 10 ? i : i - 2 * NS, P = ii / 10, mm = (ii % 10) >> 1, m = (KH != P) ? (mm + 3) % 5 : mm, cc = ii & 1; acc[m][cc] = MFMA(cat(cw.w[P][cc][0], cw.w[P][cc][1]), cp.p[P][m], acc[m][cc]); }
    }
    SB();
#pragma unroll
    for (int r = (i * 3) / 2; r < ((i + 1) * 3) / 2; ++r) {
      if (r < 18) { const int P = r / 9, q = r % 9;
        if (q < 4) { if (RDW) nw.w[P][q >> 1][q & 1] = ldtr(((q >> 1) ? a1 : a0) + (2 * KH + P) * 4096 + (q & 1) * 2048); }
        else np.p[P][q - 4] = ld128(aP + P * 1024 + (q - 4) * 4096); }
      else if (r < 20) { if (RDW) nw.ws[r - 18] = ldtr(((r - 18) ? a1 : a0) + 16384); }
      else if (r < NR) np.ps[r - 20] = ld64(aS + (M0 + r - 20) * 512);
    }
    SB();
  }
  WAITL0();
}
template <int KH>
DEV void g3_compute(unsigned lds0, int dh, int lane, char* out, int tt, int dd) {
  const int g = lane >> 4, qr = (lane >> 2) & 3, p = lane & 3;
  const int rr = 4 * g + qr;
  unsigned bW[2];
#pragma unroll
  for (int c = 0; c < 2; ++c) bW[c] = lds0 + L_O + rr * 128 + (((4 * dh + p) ^ (((rr >> 1) & 1) | (rr & 4))) << 4) + ((c ^ (p & 1)) * 8);
  const unsigned bP = lds0 + L_P + lane * 16 + KH * 2048, bS = lds0 + L_P + 20480 + lane * 8;
  G3W a0, a1, a2;
  g3_readw<KH>(a0, bW[0], bW[1]); g3_readw<KH>(a1, bW[0] + STGO, bW[1] + STGO); g3_readw<KH>(a2, bW[0] + 2 * STGO, bW[1] + 2 * STGO);
  f32x4 acc[5][2];
#pragma unroll
  for (int m = 0; m < 5; ++m) { acc[m][0] = (f32x4){0, 0, 0, 0}; acc[m][1] = (f32x4){0, 0, 0, 0}; }
  WAITL0();
  SB(); BAR(); SB();
  G3P p0, p1; G3W w0, w1;
  g3_stage<KH, false, false>(acc, p1, a0, p0, w0, bP, bS, 0, 0);
  g3_stage<KH, true, false>(acc, p0, a0, p1, w0, bP + STGP, bS + STGP, 0, 0);
  g3_stage<KH, true, false>(acc, p1, a1, p0, w0, bP + 2 * STGP, bS + 2 * STGP, 0, 0);
  g3_stage<KH, true, true>(acc, p0, a2, p1, w1, bP, bS, bW[0] + 3 * STGO, bW[1] + 3 * STGO);
  unsigned sp = STGP, sw = 4 * STGO;
  for (int j = 4; j < 16; j += 2) {
    g3_stage<KH, true, true>(acc, p1, w1, p0, w0, bP + sp, bS + sp, bW[0] + sw, bW[1] + sw);
    sp = sp == 2 * STGP ? 0 : sp + STGP; sw = sw == 4 * STGO ? 0 : sw + STGO;
    g3_stage<KH, true, true>(acc, p0, w0, p1, w1, bP + sp, bS + sp, bW[0] + sw, bW[1] + sw);
    sp = sp == 2 * STGP ? 0 : sp + STGP; sw = sw == 4 * STGO ? 0 : sw + STGO;
  }
  {
    constexpr int M0 = KH ? 3 : 0, NS = KH ? 2 : 3;
    SB(); BAR(); SB();
#pragma unroll
    for (int i = 0; i < 2 * NS; ++i) { acc[M0 + (i >> 1)][i & 1] = MFMA16(w1.ws[i & 1], p1.ps[i >> 1], acc[M0 + (i >> 1)][i & 1]); SB(); }
#pragma unroll
    for (int i = 0; i < 20; ++i) { const int P = (i >> 1) & 1, m = KH ? i >> 2 : 4 - (i >> 2), cc = i & 1; acc[m][cc] = MFMA(cat(w1.w[P][cc][0], w1.w[P][cc][1]), p1.p[P][m], acc[m][cc]); SB(); }
  }
#pragma unroll
  for (int m = 0; m < 5; ++m)
    if ((m >= 3) != KH) { *(LAS f32x4*)(size_t)(lds0 + (dh * 10 + m * 2) * 1024 + lane * 16) = acc[m][0]; *(LAS f32x4*)(size_t)(lds0 + (dh * 10 + m * 2 + 1) * 1024 + lane * 16) = acc[m][1]; }
  WL0();
  BAR(); MEMB();
  auto rO = RSRC(out, 2048 * 1152);
  const bool sw2 = g & 1;
#pragma unroll
  for (int m = 0; m < 5; ++m)
    if ((m >= 3) == KH) {
      const f32x4 a = acc[m][0] + *(LAS f32x4*)(size_t)(lds0 + (dh * 10 + m * 2) * 1024 + lane * 16), b = acc[m][1] + *(LAS f32x4*)(size_t)(lds0 + (dh * 10 + m * 2 + 1) * 1024 + lane * 16);
      const unsigned a0 = cvt_pk(a[0], a[1]), a1 = cvt_pk(a[2], a[3]), b0 = cvt_pk(b[0], b[1]), b1 = cvt_pk(b[2], b[3]);
      const u32x4 v = {sw2 ? b0 : a0, sw2 ? b1 : a1, sw2 ? a0 : b0, sw2 ? a1 : b1};
      __builtin_amdgcn_raw_buffer_store_b128(v, rO, (tt * 80 + m * 16 + (lane & 15)) * 1152 + dd * 128 + dh * 64 + g * 16, 0, OAUX);
    }
}

template <int WF>
DEV void front(unsigned lds0, int wid, int wt, int lane, int ti, int fj, char* ws) {
  const int r16 = lane & 15, g = lane >> 4, qr = (lane >> 2) & 3, p = lane & 3;
  TAddr A;
  A.w[0] = lds0 + (64 * wt + r16) * 128 + ((g ^ (r16 >> 1)) << 4); A.w[1] = A.w[0] ^ 64;
#pragma unroll
  for (int u = 0; u < 2; ++u) {
    const unsigned b = lds0 + 16384 + (16 * u + 4 * g + qr) * 288 + p * 8;
    A.z[u][0] = b + WF * 128 + (p & 1) * 32; A.z[u][1] = b + WF * 128 + 32 - (p & 1) * 32; A.s[u] = b + 256;
  }
  asm volatile("" : "+v"(A.w[0]), "+v"(A.w[1]), "+v"(A.s[0]), "+v"(A.s[1]), "+v"(A.z[0][0]), "+v"(A.z[0][1]), "+v"(A.z[1][0]), "+v"(A.z[1][1]));
  f32x4 acc[4][5];
  in_pass<WF, false>(lds0, wt, lane, A, acc, []() {});
  CST(0);
  u32x2 ua[4][4], ub[2];
  {
    f32x2 s1[4], s2[4];
#pragma unroll
    for (int mm = 0; mm < 4; ++mm) {
      const int m = mm ^ (2 * (1 - WF));
      s1[m] = (f32x2){0.f, 0.f}; s2[m] = (f32x2){0.f, 0.f};
      gelu_row<WF, true>(m, acc, ua, ub, s1[m], s2[m]);
    }
#pragma unroll
    for (int m = 0; m < 4; ++m) *(LAS f32x2*)(size_t)(lds0 + L_T + (64 * wt + 16 * m + r16) * 64 + (WF * 4 + g) * 8) = (f32x2){s1[m].x + s1[m].y, s2[m].x + s2[m].y};
  }
#pragma unroll
  for (int m = 0; m < 4; ++m) asm volatile("" : "+v"(ua[m][0]), "+v"(ua[m][1]), "+v"(ua[m][2]), "+v"(ua[m][3]));
  asm volatile("" : "+v"(ub[0]), "+v"(ub[1]));
#pragma unroll
  for (int u = 0; u < 2; ++u) { A.z[u][0] -= (p & 1) * 32; A.z[u][1] += (p & 1) * 32; }
  asm volatile("" : "+v"(A.z[0][0]), "+v"(A.z[0][1]), "+v"(A.z[1][0]), "+v"(A.z[1][1]));
  CST(1);
  in_pass<WF, true>(lds0, wt, lane, A, acc, [&]() {
    CST(2);
#pragma unroll
    for (int m = 0; m < 4; ++m) asm volatile("" : "+v"(ua[m][0]), "+v"(ua[m][1]), "+v"(ua[m][2]), "+v"(ua[m][3]));
    asm volatile("" : "+v"(ub[0]), "+v"(ub[1]));
    int lz = lane; asm volatile("" : "+v"(lz));
    auto rZ = RSRC(ws + WS_Z2, FRAG_BYTES);
    const unsigned sl = (lz >> 4) * 16 + ((lz & 15) ^ (4 * (lz >> 5))), dst = (fj * 128 + ti * 8 + wt * 4) * CH;
#pragma unroll
    for (int m = 0; m < 4; ++m) {
      const unsigned ar = lds0 + L_R + (64 * wt + 16 * m + (lz & 15)) * 32;
      const f32x4 q4 = *(LAS f32x4*)(size_t)ar + *(LAS f32x4*)(size_t)(ar + 16);
      const float mean = (q4[0] + q4[2]) * (1.0f / 2304.0f), var = fmaxf((q4[1] + q4[3]) * (1.0f / 2304.0f) - mean * mean, 0.f);
      const float rs = __builtin_amdgcn_rsqf(var + 1e-5f), sh = -mean * rs;
      auto norm = [&](unsigned v) { const f32x2 y = (f32x2){bflo(v), bfhi(v)} * rs + sh; return cvt_pk(y.x, y.y); };
#pragma unroll
      for (int pp = 0; pp < 2; ++pp)
        __builtin_amdgcn_raw_buffer_store_b128((u32x4){norm(ua[m][2 * pp].x), norm(ua[m][2 * pp].y), norm(ua[m][2 * pp + 1].x), norm(ua[m][2 * pp + 1].y)}, rZ, dst + m * CH + sl * 16 + WF * 2048 + pp * 1024, 0, 16);
      if ((m >> 1) == WF) __builtin_amdgcn_raw_buffer_store_b64((u32x2){norm(ub[m & 1].x), norm(ub[m & 1].y)}, rZ, dst + m * CH + sl * 8 + 4096, 0, 16);
    }
    CST(3);
  });
  CST(4);
#pragma unroll
  for (int m = 0; m < 4; ++m) asm volatile("" : "+v"(acc[m][0]), "+v"(acc[m][1]), "+v"(acc[m][2]), "+v"(acc[m][3]), "+v"(acc[m][4]));
  const u32x2 tg = *(LAS u32x2*)(size_t)(lds0 + L_TAG);
  const u64 tag = ((u64)(unsigned)__builtin_amdgcn_readfirstlane(tg.y) << 32) | (unsigned)__builtin_amdgcn_readfirstlane(tg.x);
  WV(0);
  if (lane == 0) __hip_atomic_store((u64*)(ws + WS_F1 + (fj * 16 + ti) * 128) + wid, tag, RLX_AGENT);
  CST(5);
  SB();
  u32x2 za[4][4], zb[2];
#pragma unroll
  for (int m = 0; m < 4; ++m) {
    f32x2 d1, d2;
    gelu_row<WF, false>(m, acc, za, zb, d1, d2);
    asm volatile("" : "+v"(za[m][0]), "+v"(za[m][1]), "+v"(za[m][2]), "+v"(za[m][3]));
  }
  asm volatile("" : "+v"(zb[0]), "+v"(zb[1]));
  CST(6);
#pragma unroll
  for (int u = 0; u < 2; ++u) {
    const unsigned sl = p * 16 + 8 * (g & 1) + 4 * (u ^ (p >> 1)) + qr;
#pragma unroll
    for (int h = 0; h < 2; ++h) A.z[u][h] = lds0 + 16384 + (g >> 1) * CH + WF * 2048 + sl * 16 + ((h ^ (p & 1)) * 8);
    A.s[u] = lds0 + 16384 + (g >> 1) * CH + 4096 + sl * 8;
  }
  asm volatile("" : "+v"(A.s[0]), "+v"(A.s[1]), "+v"(A.z[0][0]), "+v"(A.z[0][1]), "+v"(A.z[1][0]), "+v"(A.z[1][1]));
  f32x4 bias4[4];
#pragma unroll
  for (int m = 0; m < 4; ++m) { const float b = __uint_as_float((unsigned)*(LAS u16*)(size_t)(lds0 + L_BIAS + (wt * 64 + m * 16 + r16) * 2) << 16); bias4[m] = (f32x4){b, b, b, b}; }
  TFrag F;
  int dlt = 2 * STG;
  WAITL0();
  CST(7);
  t_step<WF, 1024, 0, true, true>(A, dlt, acc, F, bias4);
  CST(8);
  t_step<WF, 1024, STG, false, true>(A, dlt, acc, F);

  for (int k = 0; k < 15; ++k) {
    dlt = -dlt;
    t_step<WF, 1024, 0, false>(A, dlt, acc, F);
    t_step<WF, 1024, STG, false>(A, dlt, acc, F);
  }
  auto rP = RSRC(ws + WS_P, FRAG_BYTES);
  auto gate = [&](f32x4 a, u32x2 z) {
    const f32x2 x = (f32x2){a[0], a[1]} * (f32x2){bflo(z.x), bfhi(z.x)}, y = (f32x2){a[2], a[3]} * (f32x2){bflo(z.y), bfhi(z.y)};
    return (u32x2){cvt_pk(x.x, x.y), cvt_pk(y.x, y.y)};
  };
#pragma unroll
  for (int h = 0; h < 2; ++h) *(LAS f32x4*)(size_t)(lds0 + L_X + ((wt * 2 + WF) * 2 + h) * 1024 + lane * 16) = acc[2 * (1 - WF) + h][4];
  const unsigned dst0 = (ti * 8 + wt * 4) * 16 * CH + fj * CH;
  CST(9);
#pragma unroll
  for (int m = 0; m < 4; ++m)
#pragma unroll
    for (int n = 0; n < 4; ++n) acc[m][n] = MFMA(cat(F.tY[n][0], F.tY[n][1]), F.wY[m], acc[m][n]);
#pragma unroll
  for (int m = 0; m < 4; ++m)
#pragma unroll
    for (int pp = 0; pp < 2; ++pp) {
      const u32x2 a = gate(acc[m][2 * pp], za[m][2 * pp]), b = gate(acc[m][2 * pp + 1], za[m][2 * pp + 1]);
      __builtin_amdgcn_raw_buffer_store_b128((u32x4){a.x, a.y, b.x, b.y}, rP, dst0 + m * 16 * CH + lane * 16 + (2 * WF + pp) * 1024, 0, 16);
    }
  SB();
  WL0();
  BAR(); MEMB();
#pragma unroll
  for (int h = 0; h < 2; ++h) {
    const f32x4 o8 = *(LAS f32x4*)(size_t)(lds0 + L_X + ((wt * 2 + 1 - WF) * 2 + h) * 1024 + lane * 16);
    __builtin_amdgcn_raw_buffer_store_b64(gate(acc[2 * WF + h][4] + o8, zb[h]), rP, dst0 + (2 * WF + h) * 16 * CH + lane * 8 + 4096, 0, 16);
  }
  CST(10);
  WV(0);
  CST(11);
  if (lane == 0) __hip_atomic_store((u64*)(ws + WS_F2 + (ti * 16 + fj) * 128) + wid, tag, RLX_AGENT);
  SB(); BAR(); MEMB(); SB();
}

DEV u64 poll16(const char* base, u64 tag, int lane) {
  const u64 v = __hip_atomic_load((const u64*)(base + (lane >> 2) * 128) + (lane & 3), RLX_AGENT);
  return __builtin_amdgcn_ballot_w64(v == tag);
}
template <int NP> DEV u64 pollN(const char* base, u64 tag, int lane) {
  const u64 v = __hip_atomic_load((const u64*)(base + min(lane >> 2, NP - 1) * 128) + (lane & 3), RLX_AGENT);
  return __builtin_amdgcn_ballot_w64(v == tag) & ((1ull << (4 * NP)) - 1);
}
DEV bool got(u64 mask, int who) { return ((unsigned)(mask >> (4 * who)) & 15u) == 15u; }

__global__ __launch_bounds__(512, 2) void gmlp_fused(const char* __restrict__ X, const char* __restrict__ Win, const char* __restrict__ Wsp, const u16* __restrict__ bias,
                                                     const char* __restrict__ Wo, char* __restrict__ out, char* __restrict__ ws) {
  extern __shared__ __attribute__((aligned(16))) char smem[];
  u64 pc0; asm volatile("s_getpc_b64 %0" : "=s"(pc0));
  const int tid = threadIdx.x, wid = __builtin_amdgcn_readfirstlane(tid >> 6), lane = tid & 63;
  const int bid = blockIdx.x, xg = bid & 7, rk = bid >> 3, il = rk & 7, jl = rk >> 3, ti = (xg & 1) * 8 + il, fj = (xg >> 1) * 4 + jl;
  const int L3 = xg * 30 + rk, tt = L3 / 9, dd = L3 - tt * 9;
  const bool has3 = rk < 30 && L3 < 234;
  const unsigned lds0 = (unsigned)(size_t)(LAS char*)smem;
  if (__builtin_expect(wid >= 4, 1)) {
    __builtin_amdgcn_s_setprio(3);
    const int lw = wid - 4;
    const bool big = wid < 6;
#define WVS(STAGES, EXTRA) do { if (big) WV(9 * (STAGES) + (EXTRA)); else WV(8 * (STAGES) + (EXTRA)); } while (0)
    auto rX = RSRC(X, 2048 * 1152); auto rWi = RSRC(Win, 576 * 9216);
    unsigned vX[4], vW[5];
    const unsigned zp0 = lw < 2 ? lw * 5 : 2 + lw * 4;
    const unsigned sX = (ti * 128 + lw * 32) * 1152, sW = fj * 288;
    const int rot = ROT1 == 0 ? 0 : ROT1 == 1 ? il + 2 * jl : 3 * ((il + jl) % 3);
    const int rot8 = ROT1 == 1 ? (il + 2 * jl) & 7 : ROT1 == 2 ? (((il + jl) % 3) * 8) / 3 : 0;
    auto k1 = [&](int k) { return k == 0 ? 0 : 1 + ((k - 1 + rot8) & 7); };
    auto issue1 = [&](int half, int k) {
      const int kk = k1(k);
      const unsigned d = lds0 + ((k + 1 - half) & 3) * STG, dz = d + 16384 + zp0 * 1024, sw = sW + half * 4608 + kk * 64 * 9216;
      GL4(rX, d + lw * 4096, vX[0], vX[1], vX[2], vX[3], sX + kk * 128);
      GL4(rWi, dz, vW[0], vW[1], vW[2], vW[3], sw);
      if (big) GL(rWi, dz + 4096, vW[4], sw, 0);
    };
    {
#pragma unroll
      for (int q = 0; q < 4; ++q) { const int row = 8 * q + (lane >> 3); vX[q] = row * 1152 + (((lane & 7) ^ ((row >> 1) & 7)) << 4) - q * 1024; }
      GL4(rX, lds0 + lw * 4096, vX[0], vX[1], vX[2], vX[3], sX + k1(0) * 128);
      SB();
#pragma unroll
      for (int q = 0; q < 5; ++q) {
        const unsigned c16 = (zp0 + q) * 64 + lane, R = (c16 * 3641u) >> 16, c = (c16 - R * 18) * 16, k = (R & 32) + 8 * ((R >> 2) & 3) + 4 * ((R >> 4) & 1) + (R & 3);
        vW[q] = k * 9216 + c - (q & 3) * 1024;
      }
      const unsigned dz = lds0 + 16384 + zp0 * 1024, sw = sW + 4608 + k1(0) * 64 * 9216;
      GL4(rWi, dz, vW[0], vW[1], vW[2], vW[3], sw);
      if (big) GL(rWi, dz + 4096, vW[4], sw, 0);
    }
    const unsigned vL = lane * 16;
    auto rWs = RSRC(Wsp, 2048 * 4096); auto rWo = RSRC(Wo, 2304 * 1152);
    const unsigned e9 = lw * 21 + lane, r9 = (e9 * 7282u) >> 16, v81 = lane < 21 && e9 < 81 ? r9 * 9216 + (e9 - r9 * 9) * 128 : OOB, s81 = (bid >> 2) * 9 * 9216 + (bid & 3) * 1152;
    issue1(1, 1);
    WVS(1, T0W ? 2 : 0); LBAR();
    issue1(1, 2);
    WVS(1, 0);
    for (int k = 1; k < 5; ++k) { LBAR(); issue1(1, k + 2); WVS(1, 0); }
    LBAR(); issue1(1, 7); WVS(1, 0);
    LBAR(); issue1(1, 8); WVS(1, 0);
    LBAR(); issue1(0, 0); WVS(1, 0);
    LBAR(); issue1(0, 1);
    const u64 ep = __hip_atomic_load((const u64*)(ws + WS_EP), RLX_AGENT);
    if (lw == 0) __builtin_amdgcn_raw_ptr_buffer_load_lds(RSRC(bias, 4096), (LAS void*)(size_t)(lds0 + L_BIAS), 4, lane * 4, ti * 256, 0, 0);
    TOUCH(rWs, lane * 128, bid * 32768 + lw * 8192);
    LBAR();
    issue1(0, 2); issue1(0, 3);
    const unsigned tagA = __builtin_amdgcn_readfirstlane((unsigned)ep) + 0x9E3779B9u, tagB = __builtin_amdgcn_readfirstlane((unsigned)(ep >> 32)) + 0x85EBCA6Bu;
    const u64 tag = ((u64)tagB << 32) | tagA;
    if (lw == 0) { const u32x2 tg = {tagA, tagB}; asm volatile("ds_write_b64 %0, %1\n\ts_waitcnt lgkmcnt(0)" :: "v"(lds0 + L_TAG), "v"(tg) : "memory"); }
    unsigned vS[4];
#pragma unroll
    for (int q = 0; q < 4; ++q) { const int row = 8 * q + (lane >> 3); vS[q] = row * 4096 + (((lane & 7) ^ ((row >> 1) & 7)) << 4) - q * 1024; }
    const unsigned sWs = (ti * 128 + lw * 32) * 4096;
    auto kpos = [&](int n) { return n; };
    auto issueW = [&](int n) { GL4(rWs, lds0 + (n & 3) * STG + lw * 4096, vS[0], vS[1], vS[2], vS[3], sWs + kpos(n) * 128); };
    WVS(2, 1);
    LBAR();
    auto rS = RSRC(ws + WS_ST, 16 * 16 * 128 * 16);
    if (lane < 32) {
      const int row = lw * 32 + lane;
      f32x4 v0, v1, v2, v3;
      asm volatile("ds_read_b128 %0, %4\n\tds_read_b128 %1, %4 offset:16\n\tds_read_b128 %2, %4 offset:32\n\tds_read_b128 %3, %4 offset:48\n\ts_waitcnt lgkmcnt(0)"
                   : "=&v"(v0), "=&v"(v1), "=&v"(v2), "=&v"(v3) : "v"(lds0 + L_T + row * 64) : "memory");
      const f32x4 s = (v0 + v1) + (v2 + v3);
      u32x4 gq; gq.x = tagA; gq.y = __float_as_uint(s[0] + s[2]); gq.z = tagB; gq.w = __float_as_uint(s[1] + s[3]);
      __builtin_amdgcn_raw_buffer_store_b128(gq, rS, ((ti * 16 + fj) * 128 + row) * 16, 0, 16);
    }
    u32x4 sv[8];
    const u32x4 srd = {(unsigned)(size_t)(ws + WS_ST), (unsigned)((size_t)(ws + WS_ST) >> 32) & 0xffffu, 16 * 16 * 128 * 16, 0x00020000u};
    auto sweep = [&]() {
      const unsigned s0 = (ti * 16 + lw * 4) * 2048, v1 = vL + 4096;
      asm volatile("buffer_load_dwordx4 %0, %8, %10, %11 offen sc1\n\tbuffer_load_dwordx4 %1, %8, %10, %11 offen offset:1024 sc1\n\tbuffer_load_dwordx4 %2, %8, %10, %11 offen offset:2048 sc1\n\t"
                   "buffer_load_dwordx4 %3, %8, %10, %11 offen offset:3072 sc1\n\tbuffer_load_dwordx4 %4, %9, %10, %11 offen sc1\n\tbuffer_load_dwordx4 %5, %9, %10, %11 offen offset:1024 sc1\n\t"
                   "buffer_load_dwordx4 %6, %9, %10, %11 offen offset:2048 sc1\n\tbuffer_load_dwordx4 %7, %9, %10, %11 offen offset:3072 sc1"
                   : "=&v"(sv[0]), "=&v"(sv[1]), "=&v"(sv[2]), "=&v"(sv[3]), "=&v"(sv[4]), "=&v"(sv[5]), "=&v"(sv[6]), "=&v"(sv[7]) : "v"(vL), "v"(v1), "s"(srd), "s"(s0) : "memory");
    };
    LBAR(); issue1(0, 4); WVS(2, 1);
    LBAR(); issue1(0, 5); WVS(2, 0);
    LBAR(); issue1(0, 6); WVS(2, 0);
    LBAR(); issue1(0, 7); WVS(2, 0);
    LBAR(); sweep(); WVS(1, 8);
    LBAR(); issue1(0, 8);
    WVS(1, 8);
    LBAR();
    {
      f32x4 s;
      auto total = [&](const u32x4 (&v)[8]) {
        unsigned bad = 0;
        s = (f32x4){0.f, 0.f, 0.f, 0.f};
#pragma unroll
        for (int p = 0; p < 8; ++p) { bad |= (v[p].x ^ tagA) | (v[p].z ^ tagB); s[(p & 1) * 2] += __uint_as_float(v[p].y); s[(p & 1) * 2 + 1] += __uint_as_float(v[p].w); }
        return __builtin_amdgcn_ballot_w64(bad != 0) == 0;
      };
      WVS(1, 0);
      asm volatile("" : "+v"(sv[0]), "+v"(sv[1]), "+v"(sv[2]), "+v"(sv[3]), "+v"(sv[4]), "+v"(sv[5]), "+v"(sv[6]), "+v"(sv[7]) :: "memory");
      if (__builtin_expect(!total(sv), 0)) {
#pragma unroll 1
        for (unsigned spins = 0;; ++spins) {
          u32x4 rv[8];
          MEMB();
#pragma unroll
          for (int p = 0; p < 8; ++p) rv[p] = __builtin_amdgcn_raw_buffer_load_b128(rS, vL + (p & 1) * 1024, (ti * 16 + lw * 4 + (p >> 1)) * 2048, 16);
          if (total(rv)) break;
          if (spins > SPIN_CAP) { s[0] = s[2] = __builtin_nanf(""); break; }
        }
      }
      const f32x2 o0 = {s[0], s[1]}, o1 = {s[2], s[3]};
      asm volatile("ds_write_b64 %0, %1\n\tds_write_b64 %0, %2 offset:2048\n\ts_waitcnt lgkmcnt(0)" :: "v"(lds0 + L_R + lane * 32 + lw * 8), "v"(o0), "v"(o1) : "memory");
    }
    LBAR();
    WV(0);
    LBAR();
    issueW(0); issueW(2);
    LBAR();
    issueW(1); issueW(3);
    TOUCH(rWo, lane < 21 ? lane * 128 : OOB, bid * 10368 + lw * 2688);
    auto rZ = RSRC(ws + WS_Z2, FRAG_BYTES);
    const unsigned sZ = fj * 128 * CH + zp0 * 1024;
    u64 ready = 0;
    auto issueZ = [&](int n) {
      const int kp = kpos(n);
      if (__builtin_expect(!got(ready, kp >> 1), 0)) { for (unsigned spins = 0; !got(ready, kp >> 1) && spins < SPIN_CAP; ++spins) ready = poll16(ws + WS_F1 + fj * 16 * 128, tag, lane); }
      MEMB();
      const unsigned dz = lds0 + (n & 3) * STG + 16384 + zp0 * 1024, so = sZ + kp * 18432;
      GS4(rZ, dz, vL, so);
      if (big) GS(rZ, dz + 4096, vL, so + 4096, 0);
    };
    const char* f1 = ws + WS_F1 + fj * 16 * 128;
    for (int q = 0; q < PDELAY; ++q) __builtin_amdgcn_s_sleep(4);
    { unsigned np = 0; for (; ready != 0xfff && np < SPIN_CAP; ++np) ready = pollN<3>(f1, tag, lane); LST(0, np); }
    issueZ(0); issueZ(1); issueZ(2);
    WVS(2, -8);
    LBAR();
    u64 fv;
    asm volatile("global_load_dwordx2 %0, %1, %2 sc1" : "=&v"(fv) : "v"((lane >> 2) * 128 + (lane & 3) * 8), "s"(f1) : "memory");
    issueZ(3);
    WVS(2, -7);
    LBAR();
    issueW(4); issueZ(4);
    WVS(2, -3);
    LBAR();
    issueW(5); issueZ(5);
    WVS(2, 0);
    asm volatile("" : "+v"(fv));
    ready |= __builtin_amdgcn_ballot_w64(fv == tag);
    LBAR();
    for (int n = 6; n < 32; ++n) {
      issueW(n); issueZ(n);
      WVS(2, 0);
      LBAR();
    }
    unsigned vO[2], vOh;
#pragma unroll
    for (int q = 0; q < 2; ++q) { const int row = 36 * lw + 8 * q + (lane >> 3); vO[q] = row * 1152 + (((lane & 7) ^ (((row >> 1) & 1) | (row & 4))) << 4); }
    { const int row = 36 * lw + 32 + ((lane >> 3) & 3); vOh = row * 1152 + (((lane & 7) ^ (((row >> 1) & 1) | (row & 4))) << 4); }
    const unsigned vO1 = vO[1] - 1024, vO2 = vO[0] + 16 * 1152 - 2048, vO3 = vO[1] + 16 * 1152 - 3072;
    const unsigned sO = min(dd, 8) * 128;
    auto issueO = [&](int n, int slot) {
      const unsigned d = lds0 + L_O + slot * STGO + lw * 4608, s0 = sO + n * 144 * 1152;
      GL4(rWo, d, vO[0], vO1, vO2, vO3, s0);
      if (lane < 32) GL(rWo, d + 4096, vOh, s0, 0);
    };
    issueO(0, 0);
    WVS(1, 5); LBAR();
    issueO(1, 1); issueO(2, 2);
    WV(15); LBAR();
    LBAR();
    WV(0);
    LBAR();
    if (!has3) return;
    issueO(3, 3); issueO(4, 4);
    LBAR();
    issueO(5, 0);
    if (NPRE3 == 5) { issueO(6, 1); issueO(7, 2); }
    auto rP = RSRC(ws + WS_P, FRAG_BYTES);
    const unsigned vPs = lw < 3 ? (lane >> 5) * 16 * CH + (lane & 31) * 16 + 4096 : OOB;
    const int limS = lw == 2 ? 32 : 64;
    const unsigned sP = tt * 5 * 16 * CH;
    const int pa = (tt * 80) >> 7, pb = min((tt * 80 + 79) >> 7, 15);
    u64 rdy = 0;
    auto issueP = [&](int j, int slot) {
      if (__builtin_expect(!got(rdy, j), 0)) { for (unsigned spins = 0; !got(rdy, j) && spins < SPIN_CAP; ++spins) rdy = poll16(ws + WS_F2 + pa * 16 * 128, tag, lane) & poll16(ws + WS_F2 + pb * 16 * 128, tag, lane); }
      MEMB();
      const unsigned d = lds0 + L_P + slot * STGP, sp = sP + j * CH;
      GS4(rP, d + lw * 4096, vL, sp + lw * 16 * CH);
      GS(rP, d + 16384 + lw * 1024, vL, sp + 64 * CH + lw * 1024, 0);
      if (lane < limS) GS(rP, lw < 3 ? d + 20480 + lw * 1024 : lds0 + L_DUMMY, vPs, sp + lw * 32 * CH, 0);
    };
    const char *fa = ws + WS_F2 + pa * 16 * 128, *fb = ws + WS_F2 + pb * 16 * 128;
    { unsigned np = 0; LST(2, 0); for (; rdy != 0xfff && np < SPIN_CAP; ++np) rdy = pollN<3>(fa, tag, lane) & pollN<3>(fb, tag, lane); LST(1, np); }
    u64 fva, fvb;
    asm volatile("global_load_dwordx2 %0, %2, %3 sc1\n\tglobal_load_dwordx2 %1, %2, %4 sc1" : "=&v"(fva), "=&v"(fvb) : "v"((lane >> 2) * 128 + (lane & 3) * 8), "s"(fa), "s"(fb) : "memory");
    issueP(0, 0); issueP(1, 1); issueP(2, 2);
    WV(12);
    asm volatile("" : "+v"(fva), "+v"(fvb));
    rdy |= __builtin_amdgcn_ballot_w64(fva == tag) & __builtin_amdgcn_ballot_w64(fvb == tag);
    LBAR();
    WV(6);
    if (NPRE3 == 5) {
      LBAR(); issueP(3, 0); WV(6);
      LBAR(); issueP(4, 1); WV(6);
      LBAR(); issueP(5, 2); WV(6);
    } else {
      LBAR(); issueP(3, 0); issueO(6, 1); WV(11);
      LBAR(); issueP(4, 1); issueO(7, 2); WV(16);
      LBAR(); issueP(5, 2); WV(11);
    }
    LBAR(); issueP(6, 0); issueO(8, 3); WV(11);
    int sp3 = 1, so5 = 4;
    for (int n = 5; n < 12; ++n) {
      LBAR();
      issueP(n + 2, sp3); issueO(n + 4, so5);
      sp3 = sp3 == 2 ? 0 : sp3 + 1; so5 = so5 == 4 ? 0 : so5 + 1;
      WV(16);
    }
    LBAR(); issueP(14, 2); WV(6);
    LBAR(); issueP(15, 0); WV(6);
    LBAR(); WV(0);
    LBAR();
    LBAR(); LBAR();
    if (bid == 0 && tid == 256) __hip_atomic_store((u64*)(ws + WS_EP), tag, RLX_AGENT);
  } else {
    const int wt = wid >> 1, wf = wid & 1;
    if (wid == 0) {
      auto rC = RSRC((const char*)(pc0 & ~(u64)255), CODE_KB * 1024);
      GL(rC, lds0 + L_DUMMY, lane * 16, rk * 1024, 0); GL(rC, lds0 + L_DUMMY, lane * 16, rk * 1024 + 32768, 0);
      WV(0);
    }
    if (wf == 0) front<0>(lds0, wid, wt, lane, ti, fj, ws); else front<1>(lds0, wid, wt, lane, ti, fj, ws);
    if (!has3) return;
    if (wt == 0) g3_compute<0>(lds0, wf, lane, out, tt, dd); else g3_compute<1>(lds0, wf, lane, out, tt, dd);
  }
}

extern "C" void kernel_launch(void* const* d_in, const int* in_sizes, int n_in, void* d_out, int out_size, void* d_ws, size_t ws_size, hipStream_t stream) {
  (void)hipFuncSetAttribute((const void*)gmlp_fused, hipFuncAttributeMaxDynamicSharedMemorySize, LDS_BYTES);
  gmlp_fused<<<256, 512, LDS_BYTES, stream>>>((const char*)d_in[0], (const char*)d_in[1], (const char*)d_in[2], (const u16*)d_in[3], (const char*)d_in[4], (char*)d_out, (char*)d_ws);
}
```

```cpp
#include <hip/hip_runtime.h>
#include <hip/hip_bf16.h>
typedef unsigned short u16;
typedef unsigned long long u64;
typedef short s16x4 __attribute__((ext_vector_type(4)));
typedef short bf16x8 __attribute__((ext_vector_type(8)));
typedef float f32x2 __attribute__((ext_vector_type(2)));
typedef float f32x4 __attribute__((ext_vector_type(4)));
typedef unsigned u32x4 __attribute__((ext_vector_type(4)));
typedef unsigned u32x2 __attribute__((ext_vector_type(2)));
#define LAS __attribute__((address_space(3)))
#define DEV __device__ __forceinline__
#define BAR() __builtin_amdgcn_s_barrier()
#define SB() __builtin_amdgcn_sched_barrier(0)
#define MEMB() asm volatile("" ::: "memory")
#define WAITL0() __builtin_amdgcn_s_waitcnt(0xC07F)
#define WL0() asm volatile("s_waitcnt lgkmcnt(0)" ::: "memory")
#define WV(N) asm volatile("s_waitcnt vmcnt(%0)" ::"i"(N) : "memory")
#define MFMA(a, b, c) __builtin_amdgcn_mfma_f32_16x16x32_bf16(a, b, c, 0, 0, 0)
#define MFMA16(a, b, c) __builtin_amdgcn_mfma_f32_16x16x16bf16_1k(a, b, c, 0, 0, 0)
#define GL(R, D, V, S, I) __builtin_amdgcn_raw_ptr_buffer_load_lds(R, (LAS void*)(size_t)(D), 16, V, S, I, 0)
#define GL4(R, D, V0, V1, V2, V3, S) do { GL(R, D, V0, S, 0); GL(R, D, V1, S, 1024); GL(R, D, V2, S, 2048); GL(R, D, V3, S, 3072); } while (0)
#define GS(R, D, V, S, I) __builtin_amdgcn_raw_ptr_buffer_load_lds(R, (LAS void*)(size_t)(D), 16, V, S, I, 16)
#define GS4(R, D, V, S) do { GS(R, D, V, S, 0); GS(R, D, V, S, 1024); GS(R, D, V, S, 2048); GS(R, D, V, S, 3072); } while (0)
#define TOUCH(R, V, S) __builtin_amdgcn_raw_ptr_buffer_load_lds(R, (LAS void*)(size_t)(lds0 + L_DUMMY), 4, V, S, 0, 0)
#define RSRC(p, n) __builtin_amdgcn_make_buffer_rsrc((void*)(p), 0, (n), 0x00020000)
#define RLX_AGENT __ATOMIC_RELAXED, __HIP_MEMORY_SCOPE_AGENT
#define ROT1 2
#define NPRE3 3
#define Z1W 0
#define LS0 1
#define OAUX 16
#define WAUX 0
#define T0W 0
#define PDELAY 7
#define PSLEEP 0
#define CODE_KB 54

constexpr int CH = 4608;
constexpr int FRAG_BYTES = 16 * 128 * CH;
constexpr size_t WS_Z2 = 0, WS_P = FRAG_BYTES, WS_ST = 2 * (size_t)FRAG_BYTES, WS_F1 = WS_ST + 16 * 16 * 128 * 16, WS_F2 = WS_F1 + 256 * 128, WS_EP = WS_F2 + 256 * 128;
constexpr int STG = 34816, STGP = 23040, STGO = 18432, L_O = 0, L_P = 5 * STGO, LDS_BYTES = 163840;
constexpr int L_T = 139264, L_R = L_T + 8192, L_X = 151552, L_BIAS = 159744, L_TAG = L_BIAS + 256, L_CNT = L_TAG + 8, L_DUMMY = 161280;
constexpr unsigned SPIN_CAP = 1u << 16, OOB = 0x80000000u;
#define LBAR() BAR()
#define CST(I)
#define LST(I, X)

DEV unsigned cvt_pk(float lo, float hi) { unsigned r; asm("v_cvt_pk_bf16_f32 %0, %1, %2" : "=v"(r) : "v"(lo), "v"(hi)); return r; }
DEV bf16x8 ld128(unsigned a) { return *(LAS bf16x8*)(size_t)a; }
DEV s16x4 ld64(unsigned a) { return *(LAS s16x4*)(size_t)a; }
DEV s16x4 ldtr(unsigned a) { return __builtin_amdgcn_ds_read_tr16_b64_v4i16((LAS s16x4*)(size_t)a); }
DEV bf16x8 cat(s16x4 a, s16x4 b) { return __builtin_shufflevector(a, b, 0, 1, 2, 3, 4, 5, 6, 7); }
DEV float bflo(unsigned v) { return __uint_as_float(v << 16); }
DEV float bfhi(unsigned v) { return __uint_as_float(v & 0xffff0000u); }
template <int NB, bool SUM>
DEV void gelu_blocks(const f32x4* const (&v)[NB], u32x2* const (&o)[NB], f32x2& s1, f32x2& s2) {
  const float k1 = -2.0f * 0.7978845608028654f * 1.4426950408889634f, k2 = k1 * 0.044715f;
  f32x2 x[2 * NB], t[2 * NB];
#pragma unroll
  for (int i = 0; i < 2 * NB; ++i) { x[i] = (f32x2){(*v[i >> 1])[(i & 1) * 2], (*v[i >> 1])[(i & 1) * 2 + 1]}; t[i] = x[i] * x[i]; }
  SB();
#pragma unroll
  for (int i = 0; i < 2 * NB; ++i) t[i] = t[i] * k2 + k1;
  SB();
#pragma unroll
  for (int i = 0; i < 2 * NB; ++i) t[i] = t[i] * x[i];
  SB();
#pragma unroll
  for (int i = 0; i < 2 * NB; ++i) { t[i].x = __builtin_amdgcn_exp2f(t[i].x); t[i].y = __builtin_amdgcn_exp2f(t[i].y); }
  SB();
#pragma unroll
  for (int i = 0; i < 2 * NB; ++i) t[i] = t[i] + 1.0f;
  SB();
#pragma unroll
  for (int i = 0; i < 2 * NB; ++i) { t[i].x = __builtin_amdgcn_rcpf(t[i].x); t[i].y = __builtin_amdgcn_rcpf(t[i].y); }
  SB();
#pragma unroll
  for (int i = 0; i < 2 * NB; ++i) t[i] = t[i] * x[i];
  SB();
  f32x2 a1 = s1, a2 = s2, b1 = {0.f, 0.f}, b2 = {0.f, 0.f};
#pragma unroll
  for (int i = 0; i < NB; ++i) {
    o[i]->x = cvt_pk(t[2 * i].x, t[2 * i].y);
    if (SUM) { a1 += t[2 * i]; a2 += t[2 * i] * t[2 * i]; }
    o[i]->y = cvt_pk(t[2 * i + 1].x, t[2 * i + 1].y);
    if (SUM) { if (i == 0) { b1 = t[1]; b2 = t[1] * t[1]; } else { b1 += t[2 * i + 1]; b2 += t[2 * i + 1] * t[2 * i + 1]; } }
  }
  if (SUM) { s1 = a1 + b1; s2 = a2 + b2; }
  SB();
}
template <int WF, bool SUM>
DEV void gelu_row(int m, const f32x4 (&acc)[4][5], u32x2 (&oa)[4][4], u32x2 (&ob)[2], f32x2& s1, f32x2& s2) {
  if ((m >> 1) == WF) { const f32x4* const v[5] = {&acc[m][0], &acc[m][1], &acc[m][2], &acc[m][3], &acc[m][4]}; u32x2* const o[5] = {&oa[m][0], &oa[m][1], &oa[m][2], &oa[m][3], &ob[m & 1]}; gelu_blocks<5, SUM>(v, o, s1, s2); }
  else { const f32x4* const v[4] = {&acc[m][0], &acc[m][1], &acc[m][2], &acc[m][3]}; u32x2* const o[4] = {&oa[m][0], &oa[m][1], &oa[m][2], &oa[m][3]}; gelu_blocks<4, SUM>(v, o, s1, s2); }
}

struct TAddr { unsigned w[2], z[2][2], s[2]; };
struct TFrag { bf16x8 wX[4], wY[4]; s16x4 tX[5][2], tY[4][2]; };
template <int WF, int PS, int OFF, bool FIRST, bool SYNC = true>
DEV void t_step(TAddr& A, int dlt, f32x4 (&acc)[4][5], TFrag& F, const f32x4* init = nullptr) {
  constexpr int KX = WF, KY = 1 - WF;
  SB(); if (SYNC) BAR(); MEMB(); SB();
  auto rdz = [&](s16x4 (&t)[2], int n, int u, int K) { t[u] = n < 4 ? ldtr(A.z[u][n & 1] + OFF + K * 9216 + (n >> 1) * PS) : ldtr(A.s[u] + OFF + K * 9216); };
#pragma unroll
  for (int i = 0; i < 16; ++i) {
    const int m = i >> 2, n = i & 3;
    if (!FIRST) acc[m][n] = MFMA(cat(F.tY[n][0], F.tY[n][1]), F.wY[m], acc[m][n]);
    SB();
    if (i == 0) F.wX[0] = ld128(A.w[KX] + OFF);
    else if (i < 11) rdz(F.tX[(i - 1) >> 1], (i - 1) >> 1, (i - 1) & 1, KX);
    else if (i < 14) F.wX[i - 10] = ld128(A.w[KX] + OFF + (i - 10) * 2048);
    SB();
  }
#pragma unroll
  for (int i = 0; i < 20; ++i) {
    const int m = i / 5, n = i % 5;
    const f32x4 zero4 = {0, 0, 0, 0};
    acc[m][n] = MFMA(cat(F.tX[n][0], F.tX[n][1]), F.wX[m], !FIRST ? acc[m][n] : (PS == 1024 && (n < 4 || (m >> 1) == WF)) ? init[m] : zero4);
    SB();
    if (i == 0) F.wY[0] = ld128(A.w[KY] + OFF);
    else if (i < 9) rdz(F.tY[(i - 1) >> 1], (i - 1) >> 1, (i - 1) & 1, KY);
    else if (i < 12) F.wY[i - 8] = ld128(A.w[KY] + OFF + (i - 8) * 2048);
    else if (OFF != 0 && i < 16) {
      const int u = i & 1;
      if (i < 14) { A.w[u] += dlt; A.s[u] += dlt; asm volatile("" : "+v"(A.w[u]), "+v"(A.s[u])); }
      else { A.z[u][0] += dlt; A.z[u][1] += dlt; asm volatile("" : "+v"(A.z[u][0]), "+v"(A.z[u][1])); }
    }
    SB();
  }
  WAITL0();
}
template <int WF>
DEV void t_finish(unsigned lds0, int wt, int lane, f32x4 (&acc)[4][5], TFrag& F) {
#pragma unroll
  for (int h = 0; h < 2; ++h) *(LAS f32x4*)(size_t)(lds0 + L_X + ((wt * 2 + WF) * 2 + h) * 1024 + lane * 16) = acc[2 * (1 - WF) + h][4];
  SB();
#pragma unroll
  for (int m = 0; m < 4; ++m)
#pragma unroll
    for (int n = 0; n < 4; ++n) acc[m][n] = MFMA(cat(F.tY[n][0], F.tY[n][1]), F.wY[m], acc[m][n]);
  SB();
  WL0();
  BAR(); MEMB();
#pragma unroll
  for (int h = 0; h < 2; ++h) acc[2 * WF + h][4] += *(LAS f32x4*)(size_t)(lds0 + L_X + ((wt * 2 + 1 - WF) * 2 + h) * 1024 + lane * 16);
}
template <int WF, bool MID, class Mid>
DEV void in_pass(unsigned lds0, int wt, int lane, TAddr& A, f32x4 (&acc)[4][5], Mid mid) {
  TFrag F;
  int dlt = 2 * STG;
  WAITL0();
  if (!MID) {
    t_step<WF, 64, 0, true>(A, dlt, acc, F);
    for (int k = 0; k < 4; ++k) {
      t_step<WF, 64, STG, false>(A, dlt, acc, F);
      dlt = -dlt;
      t_step<WF, 64, 0, false>(A, dlt, acc, F);
    }
  } else {
    t_step<WF, 64, STG, true>(A, dlt, acc, F);
    for (int k = 0; k < 3; ++k) {
      dlt = -dlt;
      t_step<WF, 64, 0, false>(A, dlt, acc, F);
      t_step<WF, 64, STG, false>(A, dlt, acc, F);
    }
    t_step<WF, 64, 0, false>(A, 0, acc, F);
    SB(); BAR(); MEMB(); mid(); SB();
    t_step<WF, 64, STG, false>(A, 0, acc, F);
  }
  t_finish<WF>(lds0, wt, lane, acc, F);
}

struct G3P { bf16x8 p[2][5]; s16x4 ps[3]; };
struct G3W { s16x4 w[2][2][2], ws[2]; };
template <int KH>
DEV void g3_readw(G3W& n, unsigned a0, unsigned a1) {
#pragma unroll
  for (int q = 0; q < 8; ++q) n.w[q >> 2][(q >> 1) & 1][q & 1] = ldtr((((q >> 1) & 1) ? a1 : a0) + (2 * KH + (q >> 2)) * 4096 + (q & 1) * 2048);
  n.ws[0] = ldtr(a0 + 16384); n.ws[1] = ldtr(a1 + 16384);
}
template <int KH, bool MM, bool RDW>
DEV void g3_stage(f32x4 (&acc)[5][2], const G3P& cp, const G3W& cw, G3P& np, G3W& nw, unsigned aP, unsigned aS, unsigned a0, unsigned a1) {
  constexpr int M0 = KH ? 3 : 0, NS = KH ? 2 : 3, NM = 20 + 2 * NS, NR = 20 + NS;
  SB(); BAR(); MEMB(); SB();
#pragma unroll
  for (int i = 0; i < NM; ++i) {
    if (MM) {
      if (i >= 10 && i < 10 + 2 * NS) acc[M0 + ((i - 10) >> 1)][i & 1] = MFMA16(cw.ws[i & 1], cp.ps[(i - 10) >> 1], acc[M0 + ((i - 10) >> 1)][i & 1]);
      else { const int ii = i < 10 ? i : i - 2 * NS, P = ii / 10, mm = (ii % 10) >> 1, m = (KH != P) ? (mm + 3) % 5 : mm, cc = ii & 1; acc[m][cc] = MFMA(cat(cw.w[P][cc][0], cw.w[P][cc][1]), cp.p[P][m], acc[m][cc]); }
    }
    SB();
#pragma unroll
    for (int r = (i * 3) / 2; r < ((i + 1) * 3) / 2; ++r) {
      if (r < 18) { const int P = r / 9, q = r % 9;
        if (q < 4) { if (RDW) nw.w[P][q >> 1][q & 1] = ldtr(((q >> 1) ? a1 : a0) + (2 * KH + P) * 4096 + (q & 1) * 2048); }
        else np.p[P][q - 4] = ld128(aP + P * 1024 + (q - 4) * 4096); }
      else if (r < 20) { if (RDW) nw.ws[r - 18] = ldtr(((r - 18) ? a1 : a0) + 16384); }
      else if (r < NR) np.ps[r - 20] = ld64(aS + (M0 + r - 20) * 512);
    }
    SB();
  }
  WAITL0();
}
template <int KH>
DEV void g3_compute(unsigned lds0, int dh, int lane, char* out, int tt, int dd) {
  const int g = lane >> 4, qr = (lane >> 2) & 3, p = lane & 3;
  const int rr = 4 * g + qr;
  unsigned bW[2];
#pragma unroll
  for (int c = 0; c < 2; ++c) bW[c] = lds0 + L_O + rr * 128 + (((4 * dh + p) ^ (((rr >> 1) & 1) | (rr & 4))) << 4) + ((c ^ (p & 1)) * 8);
  const unsigned bP = lds0 + L_P + lane * 16 + KH * 2048, bS = lds0 + L_P + 20480 + lane * 8;
  G3W a0, a1, a2;
  g3_readw<KH>(a0, bW[0], bW[1]); g3_readw<KH>(a1, bW[0] + STGO, bW[1] + STGO); g3_readw<KH>(a2, bW[0] + 2 * STGO, bW[1] + 2 * STGO);
  f32x4 acc[5][2];
#pragma unroll
  for (int m = 0; m < 5; ++m) { acc[m][0] = (f32x4){0, 0, 0, 0}; acc[m][1] = (f32x4){0, 0, 0, 0}; }
  WAITL0();
  SB(); BAR(); SB();
  G3P p0, p1; G3W w0, w1;
  g3_stage<KH, false, false>(acc, p1, a0, p0, w0, bP, bS, 0, 0);
  g3_stage<KH, true, false>(acc, p0, a0, p1, w0, bP + STGP, bS + STGP, 0, 0);
  g3_stage<KH, true, false>(acc, p1, a1, p0, w0, bP + 2 * STGP, bS + 2 * STGP, 0, 0);
  g3_stage<KH, true, true>(acc, p0, a2, p1, w1, bP, bS, bW[0] + 3 * STGO, bW[1] + 3 * STGO);
  unsigned sp = STGP, sw = 4 * STGO;
  for (int j = 4; j < 16; j += 2) {
    g3_stage<KH, true, true>(acc, p1, w1, p0, w0, bP + sp, bS + sp, bW[0] + sw, bW[1] + sw);
    sp = sp == 2 * STGP ? 0 : sp + STGP; sw = sw == 4 * STGO ? 0 : sw + STGO;
    g3_stage<KH, true, true>(acc, p0, w0, p1, w1, bP + sp, bS + sp, bW[0] + sw, bW[1] + sw);
    sp = sp == 2 * STGP ? 0 : sp + STGP; sw = sw == 4 * STGO ? 0 : sw + STGO;
  }
  {
    constexpr int M0 = KH ? 3 : 0, NS = KH ? 2 : 3;
    SB(); BAR(); SB();
#pragma unroll
    for (int i = 0; i < 2 * NS; ++i) { acc[M0 + (i >> 1)][i & 1] = MFMA16(w1.ws[i & 1], p1.ps[i >> 1], acc[M0 + (i >> 1)][i & 1]); SB(); }
#pragma unroll
    for (int i = 0; i < 20; ++i) { const int P = (i >> 1) & 1, m = KH ? i >> 2 : 4 - (i >> 2), cc = i & 1; acc[m][cc] = MFMA(cat(w1.w[P][cc][0], w1.w[P][cc][1]), p1.p[P][m], acc[m][cc]); SB(); }
  }
#pragma unroll
  for (int m = 0; m < 5; ++m)
    if ((m >= 3) != KH) { *(LAS f32x4*)(size_t)(lds0 + (dh * 10 + m * 2) * 1024 + lane * 16) = acc[m][0]; *(LAS f32x4*)(size_t)(lds0 + (dh * 10 + m * 2 + 1) * 1024 + lane * 16) = acc[m][1]; }
  WL0();
  BAR(); MEMB();
  auto rO = RSRC(out, 2048 * 1152);
  const bool sw2 = g & 1;
#pragma unroll
  for (int m = 0; m < 5; ++m)
    if ((m >= 3) == KH) {
      const f32x4 a = acc[m][0] + *(LAS f32x4*)(size_t)(lds0 + (dh * 10 + m * 2) * 1024 + lane * 16), b = acc[m][1] + *(LAS f32x4*)(size_t)(lds0 + (dh * 10 + m * 2 + 1) * 1024 + lane * 16);
      const unsigned a0 = cvt_pk(a[0], a[1]), a1 = cvt_pk(a[2], a[3]), b0 = cvt_pk(b[0], b[1]), b1 = cvt_pk(b[2], b[3]);
      const u32x4 v = {sw2 ? b0 : a0, sw2 ? b1 : a1, sw2 ? a0 : b0, sw2 ? a1 : b1};
      __builtin_amdgcn_raw_buffer_store_b128(v, rO, (tt * 80 + m * 16 + (lane & 15)) * 1152 + dd * 128 + dh * 64 + g * 16, 0, OAUX);
    }
}

template <int WF>
DEV void front(unsigned lds0, int wid, int wt, int lane, int ti, int fj, char* ws) {
  const int r16 = lane & 15, g = lane >> 4, qr = (lane >> 2) & 3, p = lane & 3;
  TAddr A;
  A.w[0] = lds0 + (64 * wt + r16) * 128 + ((g ^ (r16 >> 1)) << 4); A.w[1] = A.w[0] ^ 64;
#pragma unroll
  for (int u = 0; u < 2; ++u) {
    const unsigned b = lds0 + 16384 + (16 * u + 4 * g + qr) * 288 + p * 8;
    A.z[u][0] = b + WF * 128 + (p & 1) * 32; A.z[u][1] = b + WF * 128 + 32 - (p & 1) * 32; A.s[u] = b + 256;
  }
  asm volatile("" : "+v"(A.w[0]), "+v"(A.w[1]), "+v"(A.s[0]), "+v"(A.s[1]), "+v"(A.z[0][0]), "+v"(A.z[0][1]), "+v"(A.z[1][0]), "+v"(A.z[1][1]));
  f32x4 acc[4][5];
  in_pass<WF, false>(lds0, wt, lane, A, acc, []() {});
  CST(0);
  u32x2 ua[4][4], ub[2];
  {
    f32x2 s1[4], s2[4];
#pragma unroll
    for (int mm = 0; mm < 4; ++mm) {
      const int m = mm ^ (2 * (1 - WF));
      s1[m] = (f32x2){0.f, 0.f}; s2[m] = (f32x2){0.f, 0.f};
      gelu_row<WF, true>(m, acc, ua, ub, s1[m], s2[m]);
    }
#pragma unroll
    for (int m = 0; m < 4; ++m) *(LAS f32x2*)(size_t)(lds0 + L_T + (64 * wt + 16 * m + r16) * 64 + (WF * 4 + g) * 8) = (f32x2){s1[m].x + s1[m].y, s2[m].x + s2[m].y};
  }
#pragma unroll
  for (int m = 0; m < 4; ++m) asm volatile("" : "+v"(ua[m][0]), "+v"(ua[m][1]), "+v"(ua[m][2]), "+v"(ua[m][3]));
  asm volatile("" : "+v"(ub[0]), "+v"(ub[1]));
#pragma unroll
  for (int u = 0; u < 2; ++u) { A.z[u][0] -= (p & 1) * 32; A.z[u][1] += (p & 1) * 32; }
  asm volatile("" : "+v"(A.z[0][0]), "+v"(A.z[0][1]), "+v"(A.z[1][0]), "+v"(A.z[1][1]));
  CST(1);
  in_pass<WF, true>(lds0, wt, lane, A, acc, [&]() {
    CST(2);
#pragma unroll
    for (int m = 0; m < 4; ++m) asm volatile("" : "+v"(ua[m][0]), "+v"(ua[m][1]), "+v"(ua[m][2]), "+v"(ua[m][3]));
    asm volatile("" : "+v"(ub[0]), "+v"(ub[1]));
    int lz = lane; asm volatile("" : "+v"(lz));
    auto rZ = RSRC(ws + WS_Z2, FRAG_BYTES);
    const unsigned sl = (lz >> 4) * 16 + ((lz & 15) ^ (4 * (lz >> 5))), dst = (fj * 128 + ti * 8 + wt * 4) * CH;
#pragma unroll
    for (int m = 0; m < 4; ++m) {
      const unsigned ar = lds0 + L_R + (64 * wt + 16 * m + (lz & 15)) * 32;
      const f32x4 q4 = *(LAS f32x4*)(size_t)ar + *(LAS f32x4*)(size_t)(ar + 16);
      const float mean = (q4[0] + q4[2]) * (1.0f / 2304.0f), var = fmaxf((q4[1] + q4[3]) * (1.0f / 2304.0f) - mean * mean, 0.f);
      const float rs = __builtin_amdgcn_rsqf(var + 1e-5f), sh = -mean * rs;
      auto norm = [&](unsigned v) { const f32x2 y = (f32x2){bflo(v), bfhi(v)} * rs + sh; return cvt_pk(y.x, y.y); };
#pragma unroll
      for (int pp = 0; pp < 2; ++pp)
        __builtin_amdgcn_raw_buffer_store_b128((u32x4){norm(ua[m][2 * pp].x), norm(ua[m][2 * pp].y), norm(ua[m][2 * pp + 1].x), norm(ua[m][2 * pp + 1].y)}, rZ, dst + m * CH + sl * 16 + WF * 2048 + pp * 1024, 0, 16);
      if ((m >> 1) == WF) __builtin_amdgcn_raw_buffer_store_b64((u32x2){norm(ub[m & 1].x), norm(ub[m & 1].y)}, rZ, dst + m * CH + sl * 8 + 4096, 0, 16);
    }
    CST(3);
  });
  CST(4);
#pragma unroll
  for (int m = 0; m < 4; ++m) asm volatile("" : "+v"(acc[m][0]), "+v"(acc[m][1]), "+v"(acc[m][2]), "+v"(acc[m][3]), "+v"(acc[m][4]));
  const u32x2 tg = *(LAS u32x2*)(size_t)(lds0 + L_TAG);
  const u64 tag = ((u64)(unsigned)__builtin_amdgcn_readfirstlane(tg.y) << 32) | (unsigned)__builtin_amdgcn_readfirstlane(tg.x);
  WV(0);
  if (lane == 0 && __hip_atomic_fetch_add((LAS unsigned*)(size_t)(lds0 + L_CNT), 1u, __ATOMIC_RELAXED, __HIP_MEMORY_SCOPE_WORKGROUP) == 3u)
    __hip_atomic_store((u64*)(ws + WS_F1 + (fj * 16 + ti) * 128), tag, RLX_AGENT);
  CST(5);
  SB();
  u32x2 za[4][4], zb[2];
#pragma unroll
  for (int m = 0; m < 4; ++m) {
    f32x2 d1, d2;
    gelu_row<WF, false>(m, acc, za, zb, d1, d2);
    asm volatile("" : "+v"(za[m][0]), "+v"(za[m][1]), "+v"(za[m][2]), "+v"(za[m][3]));
  }
  asm volatile("" : "+v"(zb[0]), "+v"(zb[1]));
  CST(6);
#pragma unroll
  for (int u = 0; u < 2; ++u) {
    const unsigned sl = p * 16 + 8 * (g & 1) + 4 * (u ^ (p >> 1)) + qr;
#pragma unroll
    for (int h = 0; h < 2; ++h) A.z[u][h] = lds0 + 16384 + (g >> 1) * CH + WF * 2048 + sl * 16 + ((h ^ (p & 1)) * 8);
    A.s[u] = lds0 + 16384 + (g >> 1) * CH + 4096 + sl * 8;
  }
  asm volatile("" : "+v"(A.s[0]), "+v"(A.s[1]), "+v"(A.z[0][0]), "+v"(A.z[0][1]), "+v"(A.z[1][0]), "+v"(A.z[1][1]));
  f32x4 bias4[4];
#pragma unroll
  for (int m = 0; m < 4; ++m) { const float b = __uint_as_float((unsigned)*(LAS u16*)(size_t)(lds0 + L_BIAS + (wt * 64 + m * 16 + r16) * 2) << 16); bias4[m] = (f32x4){b, b, b, b}; }
  TFrag F;
  int dlt = 2 * STG;
  WAITL0();
  CST(7);
  t_step<WF, 1024, 0, true, true>(A, dlt, acc, F, bias4);
  CST(8);
  t_step<WF, 1024, STG, false, true>(A, dlt, acc, F);

  for (int k = 0; k < 15; ++k) {
    dlt = -dlt;
    t_step<WF, 1024, 0, false>(A, dlt, acc, F);
    t_step<WF, 1024, STG, false>(A, dlt, acc, F);
  }
  auto rP = RSRC(ws + WS_P, FRAG_BYTES);
  auto gate = [&](f32x4 a, u32x2 z) {
    const f32x2 x = (f32x2){a[0], a[1]} * (f32x2){bflo(z.x), bfhi(z.x)}, y = (f32x2){a[2], a[3]} * (f32x2){bflo(z.y), bfhi(z.y)};
    return (u32x2){cvt_pk(x.x, x.y), cvt_pk(y.x, y.y)};
  };
#pragma unroll
  for (int h = 0; h < 2; ++h) *(LAS f32x4*)(size_t)(lds0 + L_X + ((wt * 2 + WF) * 2 + h) * 1024 + lane * 16) = acc[2 * (1 - WF) + h][4];
  const unsigned dst0 = (ti * 8 + wt * 4) * 16 * CH + fj * CH;
  CST(9);
#pragma unroll
  for (int m = 0; m < 4; ++m)
#pragma unroll
    for (int n = 0; n < 4; ++n) acc[m][n] = MFMA(cat(F.tY[n][0], F.tY[n][1]), F.wY[m], acc[m][n]);
#pragma unroll
  for (int m = 0; m < 4; ++m)
#pragma unroll
    for (int pp = 0; pp < 2; ++pp) {
      const u32x2 a = gate(acc[m][2 * pp], za[m][2 * pp]), b = gate(acc[m][2 * pp + 1], za[m][2 * pp + 1]);
      __builtin_amdgcn_raw_buffer_store_b128((u32x4){a.x, a.y, b.x, b.y}, rP, dst0 + m * 16 * CH + lane * 16 + (2 * WF + pp) * 1024, 0, 16);
    }
  SB();
  WL0();
  BAR(); MEMB();
#pragma unroll
  for (int h = 0; h < 2; ++h) {
    const f32x4 o8 = *(LAS f32x4*)(size_t)(lds0 + L_X + ((wt * 2 + 1 - WF) * 2 + h) * 1024 + lane * 16);
    __builtin_amdgcn_raw_buffer_store_b64(gate(acc[2 * WF + h][4] + o8, zb[h]), rP, dst0 + (2 * WF + h) * 16 * CH + lane * 8 + 4096, 0, 16);
  }
  CST(10);
  WV(0);
  CST(11);
  if (lane == 0 && __hip_atomic_fetch_add((LAS unsigned*)(size_t)(lds0 + L_CNT), 1u, __ATOMIC_RELAXED, __HIP_MEMORY_SCOPE_WORKGROUP) == 7u)
    __hip_atomic_store((u64*)(ws + WS_F2 + (ti * 16 + fj) * 128), tag, RLX_AGENT);
  SB(); BAR(); MEMB(); SB();
}

template <int NP> DEV unsigned pollN(const char* base, u64 tag, int lane) {
  const u64 v = __hip_atomic_load((const u64*)(base + min(lane, NP - 1) * 128), RLX_AGENT);
  return (unsigned)__builtin_amdgcn_ballot_w64(v == tag) & ((1u << NP) - 1);
}
DEV bool got(unsigned mask, int who) { return (mask >> who) & 1u; }

__global__ __launch_bounds__(512, 2) void gmlp_fused(const char* __restrict__ X, const char* __restrict__ Win, const char* __restrict__ Wsp, const u16* __restrict__ bias,
                                                     const char* __restrict__ Wo, char* __restrict__ out, char* __restrict__ ws) {
  extern __shared__ __attribute__((aligned(16))) char smem[];
  u64 pc0; asm volatile("s_getpc_b64 %0" : "=s"(pc0));
  const int tid = threadIdx.x, wid = __builtin_amdgcn_readfirstlane(tid >> 6), lane = tid & 63;
  const int bid = blockIdx.x, xg = bid & 7, rk = bid >> 3, il = rk & 7, jl = rk >> 3, ti = (xg & 1) * 8 + il, fj = (xg >> 1) * 4 + jl;
  const int L3 = xg * 30 + rk, tt = L3 / 9, dd = L3 - tt * 9;
  const bool has3 = rk < 30 && L3 < 234;
  const unsigned lds0 = (unsigned)(size_t)(LAS char*)smem;
  if (__builtin_expect(wid >= 4, 1)) {
    __builtin_amdgcn_s_setprio(3);
    const int lw = wid - 4;
    const bool big = wid < 6;
#define WVS(STAGES, EXTRA) do { if (big) WV(9 * (STAGES) + (EXTRA)); else WV(8 * (STAGES) + (EXTRA)); } while (0)
    auto rX = RSRC(X, 2048 * 1152); auto rWi = RSRC(Win, 576 * 9216);
    unsigned vX[4], vW[5];
    const unsigned zp0 = lw < 2 ? lw * 5 : 2 + lw * 4;
    const unsigned sX = (ti * 128 + lw * 32) * 1152, sW = fj * 288;
    const int rot = ROT1 == 0 ? 0 : ROT1 == 1 ? il + 2 * jl : 3 * ((il + jl) % 3);
    const int rot8 = ROT1 == 1 ? (il + 2 * jl) & 7 : ROT1 == 2 ? (((il + jl) % 3) * 8) / 3 : 0;
    auto k1 = [&](int k) { return k == 0 ? 0 : 1 + ((k - 1 + rot8) & 7); };
    auto issue1 = [&](int half, int k) {
      const int kk = k1(k);
      const unsigned d = lds0 + ((k + 1 - half) & 3) * STG, dz = d + 16384 + zp0 * 1024, sw = sW + half * 4608 + kk * 64 * 9216;
      GL4(rX, d + lw * 4096, vX[0], vX[1], vX[2], vX[3], sX + kk * 128);
      GL4(rWi, dz, vW[0], vW[1], vW[2], vW[3], sw);
      if (big) GL(rWi, dz + 4096, vW[4], sw, 0);
    };
    {
#pragma unroll
      for (int q = 0; q < 4; ++q) { const int row = 8 * q + (lane >> 3); vX[q] = row * 1152 + (((lane & 7) ^ ((row >> 1) & 7)) << 4) - q * 1024; }
      GL4(rX, lds0 + lw * 4096, vX[0], vX[1], vX[2], vX[3], sX + k1(0) * 128);
      SB();
#pragma unroll
      for (int q = 0; q < 5; ++q) {
        const unsigned c16 = (zp0 + q) * 64 + lane, R = (c16 * 3641u) >> 16, c = (c16 - R * 18) * 16, k = (R & 32) + 8 * ((R >> 2) & 3) + 4 * ((R >> 4) & 1) + (R & 3);
        vW[q] = k * 9216 + c - (q & 3) * 1024;
      }
      const unsigned dz = lds0 + 16384 + zp0 * 1024, sw = sW + 4608 + k1(0) * 64 * 9216;
      GL4(rWi, dz, vW[0], vW[1], vW[2], vW[3], sw);
      if (big) GL(rWi, dz + 4096, vW[4], sw, 0);
    }
    const unsigned vL = lane * 16;
    auto rWs = RSRC(Wsp, 2048 * 4096); auto rWo = RSRC(Wo, 2304 * 1152);
    const unsigned e9 = lw * 21 + lane, r9 = (e9 * 7282u) >> 16, v81 = lane < 21 && e9 < 81 ? r9 * 9216 + (e9 - r9 * 9) * 128 : OOB, s81 = (bid >> 2) * 9 * 9216 + (bid & 3) * 1152;
    issue1(1, 1);
    WVS(1, T0W ? 2 : 0); LBAR();
    issue1(1, 2);
    WVS(1, 0);
    for (int k = 1; k < 5; ++k) { LBAR(); issue1(1, k + 2); WVS(1, 0); }
    LBAR(); issue1(1, 7); WVS(1, 0);
    LBAR(); issue1(1, 8); WVS(1, 0);
    LBAR(); issue1(0, 0); WVS(1, 0);
    LBAR(); issue1(0, 1);
    const u64 ep = __hip_atomic_load((const u64*)(ws + WS_EP), RLX_AGENT);
    if (lw == 0) __builtin_amdgcn_raw_ptr_buffer_load_lds(RSRC(bias, 4096), (LAS void*)(size_t)(lds0 + L_BIAS), 4, lane * 4, ti * 256, 0, 0);
    TOUCH(rWs, lane * 128, bid * 32768 + lw * 8192);
    LBAR();
    issue1(0, 2); issue1(0, 3);
    const unsigned tagA = __builtin_amdgcn_readfirstlane((unsigned)ep) + 0x9E3779B9u, tagB = __builtin_amdgcn_readfirstlane((unsigned)(ep >> 32)) + 0x85EBCA6Bu;
    const u64 tag = ((u64)tagB << 32) | tagA;
    if (lw == 0) { const u32x4 tg = {tagA, tagB, 0u, 0u}; asm volatile("ds_write_b128 %0, %1\n\ts_waitcnt lgkmcnt(0)" :: "v"(lds0 + L_TAG), "v"(tg) : "memory"); }
    unsigned vS[4];
#pragma unroll
    for (int q = 0; q < 4; ++q) { const int row = 8 * q + (lane >> 3); vS[q] = row * 4096 + (((lane & 7) ^ ((row >> 1) & 7)) << 4) - q * 1024; }
    const unsigned sWs = (ti * 128 + lw * 32) * 4096;
    auto kpos = [&](int n) { return n; };
    auto issueW = [&](int n) { GL4(rWs, lds0 + (n & 3) * STG + lw * 4096, vS[0], vS[1], vS[2], vS[3], sWs + kpos(n) * 128); };
    WVS(2, 1);
    LBAR();
    auto rS = RSRC(ws + WS_ST, 16 * 16 * 128 * 16);
    if (lane < 32) {
      const int row = lw * 32 + lane;
      f32x4 v0, v1, v2, v3;
      asm volatile("ds_read_b128 %0, %4\n\tds_read_b128 %1, %4 offset:16\n\tds_read_b128 %2, %4 offset:32\n\tds_read_b128 %3, %4 offset:48\n\ts_waitcnt lgkmcnt(0)"
                   : "=&v"(v0), "=&v"(v1), "=&v"(v2), "=&v"(v3) : "v"(lds0 + L_T + row * 64) : "memory");
      const f32x4 s = (v0 + v1) + (v2 + v3);
      u32x4 gq; gq.x = tagA; gq.y = __float_as_uint(s[0] + s[2]); gq.z = tagB; gq.w = __float_as_uint(s[1] + s[3]);
      __builtin_amdgcn_raw_buffer_store_b128(gq, rS, ((ti * 16 + fj) * 128 + row) * 16, 0, 16);
    }
    u32x4 sv[8];
    const u32x4 srd = {(unsigned)(size_t)(ws + WS_ST), (unsigned)((size_t)(ws + WS_ST) >> 32) & 0xffffu, 16 * 16 * 128 * 16, 0x00020000u};
    auto sweep = [&]() {
      const unsigned s0 = (ti * 16 + lw * 4) * 2048, v1 = vL + 4096;
      asm volatile("buffer_load_dwordx4 %0, %8, %10, %11 offen sc1\n\tbuffer_load_dwordx4 %1, %8, %10, %11 offen offset:1024 sc1\n\tbuffer_load_dwordx4 %2, %8, %10, %11 offen offset:2048 sc1\n\t"
                   "buffer_load_dwordx4 %3, %8, %10, %11 offen offset:3072 sc1\n\tbuffer_load_dwordx4 %4, %9, %10, %11 offen sc1\n\tbuffer_load_dwordx4 %5, %9, %10, %11 offen offset:1024 sc1\n\t"
                   "buffer_load_dwordx4 %6, %9, %10, %11 offen offset:2048 sc1\n\tbuffer_load_dwordx4 %7, %9, %10, %11 offen offset:3072 sc1"
                   : "=&v"(sv[0]), "=&v"(sv[1]), "=&v"(sv[2]), "=&v"(sv[3]), "=&v"(sv[4]), "=&v"(sv[5]), "=&v"(sv[6]), "=&v"(sv[7]) : "v"(vL), "v"(v1), "s"(srd), "s"(s0) : "memory");
    };
    LBAR(); issue1(0, 4); WVS(2, 1);
    LBAR(); issue1(0, 5); WVS(2, 0);
    LBAR(); issue1(0, 6); WVS(2, 0);
    LBAR(); issue1(0, 7); WVS(2, 0);
    LBAR(); sweep(); WVS(1, 8);
    LBAR(); issue1(0, 8);
    WVS(1, 8);
    LBAR();
    {
      f32x4 s;
      auto total = [&](const u32x4 (&v)[8]) {
        unsigned bad = 0;
        s = (f32x4){0.f, 0.f, 0.f, 0.f};
#pragma unroll
        for (int p = 0; p < 8; ++p) { bad |= (v[p].x ^ tagA) | (v[p].z ^ tagB); s[(p & 1) * 2] += __uint_as_float(v[p].y); s[(p & 1) * 2 + 1] += __uint_as_float(v[p].w); }
        return __builtin_amdgcn_ballot_w64(bad != 0) == 0;
      };
      WVS(1, 0);
      asm volatile("" : "+v"(sv[0]), "+v"(sv[1]), "+v"(sv[2]), "+v"(sv[3]), "+v"(sv[4]), "+v"(sv[5]), "+v"(sv[6]), "+v"(sv[7]) :: "memory");
      if (__builtin_expect(!total(sv), 0)) {
#pragma unroll 1
        for (unsigned spins = 0;; ++spins) {
          u32x4 rv[8];
          MEMB();
#pragma unroll
          for (int p = 0; p < 8; ++p) rv[p] = __builtin_amdgcn_raw_buffer_load_b128(rS, vL + (p & 1) * 1024, (ti * 16 + lw * 4 + (p >> 1)) * 2048, 16);
          if (total(rv)) break;
          if (spins > SPIN_CAP) { s[0] = s[2] = __builtin_nanf(""); break; }
        }
      }
      const f32x2 o0 = {s[0], s[1]}, o1 = {s[2], s[3]};
      asm volatile("ds_write_b64 %0, %1\n\tds_write_b64 %0, %2 offset:2048\n\ts_waitcnt lgkmcnt(0)" :: "v"(lds0 + L_R + lane * 32 + lw * 8), "v"(o0), "v"(o1) : "memory");
    }
    LBAR();
    WV(0);
    LBAR();
    issueW(0); issueW(2);
    LBAR();
    issueW(1); issueW(3);
    TOUCH(rWo, lane < 21 ? lane * 128 : OOB, bid * 10368 + lw * 2688);
    auto rZ = RSRC(ws + WS_Z2, FRAG_BYTES);
    const unsigned sZ = fj * 128 * CH + zp0 * 1024;
    unsigned ready = 0;
    auto issueZ = [&](int n) {
      const int kp = kpos(n);
      if (__builtin_expect(!got(ready, kp >> 1), 0)) { for (unsigned spins = 0; !got(ready, kp >> 1) && spins < SPIN_CAP; ++spins) ready = pollN<16>(ws + WS_F1 + fj * 16 * 128, tag, lane); }
      MEMB();
      const unsigned dz = lds0 + (n & 3) * STG + 16384 + zp0 * 1024, so = sZ + kp * 18432;
      GS4(rZ, dz, vL, so);
      if (big) GS(rZ, dz + 4096, vL, so + 4096, 0);
    };
    const char* f1 = ws + WS_F1 + fj * 16 * 128;
    for (int q = 0; q < PDELAY; ++q) __builtin_amdgcn_s_sleep(4);
    { unsigned np = 0; for (; ready != 7u && np < SPIN_CAP; ++np) ready = pollN<3>(f1, tag, lane); LST(0, np); }
    issueZ(0); issueZ(1); issueZ(2);
    WVS(2, -8);
    LBAR();
    u64 fv;
    asm volatile("global_load_dwordx2 %0, %1, %2 sc1" : "=&v"(fv) : "v"((lane & 15) * 128), "s"(f1) : "memory");
    issueZ(3);
    WVS(2, -7);
    LBAR();
    issueW(4); issueZ(4);
    WVS(2, -3);
    LBAR();
    issueW(5); issueZ(5);
    WVS(2, 0);
    asm volatile("" : "+v"(fv));
    ready |= (unsigned)__builtin_amdgcn_ballot_w64(fv == tag) & 0xffffu;
    LBAR();
    for (int n = 6; n < 32; ++n) {
      issueW(n); issueZ(n);
      WVS(2, 0);
      LBAR();
    }
    unsigned vO[2], vOh;
#pragma unroll
    for (int q = 0; q < 2; ++q) { const int row = 36 * lw + 8 * q + (lane >> 3); vO[q] = row * 1152 + (((lane & 7) ^ (((row >> 1) & 1) | (row & 4))) << 4); }
    { const int row = 36 * lw + 32 + ((lane >> 3) & 3); vOh = row * 1152 + (((lane & 7) ^ (((row >> 1) & 1) | (row & 4))) << 4); }
    const unsigned vO1 = vO[1] - 1024, vO2 = vO[0] + 16 * 1152 - 2048, vO3 = vO[1] + 16 * 1152 - 3072;
    const unsigned sO = min(dd, 8) * 128;
    auto issueO = [&](int n, int slot) {
      const unsigned d = lds0 + L_O + slot * STGO + lw * 4608, s0 = sO + n * 144 * 1152;
      GL4(rWo, d, vO[0], vO1, vO2, vO3, s0);
      if (lane < 32) GL(rWo, d + 4096, vOh, s0, 0);
    };
    issueO(0, 0);
    WVS(1, 5); LBAR();
    issueO(1, 1); issueO(2, 2);
    WV(15); LBAR();
    LBAR();
    WV(0);
    LBAR();
    if (!has3) return;
    issueO(3, 3); issueO(4, 4);
    LBAR();
    issueO(5, 0);
    if (NPRE3 == 5) { issueO(6, 1); issueO(7, 2); }
    auto rP = RSRC(ws + WS_P, FRAG_BYTES);
    const unsigned vPs = lw < 3 ? (lane >> 5) * 16 * CH + (lane & 31) * 16 + 4096 : OOB;
    const int limS = lw == 2 ? 32 : 64;
    const unsigned sP = tt * 5 * 16 * CH;
    const int pa = (tt * 80) >> 7, pb = min((tt * 80 + 79) >> 7, 15);
    unsigned rdy = 0;
    auto issueP = [&](int j, int slot) {
      if (__builtin_expect(!got(rdy, j), 0)) { for (unsigned spins = 0; !got(rdy, j) && spins < SPIN_CAP; ++spins) rdy = pollN<16>(ws + WS_F2 + pa * 16 * 128, tag, lane) & pollN<16>(ws + WS_F2 + pb * 16 * 128, tag, lane); }
      MEMB();
      const unsigned d = lds0 + L_P + slot * STGP, sp = sP + j * CH;
      GS4(rP, d + lw * 4096, vL, sp + lw * 16 * CH);
      GS(rP, d + 16384 + lw * 1024, vL, sp + 64 * CH + lw * 1024, 0);
      if (lane < limS) GS(rP, lw < 3 ? d + 20480 + lw * 1024 : lds0 + L_DUMMY, vPs, sp + lw * 32 * CH, 0);
    };
    const char *fa = ws + WS_F2 + pa * 16 * 128, *fb = ws + WS_F2 + pb * 16 * 128;
    { unsigned np = 0; LST(2, 0); for (; rdy != 7u && np < SPIN_CAP; ++np) rdy = pollN<3>(fa, tag, lane) & pollN<3>(fb, tag, lane); LST(1, np); }
    u64 fva, fvb;
    asm volatile("global_load_dwordx2 %0, %2, %3 sc1\n\tglobal_load_dwordx2 %1, %2, %4 sc1" : "=&v"(fva), "=&v"(fvb) : "v"((lane & 15) * 128), "s"(fa), "s"(fb) : "memory");
    issueP(0, 0); issueP(1, 1); issueP(2, 2);
    WV(12);
    asm volatile("" : "+v"(fva), "+v"(fvb));
    rdy |= (unsigned)(__builtin_amdgcn_ballot_w64(fva == tag) & __builtin_amdgcn_ballot_w64(fvb == tag)) & 0xffffu;
    LBAR();
    WV(6);
    if (NPRE3 == 5) {
      LBAR(); issueP(3, 0); WV(6);
      LBAR(); issueP(4, 1); WV(6);
      LBAR(); issueP(5, 2); WV(6);
    } else {
      LBAR(); issueP(3, 0); issueO(6, 1); WV(11);
      LBAR(); issueP(4, 1); issueO(7, 2); WV(16);
      LBAR(); issueP(5, 2); WV(11);
    }
    LBAR(); issueP(6, 0); issueO(8, 3); WV(11);
    int sp3 = 1, so5 = 4;
    for (int n = 5; n < 12; ++n) {
      LBAR();
      issueP(n + 2, sp3); issueO(n + 4, so5);
      sp3 = sp3 == 2 ? 0 : sp3 + 1; so5 = so5 == 4 ? 0 : so5 + 1;
      WV(16);
    }
    LBAR(); issueP(14, 2); WV(6);
    LBAR(); issueP(15, 0); WV(6);
    LBAR(); WV(0);
    LBAR();
    LBAR(); LBAR();
    if (bid == 0 && tid == 256) __hip_atomic_store((u64*)(ws + WS_EP), tag, RLX_AGENT);
  } else {
    const int wt = wid >> 1, wf = wid & 1;
    if (wid == 0) {
      auto rC = RSRC((const char*)(pc0 & ~(u64)255), CODE_KB * 1024);
      GL(rC, lds0 + L_DUMMY, lane * 16, rk * 1024, 0); GL(rC, lds0 + L_DUMMY, lane * 16, rk * 1024 + 32768, 0);
      WV(0);
    }
    if (wf == 0) front<0>(lds0, wid, wt, lane, ti, fj, ws); else front<1>(lds0, wid, wt, lane, ti, fj, ws);
    if (!has3) return;
    if (wt == 0) g3_compute<0>(lds0, wf, lane, out, tt, dd); else g3_compute<1>(lds0, wf, lane, out, tt, dd);
  }
}

extern "C" void kernel_launch(void* const* d_in, const int* in_sizes, int n_in, void* d_out, int out_size, void* d_ws, size_t ws_size, hipStream_t stream) {
  (void)hipFuncSetAttribute((const void*)gmlp_fused, hipFuncAttributeMaxDynamicSharedMemorySize, LDS_BYTES);
  gmlp_fused<<<256, 512, LDS_BYTES, stream>>>((const char*)d_in[0], (const char*)d_in[1], (const char*)d_in[2], (const u16*)d_in[3], (const char*)d_in[4], (char*)d_out, (char*)d_ws);
}
```

```cpp
#include <hip/hip_runtime.h>
#include <hip/hip_bf16.h>
typedef unsigned short u16;
typedef unsigned long long u64;
typedef short s16x4 __attribute__((ext_vector_type(4)));
typedef short bf16x8 __attribute__((ext_vector_type(8)));
typedef float f32x2 __attribute__((ext_vector_type(2)));
typedef float f32x4 __attribute__((ext_vector_type(4)));
typedef unsigned u32x4 __attribute__((ext_vector_type(4)));
typedef unsigned u32x2 __attribute__((ext_vector_type(2)));
#define LAS __attribute__((address_space(3)))
#define DEV __device__ __forceinline__
#define BAR() __builtin_amdgcn_s_barrier()
#define SB() __builtin_amdgcn_sched_barrier(0)
#define MEMB() asm volatile("" ::: "memory")
#define WAITL0() __builtin_amdgcn_s_waitcnt(0xC07F)
#define WL0() asm volatile("s_waitcnt lgkmcnt(0)" ::: "memory")
#define WV(N) asm volatile("s_waitcnt vmcnt(%0)" ::"i"(N) : "memory")
#define MFMA(a, b, c) __builtin_amdgcn_mfma_f32_16x16x32_bf16(a, b, c, 0, 0, 0)
#define MFMA16(a, b, c) __builtin_amdgcn_mfma_f32_16x16x16bf16_1k(a, b, c, 0, 0, 0)
#define GL(R, D, V, S, I) __builtin_amdgcn_raw_ptr_buffer_load_lds(R, (LAS void*)(size_t)(D), 16, V, S, I, 0)
#define GL4(R, D, V0, V1, V2, V3, S) do { GL(R, D, V0, S, 0); GL(R, D, V1, S, 1024); GL(R, D, V2, S, 2048); GL(R, D, V3, S, 3072); } while (0)
#define GS(R, D, V, S, I) __builtin_amdgcn_raw_ptr_buffer_load_lds(R, (LAS void*)(size_t)(D), 16, V, S, I, 16)
#define GS4(R, D, V, S) do { GS(R, D, V, S, 0); GS(R, D, V, S, 1024); GS(R, D, V, S, 2048); GS(R, D, V, S, 3072); } while (0)
#define TOUCH(R, V, S) __builtin_amdgcn_raw_ptr_buffer_load_lds(R, (LAS void*)(size_t)(lds0 + L_DUMMY), 4, V, S, 0, 0)
#define RSRC(p, n) __builtin_amdgcn_make_buffer_rsrc((void*)(p), 0, (n), 0x00020000)
#define RLX_AGENT __ATOMIC_RELAXED, __HIP_MEMORY_SCOPE_AGENT
constexpr int CODE_KB = 51;

constexpr int CH = 4608;
constexpr int FRAG_BYTES = 16 * 128 * CH;
constexpr size_t WS_Z2 = 0, WS_P = FRAG_BYTES, WS_ST = 2 * (size_t)FRAG_BYTES, WS_F1 = WS_ST + 16 * 16 * 128 * 16, WS_F2 = WS_F1 + 256 * 128, WS_EP = WS_F2 + 256 * 128;
constexpr int STG = 34816, STGP = 23040, STGO = 18432, L_O = 0, L_P = 5 * STGO, LDS_BYTES = 163840;
constexpr int L_T = 139264, L_R = L_T + 8192, L_X = 151552, L_BIAS = 159744, L_TAG = L_BIAS + 256, L_CNT = L_TAG + 8, L_DUMMY = 161280;
constexpr unsigned SPIN_CAP = 1u << 16, OOB = 0x80000000u;

DEV unsigned cvt_pk(float lo, float hi) { unsigned r; asm("v_cvt_pk_bf16_f32 %0, %1, %2" : "=v"(r) : "v"(lo), "v"(hi)); return r; }
DEV bf16x8 ld128(unsigned a) { return *(LAS bf16x8*)(size_t)a; }
DEV s16x4 ld64(unsigned a) { return *(LAS s16x4*)(size_t)a; }
DEV s16x4 ldtr(unsigned a) { return __builtin_amdgcn_ds_read_tr16_b64_v4i16((LAS s16x4*)(size_t)a); }
DEV bf16x8 cat(s16x4 a, s16x4 b) { return __builtin_shufflevector(a, b, 0, 1, 2, 3, 4, 5, 6, 7); }
DEV float bflo(unsigned v) { return __uint_as_float(v << 16); }
DEV float bfhi(unsigned v) { return __uint_as_float(v & 0xffff0000u); }
template <int NB, bool SUM>
DEV void gelu_blocks(const f32x4* const (&v)[NB], u32x2* const (&o)[NB], f32x2& s1, f32x2& s2) {
  const float k1 = -2.0f * 0.7978845608028654f * 1.4426950408889634f, k2 = k1 * 0.044715f;
  f32x2 x[2 * NB], t[2 * NB];
#pragma unroll
  for (int i = 0; i < 2 * NB; ++i) { x[i] = (f32x2){(*v[i >> 1])[(i & 1) * 2], (*v[i >> 1])[(i & 1) * 2 + 1]}; t[i] = x[i] * x[i]; }
  SB();
#pragma unroll
  for (int i = 0; i < 2 * NB; ++i) t[i] = t[i] * k2 + k1;
  SB();
#pragma unroll
  for (int i = 0; i < 2 * NB; ++i) t[i] = t[i] * x[i];
  SB();
#pragma unroll
  for (int i = 0; i < 2 * NB; ++i) { t[i].x = __builtin_amdgcn_exp2f(t[i].x); t[i].y = __builtin_amdgcn_exp2f(t[i].y); }
  SB();
#pragma unroll
  for (int i = 0; i < 2 * NB; ++i) t[i] = t[i] + 1.0f;
  SB();
#pragma unroll
  for (int i = 0; i < 2 * NB; ++i) { t[i].x = __builtin_amdgcn_rcpf(t[i].x); t[i].y = __builtin_amdgcn_rcpf(t[i].y); }
  SB();
#pragma unroll
  for (int i = 0; i < 2 * NB; ++i) t[i] = t[i] * x[i];
  SB();
  f32x2 a1 = s1, a2 = s2, b1 = {0.f, 0.f}, b2 = {0.f, 0.f};
#pragma unroll
  for (int i = 0; i < NB; ++i) {
    o[i]->x = cvt_pk(t[2 * i].x, t[2 * i].y);
    if (SUM) { a1 += t[2 * i]; a2 += t[2 * i] * t[2 * i]; }
    o[i]->y = cvt_pk(t[2 * i + 1].x, t[2 * i + 1].y);
    if (SUM) { if (i == 0) { b1 = t[1]; b2 = t[1] * t[1]; } else { b1 += t[2 * i + 1]; b2 += t[2 * i + 1] * t[2 * i + 1]; } }
  }
  if (SUM) { s1 = a1 + b1; s2 = a2 + b2; }
  SB();
}
template <int WF, bool SUM>
DEV void gelu_row(int m, const f32x4 (&acc)[4][5], u32x2 (&oa)[4][4], u32x2 (&ob)[2], f32x2& s1, f32x2& s2) {
  if ((m >> 1) == WF) { const f32x4* const v[5] = {&acc[m][0], &acc[m][1], &acc[m][2], &acc[m][3], &acc[m][4]}; u32x2* const o[5] = {&oa[m][0], &oa[m][1], &oa[m][2], &oa[m][3], &ob[m & 1]}; gelu_blocks<5, SUM>(v, o, s1, s2); }
  else { const f32x4* const v[4] = {&acc[m][0], &acc[m][1], &acc[m][2], &acc[m][3]}; u32x2* const o[4] = {&oa[m][0], &oa[m][1], &oa[m][2], &oa[m][3]}; gelu_blocks<4, SUM>(v, o, s1, s2); }
}

struct TAddr { unsigned w[2], z[2][2], s[2]; };
struct TFrag { bf16x8 wX[4], wY[4]; s16x4 tX[5][2], tY[4][2]; };
template <int WF, int PS, int OFF, bool FIRST>
DEV void t_step(TAddr& A, int dlt, f32x4 (&acc)[4][5], TFrag& F, const f32x4* init = nullptr) {
  constexpr int KX = WF, KY = 1 - WF;
  SB(); BAR(); MEMB(); SB();
  auto rdz = [&](s16x4 (&t)[2], int n, int u, int K) { t[u] = n < 4 ? ldtr(A.z[u][n & 1] + OFF + K * 9216 + (n >> 1) * PS) : ldtr(A.s[u] + OFF + K * 9216); };
#pragma unroll
  for (int i = 0; i < 16; ++i) {
    const int m = i >> 2, n = i & 3;
    if (!FIRST) acc[m][n] = MFMA(cat(F.tY[n][0], F.tY[n][1]), F.wY[m], acc[m][n]);
    SB();
    if (i == 0) F.wX[0] = ld128(A.w[KX] + OFF);
    else if (i < 11) rdz(F.tX[(i - 1) >> 1], (i - 1) >> 1, (i - 1) & 1, KX);
    else if (i < 14) F.wX[i - 10] = ld128(A.w[KX] + OFF + (i - 10) * 2048);
    SB();
  }
#pragma unroll
  for (int i = 0; i < 20; ++i) {
    const int m = i / 5, n = i % 5;
    const f32x4 zero4 = {0, 0, 0, 0};
    acc[m][n] = MFMA(cat(F.tX[n][0], F.tX[n][1]), F.wX[m], !FIRST ? acc[m][n] : (PS == 1024 && (n < 4 || (m >> 1) == WF)) ? init[m] : zero4);
    SB();
    if (i == 0) F.wY[0] = ld128(A.w[KY] + OFF);
    else if (i < 9) rdz(F.tY[(i - 1) >> 1], (i - 1) >> 1, (i - 1) & 1, KY);
    else if (i < 12) F.wY[i - 8] = ld128(A.w[KY] + OFF + (i - 8) * 2048);
    else if (OFF != 0 && i < 16) {
      const int u = i & 1;
      if (i < 14) { A.w[u] += dlt; A.s[u] += dlt; asm volatile("" : "+v"(A.w[u]), "+v"(A.s[u])); }
      else { A.z[u][0] += dlt; A.z[u][1] += dlt; asm volatile("" : "+v"(A.z[u][0]), "+v"(A.z[u][1])); }
    }
    SB();
  }
  WAITL0();
}
template <int WF>
DEV void t_finish(unsigned lds0, int wt, int lane, f32x4 (&acc)[4][5], TFrag& F) {
#pragma unroll
  for (int h = 0; h < 2; ++h) *(LAS f32x4*)(size_t)(lds0 + L_X + ((wt * 2 + WF) * 2 + h) * 1024 + lane * 16) = acc[2 * (1 - WF) + h][4];
  SB();
#pragma unroll
  for (int m = 0; m < 4; ++m)
#pragma unroll
    for (int n = 0; n < 4; ++n) acc[m][n] = MFMA(cat(F.tY[n][0], F.tY[n][1]), F.wY[m], acc[m][n]);
  SB();
  WL0();
  BAR(); MEMB();
#pragma unroll
  for (int h = 0; h < 2; ++h) acc[2 * WF + h][4] += *(LAS f32x4*)(size_t)(lds0 + L_X + ((wt * 2 + 1 - WF) * 2 + h) * 1024 + lane * 16);
}
template <int WF, bool MID, class Mid>
DEV void in_pass(unsigned lds0, int wt, int lane, TAddr& A, f32x4 (&acc)[4][5], Mid mid) {
  TFrag F;
  int dlt = 2 * STG;
  WAITL0();
  if (!MID) {
    t_step<WF, 64, 0, true>(A, dlt, acc, F);
    for (int k = 0; k < 4; ++k) {
      t_step<WF, 64, STG, false>(A, dlt, acc, F);
      dlt = -dlt;
      t_step<WF, 64, 0, false>(A, dlt, acc, F);
    }
  } else {
    t_step<WF, 64, STG, true>(A, dlt, acc, F);
    for (int k = 0; k < 3; ++k) {
      dlt = -dlt;
      t_step<WF, 64, 0, false>(A, dlt, acc, F);
      t_step<WF, 64, STG, false>(A, dlt, acc, F);
    }
    t_step<WF, 64, 0, false>(A, 0, acc, F);
    SB(); BAR(); MEMB(); mid(); SB();
    t_step<WF, 64, STG, false>(A, 0, acc, F);
  }
  t_finish<WF>(lds0, wt, lane, acc, F);
}

struct G3P { bf16x8 p[2][5]; s16x4 ps[3]; };
struct G3W { s16x4 w[2][2][2], ws[2]; };
template <int KH>
DEV void g3_readw(G3W& n, unsigned a0, unsigned a1) {
#pragma unroll
  for (int q = 0; q < 8; ++q) n.w[q >> 2][(q >> 1) & 1][q & 1] = ldtr((((q >> 1) & 1) ? a1 : a0) + (2 * KH + (q >> 2)) * 4096 + (q & 1) * 2048);
  n.ws[0] = ldtr(a0 + 16384); n.ws[1] = ldtr(a1 + 16384);
}
template <int KH, bool MM, bool RDW>
DEV void g3_stage(f32x4 (&acc)[5][2], const G3P& cp, const G3W& cw, G3P& np, G3W& nw, unsigned aP, unsigned aS, unsigned a0, unsigned a1) {
  constexpr int M0 = KH ? 3 : 0, NS = KH ? 2 : 3, NM = 20 + 2 * NS, NR = 20 + NS;
  SB(); BAR(); MEMB(); SB();
#pragma unroll
  for (int i = 0; i < NM; ++i) {
    if (MM) {
      if (i >= 10 && i < 10 + 2 * NS) acc[M0 + ((i - 10) >> 1)][i & 1] = MFMA16(cw.ws[i & 1], cp.ps[(i - 10) >> 1], acc[M0 + ((i - 10) >> 1)][i & 1]);
      else { const int ii = i < 10 ? i : i - 2 * NS, P = ii / 10, mm = (ii % 10) >> 1, m = (KH != P) ? (mm + 3) % 5 : mm, cc = ii & 1; acc[m][cc] = MFMA(cat(cw.w[P][cc][0], cw.w[P][cc][1]), cp.p[P][m], acc[m][cc]); }
    }
    SB();
#pragma unroll
    for (int r = (i * 3) / 2; r < ((i + 1) * 3) / 2; ++r) {
      if (r < 18) { const int P = r / 9, q = r % 9;
        if (q < 4) { if (RDW) nw.w[P][q >> 1][q & 1] = ldtr(((q >> 1) ? a1 : a0) + (2 * KH + P) * 4096 + (q & 1) * 2048); }
        else np.p[P][q - 4] = ld128(aP + P * 1024 + (q - 4) * 4096); }
      else if (r < 20) { if (RDW) nw.ws[r - 18] = ldtr(((r - 18) ? a1 : a0) + 16384); }
      else if (r < NR) np.ps[r - 20] = ld64(aS + (M0 + r - 20) * 512);
    }
    SB();
  }
  WAITL0();
}
template <int KH>
DEV void g3_compute(unsigned lds0, int dh, int lane, char* out, int tt, int dd) {
  const int g = lane >> 4, qr = (lane >> 2) & 3, p = lane & 3;
  const int rr = 4 * g + qr;
  unsigned bW[2];
#pragma unroll
  for (int c = 0; c < 2; ++c) bW[c] = lds0 + L_O + rr * 128 + (((4 * dh + p) ^ (((rr >> 1) & 1) | (rr & 4))) << 4) + ((c ^ (p & 1)) * 8);
  const unsigned bP = lds0 + L_P + lane * 16 + KH * 2048, bS = lds0 + L_P + 20480 + lane * 8;
  G3W a0, a1, a2;
  g3_readw<KH>(a0, bW[0], bW[1]); g3_readw<KH>(a1, bW[0] + STGO, bW[1] + STGO); g3_readw<KH>(a2, bW[0] + 2 * STGO, bW[1] + 2 * STGO);
  f32x4 acc[5][2];
#pragma unroll
  for (int m = 0; m < 5; ++m) { acc[m][0] = (f32x4){0, 0, 0, 0}; acc[m][1] = (f32x4){0, 0, 0, 0}; }
  WAITL0();
  SB(); BAR(); SB();
  G3P p0, p1; G3W w0, w1;
  g3_stage<KH, false, false>(acc, p1, a0, p0, w0, bP, bS, 0, 0);
  g3_stage<KH, true, false>(acc, p0, a0, p1, w0, bP + STGP, bS + STGP, 0, 0);
  g3_stage<KH, true, false>(acc, p1, a1, p0, w0, bP + 2 * STGP, bS + 2 * STGP, 0, 0);
  g3_stage<KH, true, true>(acc, p0, a2, p1, w1, bP, bS, bW[0] + 3 * STGO, bW[1] + 3 * STGO);
  unsigned sp = STGP, sw = 4 * STGO;
  for (int j = 4; j < 16; j += 2) {
    g3_stage<KH, true, true>(acc, p1, w1, p0, w0, bP + sp, bS + sp, bW[0] + sw, bW[1] + sw);
    sp = sp == 2 * STGP ? 0 : sp + STGP; sw = sw == 4 * STGO ? 0 : sw + STGO;
    g3_stage<KH, true, true>(acc, p0, w0, p1, w1, bP + sp, bS + sp, bW[0] + sw, bW[1] + sw);
    sp = sp == 2 * STGP ? 0 : sp + STGP; sw = sw == 4 * STGO ? 0 : sw + STGO;
  }
  {
    constexpr int M0 = KH ? 3 : 0, NS = KH ? 2 : 3;
    SB(); BAR(); SB();
#pragma unroll
    for (int i = 0; i < 2 * NS; ++i) { acc[M0 + (i >> 1)][i & 1] = MFMA16(w1.ws[i & 1], p1.ps[i >> 1], acc[M0 + (i >> 1)][i & 1]); SB(); }
#pragma unroll
    for (int i = 0; i < 20; ++i) { const int P = (i >> 1) & 1, m = KH ? i >> 2 : 4 - (i >> 2), cc = i & 1; acc[m][cc] = MFMA(cat(w1.w[P][cc][0], w1.w[P][cc][1]), p1.p[P][m], acc[m][cc]); SB(); }
  }
#pragma unroll
  for (int m = 0; m < 5; ++m)
    if ((m >= 3) != KH) { *(LAS f32x4*)(size_t)(lds0 + (dh * 10 + m * 2) * 1024 + lane * 16) = acc[m][0]; *(LAS f32x4*)(size_t)(lds0 + (dh * 10 + m * 2 + 1) * 1024 + lane * 16) = acc[m][1]; }
  WL0();
  BAR(); MEMB();
  auto rO = RSRC(out, 2048 * 1152);
  const bool sw2 = g & 1;
#pragma unroll
  for (int m = 0; m < 5; ++m)
    if ((m >= 3) == KH) {
      const f32x4 a = acc[m][0] + *(LAS f32x4*)(size_t)(lds0 + (dh * 10 + m * 2) * 1024 + lane * 16), b = acc[m][1] + *(LAS f32x4*)(size_t)(lds0 + (dh * 10 + m * 2 + 1) * 1024 + lane * 16);
      const unsigned a0 = cvt_pk(a[0], a[1]), a1 = cvt_pk(a[2], a[3]), b0 = cvt_pk(b[0], b[1]), b1 = cvt_pk(b[2], b[3]);
      const u32x4 v = {sw2 ? b0 : a0, sw2 ? b1 : a1, sw2 ? a0 : b0, sw2 ? a1 : b1};
      __builtin_amdgcn_raw_buffer_store_b128(v, rO, (tt * 80 + m * 16 + (lane & 15)) * 1152 + dd * 128 + dh * 64 + g * 16, 0, 16);
    }
}

template <int WF>
DEV void front(unsigned lds0, int wt, int lane, int ti, int fj, char* ws) {
  const int r16 = lane & 15, g = lane >> 4, qr = (lane >> 2) & 3, p = lane & 3;
  TAddr A;
  A.w[0] = lds0 + (64 * wt + r16) * 128 + ((g ^ (r16 >> 1)) << 4); A.w[1] = A.w[0] ^ 64;
#pragma unroll
  for (int u = 0; u < 2; ++u) {
    const unsigned b = lds0 + 16384 + (16 * u + 4 * g + qr) * 288 + p * 8;
    A.z[u][0] = b + WF * 128 + (p & 1) * 32; A.z[u][1] = b + WF * 128 + 32 - (p & 1) * 32; A.s[u] = b + 256;
  }
  asm volatile("" : "+v"(A.w[0]), "+v"(A.w[1]), "+v"(A.s[0]), "+v"(A.s[1]), "+v"(A.z[0][0]), "+v"(A.z[0][1]), "+v"(A.z[1][0]), "+v"(A.z[1][1]));
  f32x4 acc[4][5];
  in_pass<WF, false>(lds0, wt, lane, A, acc, []() {});
  u32x2 ua[4][4], ub[2];
  {
    f32x2 s1[4], s2[4];
#pragma unroll
    for (int mm = 0; mm < 4; ++mm) {
      const int m = mm ^ (2 * (1 - WF));
      s1[m] = (f32x2){0.f, 0.f}; s2[m] = (f32x2){0.f, 0.f};
      gelu_row<WF, true>(m, acc, ua, ub, s1[m], s2[m]);
    }
#pragma unroll
    for (int m = 0; m < 4; ++m) *(LAS f32x2*)(size_t)(lds0 + L_T + (64 * wt + 16 * m + r16) * 64 + (WF * 4 + g) * 8) = (f32x2){s1[m].x + s1[m].y, s2[m].x + s2[m].y};
  }
#pragma unroll
  for (int m = 0; m < 4; ++m) asm volatile("" : "+v"(ua[m][0]), "+v"(ua[m][1]), "+v"(ua[m][2]), "+v"(ua[m][3]));
  asm volatile("" : "+v"(ub[0]), "+v"(ub[1]));
#pragma unroll
  for (int u = 0; u < 2; ++u) { A.z[u][0] -= (p & 1) * 32; A.z[u][1] += (p & 1) * 32; }
  asm volatile("" : "+v"(A.z[0][0]), "+v"(A.z[0][1]), "+v"(A.z[1][0]), "+v"(A.z[1][1]));
  in_pass<WF, true>(lds0, wt, lane, A, acc, [&]() {
#pragma unroll
    for (int m = 0; m < 4; ++m) asm volatile("" : "+v"(ua[m][0]), "+v"(ua[m][1]), "+v"(ua[m][2]), "+v"(ua[m][3]));
    asm volatile("" : "+v"(ub[0]), "+v"(ub[1]));
    int lz = lane; asm volatile("" : "+v"(lz));
    auto rZ = RSRC(ws + WS_Z2, FRAG_BYTES);
    const unsigned sl = (lz >> 4) * 16 + ((lz & 15) ^ (4 * (lz >> 5))), dst = (fj * 128 + ti * 8 + wt * 4) * CH;
#pragma unroll
    for (int m = 0; m < 4; ++m) {
      const unsigned ar = lds0 + L_R + (64 * wt + 16 * m + (lz & 15)) * 32;
      const f32x4 q4 = *(LAS f32x4*)(size_t)ar + *(LAS f32x4*)(size_t)(ar + 16);
      const float mean = (q4[0] + q4[2]) * (1.0f / 2304.0f), var = fmaxf((q4[1] + q4[3]) * (1.0f / 2304.0f) - mean * mean, 0.f);
      const float rs = __builtin_amdgcn_rsqf(var + 1e-5f), sh = -mean * rs;
      auto norm = [&](unsigned v) { const f32x2 y = (f32x2){bflo(v), bfhi(v)} * rs + sh; return cvt_pk(y.x, y.y); };
#pragma unroll
      for (int pp = 0; pp < 2; ++pp)
        __builtin_amdgcn_raw_buffer_store_b128((u32x4){norm(ua[m][2 * pp].x), norm(ua[m][2 * pp].y), norm(ua[m][2 * pp + 1].x), norm(ua[m][2 * pp + 1].y)}, rZ, dst + m * CH + sl * 16 + WF * 2048 + pp * 1024, 0, 16);
      if ((m >> 1) == WF) __builtin_amdgcn_raw_buffer_store_b64((u32x2){norm(ub[m & 1].x), norm(ub[m & 1].y)}, rZ, dst + m * CH + sl * 8 + 4096, 0, 16);
    }
  });
#pragma unroll
  for (int m = 0; m < 4; ++m) asm volatile("" : "+v"(acc[m][0]), "+v"(acc[m][1]), "+v"(acc[m][2]), "+v"(acc[m][3]), "+v"(acc[m][4]));
  const u32x2 tg = *(LAS u32x2*)(size_t)(lds0 + L_TAG);
  const u64 tag = ((u64)(unsigned)__builtin_amdgcn_readfirstlane(tg.y) << 32) | (unsigned)__builtin_amdgcn_readfirstlane(tg.x);
  WV(0);
  if (lane == 0 && __hip_atomic_fetch_add((LAS unsigned*)(size_t)(lds0 + L_CNT), 1u, __ATOMIC_RELAXED, __HIP_MEMORY_SCOPE_WORKGROUP) == 3u)
    __hip_atomic_store((u64*)(ws + WS_F1 + (fj * 16 + ti) * 128), tag, RLX_AGENT);
  SB();
  u32x2 za[4][4], zb[2];
#pragma unroll
  for (int m = 0; m < 4; ++m) {
    f32x2 d1, d2;
    gelu_row<WF, false>(m, acc, za, zb, d1, d2);
    asm volatile("" : "+v"(za[m][0]), "+v"(za[m][1]), "+v"(za[m][2]), "+v"(za[m][3]));
  }
  asm volatile("" : "+v"(zb[0]), "+v"(zb[1]));
#pragma unroll
  for (int u = 0; u < 2; ++u) {
    const unsigned sl = p * 16 + 8 * (g & 1) + 4 * (u ^ (p >> 1)) + qr;
#pragma unroll
    for (int h = 0; h < 2; ++h) A.z[u][h] = lds0 + 16384 + (g >> 1) * CH + WF * 2048 + sl * 16 + ((h ^ (p & 1)) * 8);
    A.s[u] = lds0 + 16384 + (g >> 1) * CH + 4096 + sl * 8;
  }
  asm volatile("" : "+v"(A.s[0]), "+v"(A.s[1]), "+v"(A.z[0][0]), "+v"(A.z[0][1]), "+v"(A.z[1][0]), "+v"(A.z[1][1]));
  f32x4 bias4[4];
#pragma unroll
  for (int m = 0; m < 4; ++m) { const float b = __uint_as_float((unsigned)*(LAS u16*)(size_t)(lds0 + L_BIAS + (wt * 64 + m * 16 + r16) * 2) << 16); bias4[m] = (f32x4){b, b, b, b}; }
  TFrag F;
  int dlt = 2 * STG;
  WAITL0();
  t_step<WF, 1024, 0, true>(A, dlt, acc, F, bias4);
  t_step<WF, 1024, STG, false>(A, dlt, acc, F);
  for (int k = 0; k < 15; ++k) {
    dlt = -dlt;
    t_step<WF, 1024, 0, false>(A, dlt, acc, F);
    t_step<WF, 1024, STG, false>(A, dlt, acc, F);
  }
  auto rP = RSRC(ws + WS_P, FRAG_BYTES);
  auto gate = [&](f32x4 a, u32x2 z) {
    const f32x2 x = (f32x2){a[0], a[1]} * (f32x2){bflo(z.x), bfhi(z.x)}, y = (f32x2){a[2], a[3]} * (f32x2){bflo(z.y), bfhi(z.y)};
    return (u32x2){cvt_pk(x.x, x.y), cvt_pk(y.x, y.y)};
  };
#pragma unroll
  for (int h = 0; h < 2; ++h) *(LAS f32x4*)(size_t)(lds0 + L_X + ((wt * 2 + WF) * 2 + h) * 1024 + lane * 16) = acc[2 * (1 - WF) + h][4];
  const unsigned dst0 = (ti * 8 + wt * 4) * 16 * CH + fj * CH;
#pragma unroll
  for (int m = 0; m < 4; ++m)
#pragma unroll
    for (int n = 0; n < 4; ++n) acc[m][n] = MFMA(cat(F.tY[n][0], F.tY[n][1]), F.wY[m], acc[m][n]);
#pragma unroll
  for (int m = 0; m < 4; ++m)
#pragma unroll
    for (int pp = 0; pp < 2; ++pp) {
      const u32x2 a = gate(acc[m][2 * pp], za[m][2 * pp]), b = gate(acc[m][2 * pp + 1], za[m][2 * pp + 1]);
      __builtin_amdgcn_raw_buffer_store_b128((u32x4){a.x, a.y, b.x, b.y}, rP, dst0 + m * 16 * CH + lane * 16 + (2 * WF + pp) * 1024, 0, 16);
    }
  SB();
  WL0();
  BAR(); MEMB();
#pragma unroll
  for (int h = 0; h < 2; ++h) {
    const f32x4 o8 = *(LAS f32x4*)(size_t)(lds0 + L_X + ((wt * 2 + 1 - WF) * 2 + h) * 1024 + lane * 16);
    __builtin_amdgcn_raw_buffer_store_b64(gate(acc[2 * WF + h][4] + o8, zb[h]), rP, dst0 + (2 * WF + h) * 16 * CH + lane * 8 + 4096, 0, 16);
  }
  WV(0);
  if (lane == 0 && __hip_atomic_fetch_add((LAS unsigned*)(size_t)(lds0 + L_CNT), 1u, __ATOMIC_RELAXED, __HIP_MEMORY_SCOPE_WORKGROUP) == 7u)
    __hip_atomic_store((u64*)(ws + WS_F2 + (ti * 16 + fj) * 128), tag, RLX_AGENT);
  SB(); BAR(); MEMB(); SB();
}

template <int NP> DEV unsigned pollN(const char* base, u64 tag, int lane) {
  const u64 v = __hip_atomic_load((const u64*)(base + min(lane, NP - 1) * 128), RLX_AGENT);
  return (unsigned)__builtin_amdgcn_ballot_w64(v == tag) & ((1u << NP) - 1);
}
DEV bool got(unsigned mask, int who) { return (mask >> who) & 1u; }

__global__ __launch_bounds__(512, 2) void gmlp_fused(const char* __restrict__ X, const char* __restrict__ Win, const char* __restrict__ Wsp, const u16* __restrict__ bias,
                                                     const char* __restrict__ Wo, char* __restrict__ out, char* __restrict__ ws) {
  extern __shared__ __attribute__((aligned(16))) char smem[];
  u64 pc0; asm volatile("s_getpc_b64 %0" : "=s"(pc0));
  const int tid = threadIdx.x, wid = __builtin_amdgcn_readfirstlane(tid >> 6), lane = tid & 63;
  const int bid = blockIdx.x, xg = bid & 7, rk = bid >> 3, il = rk & 7, jl = rk >> 3, ti = (xg & 1) * 8 + il, fj = (xg >> 1) * 4 + jl;
  const int L3 = xg * 30 + rk, tt = L3 / 9, dd = L3 - tt * 9;
  const bool has3 = rk < 30 && L3 < 234;
  const unsigned lds0 = (unsigned)(size_t)(LAS char*)smem;
  if (__builtin_expect(wid >= 4, 1)) {
    __builtin_amdgcn_s_setprio(3);
    const int lw = wid - 4;
    const bool big = wid < 6;
#define WVS(STAGES, EXTRA) do { if (big) WV(9 * (STAGES) + (EXTRA)); else WV(8 * (STAGES) + (EXTRA)); } while (0)
    auto rX = RSRC(X, 2048 * 1152); auto rWi = RSRC(Win, 576 * 9216);
    unsigned vX[4], vW[5];
    const unsigned zp0 = lw < 2 ? lw * 5 : 2 + lw * 4;
    const unsigned sX = (ti * 128 + lw * 32) * 1152, sW = fj * 288;
    const int rot8 = (((il + jl) % 3) * 8) / 3;
    auto k1 = [&](int k) { return k == 0 ? 0 : 1 + ((k - 1 + rot8) & 7); };
    auto issue1 = [&](int half, int k) {
      const int kk = k1(k);
      const unsigned d = lds0 + ((k + 1 - half) & 3) * STG, dz = d + 16384 + zp0 * 1024, sw = sW + half * 4608 + kk * 64 * 9216;
      GL4(rX, d + lw * 4096, vX[0], vX[1], vX[2], vX[3], sX + kk * 128);
      GL4(rWi, dz, vW[0], vW[1], vW[2], vW[3], sw);
      if (big) GL(rWi, dz + 4096, vW[4], sw, 0);
    };
    {
#pragma unroll
      for (int q = 0; q < 4; ++q) { const int row = 8 * q + (lane >> 3); vX[q] = row * 1152 + (((lane & 7) ^ ((row >> 1) & 7)) << 4) - q * 1024; }
      GL4(rX, lds0 + lw * 4096, vX[0], vX[1], vX[2], vX[3], sX + k1(0) * 128);
      SB();
#pragma unroll
      for (int q = 0; q < 5; ++q) {
        const unsigned c16 = (zp0 + q) * 64 + lane, R = (c16 * 3641u) >> 16, c = (c16 - R * 18) * 16, k = (R & 32) + 8 * ((R >> 2) & 3) + 4 * ((R >> 4) & 1) + (R & 3);
        vW[q] = k * 9216 + c - (q & 3) * 1024;
      }
      const unsigned dz = lds0 + 16384 + zp0 * 1024, sw = sW + 4608 + k1(0) * 64 * 9216;
      GL4(rWi, dz, vW[0], vW[1], vW[2], vW[3], sw);
      if (big) GL(rWi, dz + 4096, vW[4], sw, 0);
    }
    const unsigned vL = lane * 16;
    auto rWs = RSRC(Wsp, 2048 * 4096); auto rWo = RSRC(Wo, 2304 * 1152);
    issue1(1, 1);
    WVS(1, 0); BAR();
    issue1(1, 2);
    WVS(1, 0);
    for (int k = 1; k < 5; ++k) { BAR(); issue1(1, k + 2); WVS(1, 0); }
    BAR(); issue1(1, 7); WVS(1, 0);
    BAR(); issue1(1, 8); WVS(1, 0);
    BAR(); issue1(0, 0); WVS(1, 0);
    BAR(); issue1(0, 1);
    const u64 ep = __hip_atomic_load((const u64*)(ws + WS_EP), RLX_AGENT);
    if (lw == 0) __builtin_amdgcn_raw_ptr_buffer_load_lds(RSRC(bias, 4096), (LAS void*)(size_t)(lds0 + L_BIAS), 4, lane * 4, ti * 256, 0, 0);
    TOUCH(rWs, lane * 128, bid * 32768 + lw * 8192);
    BAR();
    issue1(0, 2); issue1(0, 3);
    const unsigned tagA = __builtin_amdgcn_readfirstlane((unsigned)ep) + 0x9E3779B9u, tagB = __builtin_amdgcn_readfirstlane((unsigned)(ep >> 32)) + 0x85EBCA6Bu;
    const u64 tag = ((u64)tagB << 32) | tagA;
    if (lw == 0) { const u32x4 tg = {tagA, tagB, 0u, 0u}; asm volatile("ds_write_b128 %0, %1\n\ts_waitcnt lgkmcnt(0)" :: "v"(lds0 + L_TAG), "v"(tg) : "memory"); }
    unsigned vS[4];
#pragma unroll
    for (int q = 0; q < 4; ++q) { const int row = 8 * q + (lane >> 3); vS[q] = row * 4096 + (((lane & 7) ^ ((row >> 1) & 7)) << 4) - q * 1024; }
    const unsigned sWs = (ti * 128 + lw * 32) * 4096;
    auto issueW = [&](int n) { GL4(rWs, lds0 + (n & 3) * STG + lw * 4096, vS[0], vS[1], vS[2], vS[3], sWs + n * 128); };
    WVS(2, 1);
    BAR();
    auto rS = RSRC(ws + WS_ST, 16 * 16 * 128 * 16);
    if (lane < 32) {
      const int row = lw * 32 + lane;
      f32x4 v0, v1, v2, v3;
      asm volatile("ds_read_b128 %0, %4\n\tds_read_b128 %1, %4 offset:16\n\tds_read_b128 %2, %4 offset:32\n\tds_read_b128 %3, %4 offset:48\n\ts_waitcnt lgkmcnt(0)"
                   : "=&v"(v0), "=&v"(v1), "=&v"(v2), "=&v"(v3) : "v"(lds0 + L_T + row * 64) : "memory");
      const f32x4 s = (v0 + v1) + (v2 + v3);
      u32x4 gq; gq.x = tagA; gq.y = __float_as_uint(s[0] + s[2]); gq.z = tagB; gq.w = __float_as_uint(s[1] + s[3]);
      __builtin_amdgcn_raw_buffer_store_b128(gq, rS, ((ti * 16 + fj) * 128 + row) * 16, 0, 16);
    }
    u32x4 sv[8];
    const u32x4 srd = {(unsigned)(size_t)(ws + WS_ST), (unsigned)((size_t)(ws + WS_ST) >> 32) & 0xffffu, 16 * 16 * 128 * 16, 0x00020000u};
    auto sweep = [&]() {
      const unsigned s0 = (ti * 16 + lw * 4) * 2048, v1 = vL + 4096;
      asm volatile("buffer_load_dwordx4 %0, %8, %10, %11 offen sc1\n\tbuffer_load_dwordx4 %1, %8, %10, %11 offen offset:1024 sc1\n\tbuffer_load_dwordx4 %2, %8, %10, %11 offen offset:2048 sc1\n\t"
                   "buffer_load_dwordx4 %3, %8, %10, %11 offen offset:3072 sc1\n\tbuffer_load_dwordx4 %4, %9, %10, %11 offen sc1\n\tbuffer_load_dwordx4 %5, %9, %10, %11 offen offset:1024 sc1\n\t"
                   "buffer_load_dwordx4 %6, %9, %10, %11 offen offset:2048 sc1\n\tbuffer_load_dwordx4 %7, %9, %10, %11 offen offset:3072 sc1"
                   : "=&v"(sv[0]), "=&v"(sv[1]), "=&v"(sv[2]), "=&v"(sv[3]), "=&v"(sv[4]), "=&v"(sv[5]), "=&v"(sv[6]), "=&v"(sv[7]) : "v"(vL), "v"(v1), "s"(srd), "s"(s0) : "memory");
    };
    BAR(); issue1(0, 4); WVS(2, 1);
    BAR(); issue1(0, 5); WVS(2, 0);
    BAR(); issue1(0, 6); WVS(2, 0);
    BAR(); issue1(0, 7); WVS(2, 0);
    BAR(); sweep(); WVS(1, 8);
    BAR(); issue1(0, 8);
    WVS(1, 8);
    BAR();
    {
      f32x4 s;
      auto total = [&](const u32x4 (&v)[8]) {
        unsigned bad = 0;
        s = (f32x4){0.f, 0.f, 0.f, 0.f};
#pragma unroll
        for (int p = 0; p < 8; ++p) { bad |= (v[p].x ^ tagA) | (v[p].z ^ tagB); s[(p & 1) * 2] += __uint_as_float(v[p].y); s[(p & 1) * 2 + 1] += __uint_as_float(v[p].w); }
        return __builtin_amdgcn_ballot_w64(bad != 0) == 0;
      };
      WVS(1, 0);
      asm volatile("" : "+v"(sv[0]), "+v"(sv[1]), "+v"(sv[2]), "+v"(sv[3]), "+v"(sv[4]), "+v"(sv[5]), "+v"(sv[6]), "+v"(sv[7]) :: "memory");
      if (__builtin_expect(!total(sv), 0)) {
#pragma unroll 1
        for (unsigned spins = 0;; ++spins) {
          u32x4 rv[8];
          MEMB();
#pragma unroll
          for (int p = 0; p < 8; ++p) rv[p] = __builtin_amdgcn_raw_buffer_load_b128(rS, vL + (p & 1) * 1024, (ti * 16 + lw * 4 + (p >> 1)) * 2048, 16);
          if (total(rv)) break;
          if (spins > SPIN_CAP) { s[0] = s[2] = __builtin_nanf(""); break; }
        }
      }
      const f32x2 o0 = {s[0], s[1]}, o1 = {s[2], s[3]};
      asm volatile("ds_write_b64 %0, %1\n\tds_write_b64 %0, %2 offset:2048\n\ts_waitcnt lgkmcnt(0)" :: "v"(lds0 + L_R + lane * 32 + lw * 8), "v"(o0), "v"(o1) : "memory");
    }
    BAR();
    WV(0);
    BAR();
    issueW(0); issueW(2);
    BAR();
    issueW(1); issueW(3);
    TOUCH(rWo, lane < 21 ? lane * 128 : OOB, bid * 10368 + lw * 2688);
    auto rZ = RSRC(ws + WS_Z2, FRAG_BYTES);
    const unsigned sZ = fj * 128 * CH + zp0 * 1024;
    unsigned ready = 0;
    auto issueZ = [&](int n) {
      if (__builtin_expect(!got(ready, n >> 1), 0)) { for (unsigned spins = 0; !got(ready, n >> 1) && spins < SPIN_CAP; ++spins) ready = pollN<16>(ws + WS_F1 + fj * 16 * 128, tag, lane); }
      MEMB();
      const unsigned dz = lds0 + (n & 3) * STG + 16384 + zp0 * 1024, so = sZ + n * 18432;
      GS4(rZ, dz, vL, so);
      if (big) GS(rZ, dz + 4096, vL, so + 4096, 0);
    };
    const char* f1 = ws + WS_F1 + fj * 16 * 128;
    for (int q = 0; q < 7; ++q) __builtin_amdgcn_s_sleep(4);
    for (unsigned np = 0; ready != 7u && np < SPIN_CAP; ++np) ready = pollN<3>(f1, tag, lane);
    issueZ(0); issueZ(1); issueZ(2);
    WVS(2, -8);
    BAR();
    u64 fv;
    asm volatile("global_load_dwordx2 %0, %1, %2 sc1" : "=&v"(fv) : "v"((lane & 15) * 128), "s"(f1) : "memory");
    issueZ(3);
    WVS(2, -7);
    BAR();
    issueW(4); issueZ(4);
    WVS(2, -3);
    BAR();
    issueW(5); issueZ(5);
    WVS(2, 0);
    asm volatile("" : "+v"(fv));
    ready |= (unsigned)__builtin_amdgcn_ballot_w64(fv == tag) & 0xffffu;
    BAR();
    for (int n = 6; n < 32; ++n) {
      issueW(n); issueZ(n);
      WVS(2, 0);
      BAR();
    }
    unsigned vO[2], vOh;
#pragma unroll
    for (int q = 0; q < 2; ++q) { const int row = 36 * lw + 8 * q + (lane >> 3); vO[q] = row * 1152 + (((lane & 7) ^ (((row >> 1) & 1) | (row & 4))) << 4); }
    { const int row = 36 * lw + 32 + ((lane >> 3) & 3); vOh = row * 1152 + (((lane & 7) ^ (((row >> 1) & 1) | (row & 4))) << 4); }
    const unsigned vO1 = vO[1] - 1024, vO2 = vO[0] + 16 * 1152 - 2048, vO3 = vO[1] + 16 * 1152 - 3072;
    const unsigned sO = min(dd, 8) * 128;
    auto issueO = [&](int n, int slot) {
      const unsigned d = lds0 + L_O + slot * STGO + lw * 4608, s0 = sO + n * 144 * 1152;
      GL4(rWo, d, vO[0], vO1, vO2, vO3, s0);
      if (lane < 32) GL(rWo, d + 4096, vOh, s0, 0);
    };
    issueO(0, 0);
    WVS(1, 5); BAR();
    issueO(1, 1); issueO(2, 2);
    WV(15); BAR();
    BAR();
    WV(0);
    BAR();
    if (!has3) return;
    issueO(3, 3); issueO(4, 4);
    BAR();
    issueO(5, 0);
    auto rP = RSRC(ws + WS_P, FRAG_BYTES);
    const unsigned vPs = lw < 3 ? (lane >> 5) * 16 * CH + (lane & 31) * 16 + 4096 : OOB;
    const int limS = lw == 2 ? 32 : 64;
    const unsigned sP = tt * 5 * 16 * CH;
    const int pa = (tt * 80) >> 7, pb = min((tt * 80 + 79) >> 7, 15);
    unsigned rdy = 0;
    auto issueP = [&](int j, int slot) {
      if (__builtin_expect(!got(rdy, j), 0)) { for (unsigned spins = 0; !got(rdy, j) && spins < SPIN_CAP; ++spins) rdy = pollN<16>(ws + WS_F2 + pa * 16 * 128, tag, lane) & pollN<16>(ws + WS_F2 + pb * 16 * 128, tag, lane); }
      MEMB();
      const unsigned d = lds0 + L_P + slot * STGP, sp = sP + j * CH;
      GS4(rP, d + lw * 4096, vL, sp + lw * 16 * CH);
      GS(rP, d + 16384 + lw * 1024, vL, sp + 64 * CH + lw * 1024, 0);
      if (lane < limS) GS(rP, lw < 3 ? d + 20480 + lw * 1024 : lds0 + L_DUMMY, vPs, sp + lw * 32 * CH, 0);
    };
    const char *fa = ws + WS_F2 + pa * 16 * 128, *fb = ws + WS_F2 + pb * 16 * 128;
    for (unsigned np = 0; rdy != 7u && np < SPIN_CAP; ++np) rdy = pollN<3>(fa, tag, lane) & pollN<3>(fb, tag, lane);
    u64 fva, fvb;
    asm volatile("global_load_dwordx2 %0, %2, %3 sc1\n\tglobal_load_dwordx2 %1, %2, %4 sc1" : "=&v"(fva), "=&v"(fvb) : "v"((lane & 15) * 128), "s"(fa), "s"(fb) : "memory");
    issueP(0, 0); issueP(1, 1); issueP(2, 2);
    WV(12);
    asm volatile("" : "+v"(fva), "+v"(fvb));
    rdy |= (unsigned)(__builtin_amdgcn_ballot_w64(fva == tag) & __builtin_amdgcn_ballot_w64(fvb == tag)) & 0xffffu;
    BAR();
    WV(6);
    BAR(); issueP(3, 0); issueO(6, 1); WV(11);
    BAR(); issueP(4, 1); issueO(7, 2); WV(16);
    BAR(); issueP(5, 2); WV(11);
    BAR(); issueP(6, 0); issueO(8, 3); WV(11);
    int sp3 = 1, so5 = 4;
    for (int n = 5; n < 12; ++n) {
      BAR();
      issueP(n + 2, sp3); issueO(n + 4, so5);
      sp3 = sp3 == 2 ? 0 : sp3 + 1; so5 = so5 == 4 ? 0 : so5 + 1;
      WV(16);
    }
    BAR(); issueP(14, 2); WV(6);
    BAR(); issueP(15, 0); WV(6);
    BAR(); WV(0);
    BAR();
    BAR(); BAR();
    if (bid == 0 && tid == 256) __hip_atomic_store((u64*)(ws + WS_EP), tag, RLX_AGENT);
  } else {
    const int wt = wid >> 1, wf = wid & 1;
    if (wid == 0) {
      auto rC = RSRC((const char*)(pc0 & ~(u64)255), CODE_KB * 1024);
      GL(rC, lds0 + L_DUMMY, lane * 16, rk * 1024, 0); GL(rC, lds0 + L_DUMMY, lane * 16, rk * 1024 + 32768, 0);
      WV(0);
    }
    if (wf == 0) front<0>(lds0, wt, lane, ti, fj, ws); else front<1>(lds0, wt, lane, ti, fj, ws);
    if (!has3) return;
    if (wt == 0) g3_compute<0>(lds0, wf, lane, out, tt, dd); else g3_compute<1>(lds0, wf, lane, out, tt, dd);
  }
}

extern "C" void kernel_launch(void* const* d_in, const int* in_sizes, int n_in, void* d_out, int out_size, void* d_ws, size_t ws_size, hipStream_t stream) {
  (void)hipFuncSetAttribute((const void*)gmlp_fused, hipFuncAttributeMaxDynamicSharedMemorySize, LDS_BYTES);
  gmlp_fused<<<256, 512, LDS_BYTES, stream>>>((const char*)d_in[0], (const char*)d_in[1], (const char*)d_in[2], (const u16*)d_in[3], (const char*)d_in[4], (char*)d_out, (char*)d_ws);
}
```

```cpp
#include <hip/hip_runtime.h>
#include <hip/hip_bf16.h>
typedef unsigned short u16;
typedef unsigned long long u64;
typedef short s16x4 __attribute__((ext_vector_type(4)));
typedef short bf16x8 __attribute__((ext_vector_type(8)));
typedef float f32x2 __attribute__((ext_vector_type(2)));
typedef float f32x4 __attribute__((ext_vector_type(4)));
typedef unsigned u32x4 __attribute__((ext_vector_type(4)));
typedef unsigned u32x2 __attribute__((ext_vector_type(2)));
#define LAS __attribute__((address_space(3)))
#define DEV __device__ __forceinline__
#define BAR() __builtin_amdgcn_s_barrier()
#define SB() __builtin_amdgcn_sched_barrier(0)
#define MEMB() asm volatile("" ::: "memory")
#define WAITL0() __builtin_amdgcn_s_waitcnt(0xC07F)
#define WL0() asm volatile("s_waitcnt lgkmcnt(0)" ::: "memory")
#define WV(N) asm volatile("s_waitcnt vmcnt(%0)" ::"i"(N) : "memory")
#define MFMA(a, b, c) __builtin_amdgcn_mfma_f32_16x16x32_bf16(a, b, c, 0, 0, 0)
#define MFMA16(a, b, c) __builtin_amdgcn_mfma_f32_16x16x16bf16_1k(a, b, c, 0, 0, 0)
#define GL(R, D, V, S, I) __builtin_amdgcn_raw_ptr_buffer_load_lds(R, (LAS void*)(size_t)(D), 16, V, S, I, 0)
#define GL4(R, D, V0, V1, V2, V3, S) do { GL(R, D, V0, S, 0); GL(R, D, V1, S, 1024); GL(R, D, V2, S, 2048); GL(R, D, V3, S, 3072); } while (0)
#define GS(R, D, V, S, I) __builtin_amdgcn_raw_ptr_buffer_load_lds(R, (LAS void*)(size_t)(D), 16, V, S, I, 16)
#define GS4(R, D, V, S) do { GS(R, D, V, S, 0); GS(R, D, V, S, 1024); GS(R, D, V, S, 2048); GS(R, D, V, S, 3072); } while (0)
#define TOUCH(R, V, S) __builtin_amdgcn_raw_ptr_buffer_load_lds(R, (LAS void*)(size_t)(lds0 + L_DUMMY), 4, V, S, 0, 0)
#define RSRC(p, n) __builtin_amdgcn_make_buffer_rsrc((void*)(p), 0, (n), 0x00020000)
#define RLX_AGENT __ATOMIC_RELAXED, __HIP_MEMORY_SCOPE_AGENT
#define ROT1 2
#define NPRE3 3
#define Z1W 0
#define LS0 1
#define OAUX 16
#define WAUX 0
#define T0W 0
#define PDELAY 7
#define PSLEEP 0
#define CODE_KB 48

constexpr int CH = 4608;
constexpr int FRAG_BYTES = 16 * 128 * CH;
constexpr size_t WS_Z2 = 0, WS_P = FRAG_BYTES, WS_ST = 2 * (size_t)FRAG_BYTES, WS_F1 = WS_ST + 16 * 16 * 128 * 16, WS_F2 = WS_F1 + 256 * 128, WS_EP = WS_F2 + 256 * 128;
constexpr int STG = 34816, STGP = 23040, STGO = 18432, L_O = 0, L_P = 5 * STGO, LDS_BYTES = 163840;
constexpr int L_T = 139264, L_R = L_T + 8192, L_X = 151552, L_BIAS = 159744, L_TAG = L_BIAS + 256, L_CNT = L_TAG + 8, L_DUMMY = 161280;
constexpr unsigned SPIN_CAP = 1u << 16, OOB = 0x80000000u;
#define LBAR() BAR()
#define CST(I)
#define LST(I, X)

DEV unsigned cvt_pk(float lo, float hi) { unsigned r; asm("v_cvt_pk_bf16_f32 %0, %1, %2" : "=v"(r) : "v"(lo), "v"(hi)); return r; }
DEV bf16x8 ld128(unsigned a) { return *(LAS bf16x8*)(size_t)a; }
DEV s16x4 ld64(unsigned a) { return *(LAS s16x4*)(size_t)a; }
DEV s16x4 ldtr(unsigned a) { return __builtin_amdgcn_ds_read_tr16_b64_v4i16((LAS s16x4*)(size_t)a); }
DEV bf16x8 cat(s16x4 a, s16x4 b) { return __builtin_shufflevector(a, b, 0, 1, 2, 3, 4, 5, 6, 7); }
DEV float bflo(unsigned v) { return __uint_as_float(v << 16); }
DEV float bfhi(unsigned v) { return __uint_as_float(v & 0xffff0000u); }
template <int NB, bool SUM>
DEV void gelu_blocks(const f32x4* const (&v)[NB], u32x2* const (&o)[NB], f32x2& s1, f32x2& s2) {
  const float k1 = -2.0f * 0.7978845608028654f * 1.4426950408889634f, k2 = k1 * 0.044715f;
  f32x2 x[2 * NB], t[2 * NB];
#pragma unroll
  for (int i = 0; i < 2 * NB; ++i) { x[i] = (f32x2){(*v[i >> 1])[(i & 1) * 2], (*v[i >> 1])[(i & 1) * 2 + 1]}; t[i] = x[i] * x[i]; }
  SB();
#pragma unroll
  for (int i = 0; i < 2 * NB; ++i) t[i] = t[i] * k2 + k1;
  SB();
#pragma unroll
  for (int i = 0; i < 2 * NB; ++i) t[i] = t[i] * x[i];
  SB();
#pragma unroll
  for (int i = 0; i < 2 * NB; ++i) { t[i].x = __builtin_amdgcn_exp2f(t[i].x); t[i].y = __builtin_amdgcn_exp2f(t[i].y); }
  SB();
#pragma unroll
  for (int i = 0; i < 2 * NB; ++i) t[i] = t[i] + 1.0f;
  SB();
#pragma unroll
  for (int i = 0; i < 2 * NB; ++i) { t[i].x = __builtin_amdgcn_rcpf(t[i].x); t[i].y = __builtin_amdgcn_rcpf(t[i].y); }
  SB();
#pragma unroll
  for (int i = 0; i < 2 * NB; ++i) t[i] = t[i] * x[i];
  SB();
  f32x2 a1 = s1, a2 = s2, b1 = {0.f, 0.f}, b2 = {0.f, 0.f};
#pragma unroll
  for (int i = 0; i < NB; ++i) {
    o[i]->x = cvt_pk(t[2 * i].x, t[2 * i].y);
    if (SUM) { a1 += t[2 * i]; a2 += t[2 * i] * t[2 * i]; }
    o[i]->y = cvt_pk(t[2 * i + 1].x, t[2 * i + 1].y);
    if (SUM) { if (i == 0) { b1 = t[1]; b2 = t[1] * t[1]; } else { b1 += t[2 * i + 1]; b2 += t[2 * i + 1] * t[2 * i + 1]; } }
  }
  if (SUM) { s1 = a1 + b1; s2 = a2 + b2; }
  SB();
}
template <int WF, bool SUM>
DEV void gelu_row(int m, const f32x4 (&acc)[4][5], u32x2 (&oa)[4][4], u32x2 (&ob)[2], f32x2& s1, f32x2& s2) {
  if ((m >> 1) == WF) { const f32x4* const v[5] = {&acc[m][0], &acc[m][1], &acc[m][2], &acc[m][3], &acc[m][4]}; u32x2* const o[5] = {&oa[m][0], &oa[m][1], &oa[m][2], &oa[m][3], &ob[m & 1]}; gelu_blocks<5, SUM>(v, o, s1, s2); }
  else { const f32x4* const v[4] = {&acc[m][0], &acc[m][1], &acc[m][2], &acc[m][3]}; u32x2* const o[4] = {&oa[m][0], &oa[m][1], &oa[m][2], &oa[m][3]}; gelu_blocks<4, SUM>(v, o, s1, s2); }
}

struct TAddr { unsigned w[2], z[2][2], s[2]; };
struct TSet { bf16x8 w[4]; s16x4 t[5][2]; };
struct TFrag { TSet X, Y; };
template <int WF, int PS, int IMM, bool MM, bool FIRST, bool MOVE>
DEV void t_half(TAddr& A, unsigned aw, int dlt, f32x4 (&acc)[4][5], const TSet& c, TSet& n, const f32x4* init) {
#pragma unroll
  for (int i = 0; i < 18; ++i) {
    const int m = i < 8 ? (2 * (1 - WF) + (i >> 2)) : 2 * WF + (i - 8) / 5, nb = i < 8 ? (i & 3) : (i - 8) % 5;
    const f32x4 zero4 = {0, 0, 0, 0};
    if (MM) acc[m][nb] = MFMA(cat(c.t[nb][0], c.t[nb][1]), c.w[m], !FIRST ? acc[m][nb] : PS == 1024 ? init[m] : zero4);
    SB();
    auto rd = [&](int r) {
      if (r == 0) n.w[2 * (1 - WF)] = ld128(aw + 2 * (1 - WF) * 2048);
      else if (r < 9) { const int b = (r - 1) >> 1, u = (r - 1) & 1; n.t[b][u] = ldtr(A.z[u][b & 1] + IMM + (b >> 1) * PS); }
      else if (r == 9) n.w[2 * (1 - WF) + 1] = ld128(aw + (2 * (1 - WF) + 1) * 2048);
      else if (r == 10) n.w[2 * WF] = ld128(aw + 2 * WF * 2048);
      else if (r < 13) n.t[4][r - 11] = ldtr(A.s[r - 11] + IMM);
      else n.w[2 * WF + 1] = ld128(aw + (2 * WF + 1) * 2048);
    };
    if (i < 3) { rd(2 * i); rd(2 * i + 1); }
    else if (i < 11) rd(i + 3);
    else if (MOVE && i < 15) {
      const int u = i & 1;
      if (i < 13) { A.w[u] += dlt; A.s[u] += dlt; asm volatile("" : "+v"(A.w[u]), "+v"(A.s[u])); }
      else { A.z[u][0] += dlt; A.z[u][1] += dlt; asm volatile("" : "+v"(A.z[u][0]), "+v"(A.z[u][1])); }
    }
    SB();
  }
}
template <int WF, int PS, int OFF, bool FIRST>
DEV void t_step(TAddr& A, int dlt, f32x4 (&acc)[4][5], TFrag& F, const f32x4* init = nullptr) {
  SB(); BAR(); MEMB(); SB();
  t_half<WF, PS, OFF, !FIRST, false, false>(A, A.w[0] + OFF, 0, acc, F.Y, F.X, nullptr);
  t_half<WF, PS, OFF + 9216, true, FIRST, OFF != 0>(A, A.w[1] + OFF, dlt, acc, F.X, F.Y, init);
  WAITL0();
}
template <int WF>
DEV void t_last(f32x4 (&acc)[4][5], TFrag& F) {
#pragma unroll
  for (int m = 0; m < 4; ++m)
#pragma unroll
    for (int n = 0; n < 5; ++n) if (n < 4 || (m >> 1) == WF) acc[m][n] = MFMA(cat(F.Y.t[n][0], F.Y.t[n][1]), F.Y.w[m], acc[m][n]);
}
template <int WF, bool MID, class Mid>
DEV void in_pass(unsigned lds0, int wt, int lane, TAddr& A, f32x4 (&acc)[4][5], Mid mid) {
  TFrag F;
  int dlt = 2 * STG;
  WAITL0();
  if (!MID) {
    t_step<WF, 64, 0, true>(A, dlt, acc, F);
    for (int k = 0; k < 4; ++k) {
      t_step<WF, 64, STG, false>(A, dlt, acc, F);
      dlt = -dlt;
      t_step<WF, 64, 0, false>(A, dlt, acc, F);
    }
  } else {
    t_step<WF, 64, STG, true>(A, dlt, acc, F);
    for (int k = 0; k < 3; ++k) {
      dlt = -dlt;
      t_step<WF, 64, 0, false>(A, dlt, acc, F);
      t_step<WF, 64, STG, false>(A, dlt, acc, F);
    }
    t_step<WF, 64, 0, false>(A, 0, acc, F);
    SB(); BAR(); MEMB(); mid(); SB();
    t_step<WF, 64, STG, false>(A, 0, acc, F);
  }
  SB(); BAR(); SB();
  t_last<WF>(acc, F);
}

struct G3P { bf16x8 p[2][5]; s16x4 ps[3]; };
struct G3W { s16x4 w[2][2][2], ws[2]; };
template <int KH>
DEV void g3_readw(G3W& n, unsigned a0, unsigned a1) {
#pragma unroll
  for (int q = 0; q < 8; ++q) n.w[q >> 2][(q >> 1) & 1][q & 1] = ldtr((((q >> 1) & 1) ? a1 : a0) + (2 * KH + (q >> 2)) * 4096 + (q & 1) * 2048);
  n.ws[0] = ldtr(a0 + 16384); n.ws[1] = ldtr(a1 + 16384);
}
template <int KH, bool MM, bool RDW>
DEV void g3_stage(f32x4 (&acc)[5][2], const G3P& cp, const G3W& cw, G3P& np, G3W& nw, unsigned aP, unsigned aS, unsigned a0, unsigned a1) {
  constexpr int M0 = KH ? 3 : 0, NS = KH ? 2 : 3, NM = 20 + 2 * NS, NR = 20 + NS;
  SB(); BAR(); MEMB(); SB();
#pragma unroll
  for (int i = 0; i < NM; ++i) {
    if (MM) {
      if (i >= 10 && i < 10 + 2 * NS) acc[M0 + ((i - 10) >> 1)][i & 1] = MFMA16(cw.ws[i & 1], cp.ps[(i - 10) >> 1], acc[M0 + ((i - 10) >> 1)][i & 1]);
      else { const int ii = i < 10 ? i : i - 2 * NS, P = ii / 10, mm = (ii % 10) >> 1, m = (KH != P) ? (mm + 3) % 5 : mm, cc = ii & 1; acc[m][cc] = MFMA(cat(cw.w[P][cc][0], cw.w[P][cc][1]), cp.p[P][m], acc[m][cc]); }
    }
    SB();
#pragma unroll
    for (int r = (i * 3) / 2; r < ((i + 1) * 3) / 2; ++r) {
      if (r < 18) { const int P = r / 9, q = r % 9;
        if (q < 4) { if (RDW) nw.w[P][q >> 1][q & 1] = ldtr(((q >> 1) ? a1 : a0) + (2 * KH + P) * 4096 + (q & 1) * 2048); }
        else np.p[P][q - 4] = ld128(aP + P * 1024 + (q - 4) * 4096); }
      else if (r < 20) { if (RDW) nw.ws[r - 18] = ldtr(((r - 18) ? a1 : a0) + 16384); }
      else if (r < NR) np.ps[r - 20] = ld64(aS + (M0 + r - 20) * 512);
    }
    SB();
  }
  WAITL0();
}
template <int KH>
DEV void g3_compute(unsigned lds0, int dh, int lane, char* out, int tt, int dd) {
  const int g = lane >> 4, qr = (lane >> 2) & 3, p = lane & 3;
  const int rr = 4 * g + qr;
  unsigned bW[2];
#pragma unroll
  for (int c = 0; c < 2; ++c) bW[c] = lds0 + L_O + rr * 128 + (((4 * dh + p) ^ (((rr >> 1) & 1) | (rr & 4))) << 4) + ((c ^ (p & 1)) * 8);
  const unsigned bP = lds0 + L_P + lane * 16 + KH * 2048, bS = lds0 + L_P + 20480 + lane * 8;
  G3W a0, a1, a2;
  g3_readw<KH>(a0, bW[0], bW[1]); g3_readw<KH>(a1, bW[0] + STGO, bW[1] + STGO); g3_readw<KH>(a2, bW[0] + 2 * STGO, bW[1] + 2 * STGO);
  f32x4 acc[5][2];
#pragma unroll
  for (int m = 0; m < 5; ++m) { acc[m][0] = (f32x4){0, 0, 0, 0}; acc[m][1] = (f32x4){0, 0, 0, 0}; }
  WAITL0();
  SB(); BAR(); SB();
  G3P p0, p1; G3W w0, w1;
  g3_stage<KH, false, false>(acc, p1, a0, p0, w0, bP, bS, 0, 0);
  g3_stage<KH, true, false>(acc, p0, a0, p1, w0, bP + STGP, bS + STGP, 0, 0);
  g3_stage<KH, true, false>(acc, p1, a1, p0, w0, bP + 2 * STGP, bS + 2 * STGP, 0, 0);
  g3_stage<KH, true, true>(acc, p0, a2, p1, w1, bP, bS, bW[0] + 3 * STGO, bW[1] + 3 * STGO);
  unsigned sp = STGP, sw = 4 * STGO;
  for (int j = 4; j < 16; j += 2) {
    g3_stage<KH, true, true>(acc, p1, w1, p0, w0, bP + sp, bS + sp, bW[0] + sw, bW[1] + sw);
    sp = sp == 2 * STGP ? 0 : sp + STGP; sw = sw == 4 * STGO ? 0 : sw + STGO;
    g3_stage<KH, true, true>(acc, p0, w0, p1, w1, bP + sp, bS + sp, bW[0] + sw, bW[1] + sw);
    sp = sp == 2 * STGP ? 0 : sp + STGP; sw = sw == 4 * STGO ? 0 : sw + STGO;
  }
  {
    constexpr int M0 = KH ? 3 : 0, NS = KH ? 2 : 3;
    SB(); BAR(); SB();
#pragma unroll
    for (int i = 0; i < 2 * NS; ++i) { acc[M0 + (i >> 1)][i & 1] = MFMA16(w1.ws[i & 1], p1.ps[i >> 1], acc[M0 + (i >> 1)][i & 1]); SB(); }
#pragma unroll
    for (int i = 0; i < 20; ++i) { const int P = (i >> 1) & 1, m = KH ? i >> 2 : 4 - (i >> 2), cc = i & 1; acc[m][cc] = MFMA(cat(w1.w[P][cc][0], w1.w[P][cc][1]), p1.p[P][m], acc[m][cc]); SB(); }
  }
#pragma unroll
  for (int m = 0; m < 5; ++m)
    if ((m >= 3) != KH) { *(LAS f32x4*)(size_t)(lds0 + (dh * 10 + m * 2) * 1024 + lane * 16) = acc[m][0]; *(LAS f32x4*)(size_t)(lds0 + (dh * 10 + m * 2 + 1) * 1024 + lane * 16) = acc[m][1]; }
  WL0();
  BAR(); MEMB();
  auto rO = RSRC(out, 2048 * 1152);
  const bool sw2 = g & 1;
#pragma unroll
  for (int m = 0; m < 5; ++m)
    if ((m >= 3) == KH) {
      const f32x4 a = acc[m][0] + *(LAS f32x4*)(size_t)(lds0 + (dh * 10 + m * 2) * 1024 + lane * 16), b = acc[m][1] + *(LAS f32x4*)(size_t)(lds0 + (dh * 10 + m * 2 + 1) * 1024 + lane * 16);
      const unsigned a0 = cvt_pk(a[0], a[1]), a1 = cvt_pk(a[2], a[3]), b0 = cvt_pk(b[0], b[1]), b1 = cvt_pk(b[2], b[3]);
      const u32x4 v = {sw2 ? b0 : a0, sw2 ? b1 : a1, sw2 ? a0 : b0, sw2 ? a1 : b1};
      __builtin_amdgcn_raw_buffer_store_b128(v, rO, (tt * 80 + m * 16 + (lane & 15)) * 1152 + dd * 128 + dh * 64 + g * 16, 0, OAUX);
    }
}

template <int WF>
DEV void front(unsigned lds0, int wid, int wt, int lane, int ti, int fj, char* ws) {
  const int r16 = lane & 15, g = lane >> 4, qr = (lane >> 2) & 3, p = lane & 3;
  TAddr A;
  A.w[0] = lds0 + (64 * wt + r16) * 128 + ((g ^ (r16 >> 1)) << 4); A.w[1] = A.w[0] ^ 64;
#pragma unroll
  for (int u = 0; u < 2; ++u) {
    const unsigned b = lds0 + 16384 + (16 * u + 4 * g + qr) * 288 + p * 8;
    A.z[u][0] = b + WF * 128 + (p & 1) * 32; A.z[u][1] = b + WF * 128 + 32 - (p & 1) * 32; A.s[u] = b + 256;
  }
  asm volatile("" : "+v"(A.w[0]), "+v"(A.w[1]), "+v"(A.s[0]), "+v"(A.s[1]), "+v"(A.z[0][0]), "+v"(A.z[0][1]), "+v"(A.z[1][0]), "+v"(A.z[1][1]));
  f32x4 acc[4][5];
  in_pass<WF, false>(lds0, wt, lane, A, acc, []() {});
  CST(0);
  u32x2 ua[4][4], ub[2];
  {
    f32x2 s1[4], s2[4];
#pragma unroll
    for (int mm = 0; mm < 4; ++mm) {
      const int m = mm;
      s1[m] = (f32x2){0.f, 0.f}; s2[m] = (f32x2){0.f, 0.f};
      gelu_row<WF, true>(m, acc, ua, ub, s1[m], s2[m]);
    }
#pragma unroll
    for (int m = 0; m < 4; ++m) *(LAS f32x2*)(size_t)(lds0 + L_T + (64 * wt + 16 * m + r16) * 64 + (WF * 4 + g) * 8) = (f32x2){s1[m].x + s1[m].y, s2[m].x + s2[m].y};
  }
#pragma unroll
  for (int m = 0; m < 4; ++m) asm volatile("" : "+v"(ua[m][0]), "+v"(ua[m][1]), "+v"(ua[m][2]), "+v"(ua[m][3]));
  asm volatile("" : "+v"(ub[0]), "+v"(ub[1]));
#pragma unroll
  for (int u = 0; u < 2; ++u) { A.z[u][0] -= (p & 1) * 32; A.z[u][1] += (p & 1) * 32; }
  asm volatile("" : "+v"(A.z[0][0]), "+v"(A.z[0][1]), "+v"(A.z[1][0]), "+v"(A.z[1][1]));
  CST(1);
  in_pass<WF, true>(lds0, wt, lane, A, acc, [&]() {
    CST(2);
#pragma unroll
    for (int m = 0; m < 4; ++m) asm volatile("" : "+v"(ua[m][0]), "+v"(ua[m][1]), "+v"(ua[m][2]), "+v"(ua[m][3]));
    asm volatile("" : "+v"(ub[0]), "+v"(ub[1]));
    int lz = lane; asm volatile("" : "+v"(lz));
    auto rZ = RSRC(ws + WS_Z2, FRAG_BYTES);
    const unsigned sl = (lz >> 4) * 16 + ((lz & 15) ^ (4 * (lz >> 5))), dst = (fj * 128 + ti * 8 + wt * 4) * CH;
#pragma unroll
    for (int m = 0; m < 4; ++m) {
      const unsigned ar = lds0 + L_R + (64 * wt + 16 * m + (lz & 15)) * 32;
      const f32x4 q4 = *(LAS f32x4*)(size_t)ar + *(LAS f32x4*)(size_t)(ar + 16);
      const float mean = (q4[0] + q4[2]) * (1.0f / 2304.0f), var = fmaxf((q4[1] + q4[3]) * (1.0f / 2304.0f) - mean * mean, 0.f);
      const float rs = __builtin_amdgcn_rsqf(var + 1e-5f), sh = -mean * rs;
      auto norm = [&](unsigned v) { const f32x2 y = (f32x2){bflo(v), bfhi(v)} * rs + sh; return cvt_pk(y.x, y.y); };
#pragma unroll
      for (int pp = 0; pp < 2; ++pp)
        __builtin_amdgcn_raw_buffer_store_b128((u32x4){norm(ua[m][2 * pp].x), norm(ua[m][2 * pp].y), norm(ua[m][2 * pp + 1].x), norm(ua[m][2 * pp + 1].y)}, rZ, dst + m * CH + sl * 16 + WF * 2048 + pp * 1024, 0, 16);
      if ((m >> 1) == WF) __builtin_amdgcn_raw_buffer_store_b64((u32x2){norm(ub[m & 1].x), norm(ub[m & 1].y)}, rZ, dst + m * CH + sl * 8 + 4096, 0, 16);
    }
    CST(3);
  });
  CST(4);
#pragma unroll
  for (int m = 0; m < 4; ++m) asm volatile("" : "+v"(acc[m][0]), "+v"(acc[m][1]), "+v"(acc[m][2]), "+v"(acc[m][3]));
  asm volatile("" : "+v"(acc[2 * WF][4]), "+v"(acc[2 * WF + 1][4]));
  const u32x2 tg = *(LAS u32x2*)(size_t)(lds0 + L_TAG);
  const u64 tag = ((u64)(unsigned)__builtin_amdgcn_readfirstlane(tg.y) << 32) | (unsigned)__builtin_amdgcn_readfirstlane(tg.x);
  WV(0);
  if (lane == 0 && __hip_atomic_fetch_add((LAS unsigned*)(size_t)(lds0 + L_CNT), 1u, __ATOMIC_RELAXED, __HIP_MEMORY_SCOPE_WORKGROUP) == 3u)
    __hip_atomic_store((u64*)(ws + WS_F1 + (fj * 16 + ti) * 128), tag, RLX_AGENT);
  CST(5);
  SB();
  u32x2 za[4][4], zb[2];
#pragma unroll
  for (int m = 0; m < 4; ++m) {
    f32x2 d1, d2;
    gelu_row<WF, false>(m, acc, za, zb, d1, d2);
    asm volatile("" : "+v"(za[m][0]), "+v"(za[m][1]), "+v"(za[m][2]), "+v"(za[m][3]));
  }
  asm volatile("" : "+v"(zb[0]), "+v"(zb[1]));
  CST(6);
#pragma unroll
  for (int u = 0; u < 2; ++u) {
    const unsigned sl = p * 16 + 8 * (g & 1) + 4 * (u ^ (p >> 1)) + qr;
#pragma unroll
    for (int h = 0; h < 2; ++h) A.z[u][h] = lds0 + 16384 + (g >> 1) * CH + WF * 2048 + sl * 16 + ((h ^ (p & 1)) * 8);
    A.s[u] = lds0 + 16384 + (g >> 1) * CH + 4096 + sl * 8;
  }
  asm volatile("" : "+v"(A.s[0]), "+v"(A.s[1]), "+v"(A.z[0][0]), "+v"(A.z[0][1]), "+v"(A.z[1][0]), "+v"(A.z[1][1]));
  f32x4 bias4[4];
#pragma unroll
  for (int m = 0; m < 4; ++m) { const float b = __uint_as_float((unsigned)*(LAS u16*)(size_t)(lds0 + L_BIAS + (wt * 64 + m * 16 + r16) * 2) << 16); bias4[m] = (f32x4){b, b, b, b}; }
  TFrag F;
  int dlt = 2 * STG;
  WAITL0();
  CST(7);
  t_step<WF, 1024, 0, true>(A, dlt, acc, F, bias4);
  CST(8);
  t_step<WF, 1024, STG, false>(A, dlt, acc, F);

  for (int k = 0; k < 15; ++k) {
    dlt = -dlt;
    t_step<WF, 1024, 0, false>(A, dlt, acc, F);
    t_step<WF, 1024, STG, false>(A, dlt, acc, F);
  }
  auto rP = RSRC(ws + WS_P, FRAG_BYTES);
  auto gate = [&](f32x4 a, u32x2 z) {
    const f32x2 x = (f32x2){a[0], a[1]} * (f32x2){bflo(z.x), bfhi(z.x)}, y = (f32x2){a[2], a[3]} * (f32x2){bflo(z.y), bfhi(z.y)};
    return (u32x2){cvt_pk(x.x, x.y), cvt_pk(y.x, y.y)};
  };
  const unsigned dst0 = (ti * 8 + wt * 4) * 16 * CH + fj * CH;
  CST(9);
  t_last<WF>(acc, F);
#pragma unroll
  for (int m = 0; m < 4; ++m) {
#pragma unroll
    for (int pp = 0; pp < 2; ++pp) {
      const u32x2 a = gate(acc[m][2 * pp], za[m][2 * pp]), b = gate(acc[m][2 * pp + 1], za[m][2 * pp + 1]);
      __builtin_amdgcn_raw_buffer_store_b128((u32x4){a.x, a.y, b.x, b.y}, rP, dst0 + m * 16 * CH + lane * 16 + (2 * WF + pp) * 1024, 0, 16);
    }
    if ((m >> 1) == WF) __builtin_amdgcn_raw_buffer_store_b64(gate(acc[m][4], zb[m & 1]), rP, dst0 + m * 16 * CH + lane * 8 + 4096, 0, 16);
  }
  CST(10);
  WV(0);
  CST(11);
  if (lane == 0 && __hip_atomic_fetch_add((LAS unsigned*)(size_t)(lds0 + L_CNT), 1u, __ATOMIC_RELAXED, __HIP_MEMORY_SCOPE_WORKGROUP) == 7u)
    __hip_atomic_store((u64*)(ws + WS_F2 + (ti * 16 + fj) * 128), tag, RLX_AGENT);
  SB(); BAR(); MEMB(); SB();
}

template <int NP> DEV unsigned pollN(const char* base, u64 tag, int lane) {
  const u64 v = __hip_atomic_load((const u64*)(base + min(lane, NP - 1) * 128), RLX_AGENT);
  return (unsigned)__builtin_amdgcn_ballot_w64(v == tag) & ((1u << NP) - 1);
}
DEV bool got(unsigned mask, int who) { return (mask >> who) & 1u; }

__global__ __launch_bounds__(512, 2) void gmlp_fused(const char* __restrict__ X, const char* __restrict__ Win, const char* __restrict__ Wsp, const u16* __restrict__ bias,
                                                     const char* __restrict__ Wo, char* __restrict__ out, char* __restrict__ ws) {
  extern __shared__ __attribute__((aligned(16))) char smem[];
  u64 pc0; asm volatile("s_getpc_b64 %0" : "=s"(pc0));
  const int tid = threadIdx.x, wid = __builtin_amdgcn_readfirstlane(tid >> 6), lane = tid & 63;
  const int bid = blockIdx.x, xg = bid & 7, rk = bid >> 3, il = rk & 7, jl = rk >> 3, ti = (xg & 1) * 8 + il, fj = (xg >> 1) * 4 + jl;
  const int L3 = xg * 30 + rk, tt = L3 / 9, dd = L3 - tt * 9;
  const bool has3 = rk < 30 && L3 < 234;
  const unsigned lds0 = (unsigned)(size_t)(LAS char*)smem;
  if (__builtin_expect(wid >= 4, 1)) {
    __builtin_amdgcn_s_setprio(3);
    const int lw = wid - 4;
    const bool big = wid < 6;
#define WVS(STAGES, EXTRA) do { if (big) WV(9 * (STAGES) + (EXTRA)); else WV(8 * (STAGES) + (EXTRA)); } while (0)
    auto rX = RSRC(X, 2048 * 1152); auto rWi = RSRC(Win, 576 * 9216);
    unsigned vX[4], vW[5];
    const unsigned zp0 = lw < 2 ? lw * 5 : 2 + lw * 4;
    const unsigned sX = (ti * 128 + lw * 32) * 1152, sW = fj * 288;
    const int rot = ROT1 == 0 ? 0 : ROT1 == 1 ? il + 2 * jl : 3 * ((il + jl) % 3);
    const int rot8 = ROT1 == 1 ? (il + 2 * jl) & 7 : ROT1 == 2 ? (((il + jl) % 3) * 8) / 3 : 0;
    auto k1 = [&](int k) { return k == 0 ? 0 : 1 + ((k - 1 + rot8) & 7); };
    auto issue1 = [&](int half, int k) {
      const int kk = k1(k);
      const unsigned d = lds0 + ((k + 1 - half) & 3) * STG, dz = d + 16384 + zp0 * 1024, sw = sW + half * 4608 + kk * 64 * 9216;
      GL4(rX, d + lw * 4096, vX[0], vX[1], vX[2], vX[3], sX + kk * 128);
      GL4(rWi, dz, vW[0], vW[1], vW[2], vW[3], sw);
      if (big) GL(rWi, dz + 4096, vW[4], sw, 0);
    };
    {
#pragma unroll
      for (int q = 0; q < 4; ++q) { const int row = 8 * q + (lane >> 3); vX[q] = row * 1152 + (((lane & 7) ^ ((row >> 1) & 7)) << 4) - q * 1024; }
      GL4(rX, lds0 + lw * 4096, vX[0], vX[1], vX[2], vX[3], sX + k1(0) * 128);
      SB();
#pragma unroll
      for (int q = 0; q < 5; ++q) {
        const unsigned c16 = (zp0 + q) * 64 + lane, R = (c16 * 3641u) >> 16, c = (c16 - R * 18) * 16, k = (R & 32) + 8 * ((R >> 2) & 3) + 4 * ((R >> 4) & 1) + (R & 3);
        vW[q] = k * 9216 + c - (q & 3) * 1024;
      }
      const unsigned dz = lds0 + 16384 + zp0 * 1024, sw = sW + 4608 + k1(0) * 64 * 9216;
      GL4(rWi, dz, vW[0], vW[1], vW[2], vW[3], sw);
      if (big) GL(rWi, dz + 4096, vW[4], sw, 0);
    }
    const unsigned vL = lane * 16;
    auto rWs = RSRC(Wsp, 2048 * 4096); auto rWo = RSRC(Wo, 2304 * 1152);
    const unsigned e9 = lw * 21 + lane, r9 = (e9 * 7282u) >> 16, v81 = lane < 21 && e9 < 81 ? r9 * 9216 + (e9 - r9 * 9) * 128 : OOB, s81 = (bid >> 2) * 9 * 9216 + (bid & 3) * 1152;
    issue1(1, 1);
    WVS(1, T0W ? 2 : 0); LBAR();
    issue1(1, 2);
    WVS(1, 0);
    for (int k = 1; k < 5; ++k) { LBAR(); issue1(1, k + 2); WVS(1, 0); }
    LBAR(); issue1(1, 7); WVS(1, 0);
    LBAR(); issue1(1, 8); WVS(1, 0);
    LBAR(); issue1(0, 0); WVS(1, 0);
    LBAR(); issue1(0, 1);
    const u64 ep = __hip_atomic_load((const u64*)(ws + WS_EP), RLX_AGENT);
    if (lw == 0) __builtin_amdgcn_raw_ptr_buffer_load_lds(RSRC(bias, 4096), (LAS void*)(size_t)(lds0 + L_BIAS), 4, lane * 4, ti * 256, 0, 0);
    TOUCH(rWs, lane * 128, bid * 32768 + lw * 8192);
    LBAR();
    issue1(0, 2); issue1(0, 3);
    const unsigned tagA = __builtin_amdgcn_readfirstlane((unsigned)ep) + 0x9E3779B9u, tagB = __builtin_amdgcn_readfirstlane((unsigned)(ep >> 32)) + 0x85EBCA6Bu;
    const u64 tag = ((u64)tagB << 32) | tagA;
    if (lw == 0) { const u32x4 tg = {tagA, tagB, 0u, 0u}; asm volatile("ds_write_b128 %0, %1\n\ts_waitcnt lgkmcnt(0)" :: "v"(lds0 + L_TAG), "v"(tg) : "memory"); }
    unsigned vS[4];
#pragma unroll
    for (int q = 0; q < 4; ++q) { const int row = 8 * q + (lane >> 3); vS[q] = row * 4096 + (((lane & 7) ^ ((row >> 1) & 7)) << 4) - q * 1024; }
    const unsigned sWs = (ti * 128 + lw * 32) * 4096;
    auto kpos = [&](int n) { return n; };
    auto issueW = [&](int n) { GL4(rWs, lds0 + (n & 3) * STG + lw * 4096, vS[0], vS[1], vS[2], vS[3], sWs + kpos(n) * 128); };
    WVS(2, 1);
    LBAR();
    auto rS = RSRC(ws + WS_ST, 16 * 16 * 128 * 16);
    if (lane < 32) {
      const int row = lw * 32 + lane;
      f32x4 v0, v1, v2, v3;
      asm volatile("ds_read_b128 %0, %4\n\tds_read_b128 %1, %4 offset:16\n\tds_read_b128 %2, %4 offset:32\n\tds_read_b128 %3, %4 offset:48\n\ts_waitcnt lgkmcnt(0)"
                   : "=&v"(v0), "=&v"(v1), "=&v"(v2), "=&v"(v3) : "v"(lds0 + L_T + row * 64) : "memory");
      const f32x4 s = (v0 + v1) + (v2 + v3);
      u32x4 gq; gq.x = tagA; gq.y = __float_as_uint(s[0] + s[2]); gq.z = tagB; gq.w = __float_as_uint(s[1] + s[3]);
      __builtin_amdgcn_raw_buffer_store_b128(gq, rS, ((ti * 16 + fj) * 128 + row) * 16, 0, 16);
    }
    u32x4 sv[8];
    const u32x4 srd = {(unsigned)(size_t)(ws + WS_ST), (unsigned)((size_t)(ws + WS_ST) >> 32) & 0xffffu, 16 * 16 * 128 * 16, 0x00020000u};
    auto sweep = [&]() {
      const unsigned s0 = (ti * 16 + lw * 4) * 2048, v1 = vL + 4096;
      asm volatile("buffer_load_dwordx4 %0, %8, %10, %11 offen sc1\n\tbuffer_load_dwordx4 %1, %8, %10, %11 offen offset:1024 sc1\n\tbuffer_load_dwordx4 %2, %8, %10, %11 offen offset:2048 sc1\n\t"
                   "buffer_load_dwordx4 %3, %8, %10, %11 offen offset:3072 sc1\n\tbuffer_load_dwordx4 %4, %9, %10, %11 offen sc1\n\tbuffer_load_dwordx4 %5, %9, %10, %11 offen offset:1024 sc1\n\t"
                   "buffer_load_dwordx4 %6, %9, %10, %11 offen offset:2048 sc1\n\tbuffer_load_dwordx4 %7, %9, %10, %11 offen offset:3072 sc1"
                   : "=&v"(sv[0]), "=&v"(sv[1]), "=&v"(sv[2]), "=&v"(sv[3]), "=&v"(sv[4]), "=&v"(sv[5]), "=&v"(sv[6]), "=&v"(sv[7]) : "v"(vL), "v"(v1), "s"(srd), "s"(s0) : "memory");
    };
    LBAR(); issue1(0, 4); WVS(2, 1);
    LBAR(); issue1(0, 5); WVS(2, 0);
    LBAR(); issue1(0, 6); WVS(2, 0);
    LBAR(); issue1(0, 7); WVS(2, 0);
    LBAR(); sweep(); WVS(1, 8);
    LBAR(); issue1(0, 8);
    WVS(1, 8);
    LBAR();
    {
      f32x4 s;
      auto total = [&](const u32x4 (&v)[8]) {
        unsigned bad = 0;
        s = (f32x4){0.f, 0.f, 0.f, 0.f};
#pragma unroll
        for (int p = 0; p < 8; ++p) { bad |= (v[p].x ^ tagA) | (v[p].z ^ tagB); s[(p & 1) * 2] += __uint_as_float(v[p].y); s[(p & 1) * 2 + 1] += __uint_as_float(v[p].w); }
        return __builtin_amdgcn_ballot_w64(bad != 0) == 0;
      };
      WVS(1, 0);
      asm volatile("" : "+v"(sv[0]), "+v"(sv[1]), "+v"(sv[2]), "+v"(sv[3]), "+v"(sv[4]), "+v"(sv[5]), "+v"(sv[6]), "+v"(sv[7]) :: "memory");
      if (__builtin_expect(!total(sv), 0)) {
#pragma unroll 1
        for (unsigned spins = 0;; ++spins) {
          u32x4 rv[8];
          MEMB();
#pragma unroll
          for (int p = 0; p < 8; ++p) rv[p] = __builtin_amdgcn_raw_buffer_load_b128(rS, vL + (p & 1) * 1024, (ti * 16 + lw * 4 + (p >> 1)) * 2048, 16);
          if (total(rv)) break;
          if (spins > SPIN_CAP) { s[0] = s[2] = __builtin_nanf(""); break; }
        }
      }
      const f32x2 o0 = {s[0], s[1]}, o1 = {s[2], s[3]};
      asm volatile("ds_write_b64 %0, %1\n\tds_write_b64 %0, %2 offset:2048\n\ts_waitcnt lgkmcnt(0)" :: "v"(lds0 + L_R + lane * 32 + lw * 8), "v"(o0), "v"(o1) : "memory");
    }
    LBAR();
    WV(0);
    LBAR();
    issueW(0); issueW(2);
    LBAR();
    issueW(1); issueW(3);
    TOUCH(rWo, lane < 21 ? lane * 128 : OOB, bid * 10368 + lw * 2688);
    auto rZ = RSRC(ws + WS_Z2, FRAG_BYTES);
    const unsigned sZ = fj * 128 * CH + zp0 * 1024;
    unsigned ready = 0;
    auto issueZ = [&](int n) {
      const int kp = kpos(n);
      if (__builtin_expect(!got(ready, kp >> 1), 0)) { for (unsigned spins = 0; !got(ready, kp >> 1) && spins < SPIN_CAP; ++spins) ready = pollN<16>(ws + WS_F1 + fj * 16 * 128, tag, lane); }
      MEMB();
      const unsigned dz = lds0 + (n & 3) * STG + 16384 + zp0 * 1024, so = sZ + kp * 18432;
      GS4(rZ, dz, vL, so);
      if (big) GS(rZ, dz + 4096, vL, so + 4096, 0);
    };
    const char* f1 = ws + WS_F1 + fj * 16 * 128;
    for (int q = 0; q < PDELAY; ++q) __builtin_amdgcn_s_sleep(4);
    { unsigned np = 0; for (; ready != 7u && np < SPIN_CAP; ++np) ready = pollN<3>(f1, tag, lane); LST(0, np); }
    issueZ(0); issueZ(1); issueZ(2);
    WVS(2, -8);
    LBAR();
    u64 fv;
    asm volatile("global_load_dwordx2 %0, %1, %2 sc1" : "=&v"(fv) : "v"((lane & 15) * 128), "s"(f1) : "memory");
    issueZ(3);
    WVS(2, -7);
    LBAR();
    issueW(4); issueZ(4);
    WVS(2, -3);
    LBAR();
    issueW(5); issueZ(5);
    WVS(2, 0);
    asm volatile("" : "+v"(fv));
    ready |= (unsigned)__builtin_amdgcn_ballot_w64(fv == tag) & 0xffffu;
    LBAR();
    for (int n = 6; n < 32; ++n) {
      issueW(n); issueZ(n);
      WVS(2, 0);
      LBAR();
    }
    unsigned vO[2], vOh;
#pragma unroll
    for (int q = 0; q < 2; ++q) { const int row = 36 * lw + 8 * q + (lane >> 3); vO[q] = row * 1152 + (((lane & 7) ^ (((row >> 1) & 1) | (row & 4))) << 4); }
    { const int row = 36 * lw + 32 + ((lane >> 3) & 3); vOh = row * 1152 + (((lane & 7) ^ (((row >> 1) & 1) | (row & 4))) << 4); }
    const unsigned vO1 = vO[1] - 1024, vO2 = vO[0] + 16 * 1152 - 2048, vO3 = vO[1] + 16 * 1152 - 3072;
    const unsigned sO = min(dd, 8) * 128;
    auto issueO = [&](int n, int slot) {
      const unsigned d = lds0 + L_O + slot * STGO + lw * 4608, s0 = sO + n * 144 * 1152;
      GL4(rWo, d, vO[0], vO1, vO2, vO3, s0);
      if (lane < 32) GL(rWo, d + 4096, vOh, s0, 0);
    };
    issueO(0, 0);
    WVS(1, 5); LBAR();
    issueO(1, 1); issueO(2, 2);
    WV(15); LBAR();
    WV(0);
    LBAR();
    if (!has3) return;
    issueO(3, 3); issueO(4, 4);
    LBAR();
    issueO(5, 0);
    if (NPRE3 == 5) { issueO(6, 1); issueO(7, 2); }
    auto rP = RSRC(ws + WS_P, FRAG_BYTES);
    const unsigned vPs = lw < 3 ? (lane >> 5) * 16 * CH + (lane & 31) * 16 + 4096 : OOB;
    const int limS = lw == 2 ? 32 : 64;
    const unsigned sP = tt * 5 * 16 * CH;
    const int pa = (tt * 80) >> 7, pb = min((tt * 80 + 79) >> 7, 15);
    unsigned rdy = 0;
    auto issueP = [&](int j, int slot) {
      if (__builtin_expect(!got(rdy, j), 0)) { for (unsigned spins = 0; !got(rdy, j) && spins < SPIN_CAP; ++spins) rdy = pollN<16>(ws + WS_F2 + pa * 16 * 128, tag, lane) & pollN<16>(ws + WS_F2 + pb * 16 * 128, tag, lane); }
      MEMB();
      const unsigned d = lds0 + L_P + slot * STGP, sp = sP + j * CH;
      GS4(rP, d + lw * 4096, vL, sp + lw * 16 * CH);
      GS(rP, d + 16384 + lw * 1024, vL, sp + 64 * CH + lw * 1024, 0);
      if (lane < limS) GS(rP, lw < 3 ? d + 20480 + lw * 1024 : lds0 + L_DUMMY, vPs, sp + lw * 32 * CH, 0);
    };
    const char *fa = ws + WS_F2 + pa * 16 * 128, *fb = ws + WS_F2 + pb * 16 * 128;
    { unsigned np = 0; LST(2, 0); for (; rdy != 7u && np < SPIN_CAP; ++np) rdy = pollN<3>(fa, tag, lane) & pollN<3>(fb, tag, lane); LST(1, np); }
    u64 fva, fvb;
    asm volatile("global_load_dwordx2 %0, %2, %3 sc1\n\tglobal_load_dwordx2 %1, %2, %4 sc1" : "=&v"(fva), "=&v"(fvb) : "v"((lane & 15) * 128), "s"(fa), "s"(fb) : "memory");
    issueP(0, 0); issueP(1, 1); issueP(2, 2);
    WV(12);
    asm volatile("" : "+v"(fva), "+v"(fvb));
    rdy |= (unsigned)(__builtin_amdgcn_ballot_w64(fva == tag) & __builtin_amdgcn_ballot_w64(fvb == tag)) & 0xffffu;
    LBAR();
    WV(6);
    if (NPRE3 == 5) {
      LBAR(); issueP(3, 0); WV(6);
      LBAR(); issueP(4, 1); WV(6);
      LBAR(); issueP(5, 2); WV(6);
    } else {
      LBAR(); issueP(3, 0); issueO(6, 1); WV(11);
      LBAR(); issueP(4, 1); issueO(7, 2); WV(16);
      LBAR(); issueP(5, 2); WV(11);
    }
    LBAR(); issueP(6, 0); issueO(8, 3); WV(11);
    int sp3 = 1, so5 = 4;
    for (int n = 5; n < 12; ++n) {
      LBAR();
      issueP(n + 2, sp3); issueO(n + 4, so5);
      sp3 = sp3 == 2 ? 0 : sp3 + 1; so5 = so5 == 4 ? 0 : so5 + 1;
      WV(16);
    }
    LBAR(); issueP(14, 2); WV(6);
    LBAR(); issueP(15, 0); WV(6);
    LBAR(); WV(0);
    LBAR();
    LBAR(); LBAR();
    if (bid == 0 && tid == 256) __hip_atomic_store((u64*)(ws + WS_EP), tag, RLX_AGENT);
  } else {
    const int wt = wid >> 1, wf = wid & 1;
    if (wid == 0) {
      auto rC = RSRC((const char*)(pc0 & ~(u64)255), CODE_KB * 1024);
      GL(rC, lds0 + L_DUMMY, lane * 16, rk * 1024, 0); GL(rC, lds0 + L_DUMMY, lane * 16, rk * 1024 + 32768, 0);
      WV(0);
    }
    if (wf == 0) front<0>(lds0, wid, wt, lane, ti, fj, ws); else front<1>(lds0, wid, wt, lane, ti, fj, ws);
    if (!has3) return;
    if (wt == 0) g3_compute<0>(lds0, wf, lane, out, tt, dd); else g3_compute<1>(lds0, wf, lane, out, tt, dd);
  }
}

extern "C" void kernel_launch(void* const* d_in, const int* in_sizes, int n_in, void* d_out, int out_size, void* d_ws, size_t ws_size, hipStream_t stream) {
  (void)hipFuncSetAttribute((const void*)gmlp_fused, hipFuncAttributeMaxDynamicSharedMemorySize, LDS_BYTES);
  gmlp_fused<<<256, 512, LDS_BYTES, stream>>>((const char*)d_in[0], (const char*)d_in[1], (const char*)d_in[2], (const u16*)d_in[3], (const char*)d_in[4], (char*)d_out, (char*)d_ws);
}
```

```cpp
#include <hip/hip_runtime.h>
#include <hip/hip_bf16.h>
typedef unsigned short u16;
typedef unsigned long long u64;
typedef short s16x4 __attribute__((ext_vector_type(4)));
typedef short bf16x8 __attribute__((ext_vector_type(8)));
typedef float f32x2 __attribute__((ext_vector_type(2)));
typedef float f32x4 __attribute__((ext_vector_type(4)));
typedef unsigned u32x4 __attribute__((ext_vector_type(4)));
typedef unsigned u32x2 __attribute__((ext_vector_type(2)));
#define LAS __attribute__((address_space(3)))
#define DEV __device__ __forceinline__
#define BAR() __builtin_amdgcn_s_barrier()
#define SB() __builtin_amdgcn_sched_barrier(0)
#define MEMB() asm volatile("" ::: "memory")
#define WAITL0() __builtin_amdgcn_s_waitcnt(0xC07F)
#define WL0() asm volatile("s_waitcnt lgkmcnt(0)" ::: "memory")
#define WV(N) asm volatile("s_waitcnt vmcnt(%0)" ::"i"(N) : "memory")
#define MFMA(a, b, c) __builtin_amdgcn_mfma_f32_16x16x32_bf16(a, b, c, 0, 0, 0)
#define MFMA16(a, b, c) __builtin_amdgcn_mfma_f32_16x16x16bf16_1k(a, b, c, 0, 0, 0)
#define GL(R, D, V, S, I) __builtin_amdgcn_raw_ptr_buffer_load_lds(R, (LAS void*)(size_t)(D), 16, V, S, I, 0)
#define GL4(R, D, V0, V1, V2, V3, S) do { GL(R, D, V0, S, 0); GL(R, D, V1, S, 1024); GL(R, D, V2, S, 2048); GL(R, D, V3, S, 3072); } while (0)
#define GS(R, D, V, S, I) __builtin_amdgcn_raw_ptr_buffer_load_lds(R, (LAS void*)(size_t)(D), 16, V, S, I, 16)
#define GS4(R, D, V, S) do { GS(R, D, V, S, 0); GS(R, D, V, S, 1024); GS(R, D, V, S, 2048); GS(R, D, V, S, 3072); } while (0)
#define TOUCH(R, V, S) __builtin_amdgcn_raw_ptr_buffer_load_lds(R, (LAS void*)(size_t)(lds0 + L_DUMMY), 4, V, S, 0, 0)
#define RSRC(p, n) __builtin_amdgcn_make_buffer_rsrc((void*)(p), 0, (n), 0x00020000)
#define RLX_AGENT __ATOMIC_RELAXED, __HIP_MEMORY_SCOPE_AGENT
#define ROT1 2
#define NPRE3 3
#define Z1W 0
#define LS0 1
#define OAUX 16
#define WAUX 0
#define T0W 0
#define PDELAY 7
#define PSLEEP 0
#define CODE_KB 35

constexpr int CH = 4608;
constexpr int FRAG_BYTES = 16 * 128 * CH;
constexpr size_t WS_Z2 = 0, WS_P = FRAG_BYTES, WS_ST = 2 * (size_t)FRAG_BYTES, WS_F1 = WS_ST + 16 * 16 * 128 * 16, WS_F2 = WS_F1 + 256 * 128, WS_EP = WS_F2 + 256 * 128;
constexpr int STG = 34816, STGP = 23040, STGO = 18432, L_O = 0, L_P = 5 * STGO, LDS_BYTES = 163840;
constexpr int L_T = 139264, L_R = L_T + 8192, L_X = 151552, L_BIAS = 159744, L_TAG = L_BIAS + 256, L_CNT = L_TAG + 8, L_DUMMY = 161280;
constexpr unsigned SPIN_CAP = 1u << 16, OOB = 0x80000000u;
#define LBAR() BAR()
#define CST(I)
#define LST(I, X)

DEV unsigned cvt_pk(float lo, float hi) { unsigned r; asm("v_cvt_pk_bf16_f32 %0, %1, %2" : "=v"(r) : "v"(lo), "v"(hi)); return r; }
DEV bf16x8 ld128(unsigned a) { return *(LAS bf16x8*)(size_t)a; }
DEV s16x4 ld64(unsigned a) { return *(LAS s16x4*)(size_t)a; }
DEV s16x4 ldtr(unsigned a) { return __builtin_amdgcn_ds_read_tr16_b64_v4i16((LAS s16x4*)(size_t)a); }
DEV bf16x8 cat(s16x4 a, s16x4 b) { return __builtin_shufflevector(a, b, 0, 1, 2, 3, 4, 5, 6, 7); }
DEV float bflo(unsigned v) { return __uint_as_float(v << 16); }
DEV float bfhi(unsigned v) { return __uint_as_float(v & 0xffff0000u); }
template <int NB, bool SUM>
DEV void gelu_blocks(const f32x4* const (&v)[NB], u32x2* const (&o)[NB], f32x2& s1, f32x2& s2) {
  const float k1 = -2.0f * 0.7978845608028654f * 1.4426950408889634f, k2 = k1 * 0.044715f;
  f32x2 x[2 * NB], t[2 * NB];
#pragma unroll
  for (int i = 0; i < 2 * NB; ++i) { x[i] = (f32x2){(*v[i >> 1])[(i & 1) * 2], (*v[i >> 1])[(i & 1) * 2 + 1]}; t[i] = x[i] * x[i]; }
  SB();
#pragma unroll
  for (int i = 0; i < 2 * NB; ++i) t[i] = t[i] * k2 + k1;
  SB();
#pragma unroll
  for (int i = 0; i < 2 * NB; ++i) t[i] = t[i] * x[i];
  SB();
#pragma unroll
  for (int i = 0; i < 2 * NB; ++i) { t[i].x = __builtin_amdgcn_exp2f(t[i].x); t[i].y = __builtin_amdgcn_exp2f(t[i].y); }
  SB();
#pragma unroll
  for (int i = 0; i < 2 * NB; ++i) t[i] = t[i] + 1.0f;
  SB();
#pragma unroll
  for (int i = 0; i < 2 * NB; ++i) { t[i].x = __builtin_amdgcn_rcpf(t[i].x); t[i].y = __builtin_amdgcn_rcpf(t[i].y); }
  SB();
#pragma unroll
  for (int i = 0; i < 2 * NB; ++i) t[i] = t[i] * x[i];
  SB();
  f32x2 a1 = s1, a2 = s2, b1 = {0.f, 0.f}, b2 = {0.f, 0.f};
#pragma unroll
  for (int i = 0; i < NB; ++i) {
    o[i]->x = cvt_pk(t[2 * i].x, t[2 * i].y);
    if (SUM) { a1 += t[2 * i]; a2 += t[2 * i] * t[2 * i]; }
    o[i]->y = cvt_pk(t[2 * i + 1].x, t[2 * i + 1].y);
    if (SUM) { if (i == 0) { b1 = t[1]; b2 = t[1] * t[1]; } else { b1 += t[2 * i + 1]; b2 += t[2 * i + 1] * t[2 * i + 1]; } }
  }
  if (SUM) { s1 = a1 + b1; s2 = a2 + b2; }
  SB();
}
template <bool SUM>
DEV void gelu_row(int m, const f32x4 (&acc)[4][5], u32x2 (&oa)[4][4], u32x2 (&ob)[2], f32x2& s1, f32x2& s2) {
  if (m >= 2) { const f32x4* const v[5] = {&acc[m][0], &acc[m][1], &acc[m][2], &acc[m][3], &acc[m][4]}; u32x2* const o[5] = {&oa[m][0], &oa[m][1], &oa[m][2], &oa[m][3], &ob[m & 1]}; gelu_blocks<5, SUM>(v, o, s1, s2); }
  else { const f32x4* const v[4] = {&acc[m][0], &acc[m][1], &acc[m][2], &acc[m][3]}; u32x2* const o[4] = {&oa[m][0], &oa[m][1], &oa[m][2], &oa[m][3]}; gelu_blocks<4, SUM>(v, o, s1, s2); }
}

struct TAddr { unsigned w[2][2], z[2][2], s[2]; };
struct TSet { bf16x8 w[4]; s16x4 t[5][2]; };
struct TFrag { TSet X, Y; };
template <int PS, int OFF, int KK, bool MM, bool FIRST, bool MOVE>
DEV void t_half(TAddr& A, int dlt, f32x4 (&acc)[4][5], const TSet& c, TSet& n, const f32x4* init) {
#pragma unroll
  for (int i = 0; i < 18; ++i) {
    const int m = i < 8 ? i >> 2 : 2 + (i - 8) / 5, nb = i < 8 ? (i & 3) : (i - 8) % 5;
    const f32x4 zero4 = {0, 0, 0, 0};
    if (MM) acc[m][nb] = MFMA(cat(c.t[nb][0], c.t[nb][1]), c.w[m], !FIRST ? acc[m][nb] : PS == 1024 ? init[m] : zero4);
    SB();
    auto rd = [&](int r) {
      if (r == 0) n.w[0] = ld128(A.w[KK][0] + OFF);
      else if (r < 9) { const int b = (r - 1) >> 1, u = (r - 1) & 1; n.t[b][u] = ldtr(A.z[u][b & 1] + OFF + KK * 9216 + (b >> 1) * PS); }
      else if (r == 9) n.w[1] = ld128(A.w[KK][0] + OFF + 2048);
      else if (r == 10) n.w[2] = ld128(A.w[KK][1] + OFF);
      else if (r < 13) n.t[4][r - 11] = ldtr(A.s[r - 11] + OFF + KK * 9216);
      else n.w[3] = ld128(A.w[KK][1] + OFF + 2048);
    };
    if (i < 3) { rd(2 * i); rd(2 * i + 1); }
    else if (i < 11) rd(i + 3);
    else if (MOVE && i < 16) {
      unsigned &a = i < 13 ? A.w[i - 11][0] : i < 15 ? A.z[i - 13][0] : A.s[0], &b = i < 13 ? A.w[i - 11][1] : i < 15 ? A.z[i - 13][1] : A.s[1];
      a += dlt; b += dlt; asm volatile("" : "+v"(a), "+v"(b));
    }
    SB();
  }
}
template <int PS, int OFF, bool FIRST>
DEV void t_step(TAddr& A, int dlt, f32x4 (&acc)[4][5], TFrag& F, const f32x4* init = nullptr) {
  SB(); BAR(); MEMB(); SB();
  t_half<PS, OFF, 0, !FIRST, false, false>(A, 0, acc, F.Y, F.X, nullptr);
  t_half<PS, OFF, 1, true, FIRST, OFF != 0>(A, dlt, acc, F.X, F.Y, init);
  WAITL0();
}
DEV void t_last(f32x4 (&acc)[4][5], TFrag& F) {
#pragma unroll
  for (int m = 0; m < 4; ++m)
#pragma unroll
    for (int n = 0; n < 5; ++n) if (n < 4 || m >= 2) acc[m][n] = MFMA(cat(F.Y.t[n][0], F.Y.t[n][1]), F.Y.w[m], acc[m][n]);
}
template <bool MID, class Mid>
DEV void in_pass(TAddr& A, f32x4 (&acc)[4][5], Mid mid) {
  TFrag F;
  int dlt = 2 * STG;
  WAITL0();
  if (!MID) {
    t_step<64, 0, true>(A, dlt, acc, F);
    for (int k = 0; k < 4; ++k) {
      t_step<64, STG, false>(A, dlt, acc, F);
      dlt = -dlt;
      t_step<64, 0, false>(A, dlt, acc, F);
    }
  } else {
    t_step<64, STG, true>(A, dlt, acc, F);
    for (int k = 0; k < 3; ++k) {
      dlt = -dlt;
      t_step<64, 0, false>(A, dlt, acc, F);
      t_step<64, STG, false>(A, dlt, acc, F);
    }
    t_step<64, 0, false>(A, 0, acc, F);
    SB(); BAR(); MEMB(); mid(); SB();
    t_step<64, STG, false>(A, 0, acc, F);
  }
  SB(); BAR(); SB();
  t_last(acc, F);
}

struct G3P { bf16x8 p[2][5]; s16x4 ps[3]; };
struct G3W { s16x4 w[2][2][2], ws[2]; };
template <int KH>
DEV void g3_readw(G3W& n, unsigned a0, unsigned a1) {
#pragma unroll
  for (int q = 0; q < 8; ++q) n.w[q >> 2][(q >> 1) & 1][q & 1] = ldtr((((q >> 1) & 1) ? a1 : a0) + (2 * KH + (q >> 2)) * 4096 + (q & 1) * 2048);
  n.ws[0] = ldtr(a0 + 16384); n.ws[1] = ldtr(a1 + 16384);
}
template <int KH, bool MM, bool RDW>
DEV void g3_stage(f32x4 (&acc)[5][2], const G3P& cp, const G3W& cw, G3P& np, G3W& nw, unsigned aP, unsigned aS, unsigned a0, unsigned a1) {
  constexpr int M0 = KH ? 3 : 0, NS = KH ? 2 : 3, NM = 20 + 2 * NS, NR = 20 + NS;
  SB(); BAR(); MEMB(); SB();
#pragma unroll
  for (int i = 0; i < NM; ++i) {
    if (MM) {
      if (i >= 10 && i < 10 + 2 * NS) acc[M0 + ((i - 10) >> 1)][i & 1] = MFMA16(cw.ws[i & 1], cp.ps[(i - 10) >> 1], acc[M0 + ((i - 10) >> 1)][i & 1]);
      else { const int ii = i < 10 ? i : i - 2 * NS, P = ii / 10, mm = (ii % 10) >> 1, m = (KH != P) ? (mm + 3) % 5 : mm, cc = ii & 1; acc[m][cc] = MFMA(cat(cw.w[P][cc][0], cw.w[P][cc][1]), cp.p[P][m], acc[m][cc]); }
    }
    SB();
#pragma unroll
    for (int r = (i * 3) / 2; r < ((i + 1) * 3) / 2; ++r) {
      if (r < 18) { const int P = r / 9, q = r % 9;
        if (q < 4) { if (RDW) nw.w[P][q >> 1][q & 1] = ldtr(((q >> 1) ? a1 : a0) + (2 * KH + P) * 4096 + (q & 1) * 2048); }
        else np.p[P][q - 4] = ld128(aP + P * 1024 + (q - 4) * 4096); }
      else if (r < 20) { if (RDW) nw.ws[r - 18] = ldtr(((r - 18) ? a1 : a0) + 16384); }
      else if (r < NR) np.ps[r - 20] = ld64(aS + (M0 + r - 20) * 512);
    }
    SB();
  }
  WAITL0();
}
template <int KH>
DEV void g3_compute(unsigned lds0, int dh, int lane, char* out, int tt, int dd) {
  const int g = lane >> 4, qr = (lane >> 2) & 3, p = lane & 3;
  const int rr = 4 * g + qr;
  unsigned bW[2];
#pragma unroll
  for (int c = 0; c < 2; ++c) bW[c] = lds0 + L_O + rr * 128 + (((4 * dh + p) ^ (((rr >> 1) & 1) | (rr & 4))) << 4) + ((c ^ (p & 1)) * 8);
  const unsigned bP = lds0 + L_P + lane * 16 + KH * 2048, bS = lds0 + L_P + 20480 + lane * 8;
  G3W a0, a1, a2;
  g3_readw<KH>(a0, bW[0], bW[1]); g3_readw<KH>(a1, bW[0] + STGO, bW[1] + STGO); g3_readw<KH>(a2, bW[0] + 2 * STGO, bW[1] + 2 * STGO);
  f32x4 acc[5][2];
#pragma unroll
  for (int m = 0; m < 5; ++m) { acc[m][0] = (f32x4){0, 0, 0, 0}; acc[m][1] = (f32x4){0, 0, 0, 0}; }
  WAITL0();
  SB(); BAR(); SB();
  G3P p0, p1; G3W w0, w1;
  g3_stage<KH, false, false>(acc, p1, a0, p0, w0, bP, bS, 0, 0);
  g3_stage<KH, true, false>(acc, p0, a0, p1, w0, bP + STGP, bS + STGP, 0, 0);
  g3_stage<KH, true, false>(acc, p1, a1, p0, w0, bP + 2 * STGP, bS + 2 * STGP, 0, 0);
  g3_stage<KH, true, true>(acc, p0, a2, p1, w1, bP, bS, bW[0] + 3 * STGO, bW[1] + 3 * STGO);
  unsigned sp = STGP, sw = 4 * STGO;
  for (int j = 4; j < 16; j += 2) {
    g3_stage<KH, true, true>(acc, p1, w1, p0, w0, bP + sp, bS + sp, bW[0] + sw, bW[1] + sw);
    sp = sp == 2 * STGP ? 0 : sp + STGP; sw = sw == 4 * STGO ? 0 : sw + STGO;
    g3_stage<KH, true, true>(acc, p0, w0, p1, w1, bP + sp, bS + sp, bW[0] + sw, bW[1] + sw);
    sp = sp == 2 * STGP ? 0 : sp + STGP; sw = sw == 4 * STGO ? 0 : sw + STGO;
  }
  {
    constexpr int M0 = KH ? 3 : 0, NS = KH ? 2 : 3;
    SB(); BAR(); SB();
#pragma unroll
    for (int i = 0; i < 2 * NS; ++i) { acc[M0 + (i >> 1)][i & 1] = MFMA16(w1.ws[i & 1], p1.ps[i >> 1], acc[M0 + (i >> 1)][i & 1]); SB(); }
#pragma unroll
    for (int i = 0; i < 20; ++i) { const int P = (i >> 1) & 1, m = KH ? i >> 2 : 4 - (i >> 2), cc = i & 1; acc[m][cc] = MFMA(cat(w1.w[P][cc][0], w1.w[P][cc][1]), p1.p[P][m], acc[m][cc]); SB(); }
  }
#pragma unroll
  for (int m = 0; m < 5; ++m)
    if ((m >= 3) != KH) { *(LAS f32x4*)(size_t)(lds0 + (dh * 10 + m * 2) * 1024 + lane * 16) = acc[m][0]; *(LAS f32x4*)(size_t)(lds0 + (dh * 10 + m * 2 + 1) * 1024 + lane * 16) = acc[m][1]; }
  WL0();
  BAR(); MEMB();
  auto rO = RSRC(out, 2048 * 1152);
  const bool sw2 = g & 1;
#pragma unroll
  for (int m = 0; m < 5; ++m)
    if ((m >= 3) == KH) {
      const f32x4 a = acc[m][0] + *(LAS f32x4*)(size_t)(lds0 + (dh * 10 + m * 2) * 1024 + lane * 16), b = acc[m][1] + *(LAS f32x4*)(size_t)(lds0 + (dh * 10 + m * 2 + 1) * 1024 + lane * 16);
      const unsigned a0 = cvt_pk(a[0], a[1]), a1 = cvt_pk(a[2], a[3]), b0 = cvt_pk(b[0], b[1]), b1 = cvt_pk(b[2], b[3]);
      const u32x4 v = {sw2 ? b0 : a0, sw2 ? b1 : a1, sw2 ? a0 : b0, sw2 ? a1 : b1};
      __builtin_amdgcn_raw_buffer_store_b128(v, rO, (tt * 80 + m * 16 + (lane & 15)) * 1152 + dd * 128 + dh * 64 + g * 16, 0, OAUX);
    }
}

DEV void front(unsigned lds0, int wid, int wt, int WF, int lane, int ti, int fj, char* ws) {
  const int r16 = lane & 15, g = lane >> 4, qr = (lane >> 2) & 3, p = lane & 3;
  TAddr A;
  const int t01 = 4 * wt + 2 - 2 * WF, t23 = 4 * wt + 2 * WF;
  auto tblk = [&](int m) { return (m < 2 ? t01 : t23) + (m & 1); };
  A.w[0][0] = lds0 + (16 * t01 + r16) * 128 + ((g ^ (r16 >> 1)) << 4); A.w[1][0] = A.w[0][0] ^ 64;
  A.w[0][1] = lds0 + (16 * t23 + r16) * 128 + ((g ^ (r16 >> 1)) << 4); A.w[1][1] = A.w[0][1] ^ 64;
#pragma unroll
  for (int u = 0; u < 2; ++u) {
    const unsigned b = lds0 + 16384 + (16 * u + 4 * g + qr) * 288 + p * 8;
    A.z[u][0] = b + WF * 128 + (p & 1) * 32; A.z[u][1] = b + WF * 128 + 32 - (p & 1) * 32; A.s[u] = b + 256;
  }
  asm volatile("" : "+v"(A.w[0][0]), "+v"(A.w[0][1]), "+v"(A.w[1][0]), "+v"(A.w[1][1]), "+v"(A.s[0]), "+v"(A.s[1]), "+v"(A.z[0][0]), "+v"(A.z[0][1]), "+v"(A.z[1][0]), "+v"(A.z[1][1]));
  f32x4 acc[4][5];
  in_pass<false>(A, acc, []() {});
  CST(0);
  u32x2 ua[4][4], ub[2];
  {
    f32x2 s1[4], s2[4];
#pragma unroll
    for (int mm = 0; mm < 4; ++mm) {
      const int m = mm;
      s1[m] = (f32x2){0.f, 0.f}; s2[m] = (f32x2){0.f, 0.f};
      gelu_row<true>(m, acc, ua, ub, s1[m], s2[m]);
    }
#pragma unroll
    for (int m = 0; m < 4; ++m) *(LAS f32x2*)(size_t)(lds0 + L_T + (16 * tblk(m) + r16) * 64 + (WF * 4 + g) * 8) = (f32x2){s1[m].x + s1[m].y, s2[m].x + s2[m].y};
  }
#pragma unroll
  for (int m = 0; m < 4; ++m) asm volatile("" : "+v"(ua[m][0]), "+v"(ua[m][1]), "+v"(ua[m][2]), "+v"(ua[m][3]));
  asm volatile("" : "+v"(ub[0]), "+v"(ub[1]));
#pragma unroll
  for (int u = 0; u < 2; ++u) { A.z[u][0] -= (p & 1) * 32; A.z[u][1] += (p & 1) * 32; }
  asm volatile("" : "+v"(A.z[0][0]), "+v"(A.z[0][1]), "+v"(A.z[1][0]), "+v"(A.z[1][1]));
  CST(1);
  in_pass<true>(A, acc, [&]() {
    CST(2);
#pragma unroll
    for (int m = 0; m < 4; ++m) asm volatile("" : "+v"(ua[m][0]), "+v"(ua[m][1]), "+v"(ua[m][2]), "+v"(ua[m][3]));
    asm volatile("" : "+v"(ub[0]), "+v"(ub[1]));
    int lz = lane; asm volatile("" : "+v"(lz));
    auto rZ = RSRC(ws + WS_Z2, FRAG_BYTES);
    const unsigned sl = (lz >> 4) * 16 + ((lz & 15) ^ (4 * (lz >> 5))), dst = (fj * 128 + ti * 8) * CH;
#pragma unroll
    for (int m = 0; m < 4; ++m) {
      const unsigned ar = lds0 + L_R + (16 * tblk(m) + (lz & 15)) * 32;
      const f32x4 q4 = *(LAS f32x4*)(size_t)ar + *(LAS f32x4*)(size_t)(ar + 16);
      const float mean = (q4[0] + q4[2]) * (1.0f / 2304.0f), var = fmaxf((q4[1] + q4[3]) * (1.0f / 2304.0f) - mean * mean, 0.f);
      const float rs = __builtin_amdgcn_rsqf(var + 1e-5f), sh = -mean * rs;
      auto norm = [&](unsigned v) { const f32x2 y = (f32x2){bflo(v), bfhi(v)} * rs + sh; return cvt_pk(y.x, y.y); };
#pragma unroll
      for (int pp = 0; pp < 2; ++pp)
        __builtin_amdgcn_raw_buffer_store_b128((u32x4){norm(ua[m][2 * pp].x), norm(ua[m][2 * pp].y), norm(ua[m][2 * pp + 1].x), norm(ua[m][2 * pp + 1].y)}, rZ, dst + tblk(m) * CH + sl * 16 + WF * 2048 + pp * 1024, 0, 16);
      if (m >= 2) __builtin_amdgcn_raw_buffer_store_b64((u32x2){norm(ub[m & 1].x), norm(ub[m & 1].y)}, rZ, dst + tblk(m) * CH + sl * 8 + 4096, 0, 16);
    }
    CST(3);
  });
  CST(4);
#pragma unroll
  for (int m = 0; m < 4; ++m) asm volatile("" : "+v"(acc[m][0]), "+v"(acc[m][1]), "+v"(acc[m][2]), "+v"(acc[m][3]));
  asm volatile("" : "+v"(acc[2][4]), "+v"(acc[3][4]));
  const u32x2 tg = *(LAS u32x2*)(size_t)(lds0 + L_TAG);
  const u64 tag = ((u64)(unsigned)__builtin_amdgcn_readfirstlane(tg.y) << 32) | (unsigned)__builtin_amdgcn_readfirstlane(tg.x);
  WV(0);
  if (lane == 0 && __hip_atomic_fetch_add((LAS unsigned*)(size_t)(lds0 + L_CNT), 1u, __ATOMIC_RELAXED, __HIP_MEMORY_SCOPE_WORKGROUP) == 3u)
    __hip_atomic_store((u64*)(ws + WS_F1 + (fj * 16 + ti) * 128), tag, RLX_AGENT);
  CST(5);
  SB();
  u32x2 za[4][4], zb[2];
#pragma unroll
  for (int m = 0; m < 4; ++m) {
    f32x2 d1, d2;
    gelu_row<false>(m, acc, za, zb, d1, d2);
    asm volatile("" : "+v"(za[m][0]), "+v"(za[m][1]), "+v"(za[m][2]), "+v"(za[m][3]));
  }
  asm volatile("" : "+v"(zb[0]), "+v"(zb[1]));
  CST(6);
#pragma unroll
  for (int u = 0; u < 2; ++u) {
    const unsigned sl = p * 16 + 8 * (g & 1) + 4 * (u ^ (p >> 1)) + qr;
#pragma unroll
    for (int h = 0; h < 2; ++h) A.z[u][h] = lds0 + 16384 + (g >> 1) * CH + WF * 2048 + sl * 16 + ((h ^ (p & 1)) * 8);
    A.s[u] = lds0 + 16384 + (g >> 1) * CH + 4096 + sl * 8;
  }
  asm volatile("" : "+v"(A.s[0]), "+v"(A.s[1]), "+v"(A.z[0][0]), "+v"(A.z[0][1]), "+v"(A.z[1][0]), "+v"(A.z[1][1]));
  f32x4 bias4[4];
#pragma unroll
  for (int m = 0; m < 4; ++m) { const float b = __uint_as_float((unsigned)*(LAS u16*)(size_t)(lds0 + L_BIAS + (tblk(m) * 16 + r16) * 2) << 16); bias4[m] = (f32x4){b, b, b, b}; }
  TFrag F;
  int dlt = 2 * STG;
  WAITL0();
  CST(7);
  t_step<1024, 0, true>(A, dlt, acc, F, bias4);
  CST(8);
  t_step<1024, STG, false>(A, dlt, acc, F);

  for (int k = 0; k < 15; ++k) {
    dlt = -dlt;
    t_step<1024, 0, false>(A, dlt, acc, F);
    t_step<1024, STG, false>(A, dlt, acc, F);
  }
  auto rP = RSRC(ws + WS_P, FRAG_BYTES);
  auto gate = [&](f32x4 a, u32x2 z) {
    const f32x2 x = (f32x2){a[0], a[1]} * (f32x2){bflo(z.x), bfhi(z.x)}, y = (f32x2){a[2], a[3]} * (f32x2){bflo(z.y), bfhi(z.y)};
    return (u32x2){cvt_pk(x.x, x.y), cvt_pk(y.x, y.y)};
  };
  const unsigned dst0 = ti * 8 * 16 * CH + fj * CH;
  CST(9);
  t_last(acc, F);
#pragma unroll
  for (int m = 0; m < 4; ++m) {
#pragma unroll
    for (int pp = 0; pp < 2; ++pp) {
      const u32x2 a = gate(acc[m][2 * pp], za[m][2 * pp]), b = gate(acc[m][2 * pp + 1], za[m][2 * pp + 1]);
      __builtin_amdgcn_raw_buffer_store_b128((u32x4){a.x, a.y, b.x, b.y}, rP, dst0 + tblk(m) * 16 * CH + lane * 16 + (2 * WF + pp) * 1024, 0, 16);
    }
    if (m >= 2) __builtin_amdgcn_raw_buffer_store_b64(gate(acc[m][4], zb[m & 1]), rP, dst0 + tblk(m) * 16 * CH + lane * 8 + 4096, 0, 16);
  }
  CST(10);
  WV(0);
  CST(11);
  if (lane == 0 && __hip_atomic_fetch_add((LAS unsigned*)(size_t)(lds0 + L_CNT), 1u, __ATOMIC_RELAXED, __HIP_MEMORY_SCOPE_WORKGROUP) == 7u)
    __hip_atomic_store((u64*)(ws + WS_F2 + (ti * 16 + fj) * 128), tag, RLX_AGENT);
  SB(); BAR(); MEMB(); SB();
}

template <int NP> DEV unsigned pollN(const char* base, u64 tag, int lane) {
  const u64 v = __hip_atomic_load((const u64*)(base + min(lane, NP - 1) * 128), RLX_AGENT);
  return (unsigned)__builtin_amdgcn_ballot_w64(v == tag) & ((1u << NP) - 1);
}
DEV bool got(unsigned mask, int who) { return (mask >> who) & 1u; }

__global__ __launch_bounds__(512, 2) void gmlp_fused(const char* __restrict__ X, const char* __restrict__ Win, const char* __restrict__ Wsp, const u16* __restrict__ bias,
                                                     const char* __restrict__ Wo, char* __restrict__ out, char* __restrict__ ws) {
  extern __shared__ __attribute__((aligned(16))) char smem[];
  u64 pc0; asm volatile("s_getpc_b64 %0" : "=s"(pc0));
  const int tid = threadIdx.x, wid = __builtin_amdgcn_readfirstlane(tid >> 6), lane = tid & 63;
  const int bid = blockIdx.x, xg = bid & 7, rk = bid >> 3, il = rk & 7, jl = rk >> 3, ti = (xg & 1) * 8 + il, fj = (xg >> 1) * 4 + jl;
  const int L3 = xg * 30 + rk, tt = L3 / 9, dd = L3 - tt * 9;
  const bool has3 = rk < 30 && L3 < 234;
  const unsigned lds0 = (unsigned)(size_t)(LAS char*)smem;
  if (__builtin_expect(wid >= 4, 1)) {
    __builtin_amdgcn_s_setprio(3);
    const int lw = wid - 4;
    const bool big = wid < 6;
#define WVS(STAGES, EXTRA) do { if (big) WV(9 * (STAGES) + (EXTRA)); else WV(8 * (STAGES) + (EXTRA)); } while (0)
    auto rX = RSRC(X, 2048 * 1152); auto rWi = RSRC(Win, 576 * 9216);
    unsigned vX[4], vW[5];
    const unsigned zp0 = lw < 2 ? lw * 5 : 2 + lw * 4;
    const unsigned sX = (ti * 128 + lw * 32) * 1152, sW = fj * 288;
    const int rot = ROT1 == 0 ? 0 : ROT1 == 1 ? il + 2 * jl : 3 * ((il + jl) % 3);
    const int rot8 = ROT1 == 1 ? (il + 2 * jl) & 7 : ROT1 == 2 ? (((il + jl) % 3) * 8) / 3 : 0;
    auto k1 = [&](int k) { return k == 0 ? 0 : 1 + ((k - 1 + rot8) & 7); };
    auto issue1 = [&](int half, int k) {
      const int kk = k1(k);
      const unsigned d = lds0 + ((k + 1 - half) & 3) * STG, dz = d + 16384 + zp0 * 1024, sw = sW + half * 4608 + kk * 64 * 9216;
      GL4(rX, d + lw * 4096, vX[0], vX[1], vX[2], vX[3], sX + kk * 128);
      GL4(rWi, dz, vW[0], vW[1], vW[2], vW[3], sw);
      if (big) GL(rWi, dz + 4096, vW[4], sw, 0);
    };
    {
#pragma unroll
      for (int q = 0; q < 4; ++q) { const int row = 8 * q + (lane >> 3); vX[q] = row * 1152 + (((lane & 7) ^ ((row >> 1) & 7)) << 4) - q * 1024; }
      GL4(rX, lds0 + lw * 4096, vX[0], vX[1], vX[2], vX[3], sX + k1(0) * 128);
      SB();
#pragma unroll
      for (int q = 0; q < 5; ++q) {
        const unsigned c16 = (zp0 + q) * 64 + lane, R = (c16 * 3641u) >> 16, c = (c16 - R * 18) * 16, k = (R & 32) + 8 * ((R >> 2) & 3) + 4 * ((R >> 4) & 1) + (R & 3);
        vW[q] = k * 9216 + c - (q & 3) * 1024;
      }
      const unsigned dz = lds0 + 16384 + zp0 * 1024, sw = sW + 4608 + k1(0) * 64 * 9216;
      GL4(rWi, dz, vW[0], vW[1], vW[2], vW[3], sw);
      if (big) GL(rWi, dz + 4096, vW[4], sw, 0);
    }
    const unsigned vL = lane * 16;
    auto rWs = RSRC(Wsp, 2048 * 4096); auto rWo = RSRC(Wo, 2304 * 1152);
    const unsigned e9 = lw * 21 + lane, r9 = (e9 * 7282u) >> 16, v81 = lane < 21 && e9 < 81 ? r9 * 9216 + (e9 - r9 * 9) * 128 : OOB, s81 = (bid >> 2) * 9 * 9216 + (bid & 3) * 1152;
    issue1(1, 1);
    WVS(1, T0W ? 2 : 0); LBAR();
    issue1(1, 2);
    WVS(1, 0);
    for (int k = 1; k < 5; ++k) { LBAR(); issue1(1, k + 2); WVS(1, 0); }
    LBAR(); issue1(1, 7); WVS(1, 0);
    LBAR(); issue1(1, 8); WVS(1, 0);
    LBAR(); issue1(0, 0); WVS(1, 0);
    LBAR(); issue1(0, 1);
    const u64 ep = __hip_atomic_load((const u64*)(ws + WS_EP), RLX_AGENT);
    if (lw == 0) __builtin_amdgcn_raw_ptr_buffer_load_lds(RSRC(bias, 4096), (LAS void*)(size_t)(lds0 + L_BIAS), 4, lane * 4, ti * 256, 0, 0);
    TOUCH(rWs, lane * 128, bid * 32768 + lw * 8192);
    LBAR();
    issue1(0, 2); issue1(0, 3);
    const unsigned tagA = __builtin_amdgcn_readfirstlane((unsigned)ep) + 0x9E3779B9u, tagB = __builtin_amdgcn_readfirstlane((unsigned)(ep >> 32)) + 0x85EBCA6Bu;
    const u64 tag = ((u64)tagB << 32) | tagA;
    if (lw == 0) { const u32x4 tg = {tagA, tagB, 0u, 0u}; asm volatile("ds_write_b128 %0, %1\n\ts_waitcnt lgkmcnt(0)" :: "v"(lds0 + L_TAG), "v"(tg) : "memory"); }
    unsigned vS[4];
#pragma unroll
    for (int q = 0; q < 4; ++q) { const int row = 8 * q + (lane >> 3); vS[q] = row * 4096 + (((lane & 7) ^ ((row >> 1) & 7)) << 4) - q * 1024; }
    const unsigned sWs = (ti * 128 + lw * 32) * 4096;
    auto kpos = [&](int n) { return n; };
    auto issueW = [&](int n) { GL4(rWs, lds0 + (n & 3) * STG + lw * 4096, vS[0], vS[1], vS[2], vS[3], sWs + kpos(n) * 128); };
    WVS(2, 1);
    LBAR();
    auto rS = RSRC(ws + WS_ST, 16 * 16 * 128 * 16);
    if (lane < 32) {
      const int row = lw * 32 + lane;
      f32x4 v0, v1, v2, v3;
      asm volatile("ds_read_b128 %0, %4\n\tds_read_b128 %1, %4 offset:16\n\tds_read_b128 %2, %4 offset:32\n\tds_read_b128 %3, %4 offset:48\n\ts_waitcnt lgkmcnt(0)"
                   : "=&v"(v0), "=&v"(v1), "=&v"(v2), "=&v"(v3) : "v"(lds0 + L_T + row * 64) : "memory");
      const f32x4 s = (v0 + v1) + (v2 + v3);
      u32x4 gq; gq.x = tagA; gq.y = __float_as_uint(s[0] + s[2]); gq.z = tagB; gq.w = __float_as_uint(s[1] + s[3]);
      __builtin_amdgcn_raw_buffer_store_b128(gq, rS, ((ti * 16 + fj) * 128 + row) * 16, 0, 16);
    }
    u32x4 sv[8];
    const u32x4 srd = {(unsigned)(size_t)(ws + WS_ST), (unsigned)((size_t)(ws + WS_ST) >> 32) & 0xffffu, 16 * 16 * 128 * 16, 0x00020000u};
    auto sweep = [&]() {
      const unsigned s0 = (ti * 16 + lw * 4) * 2048, v1 = vL + 4096;
      asm volatile("buffer_load_dwordx4 %0, %8, %10, %11 offen sc1\n\tbuffer_load_dwordx4 %1, %8, %10, %11 offen offset:1024 sc1\n\tbuffer_load_dwordx4 %2, %8, %10, %11 offen offset:2048 sc1\n\t"
                   "buffer_load_dwordx4 %3, %8, %10, %11 offen offset:3072 sc1\n\tbuffer_load_dwordx4 %4, %9, %10, %11 offen sc1\n\tbuffer_load_dwordx4 %5, %9, %10, %11 offen offset:1024 sc1\n\t"
                   "buffer_load_dwordx4 %6, %9, %10, %11 offen offset:2048 sc1\n\tbuffer_load_dwordx4 %7, %9, %10, %11 offen offset:3072 sc1"
                   : "=&v"(sv[0]), "=&v"(sv[1]), "=&v"(sv[2]), "=&v"(sv[3]), "=&v"(sv[4]), "=&v"(sv[5]), "=&v"(sv[6]), "=&v"(sv[7]) : "v"(vL), "v"(v1), "s"(srd), "s"(s0) : "memory");
    };
    LBAR(); issue1(0, 4); WVS(2, 1);
    LBAR(); issue1(0, 5); WVS(2, 0);
    LBAR(); issue1(0, 6); WVS(2, 0);
    LBAR(); issue1(0, 7); WVS(2, 0);
    LBAR(); sweep(); WVS(1, 8);
    LBAR(); issue1(0, 8);
    WVS(1, 8);
    LBAR();
    {
      f32x4 s;
      auto total = [&](const u32x4 (&v)[8]) {
        unsigned bad = 0;
        s = (f32x4){0.f, 0.f, 0.f, 0.f};
#pragma unroll
        for (int p = 0; p < 8; ++p) { bad |= (v[p].x ^ tagA) | (v[p].z ^ tagB); s[(p & 1) * 2] += __uint_as_float(v[p].y); s[(p & 1) * 2 + 1] += __uint_as_float(v[p].w); }
        return __builtin_amdgcn_ballot_w64(bad != 0) == 0;
      };
      WVS(1, 0);
      asm volatile("" : "+v"(sv[0]), "+v"(sv[1]), "+v"(sv[2]), "+v"(sv[3]), "+v"(sv[4]), "+v"(sv[5]), "+v"(sv[6]), "+v"(sv[7]) :: "memory");
      if (__builtin_expect(!total(sv), 0)) {
#pragma unroll 1
        for (unsigned spins = 0;; ++spins) {
          u32x4 rv[8];
          MEMB();
#pragma unroll
          for (int p = 0; p < 8; ++p) rv[p] = __builtin_amdgcn_raw_buffer_load_b128(rS, vL + (p & 1) * 1024, (ti * 16 + lw * 4 + (p >> 1)) * 2048, 16);
          if (total(rv)) break;
          if (spins > SPIN_CAP) { s[0] = s[2] = __builtin_nanf(""); break; }
        }
      }
      const f32x2 o0 = {s[0], s[1]}, o1 = {s[2], s[3]};
      asm volatile("ds_write_b64 %0, %1\n\tds_write_b64 %0, %2 offset:2048\n\ts_waitcnt lgkmcnt(0)" :: "v"(lds0 + L_R + lane * 32 + lw * 8), "v"(o0), "v"(o1) : "memory");
    }
    LBAR();
    WV(0);
    LBAR();
    issueW(0); issueW(2);
    LBAR();
    issueW(1); issueW(3);
    TOUCH(rWo, lane < 21 ? lane * 128 : OOB, bid * 10368 + lw * 2688);
    auto rZ = RSRC(ws + WS_Z2, FRAG_BYTES);
    const unsigned sZ = fj * 128 * CH + zp0 * 1024;
    unsigned ready = 0;
    auto issueZ = [&](int n) {
      const int kp = kpos(n);
      if (__builtin_expect(!got(ready, kp >> 1), 0)) { for (unsigned spins = 0; !got(ready, kp >> 1) && spins < SPIN_CAP; ++spins) ready = pollN<16>(ws + WS_F1 + fj * 16 * 128, tag, lane); }
      MEMB();
      const unsigned dz = lds0 + (n & 3) * STG + 16384 + zp0 * 1024, so = sZ + kp * 18432;
      GS4(rZ, dz, vL, so);
      if (big) GS(rZ, dz + 4096, vL, so + 4096, 0);
    };
    const char* f1 = ws + WS_F1 + fj * 16 * 128;
    for (int q = 0; q < PDELAY; ++q) __builtin_amdgcn_s_sleep(4);
    { unsigned np = 0; for (; ready != 7u && np < SPIN_CAP; ++np) ready = pollN<3>(f1, tag, lane); LST(0, np); }
    issueZ(0); issueZ(1); issueZ(2);
    WVS(2, -8);
    LBAR();
    u64 fv;
    asm volatile("global_load_dwordx2 %0, %1, %2 sc1" : "=&v"(fv) : "v"((lane & 15) * 128), "s"(f1) : "memory");
    issueZ(3);
    WVS(2, -7);
    LBAR();
    issueW(4); issueZ(4);
    WVS(2, -3);
    LBAR();
    issueW(5); issueZ(5);
    WVS(2, 0);
    asm volatile("" : "+v"(fv));
    ready |= (unsigned)__builtin_amdgcn_ballot_w64(fv == tag) & 0xffffu;
    LBAR();
    for (int n = 6; n < 32; ++n) {
      issueW(n); issueZ(n);
      WVS(2, 0);
      LBAR();
    }
    unsigned vO[2], vOh;
#pragma unroll
    for (int q = 0; q < 2; ++q) { const int row = 36 * lw + 8 * q + (lane >> 3); vO[q] = row * 1152 + (((lane & 7) ^ (((row >> 1) & 1) | (row & 4))) << 4); }
    { const int row = 36 * lw + 32 + ((lane >> 3) & 3); vOh = row * 1152 + (((lane & 7) ^ (((row >> 1) & 1) | (row & 4))) << 4); }
    const unsigned vO1 = vO[1] - 1024, vO2 = vO[0] + 16 * 1152 - 2048, vO3 = vO[1] + 16 * 1152 - 3072;
    const unsigned sO = min(dd, 8) * 128;
    auto issueO = [&](int n, int slot) {
      const unsigned d = lds0 + L_O + slot * STGO + lw * 4608, s0 = sO + n * 144 * 1152;
      GL4(rWo, d, vO[0], vO1, vO2, vO3, s0);
      if (lane < 32) GL(rWo, d + 4096, vOh, s0, 0);
    };
    issueO(0, 0);
    WVS(1, 5); LBAR();
    issueO(1, 1); issueO(2, 2);
    WV(15); LBAR();
    WV(0);
    LBAR();
    if (!has3) return;
    issueO(3, 3); issueO(4, 4);
    LBAR();
    issueO(5, 0);
    if (NPRE3 == 5) { issueO(6, 1); issueO(7, 2); }
    auto rP = RSRC(ws + WS_P, FRAG_BYTES);
    const unsigned vPs = lw < 3 ? (lane >> 5) * 16 * CH + (lane & 31) * 16 + 4096 : OOB;
    const int limS = lw == 2 ? 32 : 64;
    const unsigned sP = tt * 5 * 16 * CH;
    const int pa = (tt * 80) >> 7, pb = min((tt * 80 + 79) >> 7, 15);
    unsigned rdy = 0;
    auto issueP = [&](int j, int slot) {
      if (__builtin_expect(!got(rdy, j), 0)) { for (unsigned spins = 0; !got(rdy, j) && spins < SPIN_CAP; ++spins) rdy = pollN<16>(ws + WS_F2 + pa * 16 * 128, tag, lane) & pollN<16>(ws + WS_F2 + pb * 16 * 128, tag, lane); }
      MEMB();
      const unsigned d = lds0 + L_P + slot * STGP, sp = sP + j * CH;
      GS4(rP, d + lw * 4096, vL, sp + lw * 16 * CH);
      GS(rP, d + 16384 + lw * 1024, vL, sp + 64 * CH + lw * 1024, 0);
      if (lane < limS) GS(rP, lw < 3 ? d + 20480 + lw * 1024 : lds0 + L_DUMMY, vPs, sp + lw * 32 * CH, 0);
    };
    const char *fa = ws + WS_F2 + pa * 16 * 128, *fb = ws + WS_F2 + pb * 16 * 128;
    { unsigned np = 0; LST(2, 0); for (; rdy != 7u && np < SPIN_CAP; ++np) rdy = pollN<3>(fa, tag, lane) & pollN<3>(fb, tag, lane); LST(1, np); }
    u64 fva, fvb;
    asm volatile("global_load_dwordx2 %0, %2, %3 sc1\n\tglobal_load_dwordx2 %1, %2, %4 sc1" : "=&v"(fva), "=&v"(fvb) : "v"((lane & 15) * 128), "s"(fa), "s"(fb) : "memory");
    issueP(0, 0); issueP(1, 1); issueP(2, 2);
    WV(12);
    asm volatile("" : "+v"(fva), "+v"(fvb));
    rdy |= (unsigned)(__builtin_amdgcn_ballot_w64(fva == tag) & __builtin_amdgcn_ballot_w64(fvb == tag)) & 0xffffu;
    LBAR();
    WV(6);
    if (NPRE3 == 5) {
      LBAR(); issueP(3, 0); WV(6);
      LBAR(); issueP(4, 1); WV(6);
      LBAR(); issueP(5, 2); WV(6);
    } else {
      LBAR(); issueP(3, 0); issueO(6, 1); WV(11);
      LBAR(); issueP(4, 1); issueO(7, 2); WV(16);
      LBAR(); issueP(5, 2); WV(11);
    }
    LBAR(); issueP(6, 0); issueO(8, 3); WV(11);
    int sp3 = 1, so5 = 4;
    for (int n = 5; n < 12; ++n) {
      LBAR();
      issueP(n + 2, sp3); issueO(n + 4, so5);
      sp3 = sp3 == 2 ? 0 : sp3 + 1; so5 = so5 == 4 ? 0 : so5 + 1;
      WV(16);
    }
    LBAR(); issueP(14, 2); WV(6);
    LBAR(); issueP(15, 0); WV(6);
    LBAR(); WV(0);
    LBAR();
    LBAR(); LBAR();
    if (bid == 0 && tid == 256) __hip_atomic_store((u64*)(ws + WS_EP), tag, RLX_AGENT);
  } else {
    const int wt = wid >> 1, wf = wid & 1;
    if (wid == 0) {
      auto rC = RSRC((const char*)(pc0 & ~(u64)255), CODE_KB * 1024);
      GL(rC, lds0 + L_DUMMY, lane * 16, rk * 1024, 0); GL(rC, lds0 + L_DUMMY, lane * 16, rk * 1024 + 32768, 0);
      WV(0);
    }
    front(lds0, wid, wt, wf, lane, ti, fj, ws);
    if (!has3) return;
    if (wt == 0) g3_compute<0>(lds0, wf, lane, out, tt, dd); else g3_compute<1>(lds0, wf, lane, out, tt, dd);
  }
}

extern "C" void kernel_launch(void* const* d_in, const int* in_sizes, int n_in, void* d_out, int out_size, void* d_ws, size_t ws_size, hipStream_t stream) {
  (void)hipFuncSetAttribute((const void*)gmlp_fused, hipFuncAttributeMaxDynamicSharedMemorySize, LDS_BYTES);
  gmlp_fused<<<256, 512, LDS_BYTES, stream>>>((const char*)d_in[0], (const char*)d_in[1], (const char*)d_in[2], (const u16*)d_in[3], (const char*)d_in[4], (char*)d_out, (char*)d_ws);
}
```

```cpp
#include <hip/hip_runtime.h>
#include <hip/hip_bf16.h>
typedef unsigned short u16;
typedef unsigned long long u64;
typedef short s16x4 __attribute__((ext_vector_type(4)));
typedef short bf16x8 __attribute__((ext_vector_type(8)));
typedef float f32x2 __attribute__((ext_vector_type(2)));
typedef float f32x4 __attribute__((ext_vector_type(4)));
typedef unsigned u32x4 __attribute__((ext_vector_type(4)));
typedef unsigned u32x2 __attribute__((ext_vector_type(2)));
#define LAS __attribute__((address_space(3)))
#define DEV __device__ __forceinline__
#define BAR() __builtin_amdgcn_s_barrier()
#define SB() __builtin_amdgcn_sched_barrier(0)
#define MEMB() asm volatile("" ::: "memory")
#define WAITL0() __builtin_amdgcn_s_waitcnt(0xC07F)
#define WL0() asm volatile("s_waitcnt lgkmcnt(0)" ::: "memory")
#define WV(N) asm volatile("s_waitcnt vmcnt(%0)" ::"i"(N) : "memory")
#define MFMA(a, b, c) __builtin_amdgcn_mfma_f32_16x16x32_bf16(a, b, c, 0, 0, 0)
#define MFMA16(a, b, c) __builtin_amdgcn_mfma_f32_16x16x16bf16_1k(a, b, c, 0, 0, 0)
#define GL(R, D, V, S, I) __builtin_amdgcn_raw_ptr_buffer_load_lds(R, (LAS void*)(size_t)(D), 16, V, S, I, 0)
#define GL4(R, D, V0, V1, V2, V3, S) do { GL(R, D, V0, S, 0); GL(R, D, V1, S, 1024); GL(R, D, V2, S, 2048); GL(R, D, V3, S, 3072); } while (0)
#define GS(R, D, V, S, I) __builtin_amdgcn_raw_ptr_buffer_load_lds(R, (LAS void*)(size_t)(D), 16, V, S, I, 16)
#define GS4(R, D, V, S) do { GS(R, D, V, S, 0); GS(R, D, V, S, 1024); GS(R, D, V, S, 2048); GS(R, D, V, S, 3072); } while (0)
#define TOUCH(R, V, S) __builtin_amdgcn_raw_ptr_buffer_load_lds(R, (LAS void*)(size_t)(lds0 + L_DUMMY), 4, V, S, 0, 0)
#define RSRC(p, n) __builtin_amdgcn_make_buffer_rsrc((void*)(p), 0, (n), 0x00020000)
#define RLX_AGENT __ATOMIC_RELAXED, __HIP_MEMORY_SCOPE_AGENT
constexpr int CODE_KB = 35;

constexpr int CH = 4608;
constexpr int FRAG_BYTES = 16 * 128 * CH;
constexpr size_t WS_Z2 = 0, WS_P = FRAG_BYTES, WS_ST = 2 * (size_t)FRAG_BYTES, WS_F1 = WS_ST + 16 * 16 * 128 * 16, WS_F2 = WS_F1 + 256 * 128, WS_EP = WS_F2 + 256 * 128;
constexpr int STG = 34816, STGP = 23040, STGO = 18432, L_O = 0, L_P = 5 * STGO, LDS_BYTES = 163840;
constexpr int L_T = 139264, L_R = L_T + 8192, L_BIAS = 159744, L_TAG = L_BIAS + 256, L_CNT = L_TAG + 8, L_DUMMY = 161280;
constexpr unsigned SPIN_CAP = 1u << 16, OOB = 0x80000000u;

DEV unsigned cvt_pk(float lo, float hi) { unsigned r; asm("v_cvt_pk_bf16_f32 %0, %1, %2" : "=v"(r) : "v"(lo), "v"(hi)); return r; }
DEV bf16x8 ld128(unsigned a) { return *(LAS bf16x8*)(size_t)a; }
DEV s16x4 ld64(unsigned a) { return *(LAS s16x4*)(size_t)a; }
DEV s16x4 ldtr(unsigned a) { return __builtin_amdgcn_ds_read_tr16_b64_v4i16((LAS s16x4*)(size_t)a); }
DEV bf16x8 cat(s16x4 a, s16x4 b) { return __builtin_shufflevector(a, b, 0, 1, 2, 3, 4, 5, 6, 7); }
DEV float bflo(unsigned v) { return __uint_as_float(v << 16); }
DEV float bfhi(unsigned v) { return __uint_as_float(v & 0xffff0000u); }
template <int NB, bool SUM>
DEV void gelu_blocks(const f32x4* const (&v)[NB], u32x2* const (&o)[NB], f32x2& s1, f32x2& s2) {
  const float k1 = -2.0f * 0.7978845608028654f * 1.4426950408889634f, k2 = k1 * 0.044715f;
  f32x2 x[2 * NB], t[2 * NB];
#pragma unroll
  for (int i = 0; i < 2 * NB; ++i) { x[i] = (f32x2){(*v[i >> 1])[(i & 1) * 2], (*v[i >> 1])[(i & 1) * 2 + 1]}; t[i] = x[i] * x[i]; }
  SB();
#pragma unroll
  for (int i = 0; i < 2 * NB; ++i) t[i] = t[i] * k2 + k1;
  SB();
#pragma unroll
  for (int i = 0; i < 2 * NB; ++i) t[i] = t[i] * x[i];
  SB();
#pragma unroll
  for (int i = 0; i < 2 * NB; ++i) { t[i].x = __builtin_amdgcn_exp2f(t[i].x); t[i].y = __builtin_amdgcn_exp2f(t[i].y); }
  SB();
#pragma unroll
  for (int i = 0; i < 2 * NB; ++i) t[i] = t[i] + 1.0f;
  SB();
#pragma unroll
  for (int i = 0; i < 2 * NB; ++i) { t[i].x = __builtin_amdgcn_rcpf(t[i].x); t[i].y = __builtin_amdgcn_rcpf(t[i].y); }
  SB();
#pragma unroll
  for (int i = 0; i < 2 * NB; ++i) t[i] = t[i] * x[i];
  SB();
  f32x2 a1 = s1, a2 = s2, b1 = {0.f, 0.f}, b2 = {0.f, 0.f};
#pragma unroll
  for (int i = 0; i < NB; ++i) {
    o[i]->x = cvt_pk(t[2 * i].x, t[2 * i].y);
    if (SUM) { a1 += t[2 * i]; a2 += t[2 * i] * t[2 * i]; }
    o[i]->y = cvt_pk(t[2 * i + 1].x, t[2 * i + 1].y);
    if (SUM) { if (i == 0) { b1 = t[1]; b2 = t[1] * t[1]; } else { b1 += t[2 * i + 1]; b2 += t[2 * i + 1] * t[2 * i + 1]; } }
  }
  if (SUM) { s1 = a1 + b1; s2 = a2 + b2; }
  SB();
}
template <bool SUM>
DEV void gelu_row(int m, const f32x4 (&acc)[4][5], u32x2 (&oa)[4][4], u32x2 (&ob)[2], f32x2& s1, f32x2& s2) {
  if (m >= 2) { const f32x4* const v[5] = {&acc[m][0], &acc[m][1], &acc[m][2], &acc[m][3], &acc[m][4]}; u32x2* const o[5] = {&oa[m][0], &oa[m][1], &oa[m][2], &oa[m][3], &ob[m & 1]}; gelu_blocks<5, SUM>(v, o, s1, s2); }
  else { const f32x4* const v[4] = {&acc[m][0], &acc[m][1], &acc[m][2], &acc[m][3]}; u32x2* const o[4] = {&oa[m][0], &oa[m][1], &oa[m][2], &oa[m][3]}; gelu_blocks<4, SUM>(v, o, s1, s2); }
}

struct TAddr { unsigned w[2][2], z[2][2], s[2]; };
struct TSet { bf16x8 w[4]; s16x4 t[5][2]; };
struct TFrag { TSet X, Y; };
template <int PS, int OFF, int KK, bool MM, bool FIRST, bool MOVE>
DEV void t_half(TAddr& A, int dlt, f32x4 (&acc)[4][5], const TSet& c, TSet& n, const f32x4* init) {
#pragma unroll
  for (int i = 0; i < 18; ++i) {
    const int m = i < 8 ? i >> 2 : 2 + (i - 8) / 5, nb = i < 8 ? (i & 3) : (i - 8) % 5;
    const f32x4 zero4 = {0, 0, 0, 0};
    if (MM) acc[m][nb] = MFMA(cat(c.t[nb][0], c.t[nb][1]), c.w[m], !FIRST ? acc[m][nb] : PS == 1024 ? init[m] : zero4);
    SB();
    auto rd = [&](int r) {
      if (r == 0) n.w[0] = ld128(A.w[KK][0] + OFF);
      else if (r < 9) { const int b = (r - 1) >> 1, u = (r - 1) & 1; n.t[b][u] = ldtr(A.z[u][b & 1] + OFF + KK * 9216 + (b >> 1) * PS); }
      else if (r == 9) n.w[1] = ld128(A.w[KK][0] + OFF + 2048);
      else if (r == 10) n.w[2] = ld128(A.w[KK][1] + OFF);
      else if (r < 13) n.t[4][r - 11] = ldtr(A.s[r - 11] + OFF + KK * 9216);
      else n.w[3] = ld128(A.w[KK][1] + OFF + 2048);
    };
    if (i < 3) { rd(2 * i); rd(2 * i + 1); }
    else if (i < 11) rd(i + 3);
    else if (MOVE && i < 16) {
      unsigned &a = i < 13 ? A.w[i - 11][0] : i < 15 ? A.z[i - 13][0] : A.s[0], &b = i < 13 ? A.w[i - 11][1] : i < 15 ? A.z[i - 13][1] : A.s[1];
      a += dlt; b += dlt; asm volatile("" : "+v"(a), "+v"(b));
    }
    SB();
  }
}
template <int PS, int OFF, bool FIRST>
DEV void t_step(TAddr& A, int dlt, f32x4 (&acc)[4][5], TFrag& F, const f32x4* init = nullptr) {
  SB(); BAR(); MEMB(); SB();
  t_half<PS, OFF, 0, !FIRST, false, false>(A, 0, acc, F.Y, F.X, nullptr);
  t_half<PS, OFF, 1, true, FIRST, OFF != 0>(A, dlt, acc, F.X, F.Y, init);
  WAITL0();
}
DEV void t_last(f32x4 (&acc)[4][5], TFrag& F) {
#pragma unroll
  for (int m = 0; m < 4; ++m)
#pragma unroll
    for (int n = 0; n < 5; ++n) if (n < 4 || m >= 2) acc[m][n] = MFMA(cat(F.Y.t[n][0], F.Y.t[n][1]), F.Y.w[m], acc[m][n]);
}
template <bool MID, class Mid>
DEV void in_pass(TAddr& A, f32x4 (&acc)[4][5], Mid mid) {
  TFrag F;
  int dlt = 2 * STG;
  WAITL0();
  if (!MID) {
    t_step<64, 0, true>(A, dlt, acc, F);
    for (int k = 0; k < 4; ++k) {
      t_step<64, STG, false>(A, dlt, acc, F);
      dlt = -dlt;
      t_step<64, 0, false>(A, dlt, acc, F);
    }
  } else {
    t_step<64, STG, true>(A, dlt, acc, F);
    for (int k = 0; k < 3; ++k) {
      dlt = -dlt;
      t_step<64, 0, false>(A, dlt, acc, F);
      t_step<64, STG, false>(A, dlt, acc, F);
    }
    t_step<64, 0, false>(A, 0, acc, F);
    SB(); BAR(); MEMB(); mid(); SB();
    t_step<64, STG, false>(A, 0, acc, F);
  }
  SB(); BAR(); SB();
  t_last(acc, F);
}

struct G3P { bf16x8 p[2][5]; s16x4 ps[3]; };
struct G3W { s16x4 w[2][2][2], ws[2]; };
template <int KH>
DEV void g3_readw(G3W& n, unsigned a0, unsigned a1) {
#pragma unroll
  for (int q = 0; q < 8; ++q) n.w[q >> 2][(q >> 1) & 1][q & 1] = ldtr((((q >> 1) & 1) ? a1 : a0) + (2 * KH + (q >> 2)) * 4096 + (q & 1) * 2048);
  n.ws[0] = ldtr(a0 + 16384); n.ws[1] = ldtr(a1 + 16384);
}
template <int KH, bool MM, bool RDW>
DEV void g3_stage(f32x4 (&acc)[5][2], const G3P& cp, const G3W& cw, G3P& np, G3W& nw, unsigned aP, unsigned aS, unsigned a0, unsigned a1) {
  constexpr int M0 = KH ? 3 : 0, NS = KH ? 2 : 3, NM = 20 + 2 * NS, NR = 20 + NS;
  SB(); BAR(); MEMB(); SB();
#pragma unroll
  for (int i = 0; i < NM; ++i) {
    if (MM) {
      if (i >= 10 && i < 10 + 2 * NS) acc[M0 + ((i - 10) >> 1)][i & 1] = MFMA16(cw.ws[i & 1], cp.ps[(i - 10) >> 1], acc[M0 + ((i - 10) >> 1)][i & 1]);
      else { const int ii = i < 10 ? i : i - 2 * NS, P = ii / 10, mm = (ii % 10) >> 1, m = (KH != P) ? (mm + 3) % 5 : mm, cc = ii & 1; acc[m][cc] = MFMA(cat(cw.w[P][cc][0], cw.w[P][cc][1]), cp.p[P][m], acc[m][cc]); }
    }
    SB();
#pragma unroll
    for (int r = (i * 3) / 2; r < ((i + 1) * 3) / 2; ++r) {
      if (r < 18) { const int P = r / 9, q = r % 9;
        if (q < 4) { if (RDW) nw.w[P][q >> 1][q & 1] = ldtr(((q >> 1) ? a1 : a0) + (2 * KH + P) * 4096 + (q & 1) * 2048); }
        else np.p[P][q - 4] = ld128(aP + P * 1024 + (q - 4) * 4096); }
      else if (r < 20) { if (RDW) nw.ws[r - 18] = ldtr(((r - 18) ? a1 : a0) + 16384); }
      else if (r < NR) np.ps[r - 20] = ld64(aS + (M0 + r - 20) * 512);
    }
    SB();
  }
  WAITL0();
}
template <int KH>
DEV void g3_compute(unsigned lds0, int dh, int lane, char* out, int tt, int dd) {
  const int g = lane >> 4, qr = (lane >> 2) & 3, p = lane & 3;
  const int rr = 4 * g + qr;
  unsigned bW[2];
#pragma unroll
  for (int c = 0; c < 2; ++c) bW[c] = lds0 + L_O + rr * 128 + (((4 * dh + p) ^ (((rr >> 1) & 1) | (rr & 4))) << 4) + ((c ^ (p & 1)) * 8);
  const unsigned bP = lds0 + L_P + lane * 16 + KH * 2048, bS = lds0 + L_P + 20480 + lane * 8;
  G3W a0, a1, a2;
  g3_readw<KH>(a0, bW[0], bW[1]); g3_readw<KH>(a1, bW[0] + STGO, bW[1] + STGO); g3_readw<KH>(a2, bW[0] + 2 * STGO, bW[1] + 2 * STGO);
  f32x4 acc[5][2];
#pragma unroll
  for (int m = 0; m < 5; ++m) { acc[m][0] = (f32x4){0, 0, 0, 0}; acc[m][1] = (f32x4){0, 0, 0, 0}; }
  WAITL0();
  SB(); BAR(); SB();
  G3P p0, p1; G3W w0, w1;
  g3_stage<KH, false, false>(acc, p1, a0, p0, w0, bP, bS, 0, 0);
  g3_stage<KH, true, false>(acc, p0, a0, p1, w0, bP + STGP, bS + STGP, 0, 0);
  g3_stage<KH, true, false>(acc, p1, a1, p0, w0, bP + 2 * STGP, bS + 2 * STGP, 0, 0);
  g3_stage<KH, true, true>(acc, p0, a2, p1, w1, bP, bS, bW[0] + 3 * STGO, bW[1] + 3 * STGO);
  unsigned sp = STGP, sw = 4 * STGO;
  for (int j = 4; j < 16; j += 2) {
    g3_stage<KH, true, true>(acc, p1, w1, p0, w0, bP + sp, bS + sp, bW[0] + sw, bW[1] + sw);
    sp = sp == 2 * STGP ? 0 : sp + STGP; sw = sw == 4 * STGO ? 0 : sw + STGO;
    g3_stage<KH, true, true>(acc, p0, w0, p1, w1, bP + sp, bS + sp, bW[0] + sw, bW[1] + sw);
    sp = sp == 2 * STGP ? 0 : sp + STGP; sw = sw == 4 * STGO ? 0 : sw + STGO;
  }
  {
    constexpr int M0 = KH ? 3 : 0, NS = KH ? 2 : 3;
    SB(); BAR(); SB();
#pragma unroll
    for (int i = 0; i < 2 * NS; ++i) { acc[M0 + (i >> 1)][i & 1] = MFMA16(w1.ws[i & 1], p1.ps[i >> 1], acc[M0 + (i >> 1)][i & 1]); SB(); }
#pragma unroll
    for (int i = 0; i < 20; ++i) { const int P = (i >> 1) & 1, m = KH ? i >> 2 : 4 - (i >> 2), cc = i & 1; acc[m][cc] = MFMA(cat(w1.w[P][cc][0], w1.w[P][cc][1]), p1.p[P][m], acc[m][cc]); SB(); }
  }
#pragma unroll
  for (int m = 0; m < 5; ++m)
    if ((m >= 3) != KH) { *(LAS f32x4*)(size_t)(lds0 + (dh * 10 + m * 2) * 1024 + lane * 16) = acc[m][0]; *(LAS f32x4*)(size_t)(lds0 + (dh * 10 + m * 2 + 1) * 1024 + lane * 16) = acc[m][1]; }
  WL0();
  BAR(); MEMB();
  auto rO = RSRC(out, 2048 * 1152);
  const bool sw2 = g & 1;
#pragma unroll
  for (int m = 0; m < 5; ++m)
    if ((m >= 3) == KH) {
      const f32x4 a = acc[m][0] + *(LAS f32x4*)(size_t)(lds0 + (dh * 10 + m * 2) * 1024 + lane * 16), b = acc[m][1] + *(LAS f32x4*)(size_t)(lds0 + (dh * 10 + m * 2 + 1) * 1024 + lane * 16);
      const unsigned a0 = cvt_pk(a[0], a[1]), a1 = cvt_pk(a[2], a[3]), b0 = cvt_pk(b[0], b[1]), b1 = cvt_pk(b[2], b[3]);
      const u32x4 v = {sw2 ? b0 : a0, sw2 ? b1 : a1, sw2 ? a0 : b0, sw2 ? a1 : b1};
      __builtin_amdgcn_raw_buffer_store_b128(v, rO, (tt * 80 + m * 16 + (lane & 15)) * 1152 + dd * 128 + dh * 64 + g * 16, 0, 16);
    }
}

DEV void front(unsigned lds0, int wt, int WF, int lane, int ti, int fj, char* ws) {
  const int r16 = lane & 15, g = lane >> 4, qr = (lane >> 2) & 3, p = lane & 3;
  TAddr A;
  const int t01 = 4 * wt + 2 - 2 * WF, t23 = 4 * wt + 2 * WF;
  auto tblk = [&](int m) { return (m < 2 ? t01 : t23) + (m & 1); };
  A.w[0][0] = lds0 + (16 * t01 + r16) * 128 + ((g ^ (r16 >> 1)) << 4); A.w[1][0] = A.w[0][0] ^ 64;
  A.w[0][1] = lds0 + (16 * t23 + r16) * 128 + ((g ^ (r16 >> 1)) << 4); A.w[1][1] = A.w[0][1] ^ 64;
#pragma unroll
  for (int u = 0; u < 2; ++u) {
    const unsigned b = lds0 + 16384 + (16 * u + 4 * g + qr) * 288 + p * 8;
    A.z[u][0] = b + WF * 128 + (p & 1) * 32; A.z[u][1] = b + WF * 128 + 32 - (p & 1) * 32; A.s[u] = b + 256;
  }
  asm volatile("" : "+v"(A.w[0][0]), "+v"(A.w[0][1]), "+v"(A.w[1][0]), "+v"(A.w[1][1]), "+v"(A.s[0]), "+v"(A.s[1]), "+v"(A.z[0][0]), "+v"(A.z[0][1]), "+v"(A.z[1][0]), "+v"(A.z[1][1]));
  f32x4 acc[4][5];
  in_pass<false>(A, acc, []() {});
  u32x2 ua[4][4], ub[2];
  {
    f32x2 s1[4], s2[4];
#pragma unroll
    for (int m = 0; m < 4; ++m) {
      s1[m] = (f32x2){0.f, 0.f}; s2[m] = (f32x2){0.f, 0.f};
      gelu_row<true>(m, acc, ua, ub, s1[m], s2[m]);
    }
#pragma unroll
    for (int m = 0; m < 4; ++m) *(LAS f32x2*)(size_t)(lds0 + L_T + (16 * tblk(m) + r16) * 64 + (WF * 4 + g) * 8) = (f32x2){s1[m].x + s1[m].y, s2[m].x + s2[m].y};
  }
#pragma unroll
  for (int m = 0; m < 4; ++m) asm volatile("" : "+v"(ua[m][0]), "+v"(ua[m][1]), "+v"(ua[m][2]), "+v"(ua[m][3]));
  asm volatile("" : "+v"(ub[0]), "+v"(ub[1]));
#pragma unroll
  for (int u = 0; u < 2; ++u) { A.z[u][0] -= (p & 1) * 32; A.z[u][1] += (p & 1) * 32; }
  asm volatile("" : "+v"(A.z[0][0]), "+v"(A.z[0][1]), "+v"(A.z[1][0]), "+v"(A.z[1][1]));
  in_pass<true>(A, acc, [&]() {
#pragma unroll
    for (int m = 0; m < 4; ++m) asm volatile("" : "+v"(ua[m][0]), "+v"(ua[m][1]), "+v"(ua[m][2]), "+v"(ua[m][3]));
    asm volatile("" : "+v"(ub[0]), "+v"(ub[1]));
    int lz = lane; asm volatile("" : "+v"(lz));
    auto rZ = RSRC(ws + WS_Z2, FRAG_BYTES);
    const unsigned sl = (lz >> 4) * 16 + ((lz & 15) ^ (4 * (lz >> 5))), dst = (fj * 128 + ti * 8) * CH;
#pragma unroll
    for (int m = 0; m < 4; ++m) {
      const unsigned ar = lds0 + L_R + (16 * tblk(m) + (lz & 15)) * 32;
      const f32x4 q4 = *(LAS f32x4*)(size_t)ar + *(LAS f32x4*)(size_t)(ar + 16);
      const float mean = (q4[0] + q4[2]) * (1.0f / 2304.0f), var = fmaxf((q4[1] + q4[3]) * (1.0f / 2304.0f) - mean * mean, 0.f);
      const float rs = __builtin_amdgcn_rsqf(var + 1e-5f), sh = -mean * rs;
      auto norm = [&](unsigned v) { const f32x2 y = (f32x2){bflo(v), bfhi(v)} * rs + sh; return cvt_pk(y.x, y.y); };
#pragma unroll
      for (int pp = 0; pp < 2; ++pp)
        __builtin_amdgcn_raw_buffer_store_b128((u32x4){norm(ua[m][2 * pp].x), norm(ua[m][2 * pp].y), norm(ua[m][2 * pp + 1].x), norm(ua[m][2 * pp + 1].y)}, rZ, dst + tblk(m) * CH + sl * 16 + WF * 2048 + pp * 1024, 0, 16);
      if (m >= 2) __builtin_amdgcn_raw_buffer_store_b64((u32x2){norm(ub[m & 1].x), norm(ub[m & 1].y)}, rZ, dst + tblk(m) * CH + sl * 8 + 4096, 0, 16);
    }
  });
#pragma unroll
  for (int m = 0; m < 4; ++m) asm volatile("" : "+v"(acc[m][0]), "+v"(acc[m][1]), "+v"(acc[m][2]), "+v"(acc[m][3]));
  asm volatile("" : "+v"(acc[2][4]), "+v"(acc[3][4]));
  const u32x2 tg = *(LAS u32x2*)(size_t)(lds0 + L_TAG);
  const u64 tag = ((u64)(unsigned)__builtin_amdgcn_readfirstlane(tg.y) << 32) | (unsigned)__builtin_amdgcn_readfirstlane(tg.x);
  SB();
  u32x2 za[4][4], zb[2];
#pragma unroll
  for (int m = 0; m < 4; ++m) {
    f32x2 d1, d2;
    gelu_row<false>(m, acc, za, zb, d1, d2);
    asm volatile("" : "+v"(za[m][0]), "+v"(za[m][1]), "+v"(za[m][2]), "+v"(za[m][3]));
    if (m == 0) {
      WV(0);
      if (lane == 0 && __hip_atomic_fetch_add((LAS unsigned*)(size_t)(lds0 + L_CNT), 1u, __ATOMIC_RELAXED, __HIP_MEMORY_SCOPE_WORKGROUP) == 3u)
        __hip_atomic_store((u64*)(ws + WS_F1 + (fj * 16 + ti) * 128), tag, RLX_AGENT);
      SB();
    }
  }
  asm volatile("" : "+v"(zb[0]), "+v"(zb[1]));
#pragma unroll
  for (int u = 0; u < 2; ++u) {
    const unsigned sl = p * 16 + 8 * (g & 1) + 4 * (u ^ (p >> 1)) + qr;
#pragma unroll
    for (int h = 0; h < 2; ++h) A.z[u][h] = lds0 + 16384 + (g >> 1) * CH + WF * 2048 + sl * 16 + ((h ^ (p & 1)) * 8);
    A.s[u] = lds0 + 16384 + (g >> 1) * CH + 4096 + sl * 8;
  }
  asm volatile("" : "+v"(A.s[0]), "+v"(A.s[1]), "+v"(A.z[0][0]), "+v"(A.z[0][1]), "+v"(A.z[1][0]), "+v"(A.z[1][1]));
  f32x4 bias4[4];
#pragma unroll
  for (int m = 0; m < 4; ++m) { const float b = __uint_as_float((unsigned)*(LAS u16*)(size_t)(lds0 + L_BIAS + (tblk(m) * 16 + r16) * 2) << 16); bias4[m] = (f32x4){b, b, b, b}; }
  TFrag F;
  int dlt = 2 * STG;
  WAITL0();
  t_step<1024, 0, true>(A, dlt, acc, F, bias4);
  t_step<1024, STG, false>(A, dlt, acc, F);
  for (int k = 0; k < 15; ++k) {
    dlt = -dlt;
    t_step<1024, 0, false>(A, dlt, acc, F);
    t_step<1024, STG, false>(A, dlt, acc, F);
  }
  auto rP = RSRC(ws + WS_P, FRAG_BYTES);
  auto gate = [&](f32x4 a, u32x2 z) {
    const f32x2 x = (f32x2){a[0], a[1]} * (f32x2){bflo(z.x), bfhi(z.x)}, y = (f32x2){a[2], a[3]} * (f32x2){bflo(z.y), bfhi(z.y)};
    return (u32x2){cvt_pk(x.x, x.y), cvt_pk(y.x, y.y)};
  };
  const unsigned dst0 = ti * 8 * 16 * CH + fj * CH;
  t_last(acc, F);
#pragma unroll
  for (int m = 0; m < 4; ++m) {
#pragma unroll
    for (int pp = 0; pp < 2; ++pp) {
      const u32x2 a = gate(acc[m][2 * pp], za[m][2 * pp]), b = gate(acc[m][2 * pp + 1], za[m][2 * pp + 1]);
      __builtin_amdgcn_raw_buffer_store_b128((u32x4){a.x, a.y, b.x, b.y}, rP, dst0 + tblk(m) * 16 * CH + lane * 16 + (2 * WF + pp) * 1024, 0, 16);
    }
    if (m >= 2) __builtin_amdgcn_raw_buffer_store_b64(gate(acc[m][4], zb[m & 1]), rP, dst0 + tblk(m) * 16 * CH + lane * 8 + 4096, 0, 16);
  }
  WV(0);
  if (lane == 0 && __hip_atomic_fetch_add((LAS unsigned*)(size_t)(lds0 + L_CNT), 1u, __ATOMIC_RELAXED, __HIP_MEMORY_SCOPE_WORKGROUP) == 7u)
    __hip_atomic_store((u64*)(ws + WS_F2 + (ti * 16 + fj) * 128), tag, RLX_AGENT);
  SB(); BAR(); MEMB(); SB();
}

template <int NP> DEV unsigned pollN(const char* base, u64 tag, int lane) {
  const u64 v = __hip_atomic_load((const u64*)(base + min(lane, NP - 1) * 128), RLX_AGENT);
  return (unsigned)__builtin_amdgcn_ballot_w64(v == tag) & ((1u << NP) - 1);
}
DEV bool got(unsigned mask, int who) { return (mask >> who) & 1u; }

__global__ __launch_bounds__(512, 2) void gmlp_fused(const char* __restrict__ X, const char* __restrict__ Win, const char* __restrict__ Wsp, const u16* __restrict__ bias,
                                                     const char* __restrict__ Wo, char* __restrict__ out, char* __restrict__ ws) {
  extern __shared__ __attribute__((aligned(16))) char smem[];
  u64 pc0; asm volatile("s_getpc_b64 %0" : "=s"(pc0));
  const int tid = threadIdx.x, wid = __builtin_amdgcn_readfirstlane(tid >> 6), lane = tid & 63;
  const int bid = blockIdx.x, xg = bid & 7, rk = bid >> 3, il = rk & 7, jl = rk >> 3, ti = (xg & 1) * 8 + il, fj = (xg >> 1) * 4 + jl;
  const int L3 = xg * 30 + rk, tt = L3 / 9, dd = L3 - tt * 9;
  const bool has3 = rk < 30 && L3 < 234;
  const unsigned lds0 = (unsigned)(size_t)(LAS char*)smem;
  if (__builtin_expect(wid >= 4, 1)) {
    __builtin_amdgcn_s_setprio(3);
    const int lw = wid - 4;
    const bool big = wid < 6;
#define WVS(STAGES, EXTRA) do { if (big) WV(9 * (STAGES) + (EXTRA)); else WV(8 * (STAGES) + (EXTRA)); } while (0)
    auto rX = RSRC(X, 2048 * 1152); auto rWi = RSRC(Win, 576 * 9216);
    unsigned vX[4], vW[5];
    const unsigned zp0 = lw < 2 ? lw * 5 : 2 + lw * 4;
    const unsigned sX = (ti * 128 + lw * 32) * 1152, sW = fj * 288;
    const int rot8 = (((il + jl) % 3) * 8) / 3;
    auto k1 = [&](int k) { return k == 0 ? 0 : 1 + ((k - 1 + rot8) & 7); };
    auto issue1 = [&](int half, int k) {
      const int kk = k1(k);
      const unsigned d = lds0 + ((k + 1 - half) & 3) * STG, dz = d + 16384 + zp0 * 1024, sw = sW + half * 4608 + kk * 64 * 9216;
      GL4(rX, d + lw * 4096, vX[0], vX[1], vX[2], vX[3], sX + kk * 128);
      GL4(rWi, dz, vW[0], vW[1], vW[2], vW[3], sw);
      if (big) GL(rWi, dz + 4096, vW[4], sw, 0);
    };
    {
#pragma unroll
      for (int q = 0; q < 4; ++q) { const int row = 8 * q + (lane >> 3); vX[q] = row * 1152 + (((lane & 7) ^ ((row >> 1) & 7)) << 4) - q * 1024; }
      GL4(rX, lds0 + lw * 4096, vX[0], vX[1], vX[2], vX[3], sX + k1(0) * 128);
      SB();
#pragma unroll
      for (int q = 0; q < 5; ++q) {
        const unsigned c16 = (zp0 + q) * 64 + lane, R = (c16 * 3641u) >> 16, c = (c16 - R * 18) * 16, k = (R & 32) + 8 * ((R >> 2) & 3) + 4 * ((R >> 4) & 1) + (R & 3);
        vW[q] = k * 9216 + c - (q & 3) * 1024;
      }
      const unsigned dz = lds0 + 16384 + zp0 * 1024, sw = sW + 4608 + k1(0) * 64 * 9216;
      GL4(rWi, dz, vW[0], vW[1], vW[2], vW[3], sw);
      if (big) GL(rWi, dz + 4096, vW[4], sw, 0);
    }
    const unsigned vL = lane * 16;
    auto rWs = RSRC(Wsp, 2048 * 4096); auto rWo = RSRC(Wo, 2304 * 1152);
    issue1(1, 1);
    WVS(1, 0); BAR();
    issue1(1, 2);
    WVS(1, 0);
    for (int k = 1; k < 5; ++k) { BAR(); issue1(1, k + 2); WVS(1, 0); }
    BAR(); issue1(1, 7); WVS(1, 0);
    BAR(); issue1(1, 8); WVS(1, 0);
    BAR(); issue1(0, 0); WVS(1, 0);
    BAR(); issue1(0, 1);
    const u64 ep = __hip_atomic_load((const u64*)(ws + WS_EP), RLX_AGENT);
    if (lw == 0) __builtin_amdgcn_raw_ptr_buffer_load_lds(RSRC(bias, 4096), (LAS void*)(size_t)(lds0 + L_BIAS), 4, lane * 4, ti * 256, 0, 0);
    TOUCH(rWs, lane * 128, bid * 32768 + lw * 8192);
    BAR();
    issue1(0, 2); issue1(0, 3);
    const unsigned tagA = __builtin_amdgcn_readfirstlane((unsigned)ep) + 0x9E3779B9u, tagB = __builtin_amdgcn_readfirstlane((unsigned)(ep >> 32)) + 0x85EBCA6Bu;
    const u64 tag = ((u64)tagB << 32) | tagA;
    if (lw == 0) { const u32x4 tg = {tagA, tagB, 0u, 0u}; asm volatile("ds_write_b128 %0, %1\n\ts_waitcnt lgkmcnt(0)" :: "v"(lds0 + L_TAG), "v"(tg) : "memory"); }
    unsigned vS[4];
#pragma unroll
    for (int q = 0; q < 4; ++q) { const int row = 8 * q + (lane >> 3); vS[q] = row * 4096 + (((lane & 7) ^ ((row >> 1) & 7)) << 4) - q * 1024; }
    const unsigned sWs = (ti * 128 + lw * 32) * 4096;
    auto issueW = [&](int n) { GL4(rWs, lds0 + (n & 3) * STG + lw * 4096, vS[0], vS[1], vS[2], vS[3], sWs + n * 128); };
    WVS(2, 1);
    BAR();
    auto rS = RSRC(ws + WS_ST, 16 * 16 * 128 * 16);
    if (lane < 32) {
      const int row = lw * 32 + lane;
      f32x4 v0, v1, v2, v3;
      asm volatile("ds_read_b128 %0, %4\n\tds_read_b128 %1, %4 offset:16\n\tds_read_b128 %2, %4 offset:32\n\tds_read_b128 %3, %4 offset:48\n\ts_waitcnt lgkmcnt(0)"
                   : "=&v"(v0), "=&v"(v1), "=&v"(v2), "=&v"(v3) : "v"(lds0 + L_T + row * 64) : "memory");
      const f32x4 s = (v0 + v1) + (v2 + v3);
      u32x4 gq; gq.x = tagA; gq.y = __float_as_uint(s[0] + s[2]); gq.z = tagB; gq.w = __float_as_uint(s[1] + s[3]);
      __builtin_amdgcn_raw_buffer_store_b128(gq, rS, ((ti * 16 + fj) * 128 + row) * 16, 0, 16);
    }
    u32x4 sv[8];
    const u32x4 srd = {(unsigned)(size_t)(ws + WS_ST), (unsigned)((size_t)(ws + WS_ST) >> 32) & 0xffffu, 16 * 16 * 128 * 16, 0x00020000u};
    auto sweep = [&]() {
      const unsigned s0 = (ti * 16 + lw * 4) * 2048, v1 = vL + 4096;
      asm volatile("buffer_load_dwordx4 %0, %8, %10, %11 offen sc1\n\tbuffer_load_dwordx4 %1, %8, %10, %11 offen offset:1024 sc1\n\tbuffer_load_dwordx4 %2, %8, %10, %11 offen offset:2048 sc1\n\t"
                   "buffer_load_dwordx4 %3, %8, %10, %11 offen offset:3072 sc1\n\tbuffer_load_dwordx4 %4, %9, %10, %11 offen sc1\n\tbuffer_load_dwordx4 %5, %9, %10, %11 offen offset:1024 sc1\n\t"
                   "buffer_load_dwordx4 %6, %9, %10, %11 offen offset:2048 sc1\n\tbuffer_load_dwordx4 %7, %9, %10, %11 offen offset:3072 sc1"
                   : "=&v"(sv[0]), "=&v"(sv[1]), "=&v"(sv[2]), "=&v"(sv[3]), "=&v"(sv[4]), "=&v"(sv[5]), "=&v"(sv[6]), "=&v"(sv[7]) : "v"(vL), "v"(v1), "s"(srd), "s"(s0) : "memory");
    };
    BAR(); issue1(0, 4); WVS(2, 1);
    BAR(); issue1(0, 5); WVS(2, 0);
    BAR(); issue1(0, 6); WVS(2, 0);
    BAR(); issue1(0, 7); WVS(2, 0);
    BAR(); sweep(); WVS(1, 8);
    BAR(); issue1(0, 8);
    WVS(1, 8);
    BAR();
    {
      f32x4 s;
      auto total = [&](const u32x4 (&v)[8]) {
        unsigned bad = 0;
        s = (f32x4){0.f, 0.f, 0.f, 0.f};
#pragma unroll
        for (int p = 0; p < 8; ++p) { bad |= (v[p].x ^ tagA) | (v[p].z ^ tagB); s[(p & 1) * 2] += __uint_as_float(v[p].y); s[(p & 1) * 2 + 1] += __uint_as_float(v[p].w); }
        return __builtin_amdgcn_ballot_w64(bad != 0) == 0;
      };
      WVS(1, 0);
      asm volatile("" : "+v"(sv[0]), "+v"(sv[1]), "+v"(sv[2]), "+v"(sv[3]), "+v"(sv[4]), "+v"(sv[5]), "+v"(sv[6]), "+v"(sv[7]) :: "memory");
      if (__builtin_expect(!total(sv), 0)) {
#pragma unroll 1
        for (unsigned spins = 0;; ++spins) {
          u32x4 rv[8];
          MEMB();
#pragma unroll
          for (int p = 0; p < 8; ++p) rv[p] = __builtin_amdgcn_raw_buffer_load_b128(rS, vL + (p & 1) * 1024, (ti * 16 + lw * 4 + (p >> 1)) * 2048, 16);
          if (total(rv)) break;
          if (spins > SPIN_CAP) { s[0] = s[2] = __builtin_nanf(""); break; }
        }
      }
      const f32x2 o0 = {s[0], s[1]}, o1 = {s[2], s[3]};
      asm volatile("ds_write_b64 %0, %1\n\tds_write_b64 %0, %2 offset:2048\n\ts_waitcnt lgkmcnt(0)" :: "v"(lds0 + L_R + lane * 32 + lw * 8), "v"(o0), "v"(o1) : "memory");
    }
    BAR();
    WV(0);
    BAR();
    issueW(0); issueW(2);
    BAR();
    issueW(1); issueW(3);
    TOUCH(rWo, lane < 21 ? lane * 128 : OOB, bid * 10368 + lw * 2688);
    auto rZ = RSRC(ws + WS_Z2, FRAG_BYTES);
    const unsigned sZ = fj * 128 * CH + zp0 * 1024;
    unsigned ready = 0;
    auto issueZ = [&](int n) {
      if (__builtin_expect(!got(ready, n >> 1), 0)) { for (unsigned spins = 0; !got(ready, n >> 1) && spins < SPIN_CAP; ++spins) ready = pollN<16>(ws + WS_F1 + fj * 16 * 128, tag, lane); }
      MEMB();
      const unsigned dz = lds0 + (n & 3) * STG + 16384 + zp0 * 1024, so = sZ + n * 18432;
      GS4(rZ, dz, vL, so);
      if (big) GS(rZ, dz + 4096, vL, so + 4096, 0);
    };
    const char* f1 = ws + WS_F1 + fj * 16 * 128;
    for (int q = 0; q < 7; ++q) __builtin_amdgcn_s_sleep(4);
    for (unsigned np = 0; ready != 7u && np < SPIN_CAP; ++np) ready = pollN<3>(f1, tag, lane);
    issueZ(0); issueZ(1); issueZ(2);
    WVS(2, -8);
    BAR();
    u64 fv;
    asm volatile("global_load_dwordx2 %0, %1, %2 sc1" : "=&v"(fv) : "v"((lane & 15) * 128), "s"(f1) : "memory");
    issueZ(3);
    WVS(2, -7);
    BAR();
    issueW(4); issueZ(4);
    WVS(2, -3);
    BAR();
    issueW(5); issueZ(5);
    WVS(2, 0);
    asm volatile("" : "+v"(fv));
    ready |= (unsigned)__builtin_amdgcn_ballot_w64(fv == tag) & 0xffffu;
    BAR();
    for (int n = 6; n < 32; ++n) {
      issueW(n); issueZ(n);
      WVS(2, 0);
      BAR();
    }
    unsigned vO[2], vOh;
#pragma unroll
    for (int q = 0; q < 2; ++q) { const int row = 36 * lw + 8 * q + (lane >> 3); vO[q] = row * 1152 + (((lane & 7) ^ (((row >> 1) & 1) | (row & 4))) << 4); }
    { const int row = 36 * lw + 32 + ((lane >> 3) & 3); vOh = row * 1152 + (((lane & 7) ^ (((row >> 1) & 1) | (row & 4))) << 4); }
    const unsigned vO1 = vO[1] - 1024, vO2 = vO[0] + 16 * 1152 - 2048, vO3 = vO[1] + 16 * 1152 - 3072;
    const unsigned sO = min(dd, 8) * 128;
    auto issueO = [&](int n, int slot) {
      const unsigned d = lds0 + L_O + slot * STGO + lw * 4608, s0 = sO + n * 144 * 1152;
      GL4(rWo, d, vO[0], vO1, vO2, vO3, s0);
      if (lane < 32) GL(rWo, d + 4096, vOh, s0, 0);
    };
    issueO(0, 0);
    WVS(1, 5); BAR();
    issueO(1, 1); issueO(2, 2);
    WV(15); BAR();
    WV(0);
    BAR();
    if (!has3) return;
    issueO(3, 3); issueO(4, 4);
    BAR();
    issueO(5, 0);
    auto rP = RSRC(ws + WS_P, FRAG_BYTES);
    const unsigned vPs = lw < 3 ? (lane >> 5) * 16 * CH + (lane & 31) * 16 + 4096 : OOB;
    const int limS = lw == 2 ? 32 : 64;
    const unsigned sP = tt * 5 * 16 * CH;
    const int pa = (tt * 80) >> 7, pb = min((tt * 80 + 79) >> 7, 15);
    unsigned rdy = 0;
    auto issueP = [&](int j, int slot) {
      if (__builtin_expect(!got(rdy, j), 0)) { for (unsigned spins = 0; !got(rdy, j) && spins < SPIN_CAP; ++spins) rdy = pollN<16>(ws + WS_F2 + pa * 16 * 128, tag, lane) & pollN<16>(ws + WS_F2 + pb * 16 * 128, tag, lane); }
      MEMB();
      const unsigned d = lds0 + L_P + slot * STGP, sp = sP + j * CH;
      GS4(rP, d + lw * 4096, vL, sp + lw * 16 * CH);
      GS(rP, d + 16384 + lw * 1024, vL, sp + 64 * CH + lw * 1024, 0);
      if (lane < limS) GS(rP, lw < 3 ? d + 20480 + lw * 1024 : lds0 + L_DUMMY, vPs, sp + lw * 32 * CH, 0);
    };
    const char *fa = ws + WS_F2 + pa * 16 * 128, *fb = ws + WS_F2 + pb * 16 * 128;
    for (unsigned np = 0; rdy != 7u && np < SPIN_CAP; ++np) rdy = pollN<3>(fa, tag, lane) & pollN<3>(fb, tag, lane);
    u64 fva, fvb;
    asm volatile("global_load_dwordx2 %0, %2, %3 sc1\n\tglobal_load_dwordx2 %1, %2, %4 sc1" : "=&v"(fva), "=&v"(fvb) : "v"((lane & 15) * 128), "s"(fa), "s"(fb) : "memory");
    issueP(0, 0); issueP(1, 1); issueP(2, 2);
    WV(12);
    asm volatile("" : "+v"(fva), "+v"(fvb));
    rdy |= (unsigned)(__builtin_amdgcn_ballot_w64(fva == tag) & __builtin_amdgcn_ballot_w64(fvb == tag)) & 0xffffu;
    BAR();
    WV(6);
    BAR(); issueP(3, 0); issueO(6, 1); WV(11);
    BAR(); issueP(4, 1); issueO(7, 2); WV(16);
    BAR(); issueP(5, 2); WV(11);
    BAR(); issueP(6, 0); issueO(8, 3); WV(11);
    int sp3 = 1, so5 = 4;
    for (int n = 5; n < 12; ++n) {
      BAR();
      issueP(n + 2, sp3); issueO(n + 4, so5);
      sp3 = sp3 == 2 ? 0 : sp3 + 1; so5 = so5 == 4 ? 0 : so5 + 1;
      WV(16);
    }
    BAR(); issueP(14, 2); WV(6);
    BAR(); issueP(15, 0); WV(6);
    BAR(); WV(0);
    BAR();
    BAR(); BAR();
    if (bid == 0 && tid == 256) __hip_atomic_store((u64*)(ws + WS_EP), tag, RLX_AGENT);
  } else {
    const int wt = wid >> 1, wf = wid & 1;
    if (wid == 0) {
      auto rC = RSRC((const char*)(pc0 & ~(u64)255), CODE_KB * 1024);
      GL(rC, lds0 + L_DUMMY, lane * 16, rk * 1024, 0); GL(rC, lds0 + L_DUMMY, lane * 16, rk * 1024 + 32768, 0);
      WV(0);
    }
    front(lds0, wt, wf, lane, ti, fj, ws);
    if (!has3) return;
    if (wt == 0) g3_compute<0>(lds0, wf, lane, out, tt, dd); else g3_compute<1>(lds0, wf, lane, out, tt, dd);
  }
}

extern "C" void kernel_launch(void* const* d_in, const int* in_sizes, int n_in, void* d_out, int out_size, void* d_ws, size_t ws_size, hipStream_t stream) {
  (void)hipFuncSetAttribute((const void*)gmlp_fused, hipFuncAttributeMaxDynamicSharedMemorySize, LDS_BYTES);
  gmlp_fused<<<256, 512, LDS_BYTES, stream>>>((const char*)d_in[0], (const char*)d_in[1], (const char*)d_in[2], (const u16*)d_in[3], (const char*)d_in[4], (char*)d_out, (char*)d_ws);
}
```
